```python
import torch

def _softmax(x, axis=-1):
    return torch.softmax(x, dim=axis)
def _gelu(x):
    return torch.nn.functional.gelu(x, approximate="tanh")
def _silu(x):
    return torch.nn.functional.silu(x)
def _sigmoid(x):
    return torch.sigmoid(x)
def _layernorm(x, axis=-1, eps=1e-5):
    m = x.mean(dim=axis, keepdim=True)
    v = x.var(dim=axis, keepdim=True, unbiased=False)
    return (x - m) / torch.sqrt(v + eps)
def _scatter_add(tgt, idx, src, axis=0):
    return tgt.clone().index_add_(axis, idx, src)
def _segment_sum(src, seg, n):
    out = torch.zeros((n,) + src.shape[1:], dtype=src.dtype)
    return out.index_add_(0, seg, src)
def _pad(x, pw):
    flat = tuple(v for pair in reversed(pw) for v in pair)
    return torch.nn.functional.pad(x, flat)

kernel_name = 'gated_mix__ssm_conv_s5__glu_gated_reglu__prompt'

def setup_inputs():

    _w = (lambda a: (lambda t: t.to(torch.bfloat16) if t.is_floating_point() else t)(torch.as_tensor(a)))
    def _A_setup():
        B, S, D = 1, 2048, 1024
        P = D
        Lambda_re = -0.5 * (lambda _x: torch.exp(torch.as_tensor(_x)))((lambda *s: torch.randn(*s, dtype=torch.bfloat16) * 0.2)(P))
        Lambda_im = (lambda *s: torch.randn(*s, dtype=torch.bfloat16) * 0.4)(P) * 1.0
        B_re = (lambda *s: torch.randn(*s, dtype=torch.bfloat16) * 0.2)(P, D) * (D ** -0.5)
        B_im = (lambda *s: torch.randn(*s, dtype=torch.bfloat16) * 0.2)(P, D) * (D ** -0.5)
        C_re = (lambda *s: torch.randn(*s, dtype=torch.bfloat16) * 0.2)(D, P) * (P ** -0.5)
        C_im = (lambda *s: torch.randn(*s, dtype=torch.bfloat16) * 0.2)(D, P) * (P ** -0.5)
        log_dt = (lambda *s: torch.randn(*s, dtype=torch.bfloat16) * 0.2)(P) * 0.3 - 2.0
        Dskip = (lambda *s: torch.randn(*s, dtype=torch.bfloat16) * 0.05)(D)
        return {
            "x": _w((lambda *s: torch.randn(*s, dtype=torch.bfloat16) * 0.05)(B, S, D)),
            "Lambda_re": _w(Lambda_re),
            "Lambda_im": _w(Lambda_im),
            "B_re": _w(B_re),
            "B_im": _w(B_im),
            "C_re": _w(C_re),
            "C_im": _w(C_im),
            "log_dt": _w(log_dt),
            "Dskip": _w(Dskip),
        }
    _A_in = _A_setup()

    def _B_setup():
        B, S, D, F = 1, 2048, 1024, 3584
        return {
            "x": _w((lambda *s: torch.randn(*s, dtype=torch.bfloat16) * 0.05)(B, S, D)),
            "W_gate": _w((lambda *s: torch.randn(*s, dtype=torch.bfloat16) * 0.8)(D, F) * (2.0 / D) ** 0.5),
            "W_up": _w((lambda *s: torch.randn(*s, dtype=torch.bfloat16) * 0.8)(D, F) * (2.0 / D) ** 0.5),
            "W_down": _w((lambda *s: torch.randn(*s, dtype=torch.bfloat16) * 0.8)(F, D) * (1.0 / F) ** 0.5),
        }
    _B_in = _B_setup()

    return {
        "A_x": _A_in["x"],
        "A_Lambda_re": _A_in["Lambda_re"],
        "A_Lambda_im": _A_in["Lambda_im"],
        "A_B_re": _A_in["B_re"],
        "A_B_im": _A_in["B_im"],
        "A_C_re": _A_in["C_re"],
        "A_C_im": _A_in["C_im"],
        "A_log_dt": _A_in["log_dt"],
        "A_Dskip": _A_in["Dskip"],
        "B_x": _B_in["x"],
        "B_W_gate": _B_in["W_gate"],
        "B_W_up": _B_in["W_up"],
        "B_W_down": _B_in["W_down"],
        "gate_w": _w((lambda *s: torch.randn(*s, dtype=torch.bfloat16) * 0.05)(1024) * 16.0),
    }

def reference(A_x, A_Lambda_re, A_Lambda_im, A_B_re, A_B_im, A_C_re, A_C_im, A_log_dt, A_Dskip, B_x, B_W_gate, B_W_up, B_W_down, gate_w):
    def _A_ref(x, Lambda_re, Lambda_im, B_re, B_im, C_re, C_im, log_dt, Dskip):
        Bsz, L, H = x.shape
        Lam = Lambda_re + 1j * Lambda_im
        Bmat = B_re + 1j * B_im
        Cmat = C_re + 1j * C_im
        dt = torch.exp(log_dt)
        Lam_bar = torch.exp(dt * Lam)
        B_bar = ((Lam_bar - 1.0) / Lam)[:, None] * Bmat
        Bu = torch.einsum("bld,pd->blp", x + 0j, B_bar)
        h = torch.zeros_like(Bu[:, 0, :])
        hs = []
        for t in range(L):
            h = Lam_bar[None, :] * h + Bu[:, t, :]
            hs.append(h)
        hseq = torch.stack(hs, 1)
        y = 2.0 * torch.einsum("blp,dp->bld", hseq, Cmat).real + x * Dskip[None, None, :]
        return y

    def _B_ref(x, W_gate, W_up, W_down):
        gate = torch.einsum("bsd,df->bsf", x, W_gate)
        up = torch.einsum("bsd,df->bsf", x, W_up)
        gate = gate * (lambda x, dt: x.to(dt))(gate > 0, gate.dtype)
        h = gate * up
        return torch.einsum("bsf,fd->bsd", h, W_down)

    x = A_x
    g = _sigmoid((x * gate_w).sum(-1, keepdims=True))
    a = _A_ref(x, A_Lambda_re, A_Lambda_im, A_B_re, A_B_im, A_C_re, A_C_im, A_log_dt, A_Dskip)
    b = _B_ref(x, B_W_gate, B_W_up, B_W_down)
    return g * a + (1 - g) * b
```

```cpp
#include <hip/hip_runtime.h>
#include <hip/hip_bf16.h>
typedef unsigned short u16;
__device__ __forceinline__ float bf2f(u16 b){ return __uint_as_float(((unsigned)b)<<16); }
__device__ __forceinline__ float rbf(float f){ unsigned u=__float_as_uint(f); u += 0x7FFFu + ((u>>16)&1u); return __uint_as_float(u & 0xFFFF0000u); }
constexpr int L=2048, D=1024, P=1024, F=3584;

__global__ void n_g(const u16* x, const u16* gw, float* g){
  int t=blockIdx.x; float s=0; for(int d=threadIdx.x; d<D; d+=64) s += bf2f(x[t*D+d])*bf2f(gw[d]);
  for(int o=32;o>=1;o>>=1) s += __shfl_xor(s,o);
  if(threadIdx.x==0) g[t]=1.f/(1.f+expf(-s));
}
__global__ void n_bu(const u16* x,const u16* lre,const u16* lim,const u16* bre,const u16* bim,const u16* ldt,float2* bu){
  int p=blockIdx.x*64+threadIdx.x; int t=blockIdx.y;
  float sr=0,si=0;
  for(int d=0;d<D;++d){ float xv=bf2f(x[t*D+d]); sr+=xv*bf2f(bre[p*D+d]); si+=xv*bf2f(bim[p*D+d]); }
  float dt=rbf(expf(bf2f(ldt[p]))); float lr=bf2f(lre[p]), li=bf2f(lim[p]);
  float e=expf(dt*lr); float ar=e*cosf(dt*li), ai=e*sinf(dt*li);
  float nr=ar-1.f, ni=ai; float den=lr*lr+li*li; float cr=(nr*lr+ni*li)/den, ci=(ni*lr-nr*li)/den;
  bu[t*P+p]=make_float2(cr*sr-ci*si, cr*si+ci*sr);
}
__global__ void n_scan(const u16* lre,const u16* lim,const u16* ldt,float2* bu){
  int p=blockIdx.x*64+threadIdx.x;
  float dt=rbf(expf(bf2f(ldt[p]))); float lr=bf2f(lre[p]), li=bf2f(lim[p]);
  float e=expf(dt*lr); float ar=e*cosf(dt*li), ai=e*sinf(dt*li);
  float hr=0,hi=0;
  for(int t=0;t<L;++t){ float2 b=bu[t*P+p]; float nr=ar*hr-ai*hi+b.x, ni=ar*hi+ai*hr+b.y; hr=nr;hi=ni; bu[t*P+p]=make_float2(hr,hi); }
}
__global__ void n_ffn1(const u16* x,const u16* wg,const u16* wu,float* hf){
  int f=blockIdx.x*64+threadIdx.x; int t=blockIdx.y; float sg=0,su=0;
  for(int d=0;d<D;++d){ float xv=bf2f(x[t*D+d]); sg+=xv*bf2f(wg[d*F+f]); su+=xv*bf2f(wu[d*F+f]); }
  hf[t*F+f]=rbf(fmaxf(sg,0.f)*su);
}
__global__ void n_out(const u16* x,const u16* cre,const u16* cim,const u16* dsk,const u16* wd,const float2* h,const float* hf,const float* g,float* out){
  int d=blockIdx.x*64+threadIdx.x; int t=blockIdx.y; float s=0, b=0;
  for(int p=0;p<P;++p){ float2 hv=h[t*P+p]; s+=rbf(hv.x)*bf2f(cre[d*P+p])-rbf(hv.y)*bf2f(cim[d*P+p]); }
  for(int f=0;f<F;++f) b+=hf[t*F+f]*bf2f(wd[f*D+d]);
  float gg=g[t];
  out[t*D+d]=gg*(2.f*s+bf2f(x[t*D+d])*bf2f(dsk[d]))+(1.f-gg)*b;
}
extern "C" void kernel_launch(void* const* d_in, const int* in_sizes, int n_in, void* d_out, int out_size, void* d_ws, size_t ws_size, hipStream_t stream){
  const u16* x=(const u16*)d_in[0]; const u16* lre=(const u16*)d_in[1]; const u16* lim=(const u16*)d_in[2];
  const u16* bre=(const u16*)d_in[3]; const u16* bim=(const u16*)d_in[4]; const u16* cre=(const u16*)d_in[5]; const u16* cim=(const u16*)d_in[6];
  const u16* ldt=(const u16*)d_in[7]; const u16* dsk=(const u16*)d_in[8]; const u16* wg=(const u16*)d_in[10]; const u16* wu=(const u16*)d_in[11]; const u16* wd=(const u16*)d_in[12]; const u16* gw=(const u16*)d_in[13];
  char* ws=(char*)d_ws; float2* bu=(float2*)ws; float* hf=(float*)(ws+(size_t)L*P*8); float* g=(float*)(ws+(size_t)L*P*8+(size_t)L*F*4);
  n_g<<<L,64,0,stream>>>(x,gw,g);
  n_bu<<<dim3(P/64,L),64,0,stream>>>(x,lre,lim,bre,bim,ldt,bu);
  n_scan<<<P/64,64,0,stream>>>(lre,lim,ldt,bu);
  n_ffn1<<<dim3(F/64,L),64,0,stream>>>(x,wg,wu,hf);
  n_out<<<dim3(D/64,L),64,0,stream>>>(x,cre,cim,dsk,wd,bu,hf,g,(float*)d_out);
}
```

```cpp
#include <hip/hip_runtime.h>
#include <hip/hip_bf16.h>

typedef unsigned short u16;
typedef short bf16x8 __attribute__((ext_vector_type(8)));
typedef short s16x4 __attribute__((ext_vector_type(4)));
typedef float f32x4 __attribute__((ext_vector_type(4)));
typedef float f32x2 __attribute__((ext_vector_type(2)));
typedef unsigned u32x4 __attribute__((ext_vector_type(4)));
typedef unsigned u32x2 __attribute__((ext_vector_type(2)));
typedef unsigned long long u64;
#define LAS __attribute__((address_space(3)))
#define GAS __attribute__((address_space(1)))
#define RLX_AGENT __ATOMIC_RELAXED, __HIP_MEMORY_SCOPE_AGENT
typedef __amdgpu_buffer_rsrc_t rsrc_t;

constexpr int L_ = 2048, D_ = 1024, P_ = 1024, F_ = 3584;
constexpr int NKF = 176;
constexpr size_t WS_H = 0;
constexpr size_t H_BYTES = (size_t)16 * NKF * 8 * 1024;
constexpr size_t WS_XB = WS_H + H_BYTES;
constexpr size_t XB_BYTES = (size_t)128 * 2 * 32768;
constexpr size_t WS_G = WS_XB + XB_BYTES;
constexpr size_t WS_AG = WS_G + 8192;
constexpr size_t WS_EP = WS_AG + (size_t)32 * 8 * 2 * 32 * 8;
constexpr size_t WS_XF = WS_EP + 32 * 128;
constexpr size_t WS_END = WS_XF + 256 * 128;

__device__ __forceinline__ float bf2f(unsigned b) { return __uint_as_float(b << 16); }
__device__ __forceinline__ unsigned pk_bf16(float lo, float hi) {
  unsigned r; asm("v_cvt_pk_bf16_f32 %0, %1, %2" : "=v"(r) : "v"(lo), "v"(hi)); return r; }
__device__ __forceinline__ rsrc_t mk_rsrc(const void* p, unsigned bytes) {
  return __builtin_amdgcn_make_buffer_rsrc((void*)p, (short)0, (int)bytes, 0x00020000); }
__device__ __forceinline__ void dma16(unsigned ldsaddr, unsigned voff, rsrc_t srd, unsigned soff) {
  asm volatile("s_mov_b32 m0, %0\n\ts_nop 0\n\tbuffer_load_dwordx4 %1, %2, %3 offen lds"
               :: "s"(ldsaddr), "v"(voff), "s"(srd), "s"(soff) : "memory"); }
#define WAIT_V(n) asm volatile("s_waitcnt vmcnt(" #n ")" ::: "memory")
#define WAIT_L0() do { __builtin_amdgcn_s_waitcnt(0xC07F); asm volatile("" ::: "memory"); } while (0)
__device__ __forceinline__ int fresh_lane() { int l = __builtin_amdgcn_mbcnt_hi(~0u, __builtin_amdgcn_mbcnt_lo(~0u, 0u)); asm volatile("" : "+v"(l)); return l; }
#define BAR() do { __builtin_amdgcn_sched_barrier(0); __builtin_amdgcn_s_barrier(); __builtin_amdgcn_sched_barrier(0); } while (0)
typedef LAS char* lptr;
__device__ __forceinline__ lptr mk_l(unsigned a) { lptr p = (lptr)(size_t)a; asm volatile("" : "+v"(p)); return p; }
__device__ __forceinline__ bf16x8 lds_b128(lptr a) { return *(const LAS bf16x8*)a; }
__device__ __forceinline__ bf16x8 lds_tr2(lptr a0, lptr a1) {
  s16x4 lo = __builtin_amdgcn_ds_read_tr16_b64_v4i16((LAS s16x4*)a0);
  s16x4 hi = __builtin_amdgcn_ds_read_tr16_b64_v4i16((LAS s16x4*)a1);
  return __builtin_shufflevector(lo, hi, 0, 1, 2, 3, 4, 5, 6, 7); }
#define MFMA(a, b, c) asm volatile("v_mfma_f32_16x16x32_bf16 %0, %1, %2, %0" : "+v"(c) : "v"(a), "v"(b))
#define PIN(x) asm volatile("" : "+v"(x))
#define MFMA_DRAIN() asm volatile("s_nop 7\n\ts_nop 7" ::: "memory")

struct K1Args {
  const u16 *x, *lre, *lim, *bre, *bim, *ldt, *wg, *wu, *gw;
  char* ws;
};

constexpr unsigned K1_X = 0, K1_W = 65536, K1_BS = 131072, K1_MISC = 147456, K1_LDS = 163840;

template <int GRP>
struct K1Wave {
  static constexpr int NW = GRP == 0 ? 4 : 3;
  f32x4 ag[NW][4], au[NW][4], as[2][4];
  bf16x8 xa[4], wgf[NW], wuf[NW], bsf[2];
  lptr aX[2], aB[2], aW[NW];
  unsigned voffX, voffW, voffB;
  unsigned sX, sW, sB, ldsX, ldsW, ldsB;
  rsrc_t rx, rwg, rwu, rb;

  template <int BUF, int KS> __device__ __forceinline__ void load() {
#pragma unroll
    for (int n = 0; n < NW; ++n) {
      wgf[n] = lds_tr2(aW[n] + BUF * 32768 + KS * 8192, aW[n] + BUF * 32768 + KS * 8192 + 1024);
      wuf[n] = lds_tr2(aW[n] + BUF * 32768 + 16384 + KS * 8192, aW[n] + BUF * 32768 + 16384 + KS * 8192 + 1024);
    }
#pragma unroll
    for (int m = 0; m < 4; ++m) xa[m] = lds_b128(aX[KS] + BUF * 32768 + m * 2048);
#pragma unroll
    for (int i = 0; i < 2; ++i) bsf[i] = lds_b128(aB[KS] + BUF * 8192 + i * 2048);
  }
  __device__ __forceinline__ void compute() {
    __builtin_amdgcn_s_setprio(1);
#pragma unroll
    for (int n = 0; n < NW; ++n)
#pragma unroll
      for (int m = 0; m < 4; ++m) { MFMA(wgf[n], xa[m], ag[n][m]); MFMA(wuf[n], xa[m], au[n][m]); }
#pragma unroll
    for (int i = 0; i < 2; ++i)
#pragma unroll
      for (int m = 0; m < 4; ++m) MFMA(xa[m], bsf[i], as[i][m]);
    __builtin_amdgcn_s_setprio(0);
  }
  template <int BUF> __device__ __forceinline__ void dmaX(int T) {
#pragma unroll
    for (int s = 0; s < 4; ++s) dma16(ldsX + BUF * 32768 + s * 2048, voffX, rx, sX + T * 128 + s * 8192);
  }
  template <int BUF, int HH> __device__ __forceinline__ void dmaW(int T) {
#pragma unroll
    for (int s = 0; s < 2; ++s) {
      const unsigned so = sW + (unsigned)(T * 64 + HH * 32 + s * 4) * 7168u;
      dma16(ldsW + BUF * 32768 + HH * 8192 + s * 1024, voffW, rwg, so);
      dma16(ldsW + BUF * 32768 + 16384 + HH * 8192 + s * 1024, voffW, rwu, so);
    }
  }
  template <int BUF, int PF> __device__ __forceinline__ void dmaB(int T) {
    dma16(ldsB + BUF * 8192 + PF * 4096, voffB, rb, sB + PF * 32768 + T * 128);
  }
};

template <int BUF, bool D1, bool D2> __device__ __forceinline__ void k1_tile(K1Wave<0>& w, int T) {
  w.compute(); if (D1) WAIT_V(4); else WAIT_V(0); BAR();
  w.template load<BUF, 1>(); if (D1) w.template dmaW<BUF ^ 1, 1>(T + 1); WAIT_L0(); BAR();
  w.compute(); if (D1) WAIT_V(4); BAR();
  if (D1) { w.template load<BUF ^ 1, 0>(); if (D2) w.template dmaX<BUF>(T + 2); WAIT_L0(); }
  BAR();
}
template <int BUF, bool D1, bool D2> __device__ __forceinline__ void k1_tile(K1Wave<1>& w, int T) {
  w.template load<BUF, 0>(); if (D1) { w.template dmaX<BUF ^ 1>(T + 1); w.template dmaB<BUF ^ 1, 1>(T + 1); } WAIT_L0(); BAR();
  w.compute(); BAR();
  w.template load<BUF, 1>();
  if (D2) { w.template dmaW<BUF, 0>(T + 2); w.template dmaB<BUF, 0>(T + 2); WAIT_V(5); } else WAIT_V(0);
  WAIT_L0(); BAR();
  w.compute(); BAR();
}

__device__ __forceinline__ float pull(int idx4, float v) { return __int_as_float(__builtin_amdgcn_ds_bpermute(idx4, __float_as_int(v))); }
__device__ __forceinline__ f32x2 cmul(f32x2 a, f32x2 b) { return (f32x2){a.x * b.x - a.y * b.y, a.x * b.y + a.y * b.x}; }
__device__ __forceinline__ f32x2 cfma(f32x2 a, f32x2 b, f32x2 c) {
  return (f32x2){__builtin_fmaf(a.x, b.x, __builtin_fmaf(-a.y, b.y, c.x)), __builtin_fmaf(a.x, b.y, __builtin_fmaf(a.y, b.x, c.y))}; }

template <int GRP>
__device__ __forceinline__ void k1_body(const K1Args& A, int wid, int lane, int rch, int c) {
  K1Wave<GRP> w;
  constexpr int NW = K1Wave<GRP>::NW;
  const int wr = wid & 3;
  int r = lane & 15, g = lane >> 4, qq = (lane & 15) >> 2, pp = lane & 3;
  w.rx = mk_rsrc(A.x, L_ * D_ * 2); w.rwg = mk_rsrc(A.wg, D_ * F_ * 2); w.rwu = mk_rsrc(A.wu, D_ * F_ * 2);
  w.rb = mk_rsrc((wr >> 1) ? A.bim : A.bre, P_ * D_ * 2);
  {
    const unsigned sw8 = ((lane & 7) ^ (((wr & 1) * 4 + (lane >> 4)) & 7)) * 16;
    w.voffX = ((lane >> 5) * 16 + ((lane >> 3) & 3)) * 2048 + sw8;
    w.voffB = (lane >> 3) * 2048 + sw8;
    w.voffW = (lane >> 4) * 7168 + ((((lane & 15) >> 1) ^ ((lane >> 4) | ((wr & 1) << 2))) * 32) + (lane & 1) * 16;
    w.sX = (unsigned)(rch * 256 + (GRP * 2 + (wr >> 1)) * 64 + (wr & 1) * 32) * 2048u;
    w.ldsX = K1_X + (GRP * 16 + (wr >> 1) * 8 + (wr & 1)) * 1024;
    const int ii = (wr & 1) * 2 + (wr >> 1) * 4;
    w.sW = (unsigned)(ii * 4) * 7168u + (unsigned)((224 * c) & ~63);
    w.ldsW = K1_W + ii * 1024;
    w.sB = (unsigned)(c * 32 + (wr & 1) * 8) * 2048u;
    w.ldsB = K1_BS + wr * 1024;
#pragma unroll
    for (int ks = 0; ks < 2; ++ks) {
      const unsigned ch = ((g + 4 * ks) ^ ((r >> 1) & 7)) << 4;
      w.aX[ks] = mk_l(K1_X + (wr * 64 + r) * 128 + ch);
      w.aB[ks] = mk_l(K1_BS + (GRP * 32 + r) * 128 + ch);
    }
    const unsigned swz = qq | ((g & 1) << 2);
#pragma unroll
    for (int n = 0; n < NW; ++n) {
      int nf;
      if (GRP == 0) nf = 2 + n; else nf = (c & 1) ? (n < 2 ? 6 + n : 1) : (n < 2 ? n : 6);
      w.aW[n] = mk_l(K1_W + (8 * g + qq) * 256 + ((nf ^ swz) << 5) + pp * 8);
    }
  }
#pragma unroll
  for (int m = 0; m < 4; ++m) {
#pragma unroll
    for (int n = 0; n < NW; ++n) { w.ag[n][m] = (f32x4){0, 0, 0, 0}; w.au[n][m] = (f32x4){0, 0, 0, 0}; }
    w.as[0][m] = (f32x4){0, 0, 0, 0}; w.as[1][m] = (f32x4){0, 0, 0, 0};
  }
#define K1_PIN() _Pragma("unroll") for (int m = 0; m < 4; ++m) { _Pragma("unroll") for (int n = 0; n < NW; ++n) { PIN(w.ag[n][m]); PIN(w.au[n][m]); } PIN(w.as[0][m]); PIN(w.as[1][m]); }
  if (GRP == 0) { w.template dmaX<0>(0); w.template dmaW<0, 1>(0); w.template dmaX<1>(1); }
  else { w.template dmaW<0, 0>(0); w.template dmaB<0, 0>(0); w.template dmaX<0>(0); w.template dmaB<0, 1>(0); w.template dmaW<1, 0>(1); w.template dmaB<1, 0>(1); }
  unsigned epoch;
  {
    const unsigned ep0 = __hip_atomic_load((GAS unsigned*)(A.ws + WS_EP + c * 128), RLX_AGENT);
    const int t = rch * 256 + c * 8 + wid;
    const u32x4 x0 = *(const u32x4*)(A.x + t * D_ + lane * 8), x1 = *(const u32x4*)(A.x + t * D_ + 512 + lane * 8);
    const u32x4 g0 = *(const u32x4*)(A.gw + lane * 8), g1 = *(const u32x4*)(A.gw + 512 + lane * 8);
    float s = 0.f;
#pragma unroll
    for (int j = 0; j < 4; ++j) {
      s = __builtin_fmaf(bf2f(x0[j] & 0xffffu), bf2f(g0[j] & 0xffffu), s); s = __builtin_fmaf(__uint_as_float(x0[j] & 0xffff0000u), __uint_as_float(g0[j] & 0xffff0000u), s);
      s = __builtin_fmaf(bf2f(x1[j] & 0xffffu), bf2f(g1[j] & 0xffffu), s); s = __builtin_fmaf(__uint_as_float(x1[j] & 0xffff0000u), __uint_as_float(g1[j] & 0xffff0000u), s);
    }
#pragma unroll
    for (int o = 32; o >= 1; o >>= 1) s += __shfl_xor(s, o);
    if (lane == 0) ((float*)(A.ws + WS_G))[t] = 1.f / (1.f + __expf(-s));
    epoch = __builtin_amdgcn_readfirstlane(ep0) + 1u;
    if (wid == 0 && lane < 32) {
      const int p = c * 32 + lane;
      const float lr = bf2f(A.lre[p]), li = bf2f(A.lim[p]);
      const float dt = bf2f(pk_bf16(expf(bf2f(A.ldt[p])), 0.f) & 0xffffu);
      const float e = expf(dt * lr); float sn, cs; sincosf(dt * li, &sn, &cs);
      const float ar = e * cs, ai = e * sn, nr = ar - 1.f, den = lr * lr + li * li;
      *(LAS f32x4*)(size_t)(K1_MISC + lane * 16) = (f32x4){ar, ai, (nr * lr + ai * li) / den, (ai * lr - nr * li) / den};
    }
    if (wid == 1) {
      __hip_atomic_store((GAS u64*)(A.ws + WS_AG) + (c * 8 + rch) * 64 + lane, (u64)(epoch - 1u) << 32, RLX_AGENT);
      if (lane == 0) *(unsigned*)(A.ws + WS_XF + blockIdx.x * 128) = 0u;
    }
  }
  K1_PIN();
  WAIT_V(0); WAIT_L0(); BAR();
  if (GRP == 0) { w.template load<0, 0>(); WAIT_L0(); }
  BAR();
  for (int T = 0; T < 14; T += 2) { k1_tile<0, true, true>(w, T); k1_tile<1, true, true>(w, T + 1); }
  k1_tile<0, true, false>(w, 14);
  k1_tile<1, false, false>(w, 15);

  MFMA_DRAIN(); __builtin_amdgcn_sched_barrier(0);
  lane = fresh_lane(); r = lane & 15; g = lane >> 4; qq = (lane & 15) >> 2; pp = lane & 3;
  const f32x4 prm = *(const LAS f32x4*)(size_t)(K1_MISC + (GRP * 16 + r) * 16);
  const f32x2 lam = {prm[0], prm[1]}, coef = {prm[2], prm[3]};
  f32x2 e = {0.f, 0.f};
#pragma unroll
  for (int m = 0; m < 4; ++m)
#pragma unroll
    for (int j = 0; j < 4; ++j) e = cfma(lam, e, (f32x2){w.as[0][m][j], w.as[1][m][j]});
  f32x2 l16 = cmul(lam, lam); l16 = cmul(l16, l16); l16 = cmul(l16, l16); l16 = cmul(l16, l16);
  const f32x2 l32 = cmul(l16, l16), l64 = cmul(l32, l32), l128 = cmul(l64, l64), l256 = cmul(l128, l128);
  const int u16i = ((lane - 16) & 63) * 4, u32i = ((lane - 32) & 63) * 4;
  { f32x2 t = {pull(u16i, e.x), pull(u16i, e.y)}; if (g >= 1) e = cfma(l16, t, e); }
  { f32x2 t = {pull(u32i, e.x), pull(u32i, e.y)}; if (g >= 2) e = cfma(l32, t, e); }
  f32x2 cin = {pull(u16i, e.x), pull(u16i, e.y)}; if (g == 0) cin = (f32x2){0.f, 0.f};
  if (g == 3) *(LAS f32x2*)(size_t)(K1_MISC + 1024 + ((GRP * 16 + r) * 4 + wr) * 8) = e;
  WAIT_L0(); BAR();
  const f32x4 wa01 = *(const LAS f32x4*)(size_t)(K1_MISC + 1024 + (GRP * 16 + r) * 32);
  const f32x4 wa23 = *(const LAS f32x4*)(size_t)(K1_MISC + 1024 + (GRP * 16 + r) * 32 + 16);
  if (wr == 0 && rch < 7) {
    f32x2 a = {wa01[0], wa01[1]};
    a = cfma(l64, a, (f32x2){wa01[2], wa01[3]}); a = cfma(l64, a, (f32x2){wa23[0], wa23[1]}); a = cfma(l64, a, (f32x2){wa23[2], wa23[3]});
    if (g < 2) __hip_atomic_store((GAS u64*)(A.ws + WS_AG) + (c * 8 + rch) * 64 + g * 32 + GRP * 16 + r,
                                  ((u64)epoch << 32) | __float_as_uint(g ? a.y : a.x), RLX_AGENT);
  }
  char* Hb = A.ws + WS_H + (size_t)(2 * rch + (wr >> 1)) * NKF * 8192 + (wr & 1) * 4096 + lane * 16;
  const int kf0 = 7 * (c >> 1) + 3 * (c & 1);
#pragma unroll
  for (int m = 0; m < 4; ++m) {
    unsigned hv[NW][2];
#pragma unroll
    for (int n = 0; n < NW; ++n) {
      const f32x4 gt = w.ag[n][m], up = w.au[n][m];
      hv[n][0] = pk_bf16(fmaxf(gt[0], 0.f) * up[0], fmaxf(gt[1], 0.f) * up[1]);
      hv[n][1] = pk_bf16(fmaxf(gt[2], 0.f) * up[2], fmaxf(gt[3], 0.f) * up[3]);
    }
    if (GRP == 0) {
      *(u32x4*)(Hb + (size_t)(kf0 + 1) * 8192 + m * 1024) = (u32x4){hv[0][0], hv[0][1], hv[1][0], hv[1][1]};
      *(u32x4*)(Hb + (size_t)(kf0 + 2) * 8192 + m * 1024) = (u32x4){hv[2][0], hv[2][1], hv[NW - 1][0], hv[NW - 1][1]};
    } else {
      *(u32x4*)(Hb + (size_t)(kf0 + ((c & 1) ? 3 : 0)) * 8192 + m * 1024) = (u32x4){hv[0][0], hv[0][1], hv[1][0], hv[1][1]};
      *(u32x2*)(Hb + (size_t)(kf0 + ((c & 1) ? 0 : 3)) * 8192 + m * 1024 + ((c & 1) ? 8 : 0)) = (u32x2){hv[2][0], hv[2][1]};
    }
  }
  f32x2 cb = {0.f, 0.f};
  if (rch > 0) {
    const GAS u64* ag = (const GAS u64*)(A.ws + WS_AG) + c * 8 * 64 + GRP * 16 + r;
    u64 vr[7], vi[7];
    for (unsigned spins = 0;; ++spins) {
      bool ok = true;
#pragma unroll
      for (int cc = 0; cc < 7; ++cc) {
        vr[cc] = __hip_atomic_load(ag + cc * 64, RLX_AGENT); vi[cc] = __hip_atomic_load(ag + cc * 64 + 32, RLX_AGENT);
      }
#pragma unroll
      for (int cc = 0; cc < 7; ++cc) ok &= (cc >= rch) || ((unsigned)(vr[cc] >> 32) == epoch && (unsigned)(vi[cc] >> 32) == epoch);
      if (__all(ok) || spins > (1u << 20)) break;
      __builtin_amdgcn_s_sleep(1);
    }
#pragma unroll
    for (int cc = 0; cc < 7; ++cc) if (cc < rch) cb = cfma(l256, cb, (f32x2){__uint_as_float((unsigned)vr[cc]), __uint_as_float((unsigned)vi[cc])});
  }
  if (wr > 0) cb = cfma(l64, cb, (f32x2){wa01[0], wa01[1]});
  if (wr > 1) cb = cfma(l64, cb, (f32x2){wa01[2], wa01[3]});
  if (wr > 2) cb = cfma(l64, cb, (f32x2){wa23[0], wa23[1]});
  {
    f32x2 pq = (g & 1) ? l16 : (f32x2){1.f, 0.f};
    if (g & 2) pq = cmul(pq, l32);
    e = cfma(pq, cb, cin);
  }
  unsigned hr[8], hi[8];
#pragma unroll
  for (int m = 0; m < 4; ++m) {
    float tr[4], ti[4];
#pragma unroll
    for (int j = 0; j < 4; ++j) {
      e = cfma(lam, e, (f32x2){w.as[0][m][j], w.as[1][m][j]});
      const f32x2 h = cmul(coef, e); tr[j] = h.x; ti[j] = -h.y;
    }
    hr[2 * m] = pk_bf16(tr[0], tr[1]); hr[2 * m + 1] = pk_bf16(tr[2], tr[3]);
    hi[2 * m] = pk_bf16(ti[0], ti[1]); hi[2 * m + 1] = pk_bf16(ti[2], ti[3]);
  }
  {
    const lptr tb = mk_l(wr * 9216 + (GRP * 16 + r) * 144 + g * 32);
    *(LAS u32x4*)(tb) = (u32x4){hr[0], hr[1], hr[2], hr[3]};
    *(LAS u32x4*)(tb + 16) = (u32x4){hr[4], hr[5], hr[6], hr[7]};
    *(LAS u32x4*)(tb + 4608) = (u32x4){hi[0], hi[1], hi[2], hi[3]};
    *(LAS u32x4*)(tb + 4608 + 16) = (u32x4){hi[4], hi[5], hi[6], hi[7]};
    WAIT_L0(); BAR();
    if (rch == 7 && wid == 0 && lane == 0) __hip_atomic_store((GAS unsigned*)(A.ws + WS_EP + c * 128), epoch, RLX_AGENT);
    const lptr rb = mk_l(wr * 9216 + GRP * 4608 + (8 * g + qq) * 144 + pp * 32);
#pragma unroll
    for (int m = 0; m < 4; ++m) {
      const bf16x8 f = lds_tr2(rb + m * 8, rb + 4 * 144 + m * 8);
      *(bf16x8*)(Hb + (size_t)(112 + 32 * GRP + c) * 8192 + m * 1024) = f;
    }
  }
}

__global__ __launch_bounds__(512, 2) void k1_kernel(K1Args A) {
  const int wid = __builtin_amdgcn_readfirstlane(threadIdx.x >> 6), lane = threadIdx.x & 63;
  const int xcd = blockIdx.x & 7, idx = blockIdx.x >> 3, c = xcd * 4 + (idx >> 3), rch = idx & 7;
  if (wid < 4) k1_body<0>(A, wid, lane, rch, c); else k1_body<1>(A, wid, lane, rch, c);
}

struct K2Args {
  const u16 *x, *cre, *cim, *dsk, *wd;
  char* ws; float* out;
};
constexpr unsigned K2_LDS = 131072;
struct K2Wave {
  f32x4 af[4][4], as[4][4];
  bf16x8 ha[2][4], bb[2][4];
  lptr aH, aW[4], aC[2];
  unsigned voffH, voffW, voffC;
  unsigned sH, sW, sC, ldsA, ldsB;
  rsrc_t rh, rw, rc;
  template <int SB, bool SSM> __device__ __forceinline__ void load() {
#pragma unroll
    for (int kk = 0; kk < 2; ++kk) {
#pragma unroll
      for (int n = 0; n < 4; ++n) {
        if (SSM) bb[kk][n] = lds_b128(aC[kk] + SB * 32768 + n * 2048);
        else bb[kk][n] = lds_tr2(aW[n] + SB * 32768 + kk * 8192, aW[n] + SB * 32768 + kk * 8192 + 4096);
      }
#pragma unroll
      for (int m = 0; m < 4; ++m) ha[kk][m] = lds_b128(aH + SB * 32768 + kk * 8192 + m * 1024);
    }
  }
  template <bool SSM> __device__ __forceinline__ void compute() {
    __builtin_amdgcn_s_setprio(1);
#pragma unroll
    for (int kk = 0; kk < 2; ++kk)
#pragma unroll
      for (int n = 0; n < 4; ++n)
#pragma unroll
        for (int m = 0; m < 4; ++m) {
          if (SSM) MFMA(bb[kk][n], ha[kk][m], as[m][n]); else MFMA(bb[kk][n], ha[kk][m], af[m][n]);
        }
    __builtin_amdgcn_s_setprio(0);
  }
  template <bool SSM> __device__ __forceinline__ void dma(int st, unsigned off) {
    const unsigned hs = sH + (unsigned)(SSM ? st + 28 : st) * 16384u;
#pragma unroll
    for (int s = 0; s < 4; ++s) dma16(ldsA + off + s * 1024, voffH, rh, hs + (SSM ? hadj : 0u) + s * 1024);
#pragma unroll
    for (int s = 0; s < 4; ++s) {
      if (SSM) dma16(ldsB + off + s * 2048, voffC, rc, sC + (unsigned)(st - 28) * 128u + s * 32768u);
      else dma16(ldsB + off + s * 2048, voffW, rw, sW + (unsigned)st * 131072u + s * 16384u);
    }
  }
  unsigned hadj;
};

template <int G> __device__ __forceinline__ void k2_body(const K2Args& A, const int wid) {
  int lane = threadIdx.x & 63;
  const int xcd = blockIdx.x & 7, idx = blockIdx.x >> 3, kh = xcd & 1, tt = (xcd >> 1) * 4 + (idx >> 3), dt = idx & 7;
  const int w4 = wid & 3, wm = w4 >> 1, wn = w4 & 1;
  int r = lane & 15, g = lane >> 4; const int qq = (lane & 15) >> 2, pp = lane & 3;
  K2Wave w;
  w.rh = mk_rsrc(A.ws + WS_H, (unsigned)H_BYTES); w.rw = mk_rsrc(A.wd, F_ * D_ * 2); w.rc = mk_rsrc(kh ? A.cim : A.cre, D_ * P_ * 2);
  float gv[4];
#pragma unroll
  for (int m = 0; m < 4; ++m) gv[m] = ((const float*)(A.ws + WS_G))[tt * 128 + wm * 64 + (r >> 2) * 16 + m * 4 + (r & 3)];
  {
    w.voffH = lane * 16;
    w.voffW = (lane >> 4) * 2048 + ((((lane & 15) >> 1) ^ (((w4 & 1) << 2) | (lane >> 4))) * 32) + (lane & 1) * 16;
    w.voffC = (lane >> 3) * 2048 + (((lane & 7) ^ (((w4 & 1) * 4 + (lane >> 4)) & 7)) * 16);
    w.sH = (unsigned)(tt * NKF + 56 * kh) * 8192u + w4 * 4096u;
    w.hadj = (unsigned)(-24 * kh * 8192);
    const int i0 = (w4 & 1) + (w4 >> 1) * 8;
    w.sW = (unsigned)(kh * 1792 + i0 * 4) * 2048u + dt * 256u;
    w.sC = (unsigned)(dt * 128 + i0 * 8) * 2048u;
    w.ldsA = w4 * 4096; w.ldsB = 16384 + i0 * 1024;
    const unsigned gb = G * 65536;
    w.aH = mk_l(gb + wm * 4096 + lane * 16);
    const unsigned swz = 4 * (g & 1) + qq;
#pragma unroll
    for (int n = 0; n < 4; ++n) w.aW[n] = mk_l(gb + 16384 + (4 * g + qq) * 256 + (((wn * 4 + n) ^ swz) << 5) + pp * 8);
#pragma unroll
    for (int kk = 0; kk < 2; ++kk) w.aC[kk] = mk_l(gb + 16384 + (wn * 64 + r) * 128 + (((g + 4 * kk) ^ ((r >> 1) & 7)) << 4));
  }
#pragma unroll
  for (int m = 0; m < 4; ++m)
#pragma unroll
    for (int n = 0; n < 4; ++n) { w.af[m][n] = (f32x4){0, 0, 0, 0}; w.as[m][n] = (f32x4){0, 0, 0, 0}; }
  const unsigned ob = (G ^ 1) * 65536;
  w.template dma<false>(G ^ 1, ob); w.template dma<false>((G ^ 1) + 2, ob + 32768);
#pragma unroll
  for (int m = 0; m < 4; ++m)
#pragma unroll
    for (int n = 0; n < 4; ++n) { PIN(w.af[m][n]); PIN(w.as[m][n]); }
  WAIT_V(8); BAR();
  if (G == 1) BAR();
#define K2_STEP(SB, SSM, DMAK, n)                                                          \
  do { w.template load<SB, SSM>();                                                          \
       if (DMAK == 1) { w.template dma<false>((n) + 3, ob + ((((n) + 3) >> 1) & 1) * 32768); WAIT_V(8); } \
       else if (DMAK == 2) { w.template dma<true>((n) + 3, ob + ((((n) + 3) >> 1) & 1) * 32768); WAIT_V(8); } \
       else WAIT_V(0);                                                                      \
       WAIT_L0(); BAR(); w.template compute<SSM>(); BAR(); } while (0)
  if (G == 0) { w.template load<0, false>(); WAIT_L0(); BAR(); w.template compute<false>(); BAR(); }
  else K2_STEP(0, false, 1, 1);
  K2_STEP(1, false, 1, G + 2);
  for (int n = G + 4; n < 24; n += 4) { K2_STEP(0, false, 1, n); K2_STEP(1, false, 1, n + 2); }
  if (G == 0) K2_STEP(0, false, 1, 24); else K2_STEP(0, false, 2, 25);
  K2_STEP(1, false, 2, G + 26);
  for (int n = G + 28; n < 40; n += 4) { K2_STEP(0, true, 2, n); K2_STEP(1, true, 2, n + 2); }
  if (G == 0) K2_STEP(0, true, 2, 40); else K2_STEP(0, true, 0, 41);
  K2_STEP(1, true, 0, G + 42);
  if (G == 0) BAR();
  MFMA_DRAIN(); __builtin_amdgcn_sched_barrier(0);
  lane = fresh_lane(); r = lane & 15; g = lane >> 4;
  f32x4 v[4][4];
  const lptr rw = mk_l(wid * 8192 + lane * 16), rr = mk_l((wid ^ 4) * 8192 + lane * 16);
#pragma unroll
  for (int m = 0; m < 4; ++m)
#pragma unroll
    for (int n = 0; n < 4; ++n) v[m][n] = w.af[m][n] * (1.f - gv[m]) + w.as[m][n] * (2.f * gv[m]);
#pragma unroll
  for (int mm = 0; mm < 2; ++mm)
#pragma unroll
    for (int n = 0; n < 4; ++n) *(LAS f32x4*)(rw + (mm * 4 + n) * 1024) = G ? v[mm][n] : v[2 + mm][n];
  WAIT_L0(); BAR();
  f32x4 o[2][4];
#pragma unroll
  for (int mm = 0; mm < 2; ++mm)
#pragma unroll
    for (int n = 0; n < 4; ++n) o[mm][n] = (G ? v[2 + mm][n] : v[mm][n]) + *(const LAS f32x4*)(rr + (mm * 4 + n) * 1024);
  const rsrc_t rxb = mk_rsrc(A.ws + WS_XB, (unsigned)XB_BYTES);
  const unsigned tile = tt * 8 + dt;
  if (wm != kh) {
    const unsigned xo = (tile * 2 + kh) * 32768u + (G * 2 + wn) * 8192u + lane * 16;
#pragma unroll
    for (int mm = 0; mm < 2; ++mm)
#pragma unroll
      for (int n = 0; n < 4; ++n) __builtin_amdgcn_raw_buffer_store_b128(__builtin_bit_cast(u32x4, o[mm][n]), rxb, xo + (mm * 4 + n) * 1024, 0, 16);
    WAIT_V(0); BAR();
    if (G == 0 && wn == 0 && lane == 0) __hip_atomic_store((GAS unsigned*)(A.ws + WS_XF + blockIdx.x * 128), 1u, RLX_AGENT);
  } else {
    const int d0 = dt * 128 + wn * 64 + 4 * g;
#pragma unroll
    for (int mm = 0; mm < 2; ++mm) {
      const int t = tt * 128 + wm * 64 + (r >> 2) * 16 + (2 * G + mm) * 4 + (r & 3);
      const float gg = G ? gv[2 + mm] : gv[mm];
#pragma unroll
      for (int n = 0; n < 4; ++n) {
        const u32x2 xv = *(const u32x2*)(A.x + t * D_ + d0 + n * 16), dv = *(const u32x2*)(A.dsk + d0 + n * 16);
        const f32x4 xf = {bf2f(xv[0] & 0xffffu), __uint_as_float(xv[0] & 0xffff0000u), bf2f(xv[1] & 0xffffu), __uint_as_float(xv[1] & 0xffff0000u)};
        const f32x4 df = {bf2f(dv[0] & 0xffffu), __uint_as_float(dv[0] & 0xffff0000u), bf2f(dv[1] & 0xffffu), __uint_as_float(dv[1] & 0xffff0000u)};
        o[mm][n] += xf * df * gg;
      }
    }
    BAR();
    const GAS unsigned* fl = (const GAS unsigned*)(A.ws + WS_XF + (blockIdx.x ^ 1) * 128);
    for (unsigned spins = 0; spins < (1u << 22); ++spins) {
      if (__builtin_amdgcn_readfirstlane(__hip_atomic_load(fl, RLX_AGENT)) == 1u) break;
      __builtin_amdgcn_s_sleep(1);
    }
    asm volatile("" ::: "memory");
    const unsigned xo = (tile * 2 + (kh ^ 1)) * 32768u + (G * 2 + wn) * 8192u + lane * 16;
    u32x4 pv[2][4];
#pragma unroll
    for (int mm = 0; mm < 2; ++mm)
#pragma unroll
      for (int n = 0; n < 4; ++n) pv[mm][n] = __builtin_amdgcn_raw_buffer_load_b128(rxb, xo + (mm * 4 + n) * 1024, 0, 16);
#pragma unroll
    for (int mm = 0; mm < 2; ++mm) {
      const int t = tt * 128 + wm * 64 + (r >> 2) * 16 + (2 * G + mm) * 4 + (r & 3);
#pragma unroll
      for (int n = 0; n < 4; ++n) *(f32x4*)(A.out + (size_t)t * D_ + d0 + n * 16) = o[mm][n] + __builtin_bit_cast(f32x4, pv[mm][n]);
    }
  }
}

__global__ __launch_bounds__(512, 2) void k2_kernel(K2Args A) {
  const int wid = __builtin_amdgcn_readfirstlane(threadIdx.x >> 6);
  if (wid < 4) k2_body<0>(A, wid); else k2_body<1>(A, wid);
}

extern "C" void kernel_launch(void* const* d_in, const int* in_sizes, int n_in, void* d_out, int out_size, void* d_ws, size_t ws_size,
                              hipStream_t stream) {
  K1Args a1; a1.x = (const u16*)d_in[0]; a1.lre = (const u16*)d_in[1]; a1.lim = (const u16*)d_in[2]; a1.bre = (const u16*)d_in[3];
  a1.bim = (const u16*)d_in[4]; a1.ldt = (const u16*)d_in[7]; a1.wg = (const u16*)d_in[10]; a1.wu = (const u16*)d_in[11];
  a1.gw = (const u16*)d_in[13]; a1.ws = (char*)d_ws;
  K2Args a2; a2.x = (const u16*)d_in[0]; a2.cre = (const u16*)d_in[5]; a2.cim = (const u16*)d_in[6]; a2.dsk = (const u16*)d_in[8];
  a2.wd = (const u16*)d_in[12]; a2.ws = (char*)d_ws; a2.out = (float*)d_out;
  (void)hipFuncSetAttribute((const void*)k1_kernel, hipFuncAttributeMaxDynamicSharedMemorySize, K1_LDS);
  (void)hipFuncSetAttribute((const void*)k2_kernel, hipFuncAttributeMaxDynamicSharedMemorySize, K2_LDS);
  k1_kernel<<<256, 512, K1_LDS, stream>>>(a1);
  k2_kernel<<<256, 512, K2_LDS, stream>>>(a2);
}
```

```cpp
#include <hip/hip_runtime.h>
#include <hip/hip_bf16.h>
#define ILV 1

typedef unsigned short u16;
typedef short bf16x8 __attribute__((ext_vector_type(8)));
typedef short s16x4 __attribute__((ext_vector_type(4)));
typedef float f32x4 __attribute__((ext_vector_type(4)));
typedef float f32x2 __attribute__((ext_vector_type(2)));
typedef unsigned u32x4 __attribute__((ext_vector_type(4)));
typedef unsigned u32x2 __attribute__((ext_vector_type(2)));
typedef unsigned long long u64;
#define LAS __attribute__((address_space(3)))
#define GAS __attribute__((address_space(1)))
#define RLX_AGENT __ATOMIC_RELAXED, __HIP_MEMORY_SCOPE_AGENT
typedef __amdgpu_buffer_rsrc_t rsrc_t;

constexpr int L_ = 2048, D_ = 1024, P_ = 1024, F_ = 3584;
constexpr int NKF = 176;
constexpr size_t WS_H = 0;
constexpr size_t H_BYTES = (size_t)16 * NKF * 8 * 1024;
constexpr size_t WS_XB = WS_H + H_BYTES;
constexpr size_t XB_BYTES = (size_t)256 * 32768;
constexpr size_t WS_G = WS_XB + XB_BYTES;
constexpr size_t WS_AG = WS_G + 8192;
constexpr size_t WS_EP = WS_AG + (size_t)32 * 8 * 64 * 8;
constexpr size_t WS_FL = WS_EP + 512;
constexpr size_t WS_END = WS_FL + 256 * 128;
constexpr unsigned SPIN_CAP = 1u << 20;

__device__ __forceinline__ float bf2f(unsigned b) { return __uint_as_float(b << 16); }
__device__ __forceinline__ float bfhi(unsigned b) { return __uint_as_float(b & 0xffff0000u); }
__device__ __forceinline__ unsigned pk_bf16(float lo, float hi) {
  typedef __bf16 bf2_t __attribute__((ext_vector_type(2)));
  return __builtin_bit_cast(unsigned, __builtin_convertvector((f32x2){lo, hi}, bf2_t)); }
__device__ __forceinline__ rsrc_t mk_rsrc(const void* p, unsigned bytes) {
  return __builtin_amdgcn_make_buffer_rsrc((void*)p, (short)0, (int)bytes, 0x00020000); }
__device__ __forceinline__ void dma16(unsigned ldsaddr, unsigned voff, rsrc_t srd, unsigned soff) {
  asm volatile("s_mov_b32 m0, %0\n\ts_nop 0\n\tbuffer_load_dwordx4 %1, %2, %3 offen lds"
               :: "s"(ldsaddr), "v"(voff), "s"(srd), "s"(soff) : "memory"); }
__device__ __forceinline__ void dma16_dev(unsigned ldsaddr, unsigned voff, rsrc_t srd, unsigned soff) {
  asm volatile("s_mov_b32 m0, %0\n\ts_nop 0\n\tbuffer_load_dwordx4 %1, %2, %3 offen sc1 lds"
               :: "s"(ldsaddr), "v"(voff), "s"(srd), "s"(soff) : "memory"); }
__device__ __forceinline__ void dma4(unsigned ldsaddr, unsigned voff, rsrc_t srd, unsigned soff) {
  asm volatile("s_mov_b32 m0, %0\n\ts_nop 0\n\tbuffer_load_dword %1, %2, %3 offen lds"
               :: "s"(ldsaddr), "v"(voff), "s"(srd), "s"(soff) : "memory"); }
#define WAIT_V(n) asm volatile("s_waitcnt vmcnt(" #n ")" ::: "memory")
#define WAIT_L0() do { __builtin_amdgcn_s_waitcnt(0xC07F); asm volatile("" ::: "memory"); } while (0)
#define SCHED() __builtin_amdgcn_sched_barrier(0)
#define BAR() do { SCHED(); __builtin_amdgcn_s_barrier(); SCHED(); } while (0)
__device__ __forceinline__ int fresh_lane() { int l = __builtin_amdgcn_mbcnt_hi(~0u, __builtin_amdgcn_mbcnt_lo(~0u, 0u)); asm volatile("" : "+v"(l)); return l; }
typedef LAS char* lptr;
__device__ __forceinline__ lptr mk_l(unsigned a) { lptr p = (lptr)(size_t)a; asm volatile("" : "+v"(p)); return p; }
__device__ __forceinline__ bf16x8 lds_b128(lptr a) { return *(const LAS bf16x8*)a; }
__device__ __forceinline__ bf16x8 lds_tr2(lptr a0, lptr a1) {
  s16x4 lo = __builtin_amdgcn_ds_read_tr16_b64_v4i16((LAS s16x4*)a0);
  s16x4 hi = __builtin_amdgcn_ds_read_tr16_b64_v4i16((LAS s16x4*)a1);
  return __builtin_shufflevector(lo, hi, 0, 1, 2, 3, 4, 5, 6, 7); }
#define MFMA(a, b, c) asm volatile("v_mfma_f32_16x16x32_bf16 %0, %1, %2, %0" : "+v"(c) : "v"(a), "v"(b))
#define PIN(x) asm volatile("" : "+v"(x))
#define MFMA_DRAIN() do { asm volatile("s_nop 7\n\ts_nop 7" ::: "memory"); SCHED(); } while (0)
#define ZERO4 ((f32x4){0.f, 0.f, 0.f, 0.f})

#define ST_DECL
#define STAMP()
#define TS2()
#define ST_DUMP(ws, wid)

struct Args {
  const u16 *x, *lre, *lim, *bre, *bim, *cre, *cim, *ldt, *dsk, *wg, *wu, *wd, *gw;
  char* ws; float* out;
};

constexpr unsigned L_X = 0, L_W = 65536, L_BS = 131072, L_TR = 131072  , L_PRM = 150528  ,
                   L_WA = 152064  , L_FLG = 153088  , L_SINK = 153344, LDS_BYTES = 163840;

__device__ __forceinline__ f32x2 cmul(f32x2 a, f32x2 b) { return (f32x2){a.x * b.x - a.y * b.y, a.x * b.y + a.y * b.x}; }
__device__ __forceinline__ f32x2 cfma(f32x2 a, f32x2 b, f32x2 c) {
  return (f32x2){__builtin_fmaf(a.x, b.x, __builtin_fmaf(-a.y, b.y, c.x)), __builtin_fmaf(a.x, b.y, __builtin_fmaf(a.y, b.x, c.y))}; }
__device__ __forceinline__ void sw16(float v, float& r0, float& r1) { auto t = __builtin_amdgcn_permlane16_swap(__float_as_uint(v), __float_as_uint(v), false, false); r0 = __uint_as_float(t[0]); r1 = __uint_as_float(t[1]); }
__device__ __forceinline__ void sw32(float v, float& r0, float& r1) { auto t = __builtin_amdgcn_permlane32_swap(__float_as_uint(v), __float_as_uint(v), false, false); r0 = __uint_as_float(t[0]); r1 = __uint_as_float(t[1]); }

template <int GRP>
struct P1 {
  static constexpr int NW = GRP == 0 ? 4 : 3, NS = GRP == 0 ? 0 : 4;
  f32x4 ag[NW][4], au[NW][4], as[4][4];
  bf16x8 xa[4], wgf[NW], wuf[NW], bsf[4];
  lptr aX[2], aB[2], aW[NW];
  unsigned voffX, voffW, voffB;
  unsigned sX, sW, sB, ldsX, ldsW, ldsB;
  rsrc_t rx, rwg, rwu, rb, rxb;
  unsigned voffE; u32x4 ones;
  const char* pfp; unsigned pfs;
  __device__ __forceinline__ void pull(int T) {
    const char* p = pfp + pfs * (unsigned)(T < 7 ? T : 7);
    asm volatile("s_mov_b32 m0, %0\n\ts_nop 0\n\tglobal_load_lds_dword %1, off" :: "s"(L_SINK), "v"(p) : "memory");
  }
  __device__ __forceinline__ void clearE(int T) { __builtin_amdgcn_raw_buffer_store_b128(ones, rxb, voffE + T * 4096, 0, 16); }

  template <int BUF, int KS> __device__ __forceinline__ void load() {
#pragma unroll
    for (int n = 0; n < NW; ++n) {
      wgf[n] = lds_tr2(aW[n] + BUF * 32768 + KS * 8192, aW[n] + BUF * 32768 + KS * 8192 + 1024);
      wuf[n] = lds_tr2(aW[n] + BUF * 32768 + 16384 + KS * 8192, aW[n] + BUF * 32768 + 16384 + KS * 8192 + 1024);
    }
#pragma unroll
    for (int m = 0; m < 4; ++m) xa[m] = lds_b128(aX[KS] + BUF * 32768 + m * 2048);
#pragma unroll
    for (int i = 0; i < NS; ++i) bsf[i] = lds_b128(aB[KS] + BUF * 8192 + i * 2048);
  }
  __device__ __forceinline__ void compute() {
    __builtin_amdgcn_s_setprio(1);
#pragma unroll
    for (int i = 0; i < NS; ++i)
#pragma unroll
      for (int m = 0; m < 4; ++m) MFMA(xa[m], bsf[i], as[i][m]);
#pragma unroll
    for (int n = 0; n < NW; ++n) {
#pragma unroll
      for (int m = 0; m < 4; ++m) MFMA(wgf[n], xa[m], ag[n][m]);
#pragma unroll
      for (int m = 0; m < 4; ++m) MFMA(wuf[n], xa[3 - m], au[n][3 - m]);
    }
    __builtin_amdgcn_s_setprio(0);
  }
  template <int BUF> __device__ __forceinline__ void dmaX(int T) {
#pragma unroll
    for (int s = 0; s < 4; ++s) dma16(ldsX + BUF * 32768 + s * 2048, voffX, rx, sX + T * 128 + s * 8192);
  }
  template <int BUF, int HH> __device__ __forceinline__ void dmaW(int T) {
#pragma unroll
    for (int s = 0; s < 2; ++s) {
      const unsigned so = sW + (unsigned)(T * 64 + HH * 32 + s * 4) * 7168u;
      dma16(ldsW + BUF * 32768 + HH * 8192 + s * 1024, voffW, rwg, so);
      dma16(ldsW + BUF * 32768 + 16384 + HH * 8192 + s * 1024, voffW, rwu, so);
    }
  }
  template <int BUF> __device__ __forceinline__ void dmaB(int T) {
#pragma unroll
    for (int pf = 0; pf < 2; ++pf) dma16(ldsB + BUF * 8192 + pf * 4096, voffB, rb, sB + pf * 32768 + T * 128);
  }
};

template <int BUF> __device__ __forceinline__ void p1_tile(P1<0>& w, int T) {
  w.compute(); WAIT_V(6); BAR();
  w.template load<BUF, 1>(); w.template dmaW<BUF ^ 1, 1>(T + 1); w.clearE(T); w.pull(T); WAIT_L0(); BAR();
  w.compute(); WAIT_V(6); BAR();
  w.template load<BUF ^ 1, 0>(); w.template dmaX<BUF>(T + 2); w.template dmaB<BUF>(T + 2); WAIT_L0(); BAR();
}
template <int BUF> __device__ __forceinline__ void p1_tile(P1<1>& w, int T) {
  w.template load<BUF, 0>(); w.template dmaX<BUF ^ 1>(T + 1); WAIT_L0(); BAR();
  w.compute(); BAR();
  w.template load<BUF, 1>(); w.template dmaW<BUF, 0>(T + 2); WAIT_V(4); WAIT_L0(); BAR();
  w.compute(); BAR();
}

struct P2 {
  f32x4 af[4][4], as[4][4];
  bf16x8 ha[2][4], bb[2][4];
  lptr aH, aW[4], aC[2];
  unsigned voffH, voffW, voffC;
  unsigned sH, sW, sC, ldsA, ldsB;
  int kfa, e0, kfs;
  rsrc_t rh, rw, rc;
  __device__ __forceinline__ int kf_ffn(int st) const {
    const int e = e0 + 2 * st;
    return st < 16 ? kfa + 7 * (st & 7) + (st >> 3) : 3 * e - 2 * ((e * 43) >> 7);
  }
  __device__ __forceinline__ void dmaA1(int st, int kf, int s) {
    dma16_dev(ldsA + (st & 1) * 65536 + ((st >> 1) & 1) * 16384 + s * 1024, voffH, rh, sH + (unsigned)kf * 8192u + s * 1024);
  }
  template <bool SSM> __device__ __forceinline__ void dmaB1(int st, int kf, int s) {
    const unsigned l = ldsB + (st & 1) * 65536 + ((st >> 1) & 1) * 16384 + s * 2048;
    if (SSM) dma16(l, voffC, rc, sC + (unsigned)st * 128u + s * 32768u);
    else dma16(l, voffW, rw, sW + (unsigned)kf * 65536u + s * 16384u);
  }
  template <int SB, bool SSM, int DK> __device__ __forceinline__ void load(int ds) {
    const int kf = DK == 1 ? kf_ffn(ds) : kfs + 2 * ds;
#pragma unroll
    for (int kk = 0; kk < 2; ++kk) {
#pragma unroll
      for (int n = 0; n < 4; ++n) {
        if (DK) { if (kk == 0) dmaA1(ds, kf, n); else dmaB1<DK == 2>(ds, kf, n); }
        if (SSM) bb[kk][n] = lds_b128(aC[kk] + SB * 16384 + n * 2048);
        else bb[kk][n] = lds_tr2(aW[n] + SB * 16384 + kk * 8192, aW[n] + SB * 16384 + kk * 8192 + 4096);
        ha[kk][n] = lds_b128(aH + SB * 16384 + kk * 8192 + n * 1024);
      }
    }
  }
  template <bool SSM> __device__ __forceinline__ void compute() {
    __builtin_amdgcn_s_setprio(1);
#pragma unroll
    for (int kk = 0; kk < 2; ++kk)
#pragma unroll
      for (int n = 0; n < 4; ++n)
#pragma unroll
        for (int mm = 0; mm < 4; ++mm) {
          const int m = (n & 1) ? 3 - mm : mm;
          if (SSM) MFMA(bb[kk][n], ha[kk][m], as[m][n]); else MFMA(bb[kk][n], ha[kk][m], af[m][n]);
        }
    __builtin_amdgcn_s_setprio(0);
  }
};

__device__ __forceinline__ void arrive(char* ws, int lane, unsigned flag_off, int ctr, unsigned epoch) {
  if (lane == 0) {
    const unsigned old = __hip_atomic_fetch_add((LAS unsigned*)mk_l(L_FLG + 16 + ctr * 4), 1u, __ATOMIC_RELAXED, __HIP_MEMORY_SCOPE_WORKGROUP);
    if (old == 3u) __hip_atomic_store((GAS unsigned*)(ws + flag_off), epoch, RLX_AGENT);
  }
}
__device__ __forceinline__ void wait_flags(const char* ws, unsigned off, unsigned epoch) {
_Pragma("nounroll")
  for (unsigned spins = 0; spins < SPIN_CAP; ++spins) {
    if (__all(__hip_atomic_load((const GAS unsigned*)(ws + off), RLX_AGENT) == epoch)) break;
    __builtin_amdgcn_s_sleep(1);
  }
  asm volatile("" ::: "memory");
}

template <int GRP>
__device__ __forceinline__ void body(const Args& A, const int wid) {
  __builtin_amdgcn_s_waitcnt(0x0F70);
  ST_DECL STAMP();
  const int xcd = blockIdx.x & 7, idx = blockIdx.x >> 3, c = xcd * 4 + (idx >> 3), rch = idx & 7;
  const int kh = xcd & 1, tt = 2 * rch + ((xcd >> 1) & 1), dt = (xcd >> 2) * 4 + (idx >> 3);
  const bool sender = kh == (dt & 1);
  const int NF = sender ? 24 : 32;
  const int wr = wid & 3, wm = wr >> 1, wn = wr & 1;
  int lane = threadIdx.x & 63;
  unsigned epoch;
  P2 q;
  u32x4 hq[14]; unsigned ho[3];
  const rsrc_t rws = mk_rsrc(A.ws, (unsigned)WS_END);
  {
  P1<GRP> w;
  constexpr int NW = P1<GRP>::NW, NS = P1<GRP>::NS;
  int r = lane & 15, g = lane >> 4, qq = (lane & 15) >> 2, pp = lane & 3;
  unsigned ep0, plr = 0, pli = 0, pld = 0;
  asm volatile("global_load_dword %0, %1, %2 sc1" : "=v"(ep0) : "v"((unsigned)WS_EP), "s"(A.ws) : "memory");
  if (GRP == 1) {
    const unsigned po = (c * 32 + (lane & 31)) * 2;
    asm volatile("global_load_ushort %0, %1, %2" : "=v"(plr) : "v"(po), "s"(A.lre) : "memory");
    asm volatile("global_load_ushort %0, %1, %2" : "=v"(pli) : "v"(po), "s"(A.lim) : "memory");
    asm volatile("global_load_ushort %0, %1, %2" : "=v"(pld) : "v"(po), "s"(A.ldt) : "memory");
  }
  w.rx = mk_rsrc(A.x, L_ * D_ * 2); w.rwg = mk_rsrc(A.wg, D_ * F_ * 2); w.rwu = mk_rsrc(A.wu, D_ * F_ * 2);
  w.rb = mk_rsrc((wr >> 1) ? A.bim : A.bre, P_ * D_ * 2);
  {
    const unsigned sw8 = ((lane & 7) ^ (((wr & 1) * 4 + (lane >> 4)) & 7)) * 16;
    w.voffX = ((lane >> 5) * 16 + ((lane >> 3) & 3)) * 2048 + sw8;
    w.voffB = (lane >> 3) * 2048 + sw8;
    w.voffW = (lane >> 4) * 7168 + ((((lane & 15) >> 1) ^ ((lane >> 4) | ((wr & 1) << 2))) * 32) + (lane & 1) * 16;
    w.sX = (unsigned)(rch * 256 + (GRP * 2 + (wr >> 1)) * 64 + (wr & 1) * 32) * 2048u;
    w.ldsX = L_X + (GRP * 16 + (wr >> 1) * 8 + (wr & 1)) * 1024;
    const int ii = (wr & 1) * 2 + (wr >> 1) * 4;
    w.sW = (unsigned)(ii * 4) * 7168u + (unsigned)((224 * c) & ~63);
    w.ldsW = L_W + ii * 1024;
    w.sB = (unsigned)(c * 32 + (wr & 1) * 8) * 2048u;
    w.ldsB = L_BS + wr * 1024;
    if (GRP == 0) { w.rxb = mk_rsrc(A.ws + WS_XB + blockIdx.x * 32768u, 32768u); w.voffE = wr * 1024 + lane * 16; w.ones = (u32x4){~0u, ~0u, ~0u, ~0u}; PIN(w.ones);
      const int l = lane < 11 ? lane : 0;
      w.pfp = l < 7 ? (const char*)A.wd + blockIdx.x * 28672u + (wr * 56 + l) * 128 : (const char*)(l < 9 ? A.cre : A.cim) + blockIdx.x * 8192u + (wr * 16 + ((l - 7) & 1)) * 128;
      w.pfs = l < 7 ? 896u : 256u; }
  }
  if (GRP == 0) { w.template dmaX<0>(0); w.template dmaB<0>(0); w.template dmaW<0, 1>(0); w.template dmaX<1>(1); w.template dmaB<1>(1); }
  else { w.template dmaW<0, 0>(0); w.template dmaX<0>(0); w.template dmaW<1, 0>(1); }
  {
#pragma unroll
    for (int ks = 0; ks < 2; ++ks) {
      const unsigned ch = ((g + 4 * ks) ^ ((r >> 1) & 7)) << 4;
      w.aX[ks] = mk_l(L_X + (wr * 64 + r) * 128 + ch);
      if (NS) w.aB[ks] = mk_l(L_BS + r * 128 + ch);
    }
    const unsigned swz = qq | ((g & 1) << 2);
#pragma unroll
    for (int n = 0; n < NW; ++n) {
      unsigned col;
      if (GRP == 0) col = (((2 + n) ^ swz) << 5) + pp * 8;
      else if (n < 2) col = (((((c & 1) ? 6 : 0) + n) ^ swz) << 5) + pp * 8;
      else col = (((((c & 1) ? 0 : 6) + (pp >> 1)) ^ swz) << 5) + (c & 1) * 16 + (pp & 1) * 8;
      w.aW[n] = mk_l(L_W + (8 * g + qq) * 256 + col);
    }
  }
#pragma unroll
  for (int m = 0; m < 4; ++m) {
#pragma unroll
    for (int n = 0; n < NW; ++n) { w.ag[n][m] = ZERO4; w.au[n][m] = ZERO4; PIN(w.ag[n][m]); PIN(w.au[n][m]); }
#pragma unroll
    for (int i = 0; i < NS; ++i) { w.as[i][m] = ZERO4; PIN(w.as[i][m]); }
  }
  if (GRP == 0) asm volatile("s_waitcnt vmcnt(16)" : "+v"(ep0) :: "memory");
  else asm volatile("s_waitcnt vmcnt(12)" : "+v"(ep0), "+v"(plr), "+v"(pli), "+v"(pld) :: "memory");
  epoch = __builtin_amdgcn_readfirstlane(ep0) + 1u;
  if (GRP == 1) {
    if (wr == 0) {
      const float lr = bf2f(plr), li = bf2f(pli);
      const float dt_ = bf2f(pk_bf16(__expf(bf2f(pld)), 0.f) & 0xffffu);
      const float e = __expf(dt_ * lr), rev = __builtin_amdgcn_fractf(dt_ * li * 0.15915494309189535f);
      const f32x2 lam = {e * __builtin_amdgcn_cosf(rev), e * __builtin_amdgcn_sinf(rev)};
      const float nr = lam.x - 1.f, rden = 1.f / (lr * lr + li * li);
      f32x2 l16 = cmul(lam, lam); l16 = cmul(l16, l16); l16 = cmul(l16, l16); l16 = cmul(l16, l16);
      const f32x2 l32 = cmul(l16, l16), l64 = cmul(l32, l32), l128 = cmul(l64, l64), l256 = cmul(l128, l128);
      const lptr pb = mk_l(L_PRM + (lane & 31) * 48);
      *(LAS f32x4*)(pb) = (f32x4){lam.x, lam.y, (nr * lr + lam.y * li) * rden, (lam.y * lr - nr * li) * rden};
      *(LAS f32x4*)(pb + 16) = (f32x4){l16.x, l16.y, l32.x, l32.y};
      *(LAS f32x4*)(pb + 32) = (f32x4){l64.x, l64.y, l256.x, l256.y};
    } else if (wr == 1) {
      *(LAS unsigned*)mk_l(L_FLG + (lane & 7) * 4) = 0u;
    }
    WAIT_V(4);
    if (wr == 2) {
      __hip_atomic_store((GAS u64*)(A.ws + WS_AG) + (c * 8 + rch) * 64 + lane, (u64)(epoch - 1u) << 32, RLX_AGENT);
      if (lane < 4) __hip_atomic_store((GAS unsigned*)(A.ws + WS_FL + rch * 512 + lane * 128 + c * 4), epoch - 1u, RLX_AGENT);
    }
  } else WAIT_V(10);
  WAIT_L0(); BAR();
  if (GRP == 0) { w.template load<0, 0>(); WAIT_L0(); }
  BAR();
  STAMP();
  for (int T = 0; T < 14; T += 2) {
    TS2(); p1_tile<0>(w, T); TS2();
    p1_tile<1>(w, T + 1);
  }
  STAMP();
  lane = fresh_lane();
  q.rh = mk_rsrc(A.ws + WS_H, (unsigned)H_BYTES); q.rw = mk_rsrc(A.wd, F_ * D_ * 2); q.rc = mk_rsrc(kh ? A.cim : A.cre, D_ * P_ * 2);
  q.voffW = (lane >> 4) * 2048 + ((((lane & 15) >> 1) ^ (((wr & 1) << 2) | (lane >> 4))) * 32) + (lane & 1) * 16;
  {
    const int i0 = (wr & 1) + (wr >> 1) * 8;
    q.sH = (unsigned)(tt * NKF) * 8192u + (wr & 1) * 4096u;
    q.sW = (unsigned)((wr & 1) * 4) * 2048u + dt * 256u;
    q.sC = (unsigned)(dt * 128 + i0 * 8) * 2048u - (unsigned)NF * 128u;
    q.kfa = 56 * kh + 1 + 3 * (wr >> 1);
    q.e0 = (kh ? (sender ? 32 : 16) : 0) - 32 + (wr >> 1);
    q.kfs = 112 + 32 * kh - 2 * NF + (wr >> 1);
    q.ldsA = 32768 + wr * 4096; q.ldsB = i0 * 1024;
  }
#define P2_B(st) do { _Pragma("unroll") for (int s_ = 0; s_ < 4; ++s_) q.template dmaB1<false>(st, q.kf_ffn(st), s_); } while (0)
  u32x4 gx[4], gg[4]; u32x2 xv[2][4], dv[4];
  const int tg = rch * 256 + c * 8 + wr * 2 + (lane >> 5);
  if constexpr (GRP == 0) {
    const int trow = tt * 128 + wm * 64 + ((lane & 15) >> 2) * 16 + (lane & 3) + (sender ? 0 : 8), d0 = dt * 128 + wn * 64 + 4 * (lane >> 4);
    w.compute(); WAIT_V(6); BAR();
    w.template load<0, 1>(); w.template dmaW<1, 1>(15);
#pragma unroll
    for (int n = 0; n < 4; ++n) xv[0][n] = *(const u32x2*)(A.x + trow * D_ + d0 + n * 16);
#pragma unroll
    for (int i = 0; i < 2; ++i) gx[i] = *(const u32x4*)(A.x + tg * D_ + i * 256 + (lane & 31) * 8);
    WAIT_L0(); BAR();
    w.compute(); WAIT_V(10); BAR();
    w.template load<1, 0>(); P2_B(0);
#pragma unroll
    for (int n = 0; n < 4; ++n) xv[1][n] = *(const u32x2*)(A.x + (trow + 4) * D_ + d0 + n * 16);
#pragma unroll
    for (int i = 2; i < 4; ++i) gx[i] = *(const u32x4*)(A.x + tg * D_ + i * 256 + (lane & 31) * 8);
    WAIT_L0(); BAR();
    w.compute(); WAIT_V(16); BAR();
    w.template load<1, 1>(); P2_B(2);
#pragma unroll
    for (int n = 0; n < 4; ++n) { dv[n] = *(const u32x2*)(A.dsk + d0 + n * 16); gg[n] = *(const u32x4*)(A.gw + n * 256 + (lane & 31) * 8); }
    WAIT_L0(); BAR();
    w.compute(); BAR();
    __builtin_amdgcn_s_setprio(3);
  } else {
    w.template load<0, 0>(); w.template dmaX<1>(15); WAIT_L0(); BAR();
    w.compute(); BAR();
    w.template load<0, 1>(); WAIT_V(0); WAIT_L0(); BAR();
    w.compute(); BAR();
    w.template load<1, 0>(); P2_B(1); WAIT_L0(); BAR();
    w.compute(); BAR();
    w.template load<1, 1>(); P2_B(3); WAIT_L0(); BAR();
    w.compute();
  }
  MFMA_DRAIN();
  STAMP();
  lane = fresh_lane(); r = lane & 15; g = lane >> 4; qq = (lane & 15) >> 2; pp = lane & 3;
  const unsigned hb = (unsigned)WS_H + (unsigned)(2 * rch + (wr >> 1)) * NKF * 8192u + (wr & 1) * 4096u;
  const int kf0 = 7 * (c >> 1) + 3 * (c & 1);
  q.voffH = lane * 16;
#define FFN_H(n, m, h) pk_bf16(fmaxf(w.ag[n][m][2 * (h)], 0.f) * w.au[n][m][2 * (h)], fmaxf(w.ag[n][m][2 * (h) + 1], 0.f) * w.au[n][m][2 * (h) + 1])
  if constexpr (GRP == 0) {
#pragma unroll
    for (int f = 0; f < 2; ++f)
#pragma unroll
      for (int m = 0; m < 4; ++m) {
        __builtin_amdgcn_raw_buffer_store_b128((u32x4){FFN_H(2 * f, m, 0), FFN_H(2 * f, m, 1), FFN_H(2 * f + 1, m, 0), FFN_H(2 * f + 1, m, 1)}, rws,
                                               lane * 16 + m * 1024 + hb + (kf0 + 1 + f) * 8192u, 0, 16);
        SCHED();
      }
    __builtin_amdgcn_s_setprio(0);
    STAMP();
    {
      float s = 0.f;
#pragma unroll
      for (int i = 0; i < 4; ++i)
#pragma unroll
        for (int j = 0; j < 4; ++j) { s = __builtin_fmaf(bf2f(gx[i][j] & 0xffffu), bf2f(gg[i][j] & 0xffffu), s); s = __builtin_fmaf(bfhi(gx[i][j]), bfhi(gg[i][j]), s); }
#pragma unroll
      for (int o = 16; o >= 1; o >>= 1) s += __int_as_float(__builtin_amdgcn_ds_bpermute((lane ^ o) * 4, __float_as_int(s)));
      if ((lane & 31) == 0) __hip_atomic_store((GAS unsigned*)(A.ws + WS_G) + tg, __float_as_uint(1.f / (1.f + __expf(-s))), RLX_AGENT);
    }
#pragma unroll
    for (int n = 0; n < 4; ++n) {
      const f32x4 df = (f32x4){bf2f(dv[n][0] & 0xffffu), bfhi(dv[n][0]), bf2f(dv[n][1] & 0xffffu), bfhi(dv[n][1])} * 0.5f;
#pragma unroll
      for (int mm = 0; mm < 2; ++mm) {
        const f32x4 v = (f32x4){bf2f(xv[mm][n][0] & 0xffffu), bfhi(xv[mm][n][0]), bf2f(xv[mm][n][1] & 0xffffu), bfhi(xv[mm][n][1])} * df;
        q.as[mm][n] = sender ? v : ZERO4; q.as[2 + mm][n] = sender ? ZERO4 : v;
      }
    }
#pragma unroll
    for (int m = 0; m < 4; ++m)
#pragma unroll
      for (int n = 0; n < 4; ++n) { q.af[m][n] = ZERO4; PIN(q.af[m][n]); PIN(q.as[m][n]); }
    WAIT_V(0); arrive(A.ws, lane, (unsigned)WS_FL + rch * 512 + (c >> 4) * 128 + c * 4, 0, epoch);
    STAMP();
    wait_flags(A.ws, (unsigned)WS_FL + rch * 512 + kh * 192 + (lane & 15) * 4, epoch);
    STAMP();
#pragma unroll
    for (int st = 0; st < 4; ++st)
#pragma unroll
      for (int s = 0; s < 4; ++s) q.dmaA1(st, q.kf_ffn(st), s);
    STAMP();
  } else {
    f32x2 lam[2], coef[2], l16[2], l32[2], l64[2], l256[2], cin[2], sw[2];
    __builtin_amdgcn_s_setprio(2);
#pragma unroll
    for (int pf = 0; pf < 2; ++pf) {
      const lptr pb = mk_l(L_PRM + (pf * 16 + r) * 48);
      const f32x4 p0 = *(const LAS f32x4*)pb, p1 = *(const LAS f32x4*)(pb + 16), p2 = *(const LAS f32x4*)(pb + 32);
      lam[pf] = (f32x2){p0[0], p0[1]}; coef[pf] = (f32x2){p0[2], p0[3]}; l16[pf] = (f32x2){p1[0], p1[1]}; l32[pf] = (f32x2){p1[2], p1[3]};
      l64[pf] = (f32x2){p2[0], p2[1]}; l256[pf] = (f32x2){p2[2], p2[3]};
    }
    f32x2 e[2] = {{0.f, 0.f}, {0.f, 0.f}}, agg[2];
#pragma unroll
    for (int m = 0; m < 4; ++m)
#pragma unroll
      for (int j = 0; j < 4; ++j)
#pragma unroll
        for (int pf = 0; pf < 2; ++pf) e[pf] = cfma(lam[pf], e[pf], (f32x2){w.as[2 * pf][m][j], w.as[2 * pf + 1][m][j]});
#pragma unroll
    for (int pf = 0; pf < 2; ++pf) {
      float a0, a1, b0, b1; sw16(e[pf].x, a0, a1); sw16(e[pf].y, b0, b1);
      const f32x2 r0 = {a0, b0};
      const f32x2 xx = cfma(l16[pf], r0, (f32x2){a1, b1});
      float c0, c1, d0, d1; sw32(xx.x, c0, c1); sw32(xx.y, d0, d1);
      const f32x2 s0 = {c0, d0};
      agg[pf] = cfma(l32[pf], s0, (f32x2){c1, d1});
      const f32x2 c3 = cfma(l16[pf], s0, r0);
      cin[pf] = g == 0 ? (f32x2){0.f, 0.f} : g == 1 ? r0 : g == 2 ? s0 : c3;
    }
    if (wr < 3) {
      if (g < 2) *(LAS f32x2*)mk_l(L_WA + ((g * 16 + r) * 4 + wr) * 8) = g ? agg[1] : agg[0];
      WAIT_L0();
      *(volatile LAS unsigned*)mk_l(L_FLG + wr * 4) = 1u;
    }
    sw[0] = (f32x2){0.f, 0.f}; sw[1] = sw[0];
    if (wr > 0) {
      const lptr fp = mk_l(L_FLG);
_Pragma("nounroll")
      for (unsigned spins = 0; spins < SPIN_CAP; ++spins) {
        const u32x4 f = *(volatile LAS u32x4*)fp;
        const unsigned ok = f[0] & (wr > 1 ? f[1] : 1u) & (wr > 2 ? f[2] : 1u);
        if (__builtin_amdgcn_readfirstlane(ok)) break;
      }
      asm volatile("" ::: "memory");
#pragma unroll
      for (int pf = 0; pf < 2; ++pf) {
        const lptr ap = mk_l(L_WA + (pf * 16 + r) * 32);
        const f32x4 a01 = *(const LAS f32x4*)ap; const f32x2 a2 = *(const LAS f32x2*)(ap + 16);
        sw[pf] = (f32x2){a01[0], a01[1]};
        if (wr > 1) sw[pf] = cfma(l64[pf], sw[pf], (f32x2){a01[2], a01[3]});
        if (wr > 2) sw[pf] = cfma(l64[pf], sw[pf], a2);
      }
      if (wr == 3 && rch < 7) {
        const f32x2 t0 = cfma(l64[0], sw[0], agg[0]), t1 = cfma(l64[1], sw[1], agg[1]);
        const f32x2 t = (g & 1) ? t1 : t0;
        __hip_atomic_store((GAS u64*)(A.ws + WS_AG) + (c * 8 + rch) * 64 + lane, ((u64)epoch << 32) | __float_as_uint((g & 2) ? t.y : t.x), RLX_AGENT);
      }
    }
    STAMP();
#pragma unroll
    for (int m = 0; m < 4; ++m) { hq[m] = (u32x4){FFN_H(0, m, 0), FFN_H(0, m, 1), FFN_H(1, m, 0), FFN_H(1, m, 1)}; PIN(hq[m]); }
#pragma unroll
    for (int m = 0; m < 4; m += 2) {
      const auto s0 = __builtin_amdgcn_permlane32_swap(FFN_H(2, m, 0), FFN_H(2, m + 1, 0), false, false);
      const auto s1 = __builtin_amdgcn_permlane32_swap(FFN_H(2, m, 1), FFN_H(2, m + 1, 1), false, false);
      hq[4 + m / 2] = (u32x4){s0[0], s1[0], s0[1], s1[1]}; PIN(hq[4 + m / 2]);
    }
    ho[0] = lane * 16 + hb + (kf0 + ((c & 1) ? 3 : 0)) * 8192u;
    ho[1] = (lane & 31) * 16 + (lane >> 5) * 1024 + (c & 1) * 512 + hb + (kf0 + ((c & 1) ? 0 : 3)) * 8192u;
    ho[2] = lane * 16 + hb + (112 + c) * 8192u;
    STAMP();
    const GAS u64* agp = (const GAS u64*)(A.ws + WS_AG) + c * 8 * 64 + lane;
    u64 gv_[7];
#pragma unroll
    for (int pf = 0; pf < 2; ++pf)
#pragma unroll
      for (int m = 0; m < 4; ++m)
#pragma unroll
        for (int j = 0; j < 4; ++j) {
          const f32x2 u = cmul(coef[pf], (f32x2){w.as[2 * pf][m][j], w.as[2 * pf + 1][m][j]});
          w.as[2 * pf][m][j] = u.x; w.as[2 * pf + 1][m][j] = u.y;
        }
#pragma unroll
    for (int i = 0; i < 4; ++i)
#pragma unroll
      for (int m = 0; m < 4; ++m) PIN(w.as[i][m]);
#pragma unroll
    for (int cc = 0; cc < 7; ++cc) gv_[cc] = __hip_atomic_load(agp + cc * 64, RLX_AGENT);
    STAMP();
_Pragma("nounroll")
    for (unsigned spins = 0; spins < SPIN_CAP; ++spins) {
      bool ok = true;
#pragma unroll
      for (int cc = 0; cc < 7; ++cc) ok &= (cc >= rch) || (unsigned)(gv_[cc] >> 32) == epoch;
      if (__all(ok)) break;
      __builtin_amdgcn_s_sleep(1);
#pragma unroll
      for (int cc = 0; cc < 7; ++cc) gv_[cc] = __hip_atomic_load(agp + cc * 64, RLX_AGENT);
    }
    STAMP();
    const f32x2 lq = (g & 1) ? l256[1] : l256[0];
    f32x2 cb = {0.f, 0.f};
#pragma unroll
    for (int cc = 0; cc < 7; ++cc) {
      float are, aim; sw32(__uint_as_float((unsigned)gv_[cc]), are, aim);
      if (cc < rch) cb = cfma(lq, cb, (f32x2){are, aim});
    }
    f32x2 cbp[2]; { float a0, a1, b0, b1; sw16(cb.x, a0, a1); sw16(cb.y, b0, b1); cbp[0] = (f32x2){a0, b0}; cbp[1] = (f32x2){a1, b1}; }
#pragma unroll
    for (int pf = 0; pf < 2; ++pf) {
      f32x2 pw = (wr & 1) ? l64[pf] : (f32x2){1.f, 0.f};
      if (wr & 2) pw = cmul(pw, cmul(l64[pf], l64[pf]));
      const f32x2 ew = cfma(pw, cbp[pf], sw[pf]);
      f32x2 pq = (g & 1) ? l16[pf] : (f32x2){1.f, 0.f};
      if (g & 2) pq = cmul(pq, l32[pf]);
      e[pf] = cmul(coef[pf], cfma(pq, ew, cin[pf]));
    }
    unsigned hr[2][8], hi[2][8];
#pragma unroll
    for (int m = 0; m < 4; ++m) {
      float tr[2][4], ti[2][4];
#pragma unroll
      for (int j = 0; j < 4; ++j)
#pragma unroll
        for (int pf = 0; pf < 2; ++pf) {
          e[pf] = cfma(lam[pf], e[pf], (f32x2){w.as[2 * pf][m][j], w.as[2 * pf + 1][m][j]});
          tr[pf][j] = e[pf].x; ti[pf][j] = -e[pf].y;
        }
#pragma unroll
      for (int pf = 0; pf < 2; ++pf) {
        hr[pf][2 * m] = pk_bf16(tr[pf][0], tr[pf][1]); hr[pf][2 * m + 1] = pk_bf16(tr[pf][2], tr[pf][3]);
        hi[pf][2 * m] = pk_bf16(ti[pf][0], ti[pf][1]); hi[pf][2 * m + 1] = pk_bf16(ti[pf][2], ti[pf][3]);
      }
    }
    const lptr tb = mk_l(L_TR + wr * 4608 + r * 144 + g * 32), rb = mk_l(L_TR + wr * 4608 + (8 * g + qq) * 144 + pp * 32);
#pragma unroll
    for (int ri = 0; ri < 2; ++ri) {
#pragma unroll
      for (int pf = 0; pf < 2; ++pf) {
        const unsigned* h = ri ? hi[pf] : hr[pf];
        *(LAS u32x4*)(tb + pf * 2304) = (u32x4){h[0], h[1], h[2], h[3]};
        *(LAS u32x4*)(tb + pf * 2304 + 16) = (u32x4){h[4], h[5], h[6], h[7]};
      }
      WAIT_L0();
#pragma unroll
      for (int m = 0; m < 4; ++m) hq[6 + 4 * ri + m] = __builtin_bit_cast(u32x4, lds_tr2(rb + m * 8, rb + 4 * 144 + m * 8));
      WAIT_L0();
    }
  }
  }
  STAMP(); TS2();
  constexpr int G = GRP;
  int r = lane & 15, g = lane >> 4;
  {
    const int qq = (lane & 15) >> 2, pp = lane & 3;
    q.voffC = (lane >> 3) * 2048 + (((lane & 7) ^ (((wr & 1) * 4 + (lane >> 4)) & 7)) * 16);
    const unsigned gb = G * 65536;
    q.aH = mk_l(gb + 32768 + wm * 4096 + lane * 16);
    const unsigned swz = 4 * (g & 1) + qq;
#pragma unroll
    for (int n = 0; n < 4; ++n) q.aW[n] = mk_l(gb + (4 * g + qq) * 256 + (((wn * 4 + n) ^ swz) << 5) + pp * 8);
#pragma unroll
    for (int kk = 0; kk < 2; ++kk) q.aC[kk] = mk_l(gb + (wn * 64 + r) * 128 + (((g + 4 * kk) ^ ((r >> 1) & 7)) << 4));
  }
#pragma unroll
  for (int m = 0; m < 4; ++m)
#pragma unroll
    for (int n = 0; n < 4; ++n) if (G == 1) { q.af[m][n] = ZERO4; PIN(q.af[m][n]); }
  unsigned fl = 0u; const unsigned flo = (unsigned)WS_FL + rch * 512 + 256 + lane * 4;
#define FLAG_LOAD() asm volatile("global_load_dword %0, %1, %2 sc1" : "+v"(fl) : "v"(flo), "s"(A.ws) : "memory")
#define FLAG_CHECK(level) do { PIN(fl); const u64 bal_ = __ballot(fl == epoch); \
    if ((unsigned)((level) == 2 ? bal_ : bal_ >> 32) != ~0u) wait_flags(A.ws, (flo & ~128u) + ((level) - 2) * 128, epoch); } while (0)
#define P2_STEP(SB, SSM, DK, n) do { q.template load<SB, SSM, DK>((n) + 3); if (DK) WAIT_V(8); else WAIT_V(0); \
       WAIT_L0(); BAR(); q.template compute<SSM>(); BAR(); TS2(); } while (0)
#define HQ_ST(i) __builtin_amdgcn_raw_buffer_store_b128(hq[i], rws, (i) < 4 ? ho[0] + (i) * 1024 : (i) < 6 ? ho[1] + ((i) - 4) * 2048 : ho[2] + (((i) - 6) >> 2) * 262144 + (((i) - 6) & 3) * 1024, 0, 16)
#define RAISE(level) arrive(A.ws, lane, (unsigned)WS_FL + rch * 512 + (level) * 128 + c * 4, (level) - 1, epoch)
#define CBAR() do { WAIT_L0(); BAR(); q.template compute<false>(); BAR(); TS2(); } while (0)
  float gv[4];
#define GV_LOAD() _Pragma("unroll") for (int m = 0; m < 4; ++m) gv[m] = __uint_as_float(__hip_atomic_load((const GAS unsigned*)(A.ws + WS_G) + tt * 128 + wm * 64 + (r >> 2) * 16 + (r & 3) + m * 4, RLX_AGENT))
  if (G == 0) {
    WAIT_V(12); BAR(); STAMP(); q.template load<0, false, 0>(0); WAIT_V(8); WAIT_L0(); BAR(); q.template compute<false>(); WAIT_V(4); BAR(); TS2();
    P2_STEP(1, false, 1, 2);
    STAMP();
    for (int n = 4; n < NF - 4; n += 4) {
      q.template load<0, false, 1>(n + 3); WAIT_V(8); if (n == 12) FLAG_CHECK(2); CBAR();
      q.template load<1, false, 1>(n + 5); FLAG_LOAD(); WAIT_V(9); CBAR();
    }
    q.template load<0, false, 1>(NF - 1); WAIT_V(8); FLAG_CHECK(3); CBAR();
    if (blockIdx.x == 255 && wr == 0 && lane == 0) __hip_atomic_store((GAS unsigned*)(A.ws + WS_EP), epoch, RLX_AGENT);
    q.template load<1, false, 2>(NF + 1); GV_LOAD(); WAIT_V(12); CBAR();
  } else {
    __builtin_amdgcn_s_setprio(0);
    WAIT_V(0); BAR(); STAMP(); BAR();
    q.template load<0, false, 1>(4); HQ_ST(0); HQ_ST(1); HQ_ST(2); CBAR();
    q.template load<1, false, 1>(6); HQ_ST(3); HQ_ST(4); HQ_ST(5); WAIT_V(14); CBAR();
    STAMP();
    q.template load<0, false, 1>(8); HQ_ST(6); HQ_ST(7); WAIT_V(13); CBAR();
    q.template load<1, false, 1>(10); HQ_ST(8); HQ_ST(9); WAIT_V(12); RAISE(2); CBAR();
    q.template load<0, false, 1>(12); HQ_ST(10); HQ_ST(11); WAIT_V(12); CBAR();
    q.template load<1, false, 1>(14); HQ_ST(12); HQ_ST(13); WAIT_V(12); CBAR();
#pragma unroll
    for (int m = 0; m < 4; ++m)
#pragma unroll
      for (int n = 0; n < 4; ++n) { q.as[m][n] = ZERO4; PIN(q.as[m][n]); }
    for (int n = 13; n < NF - 4; n += 4) {
      q.template load<0, false, 1>(n + 3); WAIT_V(8); if (n == 13) RAISE(3); CBAR();
      P2_STEP(1, false, 1, n + 2);
    }
    P2_STEP(0, false, 2, NF - 3);
    q.template load<1, false, 2>(NF + 2); GV_LOAD(); WAIT_V(12); CBAR();
  }
  STAMP();
  for (int n = NF + G; n < NF + 12; n += 4) { P2_STEP(0, true, 2, n); P2_STEP(1, true, 2, n + 2); }
  if (G == 0) P2_STEP(0, true, 2, NF + 12); else P2_STEP(0, true, 0, NF + 13);
  q.template load<1, true, 0>(0); WAIT_V(0);
  lane = fresh_lane(); r = lane & 15; g = lane >> 4;
  const int trow = tt * 128 + wm * 64 + (r >> 2) * 16 + (r & 3), d0 = dt * 128 + wn * 64 + 4 * g;
  const unsigned xo = (unsigned)WS_XB + (blockIdx.x >> 1) * 65536u + wid * 8192u + lane * 16;
  u32x4 pv[2][4];
#pragma unroll
  for (int m = 0; m < 4; ++m) PIN(gv[m]);
  if (!sender) {
#pragma unroll
    for (int i = 0; i < 8; ++i) pv[i >> 2][i & 3] = __builtin_amdgcn_raw_buffer_load_b128(rws, xo + i * 1024, 0, 16);
  }
  WAIT_L0(); BAR(); q.template compute<true>();
  if (G == 0) BAR();
  MFMA_DRAIN();
  STAMP();
  const lptr rw = mk_l(wid * 8192 + lane * 16), rr = mk_l((wid ^ 4) * 8192 + lane * 16);
#pragma unroll
  for (int mm = 0; mm < 2; ++mm)
#pragma unroll
    for (int n = 0; n < 4; ++n) {
      const int m = G ? mm : 2 + mm;
      *(LAS f32x4*)(rw + (mm * 4 + n) * 1024) = q.af[m][n] * (1.f - gv[m]) + q.as[m][n] * (2.f * gv[m]);
    }
  f32x4 o[2][4];
#pragma unroll
  for (int mm = 0; mm < 2; ++mm)
#pragma unroll
    for (int n = 0; n < 4; ++n) { const int m = G ? 2 + mm : mm; o[mm][n] = q.af[m][n] * (1.f - gv[m]) + q.as[m][n] * (2.f * gv[m]); }
  WAIT_L0(); BAR();
#pragma unroll
  for (int mm = 0; mm < 2; ++mm)
#pragma unroll
    for (int n = 0; n < 4; ++n) o[mm][n] += *(const LAS f32x4*)(rr + (mm * 4 + n) * 1024);
  STAMP();
  if (sender) {
#pragma unroll
    for (int mm = 0; mm < 2; ++mm)
#pragma unroll
      for (int n = 0; n < 4; ++n) {
        u32x4 b = __builtin_bit_cast(u32x4, o[mm][n]);
#pragma unroll
        for (int j = 0; j < 4; ++j) b[j] = b[j] < 0xfffffffeu ? b[j] : 0xfffffffeu;
        __builtin_amdgcn_raw_buffer_store_b128(b, rws, xo + (mm * 4 + n) * 1024, 0, 16);
      }
    STAMP();
  } else {
_Pragma("nounroll")
    for (unsigned spins = 0; spins < SPIN_CAP; ++spins) {
      unsigned mx = 0;
#pragma unroll
      for (int mm = 0; mm < 2; ++mm)
#pragma unroll
        for (int n = 0; n < 4; ++n)
#pragma unroll
          for (int j = 0; j < 4; ++j) mx = pv[mm][n][j] > mx ? pv[mm][n][j] : mx;
      if (__all(mx != 0xffffffffu)) break;
      asm volatile("" ::: "memory");
#pragma unroll
      for (int i = 0; i < 8; ++i) pv[i >> 2][i & 3] = __builtin_amdgcn_raw_buffer_load_b128(rws, xo + i * 1024, 0, 16);
    }
    STAMP();
#pragma unroll
    for (int mm = 0; mm < 2; ++mm)
#pragma unroll
      for (int n = 0; n < 4; ++n) *(f32x4*)(A.out + (size_t)(trow + (2 * G + mm) * 4) * D_ + d0 + n * 16) = o[mm][n] + __builtin_bit_cast(f32x4, pv[mm][n]);
  }
  STAMP();
  ST_DUMP(A.ws, wid);
  __builtin_amdgcn_endpgm();
}

__global__ __launch_bounds__(512, 2) void fused_kernel(Args A) {
  const int wid = __builtin_amdgcn_readfirstlane(threadIdx.x >> 6);
  if (wid < 4) body<0>(A, wid); else body<1>(A, wid);
}

extern "C" void kernel_launch(void* const* d_in, const int* in_sizes, int n_in, void* d_out, int out_size, void* d_ws, size_t ws_size,
                              hipStream_t stream) {
  Args a; a.x = (const u16*)d_in[0]; a.lre = (const u16*)d_in[1]; a.lim = (const u16*)d_in[2]; a.bre = (const u16*)d_in[3];
  a.bim = (const u16*)d_in[4]; a.cre = (const u16*)d_in[5]; a.cim = (const u16*)d_in[6]; a.ldt = (const u16*)d_in[7]; a.dsk = (const u16*)d_in[8];
  a.wg = (const u16*)d_in[10]; a.wu = (const u16*)d_in[11]; a.wd = (const u16*)d_in[12]; a.gw = (const u16*)d_in[13];
  a.ws = (char*)d_ws; a.out = (float*)d_out;
  (void)hipFuncSetAttribute((const void*)fused_kernel, hipFuncAttributeMaxDynamicSharedMemorySize, LDS_BYTES);
  fused_kernel<<<256, 512, LDS_BYTES, stream>>>(a);
}
```

```cpp
#include <hip/hip_runtime.h>
#include <hip/hip_bf16.h>
#define PER 2
#define PFD 4
#define ILV 1

typedef unsigned short u16;
typedef short bf16x8 __attribute__((ext_vector_type(8)));
typedef short s16x4 __attribute__((ext_vector_type(4)));
typedef float f32x4 __attribute__((ext_vector_type(4)));
typedef float f32x2 __attribute__((ext_vector_type(2)));
typedef unsigned u32x4 __attribute__((ext_vector_type(4)));
typedef unsigned u32x2 __attribute__((ext_vector_type(2)));
typedef unsigned long long u64;
#define LAS __attribute__((address_space(3)))
#define GAS __attribute__((address_space(1)))
#define RLX_AGENT __ATOMIC_RELAXED, __HIP_MEMORY_SCOPE_AGENT
typedef __amdgpu_buffer_rsrc_t rsrc_t;

constexpr int L_ = 2048, D_ = 1024, P_ = 1024, F_ = 3584;
constexpr int NKF = 176;
constexpr size_t WS_H = 0;
constexpr size_t H_BYTES = (size_t)16 * NKF * 8 * 1024;
constexpr size_t WS_XB = WS_H + H_BYTES;
constexpr size_t XB_BYTES = (size_t)256 * 32768;
constexpr size_t WS_G = WS_XB + XB_BYTES;
constexpr size_t WS_AG = WS_G + 8192;
constexpr size_t WS_EP = WS_AG + (size_t)32 * 8 * 64 * 8;
constexpr size_t WS_FL = WS_EP + 512;
constexpr size_t WS_XF = WS_FL + 4096;
constexpr size_t WS_END = WS_XF + 128 * 128;
constexpr unsigned SPIN_CAP = 1u << 20;

__device__ __forceinline__ float bf2f(unsigned b) { return __uint_as_float(b << 16); }
__device__ __forceinline__ float bfhi(unsigned b) { return __uint_as_float(b & 0xffff0000u); }
__device__ __forceinline__ unsigned pk_bf16(float lo, float hi) {
  typedef __bf16 bf2_t __attribute__((ext_vector_type(2)));
  return __builtin_bit_cast(unsigned, __builtin_convertvector((f32x2){lo, hi}, bf2_t)); }
__device__ __forceinline__ float relu(float v) { const int b = __float_as_int(v); return __int_as_float(b > 0 ? b : 0); }
template <int CTRL, int BANKS> __device__ __forceinline__ float dpp(float old, float src) {
  return __int_as_float(__builtin_amdgcn_update_dpp(__float_as_int(old), __float_as_int(src), CTRL, 0xf, BANKS, false)); }
__device__ __forceinline__ rsrc_t mk_rsrc(const void* p, unsigned bytes) {
  return __builtin_amdgcn_make_buffer_rsrc((void*)p, (short)0, (int)bytes, 0x00020000); }
__device__ __forceinline__ void dma16(unsigned ldsaddr, unsigned voff, rsrc_t srd, unsigned soff) {
  asm volatile("s_mov_b32 m0, %0\n\ts_nop 0\n\tbuffer_load_dwordx4 %1, %2, %3 offen lds"
               :: "s"(ldsaddr), "v"(voff), "s"(srd), "s"(soff) : "memory"); }
__device__ __forceinline__ void dma16_dev(unsigned ldsaddr, unsigned voff, rsrc_t srd, unsigned soff) {
  asm volatile("s_mov_b32 m0, %0\n\ts_nop 0\n\tbuffer_load_dwordx4 %1, %2, %3 offen sc1 lds"
               :: "s"(ldsaddr), "v"(voff), "s"(srd), "s"(soff) : "memory"); }
__device__ __forceinline__ void dma4(unsigned ldsaddr, unsigned voff, rsrc_t srd, unsigned soff) {
  asm volatile("s_mov_b32 m0, %0\n\ts_nop 0\n\tbuffer_load_dword %1, %2, %3 offen lds"
               :: "s"(ldsaddr), "v"(voff), "s"(srd), "s"(soff) : "memory"); }
#define WAIT_V(n) asm volatile("s_waitcnt vmcnt(" #n ")" ::: "memory")
#define WAIT_L0() do { __builtin_amdgcn_s_waitcnt(0xC07F); asm volatile("" ::: "memory"); } while (0)
#define SCHED() __builtin_amdgcn_sched_barrier(0)
#define BAR() do { SCHED(); __builtin_amdgcn_s_barrier(); SCHED(); } while (0)
__device__ __forceinline__ int fresh_lane() { int l; asm volatile("v_mbcnt_lo_u32_b32 %0, -1, 0\n\tv_mbcnt_hi_u32_b32 %0, -1, %0" : "=v"(l)); return l; }
typedef LAS char* lptr;
__device__ __forceinline__ lptr mk_l(unsigned a) { lptr p = (lptr)(size_t)a; asm volatile("" : "+v"(p)); return p; }
__device__ __forceinline__ bf16x8 lds_b128(lptr a) { return *(const LAS bf16x8*)a; }
__device__ __forceinline__ bf16x8 lds_tr2(lptr a0, lptr a1) {
  s16x4 lo = __builtin_amdgcn_ds_read_tr16_b64_v4i16((LAS s16x4*)a0);
  s16x4 hi = __builtin_amdgcn_ds_read_tr16_b64_v4i16((LAS s16x4*)a1);
  return __builtin_shufflevector(lo, hi, 0, 1, 2, 3, 4, 5, 6, 7); }
#define MFMA(a, b, c) asm volatile("v_mfma_f32_16x16x32_bf16 %0, %1, %2, %0" : "+v"(c) : "v"(a), "v"(b))
#define MFMA0(a, b, c) asm volatile("v_mfma_f32_16x16x32_bf16 %0, %1, %2, 0" : "=&v"(c) : "v"(a), "v"(b))
#define PIN(x) asm volatile("" : "+v"(x))
#define MFMA_DRAIN() do { asm volatile("s_nop 7\n\ts_nop 7" ::: "memory"); SCHED(); } while (0)
#define ZERO4 ((f32x4){0.f, 0.f, 0.f, 0.f})

#define ST_DECL
#define STAMP()
#define TS2()
#define ST_DUMP(ws, wid)

struct Args {
  const u16 *x, *lre, *lim, *bre, *bim, *cre, *cim, *ldt, *dsk, *wg, *wu, *wd, *gw;
  char* ws; float* out;
};

constexpr unsigned L_X = 0, L_W = 65536, L_BS = 131072, L_TR = 114688  , L_PRM = 151552  ,
                   L_WA = 153600  , L_FLG = 154624  , L_SINK = 154880, LDS_BYTES = 163840;

__device__ __forceinline__ f32x2 cmul(f32x2 a, f32x2 b) { return (f32x2){a.x * b.x - a.y * b.y, a.x * b.y + a.y * b.x}; }
__device__ __forceinline__ f32x2 cfma(f32x2 a, f32x2 b, f32x2 c) {
  return (f32x2){__builtin_fmaf(a.x, b.x, __builtin_fmaf(-a.y, b.y, c.x)), __builtin_fmaf(a.x, b.y, __builtin_fmaf(a.y, b.x, c.y))}; }
__device__ __forceinline__ void sw16(float v, float& r0, float& r1) { auto t = __builtin_amdgcn_permlane16_swap(__float_as_uint(v), __float_as_uint(v), false, false); r0 = __uint_as_float(t[0]); r1 = __uint_as_float(t[1]); }
__device__ __forceinline__ void sw32(float v, float& r0, float& r1) { auto t = __builtin_amdgcn_permlane32_swap(__float_as_uint(v), __float_as_uint(v), false, false); r0 = __uint_as_float(t[0]); r1 = __uint_as_float(t[1]); }

__device__ __forceinline__ void touch(const char* p) {
  asm volatile("s_mov_b32 m0, %0\n\ts_nop 0\n\tglobal_load_lds_dword %1, off" :: "s"(L_SINK), "v"(p) : "memory"); }
template <int GRP>
struct P1 {
  static constexpr int NW = GRP == 0 ? 4 : 3, NS = GRP == 0 ? 0 : 4;
  f32x4 ag[NW][4], au[NW][4], as[4][4];
  bf16x8 xa[4], wgf[NW], wuf[NW], bsf[4];
  lptr aX[2], aB[2], aW[NW];
  unsigned voffX, voffW, voffB;
  unsigned sX, sW, sB, ldsX, ldsW, ldsB;
  rsrc_t rx, rwg, rwu, rb;
  const char *pfp[2], *pfc; unsigned pfs[2]; int pfo[2], pfm[2], pfg;
  __device__ __forceinline__ void pull(int T) {
    const bool ev = (T & (PER - 1)) == pfg;
#pragma unroll
    for (int k = 0; k < 2; ++k) {
      const int i = T + pfo[k];
      const char* p = pfp[k] + pfs[k] * (unsigned)(i < 0 ? 0 : i > pfm[k] ? pfm[k] : i);
      touch(ev ? p : pfc);
    }
  }

  template <int BUF, int KS> __device__ __forceinline__ void load() {
#pragma unroll
    for (int n = 0; n < NW; ++n) {
      wgf[n] = lds_tr2(aW[n] + BUF * 32768 + KS * 8192, aW[n] + BUF * 32768 + KS * 8192 + 1024);
      wuf[n] = lds_tr2(aW[n] + BUF * 32768 + 16384 + KS * 8192, aW[n] + BUF * 32768 + 16384 + KS * 8192 + 1024);
    }
#pragma unroll
    for (int m = 0; m < 4; ++m) xa[m] = lds_b128(aX[KS] + BUF * 32768 + m * 2048);
#pragma unroll
    for (int i = 0; i < NS; ++i) bsf[i] = lds_b128(aB[KS] + BUF * 8192 + i * 2048);
  }
  __device__ __forceinline__ void compute() {
    __builtin_amdgcn_s_setprio(1);
#pragma unroll
    for (int i = 0; i < NS; ++i)
#pragma unroll
      for (int m = 0; m < 4; ++m) MFMA(xa[m], bsf[i], as[i][m]);
#pragma unroll
    for (int n = 0; n < NW; ++n) {
#pragma unroll
      for (int m = 0; m < 4; ++m) MFMA(wgf[n], xa[m], ag[n][m]);
#pragma unroll
      for (int m = 0; m < 4; ++m) MFMA(wuf[n], xa[3 - m], au[n][3 - m]);
    }
    __builtin_amdgcn_s_setprio(0);
  }
  template <int BUF> __device__ __forceinline__ void dmaX(int T) {
#pragma unroll
    for (int s = 0; s < 4; ++s) dma16(ldsX + BUF * 32768 + s * 2048, voffX, rx, sX + T * 128 + s * 8192);
  }
  template <int BUF, int HH> __device__ __forceinline__ void dmaW(int T) {
#pragma unroll
    for (int s = 0; s < 2; ++s) {
      const unsigned so = sW + (unsigned)(T * 64 + HH * 32 + s * 4) * 7168u;
      dma16(ldsW + BUF * 32768 + HH * 8192 + s * 1024, voffW, rwg, so);
      dma16(ldsW + BUF * 32768 + 16384 + HH * 8192 + s * 1024, voffW, rwu, so);
    }
  }
  template <int BUF> __device__ __forceinline__ void dmaB(int T) {
#pragma unroll
    for (int pf = 0; pf < 2; ++pf) dma16(ldsB + BUF * 8192 + pf * 4096, voffB, rb, sB + pf * 32768 + T * 128);
  }
};

template <int BUF> __device__ __forceinline__ void p1_tile(P1<0>& w, int T) {
  w.compute(); WAIT_V(8); BAR();
  w.template load<BUF, 1>(); w.template dmaW<BUF ^ 1, 1>(T + 1); w.pull(T); WAIT_L0(); BAR();
  w.compute(); WAIT_V(6); BAR();
  w.template load<BUF ^ 1, 0>(); w.template dmaX<BUF>(T + 2); w.template dmaB<BUF>(T + 2); WAIT_L0(); BAR();
}
template <int BUF> __device__ __forceinline__ void p1_tile(P1<1>& w, int T) {
  w.template load<BUF, 0>(); w.template dmaX<BUF ^ 1>(T + 1); WAIT_L0(); BAR();
  w.compute(); BAR();
  w.template load<BUF, 1>(); w.template dmaW<BUF, 0>(T + 2); WAIT_V(4); WAIT_L0(); BAR();
  w.compute(); BAR();
}

struct P2 {
  f32x4 af[4][4], as[4][4];
  bf16x8 ha[2][4], bb[2][4];
  lptr aH, aW[4], aC[2];
  unsigned voffH, voffW, voffC;
  unsigned sH, sW, sC, ldsA, ldsB;
  int kfa, e0, kfs;
  rsrc_t rh, rw, rc;
  __device__ __forceinline__ int kf_ffn(int st) const {
    const int e = e0 + 2 * st;
    return st < 16 ? kfa + 7 * (st & 7) + (st >> 3) : 3 * e - 2 * ((e * 43) >> 7);
  }
  __device__ __forceinline__ void dmaA1(int st, int kf, int s) {
    dma16_dev(ldsA + (st & 1) * 65536 + ((st >> 1) & 1) * 16384 + s * 1024, voffH, rh, sH + (unsigned)kf * 8192u + s * 1024);
  }
  template <bool SSM> __device__ __forceinline__ void dmaB1(int st, int kf, int s) {
    const unsigned l = ldsB + (st & 1) * 65536 + ((st >> 1) & 1) * 16384 + s * 2048;
    if (SSM) dma16(l, voffC, rc, sC + (unsigned)st * 128u + s * 32768u);
    else dma16(l, voffW, rw, sW + (unsigned)kf * 65536u + s * 16384u);
  }
  template <int SB, bool SSM, int DK> __device__ __forceinline__ void load(int ds) {
    const int kf = DK == 1 ? kf_ffn(ds) : kfs + 2 * ds;
#pragma unroll
    for (int kk = 0; kk < 2; ++kk) {
#pragma unroll
      for (int n = 0; n < 4; ++n) {
        if (DK) { if (kk == 0) dmaA1(ds, kf, n); else dmaB1<DK == 2>(ds, kf, n); }
        if (SSM) bb[kk][n] = lds_b128(aC[kk] + SB * 16384 + n * 2048);
        else bb[kk][n] = lds_tr2(aW[n] + SB * 16384 + kk * 8192, aW[n] + SB * 16384 + kk * 8192 + 4096);
        ha[kk][n] = lds_b128(aH + SB * 16384 + kk * 8192 + n * 1024);
      }
    }
  }
  template <bool SSM, bool FIRST = false> __device__ __forceinline__ void compute() {
    __builtin_amdgcn_s_setprio(1);
#pragma unroll
    for (int kk = 0; kk < 2; ++kk)
#pragma unroll
      for (int n = 0; n < 4; ++n)
#pragma unroll
        for (int mm = 0; mm < 4; ++mm) {
          const int m = (n & 1) ? 3 - mm : mm;
          if (SSM) MFMA(bb[kk][n], ha[kk][m], as[m][n]); else if (FIRST && kk == 0) MFMA0(bb[kk][n], ha[kk][m], af[m][n]); else MFMA(bb[kk][n], ha[kk][m], af[m][n]);
        }
    __builtin_amdgcn_s_setprio(0);
  }
};

__device__ __forceinline__ void arrive(char* ws, int lane, unsigned flag_off, int ctr, unsigned epoch) {
  if (lane == 0) {
    const unsigned old = __hip_atomic_fetch_add((LAS unsigned*)mk_l(L_FLG + 16 + ctr * 4), 1u, __ATOMIC_RELAXED, __HIP_MEMORY_SCOPE_WORKGROUP);
    if (old == 3u) __hip_atomic_store((GAS unsigned*)(ws + flag_off), epoch, RLX_AGENT);
  }
}
__device__ __forceinline__ void wait_flags(const char* ws, unsigned off, unsigned epoch) {
_Pragma("nounroll")
  for (unsigned spins = 0; spins < SPIN_CAP; ++spins) {
    if (__all(__hip_atomic_load((const GAS unsigned*)(ws + off), RLX_AGENT) == epoch)) break;
    __builtin_amdgcn_s_sleep(1);
  }
  asm volatile("" ::: "memory");
}

template <int GRP>
__device__ __forceinline__ void body(const Args& A, const int wid, const char* code, const unsigned code_len) {
  __builtin_amdgcn_s_waitcnt(0x0F70);
  ST_DECL STAMP();
  const int xcd = blockIdx.x & 7, idx = blockIdx.x >> 3, c = xcd * 4 + (idx >> 3), rch = idx & 7;
  const int kh = xcd & 1, tt = 2 * rch + ((xcd >> 1) & 1), dt = (xcd >> 2) * 4 + (idx >> 3);
  const bool sender = kh == (dt & 1);
  const int NF = sender ? 24 : 32;
  const int wr = wid & 3, wm = wr >> 1, wn = wr & 1;
  int lane = fresh_lane();
  unsigned epoch;
  P2 q;
  u32x4 hq[14]; unsigned ho[3];
  const rsrc_t rws = mk_rsrc(A.ws, (unsigned)WS_END);
  {
  P1<GRP> w;
  constexpr int NW = P1<GRP>::NW, NS = P1<GRP>::NS;
  int r = lane & 15, g = lane >> 4, qq = (lane & 15) >> 2, pp = lane & 3;
  unsigned ep0, plr = 0, pli = 0, pld = 0;
  asm volatile("global_load_dword %0, %1, %2 sc1" : "=v"(ep0) : "v"((unsigned)WS_EP), "s"(A.ws) : "memory");
  if (GRP == 1) {
    const unsigned po = (c * 32 + (lane & 31)) * 2;
    asm volatile("global_load_ushort %0, %1, %2" : "=v"(plr) : "v"(po), "s"(A.lre) : "memory");
    asm volatile("global_load_ushort %0, %1, %2" : "=v"(pli) : "v"(po), "s"(A.lim) : "memory");
    asm volatile("global_load_ushort %0, %1, %2" : "=v"(pld) : "v"(po), "s"(A.ldt) : "memory");
  }
  w.rx = mk_rsrc(A.x, L_ * D_ * 2); w.rwg = mk_rsrc(A.wg, D_ * F_ * 2); w.rwu = mk_rsrc(A.wu, D_ * F_ * 2);
  w.rb = mk_rsrc((wr >> 1) ? A.bim : A.bre, P_ * D_ * 2);
  {
    const unsigned sw8 = ((lane & 7) ^ (((wr & 1) * 4 + (lane >> 4)) & 7)) * 16;
    w.voffX = ((lane >> 5) * 16 + ((lane >> 3) & 3)) * 2048 + sw8;
    w.voffB = (lane >> 3) * 2048 + sw8;
    w.voffW = (lane >> 4) * 7168 + ((((lane & 15) >> 1) ^ ((lane >> 4) | ((wr & 1) << 2))) * 32) + (lane & 1) * 16;
    w.sX = (unsigned)(rch * 256 + (GRP * 2 + (wr >> 1)) * 64 + (wr & 1) * 32) * 2048u;
    w.ldsX = L_X + (GRP * 16 + (wr >> 1) * 8 + (wr & 1)) * 1024;
    const int ii = (wr & 1) * 2 + (wr >> 1) * 4;
    w.sW = (unsigned)(ii * 4) * 7168u + (unsigned)((224 * c) & ~63);
    w.ldsW = L_W + ii * 1024;
    w.sB = (unsigned)(c * 32 + (wr & 1) * 8) * 2048u;
    w.ldsB = L_BS + wr * 1024;
  }
  if (GRP == 0) { w.template dmaX<0>(0); w.template dmaB<0>(0); w.template dmaW<0, 1>(0); w.template dmaX<1>(1); w.template dmaB<1>(1); }
  else { w.template dmaW<0, 0>(0); w.template dmaX<0>(0); w.template dmaW<1, 0>(1); }
  if (GRP == 0) {
    {
      const char* wbase = (const char*)((wr & 1) ? A.wu : A.wg) + (blockIdx.x * 14 + (wr >> 1) * 7) * 128;
      const char* rbase = wr < 2 ? (const char*)A.x + (blockIdx.x * 8 + wr * 4) * 2048 : (const char*)(wr == 2 ? A.bre : A.bim) + blockIdx.x * 4 * 2048;
      const int l2 = lane < 7 * (PFD - 2) ? lane : 0;
      touch(lane < 56 ? wbase + (size_t)(2 + l2 / 7) * 458752 + (l2 % 7) * 128 : rbase + ((lane - 56) >> 1) * 2048 + (2 + (lane & 1)) * 128);
    }
    constexpr int NG = 256 / PER;
    w.pfg = idx & (PER - 1);
    const unsigned rk = (blockIdx.x & 7) + (idx / PER) * 8, gw = rk * 4 + wr;
    const unsigned co = ((idx * 4 + wr) * 4 + (lane & 3)) * 128u;
    w.pfc = lane < 4 ? code + (co < code_len ? co : 0u) : (const char*)((idx & 4) ? A.dsk : A.gw) + ((idx & 3) * 4 + wr) * 128;
    constexpr int LW = 7 * PER, LX = 8 * PER;
    if (lane < LW) { w.pfp[0] = (const char*)((wr & 1) ? A.wu : A.wg) + ((rk * 2 + (wr >> 1)) * LW + lane) * 128; w.pfs[0] = 458752u; w.pfo[0] = PFD; w.pfm[0] = 15; }
    else {
      const int l = lane < LW + LX ? lane - LW : 0;
      const char* rb_ = wr < 2 ? (const char*)A.x + (rk * 2 + wr) * (LX / 2) * 2048 : (const char*)(wr == 2 ? A.bre : A.bim) + rk * (LX / 2) * 2048;
      w.pfp[0] = rb_ + (l >> 1) * 2048 + (l & 1) * 128; w.pfs[0] = 256u; w.pfo[0] = 2; w.pfm[0] = 7;
    }
    constexpr int LD = 4 * PER;
    if (lane < LD) { w.pfp[1] = (const char*)A.wd + gw * (LD * 128u) + lane * 128; w.pfs[1] = 524288u; w.pfo[1] = 0; w.pfm[1] = 13; }
    else { const int l = lane < 2 * LD ? lane - LD : 0;
      w.pfp[1] = (const char*)(l < LD / 2 ? A.cre : A.cim) + gw * (LD * 64u) + (l & (LD / 2 - 1)) * 128; w.pfs[1] = 262144u; w.pfo[1] = -6; w.pfm[1] = 7; }
    static_assert(LW + LX <= 64 && NG * PER == 256, "");
    touch(w.pfc);
  }
  {
#pragma unroll
    for (int ks = 0; ks < 2; ++ks) {
      const unsigned ch = ((g + 4 * ks) ^ ((r >> 1) & 7)) << 4;
      w.aX[ks] = mk_l(L_X + (wr * 64 + r) * 128 + ch);
      if (NS) w.aB[ks] = mk_l(L_BS + r * 128 + ch);
    }
    const unsigned swz = qq | ((g & 1) << 2);
#pragma unroll
    for (int n = 0; n < NW; ++n) {
      unsigned col;
      if (GRP == 0) col = (((2 + n) ^ swz) << 5) + pp * 8;
      else if (n < 2) col = (((((c & 1) ? 6 : 0) + n) ^ swz) << 5) + pp * 8;
      else col = (((((c & 1) ? 0 : 6) + (pp >> 1)) ^ swz) << 5) + (c & 1) * 16 + (pp & 1) * 8;
      w.aW[n] = mk_l(L_W + (8 * g + qq) * 256 + col);
    }
  }
#pragma unroll
  for (int m = 0; m < 4; ++m) {
#pragma unroll
    for (int n = 0; n < NW; ++n) { w.ag[n][m] = ZERO4; w.au[n][m] = ZERO4; PIN(w.ag[n][m]); PIN(w.au[n][m]); }
#pragma unroll
    for (int i = 0; i < NS; ++i) { w.as[i][m] = ZERO4; PIN(w.as[i][m]); }
  }
  if (GRP == 0) asm volatile("s_waitcnt vmcnt(18)" : "+v"(ep0) :: "memory");
  else asm volatile("s_waitcnt vmcnt(12)" : "+v"(ep0), "+v"(plr), "+v"(pli), "+v"(pld) :: "memory");
  epoch = __builtin_amdgcn_readfirstlane(ep0) + 1u;
  if (GRP == 1) {
    if (wr == 0) {
      const float lr = bf2f(plr), li = bf2f(pli);
      const float dt_ = bf2f(pk_bf16(__expf(bf2f(pld)), 0.f) & 0xffffu);
      const float e = __expf(dt_ * lr), rev = __builtin_amdgcn_fractf(dt_ * li * 0.15915494309189535f);
      const f32x2 lam = {e * __builtin_amdgcn_cosf(rev), e * __builtin_amdgcn_sinf(rev)};
      const float nr = lam.x - 1.f, rden = 1.f / (lr * lr + li * li);
      const f32x2 l2 = cmul(lam, lam);
      f32x2 l16 = cmul(l2, l2); l16 = cmul(l16, l16); l16 = cmul(l16, l16);
      const f32x2 l32 = cmul(l16, l16), l64 = cmul(l32, l32), l128 = cmul(l64, l64), l256 = cmul(l128, l128);
      const lptr pb = mk_l(L_PRM + (lane & 31) * 64);
      *(LAS f32x4*)(pb) = (f32x4){lam.x, lam.y, (nr * lr + lam.y * li) * rden, (lam.y * lr - nr * li) * rden};
      *(LAS f32x4*)(pb + 16) = (f32x4){l16.x, l16.y, l32.x, l32.y};
      *(LAS f32x4*)(pb + 32) = (f32x4){l64.x, l64.y, l256.x, l256.y};

    } else if (wr == 1) {
      *(LAS unsigned*)mk_l(L_FLG + (lane & 7) * 4) = 0u;
    }
    WAIT_V(4);
    if (wr == 2) {
      __hip_atomic_store((GAS u64*)(A.ws + WS_AG) + (c * 8 + rch) * 64 + lane, (u64)(epoch - 1u) << 32, RLX_AGENT);
      if (lane < 4) __hip_atomic_store((GAS unsigned*)(A.ws + WS_FL + rch * 512 + lane * 128 + c * 4), epoch - 1u, RLX_AGENT);
      else if (lane < 12 && sender) __hip_atomic_store((GAS unsigned*)(A.ws + WS_XF + (blockIdx.x >> 1) * 128) + (lane - 4), epoch - 1u, RLX_AGENT);
    }
  } else WAIT_V(12);
  WAIT_L0(); BAR();
  if (GRP == 0) { w.template load<0, 0>(); WAIT_L0(); }
  BAR();
  STAMP();
  for (int T = 0; T < 14; T += 2) {
    TS2(); p1_tile<0>(w, T); TS2();
    p1_tile<1>(w, T + 1);
  }
  STAMP();
  lane = fresh_lane();
  q.rh = mk_rsrc(A.ws + WS_H, (unsigned)H_BYTES); q.rw = mk_rsrc(A.wd, F_ * D_ * 2); q.rc = mk_rsrc(kh ? A.cim : A.cre, D_ * P_ * 2);
  q.voffW = (lane >> 4) * 2048 + ((((lane & 15) >> 1) ^ (((wr & 1) << 2) | (lane >> 4))) * 32) + (lane & 1) * 16;
  {
    const int i0 = (wr & 1) + (wr >> 1) * 8;
    q.sH = (unsigned)(tt * NKF) * 8192u + (wr & 1) * 4096u;
    q.sW = (unsigned)((wr & 1) * 4) * 2048u + dt * 256u;
    q.sC = (unsigned)(dt * 128 + i0 * 8) * 2048u - (unsigned)NF * 128u;
    q.kfa = 56 * kh + 1 + 3 * (wr >> 1);
    q.e0 = (kh ? (sender ? 32 : 16) : 0) - 32 + (wr >> 1);
    q.kfs = 112 + 32 * kh - 2 * NF + (wr >> 1);
    q.ldsA = 32768 + wr * 4096; q.ldsB = i0 * 1024;
  }
#define P2_B(st) do { _Pragma("unroll") for (int s_ = 0; s_ < 4; ++s_) q.template dmaB1<false>(st, q.kf_ffn(st), s_); } while (0)
  u32x4 gx[4], gg[4];
  const int tg = rch * 256 + c * 8 + wr * 2 + (lane >> 5);
  if constexpr (GRP == 0) {
    w.compute(); WAIT_V(8); BAR();
    w.template load<0, 1>(); w.template dmaW<1, 1>(15);
#pragma unroll
    for (int i = 0; i < 4; ++i) gx[i] = *(const u32x4*)(A.x + tg * D_ + i * 256 + (lane & 31) * 8);
    WAIT_L0(); BAR();
    w.compute(); WAIT_V(8); BAR();
    w.template load<1, 0>(); P2_B(0);
#pragma unroll
    for (int i = 0; i < 4; ++i) gg[i] = *(const u32x4*)(A.gw + i * 256 + (lane & 31) * 8);
    WAIT_L0(); BAR();
    w.compute(); WAIT_V(12); BAR();
    w.template load<1, 1>();
    {
      typedef __bf16 bf2_t __attribute__((ext_vector_type(2)));
      float s0 = 0.f, s1 = 0.f;
#pragma unroll
      for (int i = 0; i < 4; ++i)
#pragma unroll
        for (int j = 0; j < 4; j += 2) {
          const unsigned a0 = gx[i][j], b0 = gg[i][j], a1 = gx[i][j + 1], b1 = gg[i][j + 1];
          s0 = __builtin_amdgcn_fdot2_f32_bf16(__builtin_bit_cast(bf2_t, a0), __builtin_bit_cast(bf2_t, b0), s0, false);
          s1 = __builtin_amdgcn_fdot2_f32_bf16(__builtin_bit_cast(bf2_t, a1), __builtin_bit_cast(bf2_t, b1), s1, false);
        }
      float s = s0 + s1;
      s += dpp<0x128, 0xf>(0.f, s); s += dpp<0x124, 0xf>(0.f, s); s += dpp<0x122, 0xf>(0.f, s); s += dpp<0x121, 0xf>(0.f, s);
      float t0, t1; sw16(s, t0, t1); s = t0 + t1;
      if ((lane & 31) == 0) __hip_atomic_store((GAS unsigned*)(A.ws + WS_G) + tg, __float_as_uint(1.f / (1.f + __expf(-s))), RLX_AGENT);
    }
    P2_B(2);
    WAIT_L0(); BAR();
    w.compute();
    __builtin_amdgcn_s_setprio(3);
  } else {
    w.template load<0, 0>(); w.template dmaX<1>(15); WAIT_L0(); BAR();
    w.compute(); BAR();
    w.template load<0, 1>(); WAIT_V(0); WAIT_L0(); BAR();
    w.compute(); BAR();
  }
  lane = fresh_lane(); r = lane & 15; g = lane >> 4; qq = (lane & 15) >> 2; pp = lane & 3;
  const unsigned hb = (unsigned)WS_H + (unsigned)(2 * rch + (wr >> 1)) * NKF * 8192u + (wr & 1) * 4096u;
  const int kf0 = 7 * (c >> 1) + 3 * (c & 1);
  q.voffH = lane * 16;
#define FFN_H(n, m, h) pk_bf16(relu(w.ag[n][m][2 * (h)]) * w.au[n][m][2 * (h)], relu(w.ag[n][m][2 * (h) + 1]) * w.au[n][m][2 * (h) + 1])
  if constexpr (GRP == 0) {
    MFMA_DRAIN();
    STAMP();
#pragma unroll
    for (int f = 0; f < 2; ++f)
#pragma unroll
      for (int m = 0; m < 4; ++m) {
        __builtin_amdgcn_raw_buffer_store_b128((u32x4){FFN_H(2 * f, m, 0), FFN_H(2 * f, m, 1), FFN_H(2 * f + 1, m, 0), FFN_H(2 * f + 1, m, 1)}, rws,
                                               lane * 16 + m * 1024 + hb + (kf0 + 1 + f) * 8192u, 0, 16);
        SCHED();
      }
    STAMP();
    WAIT_V(0); arrive(A.ws, lane, (unsigned)WS_FL + rch * 512 + (c >> 4) * 128 + c * 4, 0, epoch);
    __builtin_amdgcn_s_setprio(0);
    BAR();
    STAMP();
#pragma unroll
    for (int m = 0; m < 4; ++m)
#pragma unroll
      for (int n = 0; n < 4; ++n) { q.as[m][n] = ZERO4; PIN(q.as[m][n]); }
    wait_flags(A.ws, (unsigned)WS_FL + rch * 512 + kh * 192 + (lane & 15) * 4, epoch);
    STAMP();
#pragma unroll
    for (int st = 0; st < 2; ++st)
#pragma unroll
      for (int s = 0; s < 4; ++s) q.dmaA1(st, q.kf_ffn(st), s);
    STAMP();
  } else {
    bf16x8 xb[4], bs1[4], wg1[3], wu1[3];
#pragma unroll
    for (int m = 0; m < 4; ++m) { w.xa[m] = lds_b128(w.aX[0] + 32768 + m * 2048); xb[m] = lds_b128(w.aX[1] + 32768 + m * 2048); }
#pragma unroll
    for (int i = 0; i < 4; ++i) { w.bsf[i] = lds_b128(w.aB[0] + 8192 + i * 2048); bs1[i] = lds_b128(w.aB[1] + 8192 + i * 2048); }
    w.wgf[0] = lds_tr2(w.aW[0] + 32768, w.aW[0] + 32768 + 1024); w.wuf[0] = lds_tr2(w.aW[0] + 49152, w.aW[0] + 49152 + 1024);
    P2_B(1); WAIT_L0(); BAR();
    __builtin_amdgcn_s_setprio(1);
#pragma unroll
    for (int i = 0; i < 4; ++i)
#pragma unroll
      for (int m = 0; m < 4; ++m) MFMA(w.xa[m], w.bsf[i], w.as[i][m]);
#pragma unroll
    for (int i = 0; i < 4; ++i)
#pragma unroll
      for (int m = 0; m < 4; ++m) MFMA(xb[m], bs1[i], w.as[i][m]);
#pragma unroll
    for (int m = 0; m < 4; ++m) MFMA(w.wgf[0], w.xa[m], w.ag[0][m]);
#pragma unroll
    for (int m = 0; m < 4; ++m) MFMA(w.wuf[0], w.xa[m], w.au[0][m]);
    __builtin_amdgcn_s_setprio(0);
    BAR();
    STAMP();
    f32x4 pl[2], pm[2];
    f32x2 l64[2], cin[2], sw[2];
#pragma unroll
    for (int pf = 0; pf < 2; ++pf) {
      const lptr pb = mk_l(L_PRM + (pf * 16 + r) * 64);
      pl[pf] = *(const LAS f32x4*)pb; pm[pf] = *(const LAS f32x4*)(pb + 16);
    }
#pragma unroll
    for (int n = 1; n < 3; ++n) { w.wgf[n] = lds_tr2(w.aW[n] + 32768, w.aW[n] + 32768 + 1024); w.wuf[n] = lds_tr2(w.aW[n] + 49152, w.aW[n] + 49152 + 1024); }
    SCHED();
    f32x2 agg[2];
    {
      f32x2 e2[2];
#pragma unroll
      for (int k = 0; k < 16; ++k)
#pragma unroll
        for (int pf = 0; pf < 2; ++pf) {
          const f32x2 u = {w.as[2 * pf][k >> 2][k & 3], w.as[2 * pf + 1][k >> 2][k & 3]};
          e2[pf] = k ? cfma((f32x2){pl[pf][0], pl[pf][1]}, e2[pf], u) : u;
        }
#pragma unroll
      for (int pf = 0; pf < 2; ++pf) {
        const f32x2 e = e2[pf];
        const f32x2 l16 = {pm[pf][0], pm[pf][1]}, l32 = {pm[pf][2], pm[pf][3]};
        float a0, a1, b0, b1; sw16(e.x, a0, a1); sw16(e.y, b0, b1);
        const f32x2 r0 = {a0, b0};
        const f32x2 xx = cfma(l16, r0, (f32x2){a1, b1});
        float c0, c1, d0, d1; sw32(xx.x, c0, c1); sw32(xx.y, d0, d1);
        const f32x2 s0 = {c0, d0};
        agg[pf] = cfma(l32, s0, (f32x2){c1, d1});
        const f32x2 c3 = cfma(l16, s0, r0);
        cin[pf] = g == 0 ? (f32x2){0.f, 0.f} : g == 1 ? r0 : g == 2 ? s0 : c3;
      }
    }
    f32x2 aggq = (g & 1) ? agg[1] : agg[0]; PIN(aggq); PIN(cin[0]); PIN(cin[1]);
    if (wr < 3) {
      if (g < 2) *(LAS f32x2*)mk_l(L_WA + ((g * 16 + r) * 4 + wr) * 8) = aggq;
      WAIT_L0();
      *(volatile LAS unsigned*)mk_l(L_FLG + wr * 4) = 1u;
    }
    SCHED();
#pragma unroll
    for (int n = 0; n < 3; ++n) { wg1[n] = lds_tr2(w.aW[n] + 40960, w.aW[n] + 40960 + 1024); wu1[n] = lds_tr2(w.aW[n] + 57344, w.aW[n] + 57344 + 1024); }
    if (wr == 3) {
      const lptr fp = mk_l(L_FLG);
_Pragma("nounroll")
      for (unsigned spins = 0; spins < SPIN_CAP; ++spins) {
        const u32x4 f = *(volatile LAS u32x4*)fp;
        if (__builtin_amdgcn_readfirstlane(f[0] & f[1] & f[2])) break;
      }
      asm volatile("" ::: "memory");
      const lptr ap = mk_l(L_WA + ((g & 1) * 16 + r) * 32);
      const f32x4 a01 = *(const LAS f32x4*)ap; const f32x2 a2 = *(const LAS f32x2*)(ap + 16);
      const f32x2 lw = *(const LAS f32x2*)mk_l(L_PRM + ((g & 1) * 16 + r) * 64 + 32);
      const f32x2 t = cfma(lw, cfma(lw, cfma(lw, (f32x2){a01[0], a01[1]}, (f32x2){a01[2], a01[3]}), a2), aggq);
      if (rch < 7) __hip_atomic_store((GAS u64*)(A.ws + WS_AG) + (c * 8 + rch) * 64 + lane, ((u64)epoch << 32) | __float_as_uint((g & 2) ? t.y : t.x), RLX_AGENT);
    }
    P2_B(3);
    WAIT_L0(); SCHED();
    STAMP();
    __builtin_amdgcn_s_setprio(1);
#pragma unroll
    for (int n = 1; n < 3; ++n) {
#pragma unroll
      for (int m = 0; m < 4; ++m) MFMA(w.wgf[n], w.xa[m], w.ag[n][m]);
#pragma unroll
      for (int m = 0; m < 4; ++m) MFMA(w.wuf[n], w.xa[m], w.au[n][m]);
    }
#pragma unroll
    for (int n = 0; n < 3; ++n) {
#pragma unroll
      for (int m = 0; m < 4; ++m) MFMA(wg1[n], xb[m], w.ag[n][m]);
#pragma unroll
      for (int m = 0; m < 4; ++m) MFMA(wu1[n], xb[m], w.au[n][m]);
    }
    BAR();
    __builtin_amdgcn_s_setprio(2);
    lane = fresh_lane(); r = lane & 15; g = lane >> 4; qq = (lane & 15) >> 2; pp = lane & 3;
    q.voffH = lane * 16;
    const GAS u64* agp = (const GAS u64*)(A.ws + WS_AG) + c * 8 * 64 + lane;
    u64 gv_[7];
#pragma unroll
    for (int cc = 0; cc < 7; ++cc) gv_[cc] = __hip_atomic_load(agp + cc * 64, RLX_AGENT);
    STAMP();
    f32x2 coef[2], l256q;
    {
      l256q = *(const LAS f32x2*)mk_l(L_PRM + ((g & 1) * 16 + r) * 64 + 40);
#pragma unroll
      for (int pf = 0; pf < 2; ++pf) {
        const lptr pb = mk_l(L_PRM + (pf * 16 + r) * 64);
        pl[pf] = *(const LAS f32x4*)pb; pm[pf] = *(const LAS f32x4*)(pb + 16); l64[pf] = *(const LAS f32x2*)(pb + 32); coef[pf] = (f32x2){pl[pf][2], pl[pf][3]};
        const lptr ap = mk_l(L_WA + (pf * 16 + r) * 32);
        const f32x4 a01 = *(const LAS f32x4*)ap; const f32x2 a2 = *(const LAS f32x2*)(ap + 16);
        sw[pf] = wr > 0 ? (f32x2){a01[0], a01[1]} : (f32x2){0.f, 0.f};
        if (wr > 1) sw[pf] = cfma(l64[pf], sw[pf], (f32x2){a01[2], a01[3]});
        if (wr > 2) sw[pf] = cfma(l64[pf], sw[pf], a2);
      }
    }
#pragma unroll
    for (int pf = 0; pf < 2; ++pf)
#pragma unroll
      for (int m = 0; m < 4; ++m) {
        const f32x4 re = w.as[2 * pf][m], im = w.as[2 * pf + 1][m];
        w.as[2 * pf][m] = re * coef[pf].x - im * coef[pf].y; w.as[2 * pf + 1][m] = -(re * coef[pf].y) - im * coef[pf].x;
        PIN(w.as[2 * pf][m]); PIN(w.as[2 * pf + 1][m]);
      }
    f32x2 ca[2], cm[2];
#pragma unroll
    for (int pf = 0; pf < 2; ++pf) {
      const f32x2 l16 = {pm[pf][0], pm[pf][1]}, l32 = {pm[pf][2], pm[pf][3]};
      f32x2 pw = (wr & 1) ? l64[pf] : (f32x2){1.f, 0.f};
      if (wr & 2) pw = cmul(pw, cmul(l64[pf], l64[pf]));
      f32x2 pq = (g & 1) ? l16 : (f32x2){1.f, 0.f};
      if (g & 2) pq = cmul(pq, l32);
      ca[pf] = cmul(coef[pf], cfma(pq, sw[pf], cin[pf])); cm[pf] = cmul(coef[pf], cmul(pq, pw));
      PIN(ca[pf]); PIN(cm[pf]);
    }
    const lptr tb = mk_l(L_TR + wr * 9216 + r * 144 + g * 32), rb = mk_l(L_TR + wr * 9216 + (8 * g + qq) * 144 + pp * 32);
    STAMP();
_Pragma("nounroll")
    for (unsigned spins = 0; spins < SPIN_CAP; ++spins) {
      bool ok = true;
#pragma unroll
      for (int cc = 0; cc < 7; ++cc) ok &= (cc >= rch) || (unsigned)(gv_[cc] >> 32) == epoch;
      if (__all(ok)) break;
      __builtin_amdgcn_s_sleep(1);
#pragma unroll
      for (int cc = 0; cc < 7; ++cc) gv_[cc] = __hip_atomic_load(agp + cc * 64, RLX_AGENT);
    }
    STAMP();
    f32x2 cb = {0.f, 0.f};
#pragma unroll
    for (int cc = 0; cc < 7; ++cc) {
      float are, aim; sw32(__uint_as_float((unsigned)gv_[cc]), are, aim);
      if (cc < rch) cb = cfma(l256q, cb, (f32x2){are, aim});
    }
    f32x2 e[2];
    { float a0, a1, b0, b1; sw16(cb.x, a0, a1); sw16(cb.y, b0, b1);
      e[0] = cfma(cm[0], (f32x2){a0, b0}, ca[0]); e[1] = cfma(cm[1], (f32x2){a1, b1}, ca[1]); e[0].y = -e[0].y; e[1].y = -e[1].y; }
    unsigned hr[2][8], hi[2][8];
#pragma unroll
    for (int k = 0; k < 8; ++k)
#pragma unroll
      for (int pf = 0; pf < 2; ++pf) {
        const f32x2 lc = {pl[pf][0], -pl[pf][1]};
        const f32x2 h0 = cfma(lc, e[pf], (f32x2){w.as[2 * pf][k >> 1][(k & 1) * 2], w.as[2 * pf + 1][k >> 1][(k & 1) * 2]});
        e[pf] = cfma(lc, h0, (f32x2){w.as[2 * pf][k >> 1][(k & 1) * 2 + 1], w.as[2 * pf + 1][k >> 1][(k & 1) * 2 + 1]});
        hr[pf][k] = pk_bf16(h0.x, e[pf].x); hi[pf][k] = pk_bf16(h0.y, e[pf].y);
      }
#pragma unroll
    for (int ri = 0; ri < 2; ++ri)
#pragma unroll
      for (int pf = 0; pf < 2; ++pf) {
        const unsigned* h = ri ? hi[pf] : hr[pf];
        *(LAS u32x4*)(tb + ri * 4608 + pf * 2304) = (u32x4){h[0], h[1], h[2], h[3]};
        *(LAS u32x4*)(tb + ri * 4608 + pf * 2304 + 16) = (u32x4){h[4], h[5], h[6], h[7]};
      }
    WAIT_L0();
#pragma unroll
    for (int i = 0; i < 8; ++i) hq[6 + i] = __builtin_bit_cast(u32x4, lds_tr2(rb + (i >> 2) * 4608 + (i & 3) * 8, rb + (i >> 2) * 4608 + 4 * 144 + (i & 3) * 8));
    WAIT_L0();
    STAMP(); TS2();
    WAIT_V(0); BAR();
    STAMP();
#pragma unroll
    for (int m = 0; m < 4; ++m) { hq[m] = (u32x4){FFN_H(0, m, 0), FFN_H(0, m, 1), FFN_H(1, m, 0), FFN_H(1, m, 1)}; PIN(hq[m]); }
#pragma unroll
    for (int m = 0; m < 4; m += 2) {
      const auto s0 = __builtin_amdgcn_permlane32_swap(FFN_H(2, m, 0), FFN_H(2, m + 1, 0), false, false);
      const auto s1 = __builtin_amdgcn_permlane32_swap(FFN_H(2, m, 1), FFN_H(2, m + 1, 1), false, false);
      hq[4 + m / 2] = (u32x4){s0[0], s1[0], s0[1], s1[1]}; PIN(hq[4 + m / 2]);
    }
    ho[0] = lane * 16 + hb + (kf0 + ((c & 1) ? 3 : 0)) * 8192u;
    ho[1] = (lane & 31) * 16 + (lane >> 5) * 1024 + (c & 1) * 512 + hb + (kf0 + ((c & 1) ? 0 : 3)) * 8192u;
    ho[2] = lane * 16 + hb + (112 + c) * 8192u;
  }
  }
  if (GRP == 0) { STAMP(); TS2(); }
  constexpr int G = GRP;
  int r = lane & 15, g = lane >> 4;
  {
    const int qq = (lane & 15) >> 2, pp = lane & 3;
    q.voffC = (lane >> 3) * 2048 + (((lane & 7) ^ (((wr & 1) * 4 + (lane >> 4)) & 7)) * 16);
    const unsigned gb = G * 65536;
    q.aH = mk_l(gb + 32768 + wm * 4096 + lane * 16);
    const unsigned swz = 4 * (g & 1) + qq;
#pragma unroll
    for (int n = 0; n < 4; ++n) q.aW[n] = mk_l(gb + (4 * g + qq) * 256 + (((wn * 4 + n) ^ swz) << 5) + pp * 8);
#pragma unroll
    for (int kk = 0; kk < 2; ++kk) q.aC[kk] = mk_l(gb + (wn * 64 + r) * 128 + (((g + 4 * kk) ^ ((r >> 1) & 7)) << 4));
  }
  unsigned fl = 0u; const unsigned flo = (unsigned)WS_FL + rch * 512 + 256 + lane * 4;
#define FLAG_LOAD() asm volatile("global_load_dword %0, %1, %2 sc1" : "+v"(fl) : "v"(flo), "s"(A.ws) : "memory")
#define FLAG_CHECK(level) do { PIN(fl); const u64 bal_ = __ballot(fl == epoch); \
    if ((unsigned)((level) == 2 ? bal_ : bal_ >> 32) != ~0u) wait_flags(A.ws, (flo & ~128u) + ((level) - 2) * 128, epoch); } while (0)
#define P2_STEP(SB, SSM, DK, n) do { q.template load<SB, SSM, DK>((n) + 3); if (DK) WAIT_V(8); else WAIT_V(0); \
       WAIT_L0(); BAR(); q.template compute<SSM>(); BAR(); TS2(); } while (0)
#define HQ_ST(i) __builtin_amdgcn_raw_buffer_store_b128(hq[i], rws, (i) < 4 ? ho[0] + (i) * 1024 : (i) < 6 ? ho[1] + ((i) - 4) * 2048 : ho[2] + (((i) - 6) >> 2) * 262144 + (((i) - 6) & 3) * 1024, 0, 16)
#define RAISE(level) arrive(A.ws, lane, (unsigned)WS_FL + rch * 512 + (level) * 128 + c * 4, (level) - 1, epoch)
#define CBAR() do { WAIT_L0(); BAR(); q.template compute<false>(); BAR(); TS2(); } while (0)
  float gv[4];
#define GV_LOAD() do { const unsigned go_ = (unsigned)WS_G + (tt * 128 + wm * 64 + (r >> 2) * 16 + (r & 3)) * 4;     \
    asm volatile("global_load_dword %0, %4, %5 sc1\n\tglobal_load_dword %1, %4, %5 offset:16 sc1\n\tglobal_load_dword %2, %4, %5 offset:32 sc1\n\tglobal_load_dword %3, %4, %5 offset:48 sc1" \
                 : "=&v"(gv[0]), "=&v"(gv[1]), "=&v"(gv[2]), "=&v"(gv[3]) : "v"(go_), "s"(A.ws) : "memory"); } while (0)
  if (G == 0) {
    __builtin_amdgcn_s_setprio(0);
    WAIT_V(4); BAR(); STAMP(); q.template load<0, false, 0>(0);
#pragma unroll
    for (int st = 2; st < 4; ++st)
#pragma unroll
      for (int s = 0; s < 4; ++s) q.dmaA1(st, q.kf_ffn(st), s);
    WAIT_V(8); WAIT_L0(); BAR(); q.template compute<false, true>(); WAIT_V(4); BAR(); TS2();
    P2_STEP(1, false, 1, 2);
    STAMP();
    for (int n = 4; n < NF - 4; n += 4) {
      q.template load<0, false, 1>(n + 3); WAIT_V(8); if (n == 12) FLAG_CHECK(2); CBAR();
      q.template load<1, false, 1>(n + 5); FLAG_LOAD(); WAIT_V(9); CBAR();
    }
    q.template load<0, false, 1>(NF - 1); WAIT_V(8); FLAG_CHECK(3); CBAR();
    if (blockIdx.x == 255 && wr == 0 && lane == 0) __hip_atomic_store((GAS unsigned*)(A.ws + WS_EP), epoch, RLX_AGENT);
    q.template load<1, false, 2>(NF + 1); GV_LOAD(); WAIT_V(12); CBAR();
  } else {
    __builtin_amdgcn_s_setprio(0);
    BAR();
    q.template load<0, false, 1>(4); HQ_ST(0); HQ_ST(1); HQ_ST(2); WAIT_L0(); BAR(); q.template compute<false, true>(); BAR(); TS2();
    q.template load<1, false, 1>(6); HQ_ST(3); HQ_ST(4); HQ_ST(5); WAIT_V(14); CBAR();
    STAMP();
    q.template load<0, false, 1>(8); HQ_ST(6); HQ_ST(7); WAIT_V(13); CBAR();
    q.template load<1, false, 1>(10); HQ_ST(8); HQ_ST(9); WAIT_V(12); RAISE(2); CBAR();
    q.template load<0, false, 1>(12); HQ_ST(10); HQ_ST(11); WAIT_V(12); CBAR();
    q.template load<1, false, 1>(14); HQ_ST(12); HQ_ST(13); WAIT_V(12); CBAR();
#pragma unroll
    for (int m = 0; m < 4; ++m)
#pragma unroll
      for (int n = 0; n < 4; ++n) { q.as[m][n] = ZERO4; PIN(q.as[m][n]); }
    for (int n = 13; n < NF - 4; n += 4) {
      q.template load<0, false, 1>(n + 3); WAIT_V(8); if (n == 13) RAISE(3); CBAR();
      P2_STEP(1, false, 1, n + 2);
    }
    P2_STEP(0, false, 2, NF - 3);
    q.template load<1, false, 2>(NF + 2); GV_LOAD(); WAIT_V(12); CBAR();
  }
  STAMP();
  for (int n = NF + G; n < NF + 12; n += 4) { P2_STEP(0, true, 2, n); P2_STEP(1, true, 2, n + 2); }
  q.template load<0, true, G == 0 ? 2 : 0>(NF + 15);
  if (G == 0) WAIT_V(8); else WAIT_V(0);
#pragma unroll
  for (int m = 0; m < 4; ++m) {
    asm volatile("" : "+v"(gv[m]));
#pragma unroll
    for (int n = 0; n < 4; ++n) { q.af[m][n] *= 1.f - gv[m]; PIN(q.af[m][n]); }
  }
  WAIT_L0(); BAR(); q.template compute<true>(); BAR(); TS2();
  unsigned xfl;
  asm volatile("global_load_dword %0, %1, %2 sc1" : "=v"(xfl) : "v"((unsigned)WS_XF + (blockIdx.x >> 1) * 128u + wid * 4), "s"(A.ws) : "memory");
  q.template load<1, true, 0>(0);
  asm volatile("s_waitcnt vmcnt(0)" : "+v"(xfl) :: "memory");
  const bool early = __builtin_amdgcn_readfirstlane(xfl) == epoch;
  lane = fresh_lane(); r = lane & 15; g = lane >> 4;
  const unsigned xo = (unsigned)WS_XB + (blockIdx.x >> 1) * 65536u + wid * 8192u + lane * 16, xf = (unsigned)WS_XF + (blockIdx.x >> 1) * 128u + wid * 4;
  const int t0 = tt * 128 + wm * 64 + (2 * G + wn) * 4 + (lane >> 5), dc = dt * 128 + (lane & 31) * 4;
  u32x4 pv[8]; u32x2 dv = {0u, 0u};
  if (!sender) {
    if (early) {
#pragma unroll
      for (int i = 0; i < 8; ++i) pv[i] = __builtin_amdgcn_raw_buffer_load_b128(rws, xo + i * 1024, 0, 16);
    }
  } else {
    dv = *(const u32x2*)(A.dsk + dc);
    const int gsel = __float_as_int(wn ? gv[2 * G + 1] : gv[2 * G]);
#pragma unroll
    for (int i = 0; i < 8; ++i) {
      const u32x2 xv = *(const u32x2*)(A.x + (t0 + (i >> 1) * 16 + (i & 1) * 2) * D_ + dc);
      pv[i] = (u32x4){xv[0], xv[1], (unsigned)__builtin_amdgcn_ds_bpermute((2 * i + (lane >> 5)) * 4, gsel), 0u};
    }
  }
  lptr rw[2];
#pragma unroll
  for (int h = 0; h < 2; ++h) rw[h] = mk_l(G * 65536 + (wm * 64 + r) * 512 + wn * 256 + (((h * 4 + g) ^ (r & 7)) << 4));
#pragma unroll
  for (int m = 0; m < 4; ++m) { gv[m] *= 2.f; PIN(gv[m]); }
  WAIT_L0(); BAR();
  __builtin_amdgcn_s_setprio(1);
#pragma unroll
  for (int k = 0; k < 19; ++k) {
    if (k < 16) { MFMA(q.bb[0][k >> 2], q.ha[0][k & 3], q.as[k & 3][k >> 2]); MFMA(q.bb[1][k >> 2], q.ha[1][k & 3], q.as[k & 3][k >> 2]); }
    if (k == 3 || k >= 16) asm volatile("s_nop 3");
    SCHED();
    if (k >= 3) {
      const int m = (k - 3) & 3, n = (k - 3) >> 2;
      f32x4 v;
#pragma unroll
      for (int j = 0; j < 4; ++j) v[j] = __builtin_fmaf(q.as[m][n][j], gv[m], q.af[m][n][j]);
      *(LAS f32x4*)(rw[n & 1] + m * 8192 + (n >> 1) * 128) = v;
    }
    SCHED();
  }
  __builtin_amdgcn_s_setprio(0);
  if (G == 0) BAR();
  STAMP();
  lptr rr[4], rr2[4];
#pragma unroll
  for (int i = 0; i < 4; ++i) {
    const unsigned a_ = (wm * 64 + (2 * G + wn) * 16 + 2 * i + (lane >> 5)) * 512 + (((lane & 31) ^ (2 * i + (lane >> 5))) << 4);
    rr[i] = mk_l(a_); rr2[i] = mk_l(a_ + 65536);
  }
  WAIT_L0(); BAR();
  f32x4 o[8];
#pragma unroll
  for (int i = 0; i < 8; ++i) o[i] = *(const LAS f32x4*)(rr[i & 3] + (i >> 2) * 4096) + *(const LAS f32x4*)(rr2[i & 3] + (i >> 2) * 4096);
  STAMP();
  __builtin_amdgcn_s_waitcnt(0x0F70);
  if (sender) {
    const f32x4 df = {bf2f(dv[0] & 0xffffu), bfhi(dv[0]), bf2f(dv[1] & 0xffffu), bfhi(dv[1])};
#pragma unroll
    for (int i = 0; i < 8; ++i) {
      const f32x4 xv = {bf2f(pv[i][0] & 0xffffu), bfhi(pv[i][0]), bf2f(pv[i][1] & 0xffffu), bfhi(pv[i][1])};
      __builtin_amdgcn_raw_buffer_store_b128(__builtin_bit_cast(u32x4, o[i] + xv * df * __uint_as_float(pv[i][2])), rws, xo + i * 1024, 0, 16);
    }
    STAMP();
    WAIT_V(0);
    if (lane == 0) __hip_atomic_store((GAS unsigned*)(A.ws + xf), epoch, RLX_AGENT);
  } else {
    if (!early) {
      wait_flags(A.ws, xf, epoch);
#pragma unroll
      for (int i = 0; i < 8; ++i) pv[i] = __builtin_amdgcn_raw_buffer_load_b128(rws, xo + i * 1024, 0, 16);
    }
    STAMP();
    __builtin_amdgcn_s_waitcnt(0x0F70);
#pragma unroll
    for (int i = 0; i < 8; ++i) *(f32x4*)(A.out + (size_t)(t0 + (i >> 1) * 16 + (i & 1) * 2) * D_ + dc) = o[i] + __builtin_bit_cast(f32x4, pv[i]);
  }
  STAMP();
  ST_DUMP(A.ws, wid);
  if (GRP == 0) asm volatile(".Lgm_end0:"); else asm volatile(".Lgm_end1:");
  __builtin_amdgcn_endpgm();
  __builtin_unreachable();
}

__global__ __launch_bounds__(512, 2) void fused_kernel(Args A) {
  const int wid = __builtin_amdgcn_readfirstlane(threadIdx.x >> 6);
  const char* code; unsigned code_len;
  asm volatile("s_getpc_b64 %0\n.Lgm_pc:\n\ts_mov_b32 %1, .Lgm_end0-.Lgm_pc\n\ts_max_u32 %1, %1, .Lgm_end1-.Lgm_pc" : "=s"(code), "=s"(code_len) :: "scc");
  if (wid < 4) body<0>(A, wid, code, code_len); else body<1>(A, wid, code, code_len);
}

extern "C" void kernel_launch(void* const* d_in, const int* in_sizes, int n_in, void* d_out, int out_size, void* d_ws, size_t ws_size,
                              hipStream_t stream) {
  Args a; a.x = (const u16*)d_in[0]; a.lre = (const u16*)d_in[1]; a.lim = (const u16*)d_in[2]; a.bre = (const u16*)d_in[3];
  a.bim = (const u16*)d_in[4]; a.cre = (const u16*)d_in[5]; a.cim = (const u16*)d_in[6]; a.ldt = (const u16*)d_in[7]; a.dsk = (const u16*)d_in[8];
  a.wg = (const u16*)d_in[10]; a.wu = (const u16*)d_in[11]; a.wd = (const u16*)d_in[12]; a.gw = (const u16*)d_in[13];
  a.ws = (char*)d_ws; a.out = (float*)d_out;
  (void)hipFuncSetAttribute((const void*)fused_kernel, hipFuncAttributeMaxDynamicSharedMemorySize, LDS_BYTES);
  fused_kernel<<<256, 512, LDS_BYTES, stream>>>(a);
}
```

```cpp
#include <hip/hip_runtime.h>
#include <hip/hip_bf16.h>
#define ORD 0
#define OAUX 0
#define PER 2
#define PFD 4
#define ILV 1

typedef unsigned short u16;
typedef short bf16x8 __attribute__((ext_vector_type(8)));
typedef short s16x4 __attribute__((ext_vector_type(4)));
typedef float f32x4 __attribute__((ext_vector_type(4)));
typedef float f32x2 __attribute__((ext_vector_type(2)));
typedef unsigned u32x4 __attribute__((ext_vector_type(4)));
typedef unsigned u32x2 __attribute__((ext_vector_type(2)));
typedef unsigned long long u64;
#define LAS __attribute__((address_space(3)))
#define GAS __attribute__((address_space(1)))
#define RLX_AGENT __ATOMIC_RELAXED, __HIP_MEMORY_SCOPE_AGENT
typedef __amdgpu_buffer_rsrc_t rsrc_t;

constexpr int L_ = 2048, D_ = 1024, P_ = 1024, F_ = 3584;
constexpr int NKF = 176;
constexpr size_t WS_H = 0;
constexpr size_t H_BYTES = (size_t)16 * NKF * 8 * 1024;
constexpr size_t WS_XB = WS_H + H_BYTES;
constexpr size_t XB_BYTES = (size_t)256 * 32768;
constexpr size_t WS_G = WS_XB + XB_BYTES;
constexpr size_t WS_AG = WS_G + 8192;
constexpr size_t WS_EP = WS_AG + (size_t)32 * 8 * 64 * 8;
constexpr size_t WS_FL = WS_EP + 512;
constexpr size_t WS_XF = WS_FL + 4096;
constexpr size_t WS_END = WS_XF + 128 * 128;
constexpr unsigned SPIN_CAP = 1u << 20;

__device__ __forceinline__ float bf2f(unsigned b) { return __uint_as_float(b << 16); }
__device__ __forceinline__ float bfhi(unsigned b) { return __uint_as_float(b & 0xffff0000u); }
__device__ __forceinline__ unsigned pk_bf16(float lo, float hi) {
  typedef __bf16 bf2_t __attribute__((ext_vector_type(2)));
  return __builtin_bit_cast(unsigned, __builtin_convertvector((f32x2){lo, hi}, bf2_t)); }
__device__ __forceinline__ float relu(float v) { const int b = __float_as_int(v); return __int_as_float(b > 0 ? b : 0); }
template <int CTRL, int BANKS> __device__ __forceinline__ float dpp(float old, float src) {
  return __int_as_float(__builtin_amdgcn_update_dpp(__float_as_int(old), __float_as_int(src), CTRL, 0xf, BANKS, false)); }
__device__ __forceinline__ rsrc_t mk_rsrc(const void* p, unsigned bytes) {
  return __builtin_amdgcn_make_buffer_rsrc((void*)p, (short)0, (int)bytes, 0x00020000); }
__device__ __forceinline__ void dma16(unsigned ldsaddr, unsigned voff, rsrc_t srd, unsigned soff) {
  asm volatile("s_mov_b32 m0, %0\n\ts_nop 0\n\tbuffer_load_dwordx4 %1, %2, %3 offen lds"
               :: "s"(ldsaddr), "v"(voff), "s"(srd), "s"(soff) : "memory"); }
__device__ __forceinline__ void dma16_dev(unsigned ldsaddr, unsigned voff, rsrc_t srd, unsigned soff) {
  asm volatile("s_mov_b32 m0, %0\n\ts_nop 0\n\tbuffer_load_dwordx4 %1, %2, %3 offen sc1 lds"
               :: "s"(ldsaddr), "v"(voff), "s"(srd), "s"(soff) : "memory"); }
__device__ __forceinline__ void dma4(unsigned ldsaddr, unsigned voff, rsrc_t srd, unsigned soff) {
  asm volatile("s_mov_b32 m0, %0\n\ts_nop 0\n\tbuffer_load_dword %1, %2, %3 offen lds"
               :: "s"(ldsaddr), "v"(voff), "s"(srd), "s"(soff) : "memory"); }
#define WAIT_V(n) asm volatile("s_waitcnt vmcnt(" #n ")" ::: "memory")
#define WAIT_L0() do { __builtin_amdgcn_s_waitcnt(0xC07F); asm volatile("" ::: "memory"); } while (0)
#define SCHED() __builtin_amdgcn_sched_barrier(0)
#define BAR() do { SCHED(); __builtin_amdgcn_s_barrier(); SCHED(); } while (0)
__device__ __forceinline__ int fresh_lane() { int l; asm volatile("v_mbcnt_lo_u32_b32 %0, -1, 0\n\tv_mbcnt_hi_u32_b32 %0, -1, %0" : "=v"(l)); return l; }
typedef LAS char* lptr;
__device__ __forceinline__ lptr mk_l(unsigned a) { lptr p = (lptr)(size_t)a; asm volatile("" : "+v"(p)); return p; }
__device__ __forceinline__ bf16x8 lds_b128(lptr a) { return *(const LAS bf16x8*)a; }
__device__ __forceinline__ bf16x8 lds_tr2(lptr a0, lptr a1) {
  s16x4 lo = __builtin_amdgcn_ds_read_tr16_b64_v4i16((LAS s16x4*)a0);
  s16x4 hi = __builtin_amdgcn_ds_read_tr16_b64_v4i16((LAS s16x4*)a1);
  return __builtin_shufflevector(lo, hi, 0, 1, 2, 3, 4, 5, 6, 7); }
#define MFMA(a, b, c) asm volatile("v_mfma_f32_16x16x32_bf16 %0, %1, %2, %0" : "+v"(c) : "v"(a), "v"(b))
#define MFMA0(a, b, c) asm volatile("v_mfma_f32_16x16x32_bf16 %0, %1, %2, 0" : "=&v"(c) : "v"(a), "v"(b))
#define PIN(x) asm volatile("" : "+v"(x))
#define MFMA_DRAIN() do { asm volatile("s_nop 7\n\ts_nop 7" ::: "memory"); SCHED(); } while (0)
#define ZERO4 ((f32x4){0.f, 0.f, 0.f, 0.f})

#define ST_DECL
#define STAMP()
#define TS2()
#define ST_DUMP(ws, wid)

struct Args {
  const u16 *x, *lre, *lim, *bre, *bim, *cre, *cim, *ldt, *dsk, *wg, *wu, *wd, *gw;
  char* ws; float* out;
};

constexpr unsigned L_X = 0, L_W = 65536, L_BS = 131072, L_TR = 114688  , L_PRM = 151552  ,
                   L_WA = 153600  , L_FLG = 154624  , L_SINK = 154880, LDS_BYTES = 163840;

__device__ __forceinline__ f32x2 cmul(f32x2 a, f32x2 b) { return (f32x2){a.x * b.x - a.y * b.y, a.x * b.y + a.y * b.x}; }
__device__ __forceinline__ f32x2 cfma(f32x2 a, f32x2 b, f32x2 c) {
  return (f32x2){__builtin_fmaf(a.x, b.x, __builtin_fmaf(-a.y, b.y, c.x)), __builtin_fmaf(a.x, b.y, __builtin_fmaf(a.y, b.x, c.y))}; }
__device__ __forceinline__ void sw16(float v, float& r0, float& r1) { auto t = __builtin_amdgcn_permlane16_swap(__float_as_uint(v), __float_as_uint(v), false, false); r0 = __uint_as_float(t[0]); r1 = __uint_as_float(t[1]); }
__device__ __forceinline__ void sw32(float v, float& r0, float& r1) { auto t = __builtin_amdgcn_permlane32_swap(__float_as_uint(v), __float_as_uint(v), false, false); r0 = __uint_as_float(t[0]); r1 = __uint_as_float(t[1]); }

__device__ __forceinline__ void touch(const char* p) {
  asm volatile("s_mov_b32 m0, %0\n\ts_nop 0\n\tglobal_load_lds_dword %1, off" :: "s"(L_SINK), "v"(p) : "memory"); }
template <int GRP>
struct P1 {
  static constexpr int NW = GRP == 0 ? 4 : 3, NS = GRP == 0 ? 0 : 4;
  f32x4 ag[NW][4], au[NW][4], as[4][4];
  bf16x8 xa[4], wgf[NW], wuf[NW], bsf[4];
  lptr aX[2], aB[2], aW[NW];
  unsigned voffX, voffW, voffB;
  unsigned sX, sW, sB, ldsX, ldsW, ldsB;
  rsrc_t rx, rwg, rwu, rb;
  const char *pfp[2], *pfc; unsigned pfs[2]; int pfo[2], pfm[2], pfg;
  __device__ __forceinline__ void pull(int T) {
    const bool ev = (T & (PER - 1)) == pfg;
#pragma unroll
    for (int k = 0; k < 2; ++k) {
      const int i = T + pfo[k];
      const char* p = pfp[k] + pfs[k] * (unsigned)(i < 0 ? 0 : i > pfm[k] ? pfm[k] : i);
      touch(ev ? p : pfc);
    }
  }

  template <int BUF, int KS> __device__ __forceinline__ void load() {
#pragma unroll
    for (int n = 0; n < NW; ++n) {
      wgf[n] = lds_tr2(aW[n] + BUF * 32768 + KS * 8192, aW[n] + BUF * 32768 + KS * 8192 + 1024);
      wuf[n] = lds_tr2(aW[n] + BUF * 32768 + 16384 + KS * 8192, aW[n] + BUF * 32768 + 16384 + KS * 8192 + 1024);
    }
#pragma unroll
    for (int m = 0; m < 4; ++m) xa[m] = lds_b128(aX[KS] + BUF * 32768 + m * 2048);
#pragma unroll
    for (int i = 0; i < NS; ++i) bsf[i] = lds_b128(aB[KS] + BUF * 8192 + i * 2048);
  }
  __device__ __forceinline__ void compute() {
    __builtin_amdgcn_s_setprio(1);
#pragma unroll
    for (int i = 0; i < NS; ++i)
#pragma unroll
      for (int m = 0; m < 4; ++m) MFMA(xa[m], bsf[i], as[i][m]);
#pragma unroll
    for (int n = 0; n < NW; ++n) {
#pragma unroll
      for (int m = 0; m < 4; ++m) MFMA(wgf[n], xa[m], ag[n][m]);
#pragma unroll
      for (int m = 0; m < 4; ++m) MFMA(wuf[n], xa[3 - m], au[n][3 - m]);
    }
    __builtin_amdgcn_s_setprio(0);
  }
  template <int BUF> __device__ __forceinline__ void dmaX(int T) {
#pragma unroll
    for (int s = 0; s < 4; ++s) dma16(ldsX + BUF * 32768 + s * 2048, voffX, rx, sX + T * 128 + s * 8192);
  }
  template <int BUF, int HH> __device__ __forceinline__ void dmaW(int T) {
#pragma unroll
    for (int s = 0; s < 2; ++s) {
      const unsigned so = sW + (unsigned)(T * 64 + HH * 32 + s * 4) * 7168u;
      dma16(ldsW + BUF * 32768 + HH * 8192 + s * 1024, voffW, rwg, so);
      dma16(ldsW + BUF * 32768 + 16384 + HH * 8192 + s * 1024, voffW, rwu, so);
    }
  }
  template <int BUF> __device__ __forceinline__ void dmaB(int T) {
#pragma unroll
    for (int pf = 0; pf < 2; ++pf) dma16(ldsB + BUF * 8192 + pf * 4096, voffB, rb, sB + pf * 32768 + T * 128);
  }
};

template <int BUF> __device__ __forceinline__ void p1_tile(P1<0>& w, int T) {
  w.compute(); WAIT_V(8); BAR();
  w.template load<BUF, 1>(); w.template dmaW<BUF ^ 1, 1>(T + 1); w.pull(T); WAIT_L0(); BAR();
  w.compute(); WAIT_V(6); BAR();
  w.template load<BUF ^ 1, 0>(); w.template dmaX<BUF>(T + 2); w.template dmaB<BUF>(T + 2); WAIT_L0(); BAR();
}
template <int BUF> __device__ __forceinline__ void p1_tile(P1<1>& w, int T) {
  w.template load<BUF, 0>(); w.template dmaX<BUF ^ 1>(T + 1); WAIT_L0(); BAR();
  w.compute(); BAR();
  w.template load<BUF, 1>(); w.template dmaW<BUF, 0>(T + 2); WAIT_V(4); WAIT_L0(); BAR();
  w.compute(); BAR();
}

struct P2 {
  f32x4 af[4][4], as[4][4];
  bf16x8 ha[2][4], bb[2][4];
  lptr aH, aW[4], aC[2];
  unsigned voffH, voffW, voffC;
  unsigned sH, sW, sC, ldsA, ldsB;
  int kfa, e0, kfs;
  rsrc_t rh, rw, rc;
  __device__ __forceinline__ int kf_ffn(int st) const {
    const int e = e0 + 2 * st;
    return st < 16 ? kfa + 7 * (st & 7) + (st >> 3) : 3 * e - 2 * ((e * 43) >> 7);
  }
  __device__ __forceinline__ void dmaA1(int st, int kf, int s) {
    dma16_dev(ldsA + (st & 1) * 65536 + ((st >> 1) & 1) * 16384 + s * 1024, voffH, rh, sH + (unsigned)kf * 8192u + s * 1024);
  }
  template <bool SSM> __device__ __forceinline__ void dmaB1(int st, int kf, int s) {
    const unsigned l = ldsB + (st & 1) * 65536 + ((st >> 1) & 1) * 16384 + s * 2048;
    if (SSM) dma16(l, voffC, rc, sC + (unsigned)st * 128u + s * 32768u);
    else dma16(l, voffW, rw, sW + (unsigned)kf * 65536u + s * 16384u);
  }
  template <int SB, bool SSM, int DK> __device__ __forceinline__ void load(int ds) {
    const int kf = DK == 1 ? kf_ffn(ds) : kfs + 2 * ds;
#pragma unroll
    for (int kk = 0; kk < 2; ++kk) {
#pragma unroll
      for (int n = 0; n < 4; ++n) {
        if (DK) { if (kk == 0) dmaA1(ds, kf, n); else dmaB1<DK == 2>(ds, kf, n); }
        if (SSM) bb[kk][n] = lds_b128(aC[kk] + SB * 16384 + n * 2048);
        else bb[kk][n] = lds_tr2(aW[n] + SB * 16384 + kk * 8192, aW[n] + SB * 16384 + kk * 8192 + 4096);
        ha[kk][n] = lds_b128(aH + SB * 16384 + kk * 8192 + n * 1024);
      }
    }
  }
  template <bool SSM, bool FIRST = false> __device__ __forceinline__ void compute() {
    __builtin_amdgcn_s_setprio(1);
#pragma unroll
    for (int kk = 0; kk < 2; ++kk)
#pragma unroll
      for (int n = 0; n < 4; ++n)
#pragma unroll
        for (int mm = 0; mm < 4; ++mm) {
          const int m = (n & 1) ? 3 - mm : mm;
          if (SSM) MFMA(bb[kk][n], ha[kk][m], as[m][n]); else if (FIRST && kk == 0) MFMA0(bb[kk][n], ha[kk][m], af[m][n]); else MFMA(bb[kk][n], ha[kk][m], af[m][n]);
        }
    __builtin_amdgcn_s_setprio(0);
  }
};

__device__ __forceinline__ void arrive(char* ws, int lane, unsigned flag_off, int ctr, unsigned epoch) {
  if (lane == 0) {
    const unsigned old = __hip_atomic_fetch_add((LAS unsigned*)mk_l(L_FLG + 16 + ctr * 4), 1u, __ATOMIC_RELAXED, __HIP_MEMORY_SCOPE_WORKGROUP);
    if (old == 3u) __hip_atomic_store((GAS unsigned*)(ws + flag_off), epoch, RLX_AGENT);
  }
}
__device__ __forceinline__ void wait_flags(const char* ws, unsigned off, unsigned epoch) {
_Pragma("nounroll")
  for (unsigned spins = 0; spins < SPIN_CAP; ++spins) {
    if (__all(__hip_atomic_load((const GAS unsigned*)(ws + off), RLX_AGENT) == epoch)) break;
    __builtin_amdgcn_s_sleep(1);
  }
  asm volatile("" ::: "memory");
}

template <int GRP>
__device__ __forceinline__ void body(const Args& A, const int wid, const char* code, const unsigned code_len) {
  __builtin_amdgcn_s_waitcnt(0x0F70);
  ST_DECL STAMP();
  const int xcd = blockIdx.x & 7, idx = blockIdx.x >> 3, c = xcd * 4 + (idx >> 3), rch = idx & 7;
  const int kh = xcd & 1, tt = 2 * rch + ((xcd >> 1) & 1), dt = (xcd >> 2) * 4 + (idx >> 3);
  const bool sender = kh == (dt & 1);
  const int NF = sender ? 24 : 32;
  const int wr = wid & 3, wm = wr >> 1, wn = wr & 1;
  int lane = fresh_lane();
  unsigned epoch;
  P2 q;
  u32x4 hq[14]; unsigned ho[3];
  const rsrc_t rws = mk_rsrc(A.ws, (unsigned)WS_END);
  {
  P1<GRP> w;
  constexpr int NW = P1<GRP>::NW, NS = P1<GRP>::NS;
  int r = lane & 15, g = lane >> 4, qq = (lane & 15) >> 2, pp = lane & 3;
  unsigned ep0, plr = 0, pli = 0, pld = 0;
  asm volatile("global_load_dword %0, %1, %2 sc1" : "=v"(ep0) : "v"((unsigned)WS_EP), "s"(A.ws) : "memory");
  if (GRP == 1) {
    const unsigned po = (c * 32 + (lane & 31)) * 2;
    asm volatile("global_load_ushort %0, %1, %2" : "=v"(plr) : "v"(po), "s"(A.lre) : "memory");
    asm volatile("global_load_ushort %0, %1, %2" : "=v"(pli) : "v"(po), "s"(A.lim) : "memory");
    asm volatile("global_load_ushort %0, %1, %2" : "=v"(pld) : "v"(po), "s"(A.ldt) : "memory");
  }
  w.rx = mk_rsrc(A.x, L_ * D_ * 2); w.rwg = mk_rsrc(A.wg, D_ * F_ * 2); w.rwu = mk_rsrc(A.wu, D_ * F_ * 2);
  w.rb = mk_rsrc((wr >> 1) ? A.bim : A.bre, P_ * D_ * 2);
  {
    const unsigned sw8 = ((lane & 7) ^ (((wr & 1) * 4 + (lane >> 4)) & 7)) * 16;
    w.voffX = ((lane >> 5) * 16 + ((lane >> 3) & 3)) * 2048 + sw8;
    w.voffB = (lane >> 3) * 2048 + sw8;
    w.voffW = (lane >> 4) * 7168 + ((((lane & 15) >> 1) ^ ((lane >> 4) | ((wr & 1) << 2))) * 32) + (lane & 1) * 16;
    w.sX = (unsigned)(rch * 256 + (GRP * 2 + (wr >> 1)) * 64 + (wr & 1) * 32) * 2048u;
    w.ldsX = L_X + (GRP * 16 + (wr >> 1) * 8 + (wr & 1)) * 1024;
    const int ii = (wr & 1) * 2 + (wr >> 1) * 4;
    w.sW = (unsigned)(ii * 4) * 7168u + (unsigned)((224 * c) & ~63);
    w.ldsW = L_W + ii * 1024;
    w.sB = (unsigned)(c * 32 + (wr & 1) * 8) * 2048u;
    w.ldsB = L_BS + wr * 1024;
  }
  if (GRP == 0) { w.template dmaX<0>(0); w.template dmaB<0>(0); w.template dmaW<0, 1>(0); w.template dmaX<1>(1); w.template dmaB<1>(1); }
  else { w.template dmaW<0, 0>(0); w.template dmaX<0>(0); w.template dmaW<1, 0>(1); }
  if (GRP == 0) {
    {
      const char* wbase = (const char*)((wr & 1) ? A.wu : A.wg) + (blockIdx.x * 14 + (wr >> 1) * 7) * 128;
      const char* rbase = wr < 2 ? (const char*)A.x + (blockIdx.x * 8 + wr * 4) * 2048 : (const char*)(wr == 2 ? A.bre : A.bim) + blockIdx.x * 4 * 2048;
      const int l2 = lane < 7 * (PFD - 2) ? lane : 0;
      touch(lane < 56 ? wbase + (size_t)(2 + l2 / 7) * 458752 + (l2 % 7) * 128 : rbase + ((lane - 56) >> 1) * 2048 + (2 + (lane & 1)) * 128);
    }
    constexpr int NG = 256 / PER;
    w.pfg = idx & (PER - 1);
    const unsigned rk = (blockIdx.x & 7) + (idx / PER) * 8, gw = rk * 4 + wr;
    const unsigned co = ((idx * 4 + wr) * 4 + (lane & 3)) * 128u;
    w.pfc = lane < 4 ? code + (co < code_len ? co : 0u) : (const char*)((idx & 4) ? A.dsk : A.gw) + ((idx & 3) * 4 + wr) * 128;
    constexpr int LW = 7 * PER, LX = 8 * PER;
    if (lane < LW) { w.pfp[0] = (const char*)((wr & 1) ? A.wu : A.wg) + ((rk * 2 + (wr >> 1)) * LW + lane) * 128; w.pfs[0] = 458752u; w.pfo[0] = PFD; w.pfm[0] = 15; }
    else {
      const int l = lane < LW + LX ? lane - LW : 0;
      const char* rb_ = wr < 2 ? (const char*)A.x + (rk * 2 + wr) * (LX / 2) * 2048 : (const char*)(wr == 2 ? A.bre : A.bim) + rk * (LX / 2) * 2048;
      w.pfp[0] = rb_ + (l >> 1) * 2048 + (l & 1) * 128; w.pfs[0] = 256u; w.pfo[0] = 2; w.pfm[0] = 7;
    }
    constexpr int LD = 4 * PER;
    if (lane < LD) { w.pfp[1] = (const char*)A.wd + gw * (LD * 128u) + lane * 128; w.pfs[1] = 524288u; w.pfo[1] = 0; w.pfm[1] = 13; }
    else { const int l = lane < 2 * LD ? lane - LD : 0;
      w.pfp[1] = (const char*)(l < LD / 2 ? A.cre : A.cim) + gw * (LD * 64u) + (l & (LD / 2 - 1)) * 128; w.pfs[1] = 262144u; w.pfo[1] = -6; w.pfm[1] = 7; }
    static_assert(LW + LX <= 64 && NG * PER == 256, "");
    touch(w.pfc);
  }
  {
#pragma unroll
    for (int ks = 0; ks < 2; ++ks) {
      const unsigned ch = ((g + 4 * ks) ^ ((r >> 1) & 7)) << 4;
      w.aX[ks] = mk_l(L_X + (wr * 64 + r) * 128 + ch);
      if (NS) w.aB[ks] = mk_l(L_BS + r * 128 + ch);
    }
    const unsigned swz = qq | ((g & 1) << 2);
#pragma unroll
    for (int n = 0; n < NW; ++n) {
      unsigned col;
      if (GRP == 0) col = (((2 + n) ^ swz) << 5) + pp * 8;
      else if (n < 2) col = (((((c & 1) ? 6 : 0) + n) ^ swz) << 5) + pp * 8;
      else col = (((((c & 1) ? 0 : 6) + (pp >> 1)) ^ swz) << 5) + (c & 1) * 16 + (pp & 1) * 8;
      w.aW[n] = mk_l(L_W + (8 * g + qq) * 256 + col);
    }
  }
#pragma unroll
  for (int m = 0; m < 4; ++m) {
#pragma unroll
    for (int n = 0; n < NW; ++n) { w.ag[n][m] = ZERO4; w.au[n][m] = ZERO4; PIN(w.ag[n][m]); PIN(w.au[n][m]); }
#pragma unroll
    for (int i = 0; i < NS; ++i) { w.as[i][m] = ZERO4; PIN(w.as[i][m]); }
  }
  if (GRP == 0) asm volatile("s_waitcnt vmcnt(18)" : "+v"(ep0) :: "memory");
  else asm volatile("s_waitcnt vmcnt(12)" : "+v"(ep0), "+v"(plr), "+v"(pli), "+v"(pld) :: "memory");
  epoch = __builtin_amdgcn_readfirstlane(ep0) + 1u;
  if (GRP == 1) {
    if (wr == 0) {
      const float lr = bf2f(plr), li = bf2f(pli);
      const float dt_ = bf2f(pk_bf16(__expf(bf2f(pld)), 0.f) & 0xffffu);
      const float e = __expf(dt_ * lr), rev = __builtin_amdgcn_fractf(dt_ * li * 0.15915494309189535f);
      const f32x2 lam = {e * __builtin_amdgcn_cosf(rev), e * __builtin_amdgcn_sinf(rev)};
      const float nr = lam.x - 1.f, rden = 1.f / (lr * lr + li * li);
      const f32x2 l2 = cmul(lam, lam);
      f32x2 l16 = cmul(l2, l2); l16 = cmul(l16, l16); l16 = cmul(l16, l16);
      const f32x2 l32 = cmul(l16, l16), l64 = cmul(l32, l32), l128 = cmul(l64, l64), l256 = cmul(l128, l128);
      const lptr pb = mk_l(L_PRM + (lane & 31) * 64);
      *(LAS f32x4*)(pb) = (f32x4){lam.x, lam.y, (nr * lr + lam.y * li) * rden, (lam.y * lr - nr * li) * rden};
      *(LAS f32x4*)(pb + 16) = (f32x4){l16.x, l16.y, l32.x, l32.y};
      *(LAS f32x4*)(pb + 32) = (f32x4){l64.x, l64.y, l256.x, l256.y};

    } else if (wr == 1) {
      *(LAS unsigned*)mk_l(L_FLG + (lane & 7) * 4) = 0u;
    }
    WAIT_V(4);
    if (wr == 2) {
      __hip_atomic_store((GAS u64*)(A.ws + WS_AG) + (c * 8 + rch) * 64 + lane, (u64)(epoch - 1u) << 32, RLX_AGENT);
      if (lane < 4) __hip_atomic_store((GAS unsigned*)(A.ws + WS_FL + rch * 512 + lane * 128 + c * 4), epoch - 1u, RLX_AGENT);
      else if (lane < 12 && sender) __hip_atomic_store((GAS unsigned*)(A.ws + WS_XF + (blockIdx.x >> 1) * 128) + (lane - 4), epoch - 1u, RLX_AGENT);
    }
  } else WAIT_V(12);
  WAIT_L0(); BAR();
  if (GRP == 0) { w.template load<0, 0>(); WAIT_L0(); }
  BAR();
  STAMP();
  for (int T = 0; T < 14; T += 2) {
    TS2(); p1_tile<0>(w, T); TS2();
    p1_tile<1>(w, T + 1);
  }
  STAMP();
  lane = fresh_lane();
  q.rh = mk_rsrc(A.ws + WS_H, (unsigned)H_BYTES); q.rw = mk_rsrc(A.wd, F_ * D_ * 2); q.rc = mk_rsrc(kh ? A.cim : A.cre, D_ * P_ * 2);
  q.voffW = (lane >> 4) * 2048 + ((((lane & 15) >> 1) ^ (((wr & 1) << 2) | (lane >> 4))) * 32) + (lane & 1) * 16;
  {
    const int i0 = (wr & 1) + (wr >> 1) * 8;
    q.sH = (unsigned)(tt * NKF) * 8192u + (wr & 1) * 4096u;
    q.sW = (unsigned)((wr & 1) * 4) * 2048u + dt * 256u;
    q.sC = (unsigned)(dt * 128 + i0 * 8) * 2048u - (unsigned)NF * 128u;
    q.kfa = 56 * kh + 1 + 3 * (wr >> 1);
    q.e0 = (kh ? (sender ? 32 : 16) : 0) - 32 + (wr >> 1);
    q.kfs = 112 + 32 * kh - 2 * NF + (wr >> 1);
    q.ldsA = 32768 + wr * 4096; q.ldsB = i0 * 1024;
  }
#define P2_B(st) do { _Pragma("unroll") for (int s_ = 0; s_ < 4; ++s_) q.template dmaB1<false>(st, q.kf_ffn(st), s_); } while (0)
  u32x4 gx[4], gg[4];
  const int tg = rch * 256 + c * 8 + wr * 2 + (lane >> 5);
  if constexpr (GRP == 0) {
    w.compute(); WAIT_V(8); BAR();
    w.template load<0, 1>(); w.template dmaW<1, 1>(15);
#pragma unroll
    for (int i = 0; i < 4; ++i) gx[i] = *(const u32x4*)(A.x + tg * D_ + i * 256 + (lane & 31) * 8);
    WAIT_L0(); BAR();
    w.compute(); WAIT_V(8); BAR();
    w.template load<1, 0>(); P2_B(0);
#pragma unroll
    for (int i = 0; i < 4; ++i) gg[i] = *(const u32x4*)(A.gw + i * 256 + (lane & 31) * 8);
    WAIT_L0(); BAR();
    w.compute(); WAIT_V(12); BAR();
    w.template load<1, 1>();
    {
      typedef __bf16 bf2_t __attribute__((ext_vector_type(2)));
      float s0 = 0.f, s1 = 0.f;
#pragma unroll
      for (int i = 0; i < 4; ++i)
#pragma unroll
        for (int j = 0; j < 4; j += 2) {
          const unsigned a0 = gx[i][j], b0 = gg[i][j], a1 = gx[i][j + 1], b1 = gg[i][j + 1];
          s0 = __builtin_amdgcn_fdot2_f32_bf16(__builtin_bit_cast(bf2_t, a0), __builtin_bit_cast(bf2_t, b0), s0, false);
          s1 = __builtin_amdgcn_fdot2_f32_bf16(__builtin_bit_cast(bf2_t, a1), __builtin_bit_cast(bf2_t, b1), s1, false);
        }
      float s = s0 + s1;
      s += dpp<0x128, 0xf>(0.f, s); s += dpp<0x124, 0xf>(0.f, s); s += dpp<0x122, 0xf>(0.f, s); s += dpp<0x121, 0xf>(0.f, s);
      float t0, t1; sw16(s, t0, t1); s = t0 + t1;
      if ((lane & 31) == 0) __hip_atomic_store((GAS unsigned*)(A.ws + WS_G) + tg, __float_as_uint(1.f / (1.f + __expf(-s))), RLX_AGENT);
    }
    P2_B(2);
    WAIT_L0(); BAR();
    w.compute();
    __builtin_amdgcn_s_setprio(3);
  } else {
    w.template load<0, 0>(); w.template dmaX<1>(15); WAIT_L0(); BAR();
    w.compute(); BAR();
    w.template load<0, 1>(); WAIT_V(0); WAIT_L0(); BAR();
    w.compute(); BAR();
  }
  lane = fresh_lane(); r = lane & 15; g = lane >> 4; qq = (lane & 15) >> 2; pp = lane & 3;
  const unsigned hb = (unsigned)WS_H + (unsigned)(2 * rch + (wr >> 1)) * NKF * 8192u + (wr & 1) * 4096u;
  const int kf0 = 7 * (c >> 1) + 3 * (c & 1);
  q.voffH = lane * 16;
#define FFN_H(n, m, h) pk_bf16(relu(w.ag[n][m][2 * (h)]) * w.au[n][m][2 * (h)], relu(w.ag[n][m][2 * (h) + 1]) * w.au[n][m][2 * (h) + 1])
  if constexpr (GRP == 0) {
    MFMA_DRAIN();
    STAMP();
#pragma unroll
    for (int f = 0; f < 2; ++f)
#pragma unroll
      for (int m = 0; m < 4; ++m) {
        __builtin_amdgcn_raw_buffer_store_b128((u32x4){FFN_H(2 * f, m, 0), FFN_H(2 * f, m, 1), FFN_H(2 * f + 1, m, 0), FFN_H(2 * f + 1, m, 1)}, rws,
                                               lane * 16 + m * 1024 + hb + (kf0 + 1 + f) * 8192u, 0, 16);
        SCHED();
      }
    STAMP();
    WAIT_V(0); arrive(A.ws, lane, (unsigned)WS_FL + rch * 512 + (c >> 4) * 128 + c * 4, 0, epoch);
    __builtin_amdgcn_s_setprio(0);
    BAR();
    STAMP();
#pragma unroll
    for (int m = 0; m < 4; ++m)
#pragma unroll
      for (int n = 0; n < 4; ++n) { q.as[m][n] = ZERO4; PIN(q.as[m][n]); }
    wait_flags(A.ws, (unsigned)WS_FL + rch * 512 + kh * 192 + (lane & 15) * 4, epoch);
    STAMP();
#pragma unroll
    for (int st = 0; st < 2; ++st)
#pragma unroll
      for (int s = 0; s < 4; ++s) q.dmaA1(st, q.kf_ffn(st), s);
    STAMP();
  } else {
    bf16x8 xb[4], bs1[4], wg1[3], wu1[3];
#pragma unroll
    for (int m = 0; m < 4; ++m) { w.xa[m] = lds_b128(w.aX[0] + 32768 + m * 2048); xb[m] = lds_b128(w.aX[1] + 32768 + m * 2048); }
#pragma unroll
    for (int i = 0; i < 4; ++i) { w.bsf[i] = lds_b128(w.aB[0] + 8192 + i * 2048); bs1[i] = lds_b128(w.aB[1] + 8192 + i * 2048); }
    w.wgf[0] = lds_tr2(w.aW[0] + 32768, w.aW[0] + 32768 + 1024); w.wuf[0] = lds_tr2(w.aW[0] + 49152, w.aW[0] + 49152 + 1024);
    P2_B(1); WAIT_L0(); BAR();
    __builtin_amdgcn_s_setprio(1);
#pragma unroll
    for (int i = 0; i < 4; ++i)
#pragma unroll
      for (int m = 0; m < 4; ++m) MFMA(w.xa[m], w.bsf[i], w.as[i][m]);
#pragma unroll
    for (int i = 0; i < 4; ++i)
#pragma unroll
      for (int m = 0; m < 4; ++m) MFMA(xb[m], bs1[i], w.as[i][m]);
#pragma unroll
    for (int m = 0; m < 4; ++m) MFMA(w.wgf[0], w.xa[m], w.ag[0][m]);
#pragma unroll
    for (int m = 0; m < 4; ++m) MFMA(w.wuf[0], w.xa[m], w.au[0][m]);
    __builtin_amdgcn_s_setprio(0);
    BAR();
    STAMP();
    f32x4 pl[2], pm[2];
    f32x2 l64[2], cin[2], sw[2];
#pragma unroll
    for (int pf = 0; pf < 2; ++pf) {
      const lptr pb = mk_l(L_PRM + (pf * 16 + r) * 64);
      pl[pf] = *(const LAS f32x4*)pb; pm[pf] = *(const LAS f32x4*)(pb + 16);
    }
#pragma unroll
    for (int n = 1; n < 3; ++n) { w.wgf[n] = lds_tr2(w.aW[n] + 32768, w.aW[n] + 32768 + 1024); w.wuf[n] = lds_tr2(w.aW[n] + 49152, w.aW[n] + 49152 + 1024); }
    SCHED();
    f32x2 agg[2];
    {
      f32x2 e2[2];
#pragma unroll
      for (int k = 0; k < 16; ++k)
#pragma unroll
        for (int pf = 0; pf < 2; ++pf) {
          const f32x2 u = {w.as[2 * pf][k >> 2][k & 3], w.as[2 * pf + 1][k >> 2][k & 3]};
          e2[pf] = k ? cfma((f32x2){pl[pf][0], pl[pf][1]}, e2[pf], u) : u;
        }
#pragma unroll
      for (int pf = 0; pf < 2; ++pf) {
        const f32x2 e = e2[pf];
        const f32x2 l16 = {pm[pf][0], pm[pf][1]}, l32 = {pm[pf][2], pm[pf][3]};
        float a0, a1, b0, b1; sw16(e.x, a0, a1); sw16(e.y, b0, b1);
        const f32x2 r0 = {a0, b0};
        const f32x2 xx = cfma(l16, r0, (f32x2){a1, b1});
        float c0, c1, d0, d1; sw32(xx.x, c0, c1); sw32(xx.y, d0, d1);
        const f32x2 s0 = {c0, d0};
        agg[pf] = cfma(l32, s0, (f32x2){c1, d1});
        const f32x2 c3 = cfma(l16, s0, r0);
        cin[pf] = g == 0 ? (f32x2){0.f, 0.f} : g == 1 ? r0 : g == 2 ? s0 : c3;
      }
    }
    f32x2 aggq = (g & 1) ? agg[1] : agg[0]; PIN(aggq); PIN(cin[0]); PIN(cin[1]);
    if (wr < 3) {
      if (g < 2) *(LAS f32x2*)mk_l(L_WA + ((g * 16 + r) * 4 + wr) * 8) = aggq;
      WAIT_L0();
      *(volatile LAS unsigned*)mk_l(L_FLG + wr * 4) = 1u;
    }
    SCHED();
#pragma unroll
    for (int n = 0; n < 3; ++n) { wg1[n] = lds_tr2(w.aW[n] + 40960, w.aW[n] + 40960 + 1024); wu1[n] = lds_tr2(w.aW[n] + 57344, w.aW[n] + 57344 + 1024); }
    if (wr == 3) {
      const lptr fp = mk_l(L_FLG);
_Pragma("nounroll")
      for (unsigned spins = 0; spins < SPIN_CAP; ++spins) {
        const u32x4 f = *(volatile LAS u32x4*)fp;
        if (__builtin_amdgcn_readfirstlane(f[0] & f[1] & f[2])) break;
      }
      asm volatile("" ::: "memory");
      const lptr ap = mk_l(L_WA + ((g & 1) * 16 + r) * 32);
      const f32x4 a01 = *(const LAS f32x4*)ap; const f32x2 a2 = *(const LAS f32x2*)(ap + 16);
      const f32x2 lw = *(const LAS f32x2*)mk_l(L_PRM + ((g & 1) * 16 + r) * 64 + 32);
      const f32x2 t = cfma(lw, cfma(lw, cfma(lw, (f32x2){a01[0], a01[1]}, (f32x2){a01[2], a01[3]}), a2), aggq);
      if (rch < 7) __hip_atomic_store((GAS u64*)(A.ws + WS_AG) + (c * 8 + rch) * 64 + lane, ((u64)epoch << 32) | __float_as_uint((g & 2) ? t.y : t.x), RLX_AGENT);
    }
    P2_B(3);
    WAIT_L0(); SCHED();
    STAMP();
    __builtin_amdgcn_s_setprio(1);
#pragma unroll
    for (int n = 1; n < 3; ++n) {
#pragma unroll
      for (int m = 0; m < 4; ++m) MFMA(w.wgf[n], w.xa[m], w.ag[n][m]);
#pragma unroll
      for (int m = 0; m < 4; ++m) MFMA(w.wuf[n], w.xa[m], w.au[n][m]);
    }
#pragma unroll
    for (int n = 0; n < 3; ++n) {
#pragma unroll
      for (int m = 0; m < 4; ++m) MFMA(wg1[n], xb[m], w.ag[n][m]);
#pragma unroll
      for (int m = 0; m < 4; ++m) MFMA(wu1[n], xb[m], w.au[n][m]);
    }
    BAR();
    __builtin_amdgcn_s_setprio(2);
    lane = fresh_lane(); r = lane & 15; g = lane >> 4; qq = (lane & 15) >> 2; pp = lane & 3;
    q.voffH = lane * 16;
    const GAS u64* agp = (const GAS u64*)(A.ws + WS_AG) + c * 8 * 64 + lane;
    u64 gv_[7];
#pragma unroll
    for (int cc = 0; cc < 7; ++cc) gv_[cc] = __hip_atomic_load(agp + cc * 64, RLX_AGENT);
    STAMP();
    f32x2 coef[2], l256q;
    {
      l256q = *(const LAS f32x2*)mk_l(L_PRM + ((g & 1) * 16 + r) * 64 + 40);
#pragma unroll
      for (int pf = 0; pf < 2; ++pf) {
        const lptr pb = mk_l(L_PRM + (pf * 16 + r) * 64);
        pl[pf] = *(const LAS f32x4*)pb; pm[pf] = *(const LAS f32x4*)(pb + 16); l64[pf] = *(const LAS f32x2*)(pb + 32); coef[pf] = (f32x2){pl[pf][2], pl[pf][3]};
        const lptr ap = mk_l(L_WA + (pf * 16 + r) * 32);
        const f32x4 a01 = *(const LAS f32x4*)ap; const f32x2 a2 = *(const LAS f32x2*)(ap + 16);
        sw[pf] = wr > 0 ? (f32x2){a01[0], a01[1]} : (f32x2){0.f, 0.f};
        if (wr > 1) sw[pf] = cfma(l64[pf], sw[pf], (f32x2){a01[2], a01[3]});
        if (wr > 2) sw[pf] = cfma(l64[pf], sw[pf], a2);
      }
    }
#pragma unroll
    for (int pf = 0; pf < 2; ++pf)
#pragma unroll
      for (int m = 0; m < 4; ++m) {
        const f32x4 re = w.as[2 * pf][m], im = w.as[2 * pf + 1][m];
        w.as[2 * pf][m] = re * coef[pf].x - im * coef[pf].y; w.as[2 * pf + 1][m] = -(re * coef[pf].y) - im * coef[pf].x;
        PIN(w.as[2 * pf][m]); PIN(w.as[2 * pf + 1][m]);
      }
    f32x2 ca[2], cm[2];
#pragma unroll
    for (int pf = 0; pf < 2; ++pf) {
      const f32x2 l16 = {pm[pf][0], pm[pf][1]}, l32 = {pm[pf][2], pm[pf][3]};
      f32x2 pw = (wr & 1) ? l64[pf] : (f32x2){1.f, 0.f};
      if (wr & 2) pw = cmul(pw, cmul(l64[pf], l64[pf]));
      f32x2 pq = (g & 1) ? l16 : (f32x2){1.f, 0.f};
      if (g & 2) pq = cmul(pq, l32);
      ca[pf] = cmul(coef[pf], cfma(pq, sw[pf], cin[pf])); cm[pf] = cmul(coef[pf], cmul(pq, pw));
      PIN(ca[pf]); PIN(cm[pf]);
    }
    const lptr tb = mk_l(L_TR + wr * 9216 + r * 144 + g * 32), rb = mk_l(L_TR + wr * 9216 + (8 * g + qq) * 144 + pp * 32);
    STAMP();
_Pragma("nounroll")
    for (unsigned spins = 0; spins < SPIN_CAP; ++spins) {
      bool ok = true;
#pragma unroll
      for (int cc = 0; cc < 7; ++cc) ok &= (cc >= rch) || (unsigned)(gv_[cc] >> 32) == epoch;
      if (__all(ok)) break;
      __builtin_amdgcn_s_sleep(1);
#pragma unroll
      for (int cc = 0; cc < 7; ++cc) gv_[cc] = __hip_atomic_load(agp + cc * 64, RLX_AGENT);
    }
    STAMP();
    f32x2 cb = {0.f, 0.f};
#pragma unroll
    for (int cc = 0; cc < 7; ++cc) {
      float are, aim; sw32(__uint_as_float((unsigned)gv_[cc]), are, aim);
      if (cc < rch) cb = cfma(l256q, cb, (f32x2){are, aim});
    }
    f32x2 e[2];
    { float a0, a1, b0, b1; sw16(cb.x, a0, a1); sw16(cb.y, b0, b1);
      e[0] = cfma(cm[0], (f32x2){a0, b0}, ca[0]); e[1] = cfma(cm[1], (f32x2){a1, b1}, ca[1]); e[0].y = -e[0].y; e[1].y = -e[1].y; }
    unsigned hr[2][8], hi[2][8];
#pragma unroll
    for (int k = 0; k < 8; ++k)
#pragma unroll
      for (int pf = 0; pf < 2; ++pf) {
        const f32x2 lc = {pl[pf][0], -pl[pf][1]};
        const f32x2 h0 = cfma(lc, e[pf], (f32x2){w.as[2 * pf][k >> 1][(k & 1) * 2], w.as[2 * pf + 1][k >> 1][(k & 1) * 2]});
        e[pf] = cfma(lc, h0, (f32x2){w.as[2 * pf][k >> 1][(k & 1) * 2 + 1], w.as[2 * pf + 1][k >> 1][(k & 1) * 2 + 1]});
        hr[pf][k] = pk_bf16(h0.x, e[pf].x); hi[pf][k] = pk_bf16(h0.y, e[pf].y);
      }
#pragma unroll
    for (int ri = 0; ri < 2; ++ri)
#pragma unroll
      for (int pf = 0; pf < 2; ++pf) {
        const unsigned* h = ri ? hi[pf] : hr[pf];
        *(LAS u32x4*)(tb + ri * 4608 + pf * 2304) = (u32x4){h[0], h[1], h[2], h[3]};
        *(LAS u32x4*)(tb + ri * 4608 + pf * 2304 + 16) = (u32x4){h[4], h[5], h[6], h[7]};
      }
    WAIT_L0();
#pragma unroll
    for (int i = 0; i < 8; ++i) hq[6 + i] = __builtin_bit_cast(u32x4, lds_tr2(rb + (i >> 2) * 4608 + (i & 3) * 8, rb + (i >> 2) * 4608 + 4 * 144 + (i & 3) * 8));
    WAIT_L0();
    STAMP(); TS2();
    WAIT_V(0); BAR();
    STAMP();
#pragma unroll
    for (int m = 0; m < 4; ++m) { hq[m] = (u32x4){FFN_H(0, m, 0), FFN_H(0, m, 1), FFN_H(1, m, 0), FFN_H(1, m, 1)}; PIN(hq[m]); }
#pragma unroll
    for (int m = 0; m < 4; m += 2) {
      const auto s0 = __builtin_amdgcn_permlane32_swap(FFN_H(2, m, 0), FFN_H(2, m + 1, 0), false, false);
      const auto s1 = __builtin_amdgcn_permlane32_swap(FFN_H(2, m, 1), FFN_H(2, m + 1, 1), false, false);
      hq[4 + m / 2] = (u32x4){s0[0], s1[0], s0[1], s1[1]}; PIN(hq[4 + m / 2]);
    }
    ho[0] = lane * 16 + hb + (kf0 + ((c & 1) ? 3 : 0)) * 8192u;
    ho[1] = (lane & 31) * 16 + (lane >> 5) * 1024 + (c & 1) * 512 + hb + (kf0 + ((c & 1) ? 0 : 3)) * 8192u;
    ho[2] = lane * 16 + hb + (112 + c) * 8192u;
  }
  }
  if (GRP == 0) { STAMP(); TS2(); }
  constexpr int G = GRP;
  int r = lane & 15, g = lane >> 4;
  {
    const int qq = (lane & 15) >> 2, pp = lane & 3;
    q.voffC = (lane >> 3) * 2048 + (((lane & 7) ^ (((wr & 1) * 4 + (lane >> 4)) & 7)) * 16);
    const unsigned gb = G * 65536;
    q.aH = mk_l(gb + 32768 + wm * 4096 + lane * 16);
    const unsigned swz = 4 * (g & 1) + qq;
#pragma unroll
    for (int n = 0; n < 4; ++n) q.aW[n] = mk_l(gb + (4 * g + qq) * 256 + (((wn * 4 + n) ^ swz) << 5) + pp * 8);
#pragma unroll
    for (int kk = 0; kk < 2; ++kk) q.aC[kk] = mk_l(gb + (wn * 64 + r) * 128 + (((g + 4 * kk) ^ ((r >> 1) & 7)) << 4));
  }
  unsigned fl = 0u; const unsigned flo = (unsigned)WS_FL + rch * 512 + 256 + lane * 4;
#define FLAG_LOAD() asm volatile("global_load_dword %0, %1, %2 sc1" : "+v"(fl) : "v"(flo), "s"(A.ws) : "memory")
#define FLAG_CHECK(level) do { PIN(fl); const u64 bal_ = __ballot(fl == epoch); \
    if ((unsigned)((level) == 2 ? bal_ : bal_ >> 32) != ~0u) wait_flags(A.ws, (flo & ~128u) + ((level) - 2) * 128, epoch); } while (0)
#define P2_STEP(SB, SSM, DK, n) do { q.template load<SB, SSM, DK>((n) + 3); if (DK) WAIT_V(8); else WAIT_V(0); \
       WAIT_L0(); BAR(); q.template compute<SSM>(); BAR(); TS2(); } while (0)
#define HQ_ST(i) __builtin_amdgcn_raw_buffer_store_b128(hq[i], rws, (i) < 4 ? ho[0] + (i) * 1024 : (i) < 6 ? ho[1] + ((i) - 4) * 2048 : ho[2] + (((i) - 6) >> 2) * 262144 + (((i) - 6) & 3) * 1024, 0, 16)
#define RAISE(level) arrive(A.ws, lane, (unsigned)WS_FL + rch * 512 + (level) * 128 + c * 4, (level) - 1, epoch)
#define CBAR() do { WAIT_L0(); BAR(); q.template compute<false>(); BAR(); TS2(); } while (0)
  float gv[4];
#define GV_LOAD() do { const unsigned go_ = (unsigned)WS_G + (tt * 128 + wm * 64 + (r >> 2) * 16 + (r & 3)) * 4;     \
    asm volatile("global_load_dword %0, %4, %5 sc1\n\tglobal_load_dword %1, %4, %5 offset:16 sc1\n\tglobal_load_dword %2, %4, %5 offset:32 sc1\n\tglobal_load_dword %3, %4, %5 offset:48 sc1" \
                 : "=&v"(gv[0]), "=&v"(gv[1]), "=&v"(gv[2]), "=&v"(gv[3]) : "v"(go_), "s"(A.ws) : "memory"); } while (0)
  if (G == 0) {
    __builtin_amdgcn_s_setprio(0);
    WAIT_V(4); BAR(); STAMP(); q.template load<0, false, 0>(0);
#pragma unroll
    for (int st = 2; st < 4; ++st)
#pragma unroll
      for (int s = 0; s < 4; ++s) q.dmaA1(st, q.kf_ffn(st), s);
    WAIT_V(8); WAIT_L0(); BAR(); q.template compute<false, true>(); WAIT_V(4); BAR(); TS2();
    P2_STEP(1, false, 1, 2);
    STAMP();
    for (int n = 4; n < NF - 4; n += 4) {
      q.template load<0, false, 1>(n + 3); WAIT_V(8); if (n == 12) FLAG_CHECK(2); CBAR();
      q.template load<1, false, 1>(n + 5); FLAG_LOAD(); WAIT_V(9); CBAR();
    }
    q.template load<0, false, 1>(NF - 1); WAIT_V(8); FLAG_CHECK(3); CBAR();
    if (blockIdx.x == 255 && wr == 0 && lane == 0) __hip_atomic_store((GAS unsigned*)(A.ws + WS_EP), epoch, RLX_AGENT);
    q.template load<1, false, 2>(NF + 1); GV_LOAD(); WAIT_V(12); CBAR();
  } else {
    __builtin_amdgcn_s_setprio(0);
    BAR();
    q.template load<0, false, 1>(4); HQ_ST(0); HQ_ST(1); HQ_ST(2); WAIT_L0(); BAR(); q.template compute<false, true>(); BAR(); TS2();
    q.template load<1, false, 1>(6); HQ_ST(3); HQ_ST(4); HQ_ST(5); WAIT_V(14); CBAR();
    STAMP();
    q.template load<0, false, 1>(8); HQ_ST(6); HQ_ST(7); WAIT_V(13); CBAR();
    q.template load<1, false, 1>(10); HQ_ST(8); HQ_ST(9); WAIT_V(12); RAISE(2); CBAR();
    q.template load<0, false, 1>(12); HQ_ST(10); HQ_ST(11); WAIT_V(12); CBAR();
    q.template load<1, false, 1>(14); HQ_ST(12); HQ_ST(13); WAIT_V(12); CBAR();
#pragma unroll
    for (int m = 0; m < 4; ++m)
#pragma unroll
      for (int n = 0; n < 4; ++n) { q.as[m][n] = ZERO4; PIN(q.as[m][n]); }
    for (int n = 13; n < NF - 4; n += 4) {
      q.template load<0, false, 1>(n + 3); WAIT_V(8); if (n == 13) RAISE(3); CBAR();
      P2_STEP(1, false, 1, n + 2);
    }
    P2_STEP(0, false, 2, NF - 3);
    q.template load<1, false, 2>(NF + 2); GV_LOAD(); WAIT_V(12); CBAR();
  }
  STAMP();
  for (int n = NF + G; n < NF + 12; n += 4) { P2_STEP(0, true, 2, n); P2_STEP(1, true, 2, n + 2); }
  q.template load<0, true, G == 0 ? 2 : 0>(NF + 15);
  if (G == 0) WAIT_V(8); else WAIT_V(0);
#pragma unroll
  for (int m = 0; m < 4; ++m) {
    asm volatile("" : "+v"(gv[m]));
#pragma unroll
    for (int n = 0; n < 4; ++n) { q.af[m][n] *= 1.f - gv[m]; PIN(q.af[m][n]); }
  }
  WAIT_L0(); BAR(); q.template compute<true>(); BAR(); TS2();
  unsigned xfl;
  asm volatile("global_load_dword %0, %1, %2 sc1" : "=v"(xfl) : "v"((unsigned)WS_XF + (blockIdx.x >> 1) * 128u + wid * 4), "s"(A.ws) : "memory");
  q.template load<1, true, 0>(0);
  asm volatile("s_waitcnt vmcnt(0)" : "+v"(xfl) :: "memory");
  const bool early = __builtin_amdgcn_readfirstlane(xfl) == epoch;
  lane = fresh_lane(); r = lane & 15; g = lane >> 4;
  const unsigned xo = (unsigned)WS_XB + (blockIdx.x >> 1) * 65536u + wid * 8192u + lane * 16, xf = (unsigned)WS_XF + (blockIdx.x >> 1) * 128u + wid * 4;
  const int t0 = tt * 128 + wm * 64 + (2 * G + wn) * 4 + (lane >> 5), dc = dt * 128 + (lane & 31) * 4;
  u32x4 pv[8]; u32x2 dv = {0u, 0u};
  if (!sender) {
    if (early) {
#pragma unroll
      for (int i = 0; i < 8; ++i) pv[i] = __builtin_amdgcn_raw_buffer_load_b128(rws, xo + i * 1024, 0, 16);
    }
  } else {
    dv = *(const u32x2*)(A.dsk + dc);
    const int gsel = __float_as_int(wn ? gv[2 * G + 1] : gv[2 * G]);
#pragma unroll
    for (int i = 0; i < 8; ++i) {
      const u32x2 xv = *(const u32x2*)(A.x + (t0 + (i >> 1) * 16 + (i & 1) * 2) * D_ + dc);
      pv[i] = (u32x4){xv[0], xv[1], (unsigned)__builtin_amdgcn_ds_bpermute((2 * i + (lane >> 5)) * 4, gsel), 0u};
    }
  }
  lptr rw[2];
#pragma unroll
  for (int h = 0; h < 2; ++h) rw[h] = mk_l(G * 65536 + (wm * 64 + r) * 512 + wn * 256 + (((h * 4 + g) ^ (r & 7)) << 4));
#pragma unroll
  for (int m = 0; m < 4; ++m) { gv[m] *= 2.f; PIN(gv[m]); }
  WAIT_L0(); BAR();
  __builtin_amdgcn_s_setprio(1);
#pragma unroll
  for (int k = 0; k < 19; ++k) {
    if (k < 16) { MFMA(q.bb[0][k >> 2], q.ha[0][k & 3], q.as[k & 3][k >> 2]); MFMA(q.bb[1][k >> 2], q.ha[1][k & 3], q.as[k & 3][k >> 2]); }
    if (k == 3 || k >= 16) asm volatile("s_nop 3");
    SCHED();
    if (k >= 3) {
      const int m = (k - 3) & 3, n = (k - 3) >> 2;
      f32x4 v;
#pragma unroll
      for (int j = 0; j < 4; ++j) v[j] = __builtin_fmaf(q.as[m][n][j], gv[m], q.af[m][n][j]);
      *(LAS f32x4*)(rw[n & 1] + m * 8192 + (n >> 1) * 128) = v;
    }
    SCHED();
  }
  __builtin_amdgcn_s_setprio(0);
  if (G == 0) BAR();
  STAMP();
  lptr rr[4], rr2[4];
#pragma unroll
  for (int i = 0; i < 4; ++i) {
    const unsigned a_ = (wm * 64 + (2 * G + wn) * 16 + 2 * i + (lane >> 5)) * 512 + (((lane & 31) ^ (2 * i + (lane >> 5))) << 4);
    rr[i] = mk_l(a_); rr2[i] = mk_l(a_ + 65536);
  }
  WAIT_L0(); BAR();
  f32x4 o[8];
#pragma unroll
  for (int i = 0; i < 8; ++i) o[i] = *(const LAS f32x4*)(rr[i & 3] + (i >> 2) * 4096) + *(const LAS f32x4*)(rr2[i & 3] + (i >> 2) * 4096);
  STAMP();
  __builtin_amdgcn_s_waitcnt(0x0F70);
  if (sender) {
    const f32x4 df = {bf2f(dv[0] & 0xffffu), bfhi(dv[0]), bf2f(dv[1] & 0xffffu), bfhi(dv[1])};
#pragma unroll
    for (int i = 0; i < 8; ++i) {
      const f32x4 xv = {bf2f(pv[i][0] & 0xffffu), bfhi(pv[i][0]), bf2f(pv[i][1] & 0xffffu), bfhi(pv[i][1])};
      __builtin_amdgcn_raw_buffer_store_b128(__builtin_bit_cast(u32x4, o[i] + xv * df * __uint_as_float(pv[i][2])), rws, xo + i * 1024, 0, 16);
    }
    STAMP();
    WAIT_V(0);
    if (lane == 0) __hip_atomic_store((GAS unsigned*)(A.ws + xf), epoch, RLX_AGENT);
  } else {
    if (!early) {
      wait_flags(A.ws, xf, epoch);
#pragma unroll
      for (int i = 0; i < 8; ++i) pv[i] = __builtin_amdgcn_raw_buffer_load_b128(rws, xo + i * 1024, 0, 16);
    }
    STAMP();
    __builtin_amdgcn_s_waitcnt(0x0F70);
#pragma unroll
    for (int i = 0; i < 8; ++i)
      __builtin_amdgcn_raw_buffer_store_b128(__builtin_bit_cast(u32x4, o[i] + __builtin_bit_cast(f32x4, pv[i])), mk_rsrc(A.out, L_ * D_ * 4),
                                             ((t0 + (i >> 1) * 16 + (i & 1) * 2) * D_ + dc) * 4, 0, OAUX);
  }
  STAMP();
  ST_DUMP(A.ws, wid);
  if (GRP == 0) asm volatile(".Lgm_end0:"); else asm volatile(".Lgm_end1:");
  __builtin_amdgcn_endpgm();
  __builtin_unreachable();
}

__global__ __launch_bounds__(512, 2) void fused_kernel(Args A) {
  const int wid = __builtin_amdgcn_readfirstlane(threadIdx.x >> 6);
  const char* code; unsigned code_len;
  asm volatile("s_getpc_b64 %0\n.Lgm_pc:\n\ts_mov_b32 %1, .Lgm_end0-.Lgm_pc\n\ts_max_u32 %1, %1, .Lgm_end1-.Lgm_pc" : "=s"(code), "=s"(code_len) :: "scc");
  if (wid < 4) body<0>(A, wid, code, code_len); else body<1>(A, wid, code, code_len);
}

extern "C" void kernel_launch(void* const* d_in, const int* in_sizes, int n_in, void* d_out, int out_size, void* d_ws, size_t ws_size,
                              hipStream_t stream) {
  Args a; a.x = (const u16*)d_in[0]; a.lre = (const u16*)d_in[1]; a.lim = (const u16*)d_in[2]; a.bre = (const u16*)d_in[3];
  a.bim = (const u16*)d_in[4]; a.cre = (const u16*)d_in[5]; a.cim = (const u16*)d_in[6]; a.ldt = (const u16*)d_in[7]; a.dsk = (const u16*)d_in[8];
  a.wg = (const u16*)d_in[10]; a.wu = (const u16*)d_in[11]; a.wd = (const u16*)d_in[12]; a.gw = (const u16*)d_in[13];
  a.ws = (char*)d_ws; a.out = (float*)d_out;
  (void)hipFuncSetAttribute((const void*)fused_kernel, hipFuncAttributeMaxDynamicSharedMemorySize, LDS_BYTES);
  fused_kernel<<<256, 512, LDS_BYTES, stream>>>(a);
}
```

```cpp
#include <hip/hip_runtime.h>
#include <hip/hip_bf16.h>
#include <type_traits>
#define TCH 0
#define SO 1
#define EB 0
#define PER 2
#define PFD 4

typedef unsigned short u16;
typedef short bf16x8 __attribute__((ext_vector_type(8)));
typedef short s16x4 __attribute__((ext_vector_type(4)));
typedef float f32x4 __attribute__((ext_vector_type(4)));
typedef float f32x2 __attribute__((ext_vector_type(2)));
typedef unsigned u32x4 __attribute__((ext_vector_type(4)));
typedef unsigned u32x2 __attribute__((ext_vector_type(2)));
typedef unsigned long long u64;
#define LAS __attribute__((address_space(3)))
#define GAS __attribute__((address_space(1)))
#define RLX_AGENT __ATOMIC_RELAXED, __HIP_MEMORY_SCOPE_AGENT
typedef __amdgpu_buffer_rsrc_t rsrc_t;

constexpr int L_ = 2048, D_ = 1024, P_ = 1024, F_ = 3584;
constexpr int NKF = 176;
constexpr size_t WS_H = 0;
constexpr size_t H_BYTES = (size_t)16 * NKF * 8 * 1024;
constexpr size_t WS_XB = WS_H + H_BYTES;
constexpr size_t XB_BYTES = (size_t)64 * 4 * 3 * 16384;
constexpr size_t WS_G = WS_XB + XB_BYTES;
constexpr size_t WS_AG = WS_G + 8192;
constexpr size_t WS_EP = WS_AG + (size_t)32 * 8 * 64 * 8;
constexpr size_t WS_FL = WS_EP + 512;
constexpr size_t WS_XF = WS_FL + 8192;
constexpr size_t WS_END = WS_XF + 256 * 128;
constexpr unsigned SPIN_CAP = 1u << 20;

__device__ __forceinline__ float bf2f(unsigned b) { return __uint_as_float(b << 16); }
__device__ __forceinline__ float bfhi(unsigned b) { return __uint_as_float(b & 0xffff0000u); }
__device__ __forceinline__ unsigned pk_bf16(float lo, float hi) {
  typedef __bf16 bf2_t __attribute__((ext_vector_type(2)));
  return __builtin_bit_cast(unsigned, __builtin_convertvector((f32x2){lo, hi}, bf2_t)); }
__device__ __forceinline__ float relu(float v) { const int b = __float_as_int(v); return __int_as_float(b > 0 ? b : 0); }
template <int CTRL, int BANKS> __device__ __forceinline__ float dpp(float old, float src) {
  return __int_as_float(__builtin_amdgcn_update_dpp(__float_as_int(old), __float_as_int(src), CTRL, 0xf, BANKS, false)); }
__device__ __forceinline__ rsrc_t mk_rsrc(const void* p, unsigned bytes) {
  return __builtin_amdgcn_make_buffer_rsrc((void*)p, (short)0, (int)bytes, 0x00020000); }
__device__ __forceinline__ void dma16(unsigned ldsaddr, unsigned voff, rsrc_t srd, unsigned soff) {
  asm volatile("s_mov_b32 m0, %0\n\ts_nop 0\n\tbuffer_load_dwordx4 %1, %2, %3 offen lds"
               :: "s"(ldsaddr), "v"(voff), "s"(srd), "s"(soff) : "memory"); }
__device__ __forceinline__ void dma16_dev(unsigned ldsaddr, unsigned voff, rsrc_t srd, unsigned soff) {
  asm volatile("s_mov_b32 m0, %0\n\ts_nop 0\n\tbuffer_load_dwordx4 %1, %2, %3 offen sc1 lds"
               :: "s"(ldsaddr), "v"(voff), "s"(srd), "s"(soff) : "memory"); }
__device__ __forceinline__ void dma4(unsigned ldsaddr, unsigned voff, rsrc_t srd, unsigned soff) {
  asm volatile("s_mov_b32 m0, %0\n\ts_nop 0\n\tbuffer_load_dword %1, %2, %3 offen lds"
               :: "s"(ldsaddr), "v"(voff), "s"(srd), "s"(soff) : "memory"); }
#define WAIT_V(n) asm volatile("s_waitcnt vmcnt(" #n ")" ::: "memory")
#define WAIT_VN(n) asm volatile("s_waitcnt vmcnt(%0)" :: "i"(n) : "memory")
#define WAIT_L0() do { __builtin_amdgcn_s_waitcnt(0xC07F); asm volatile("" ::: "memory"); } while (0)
#define SCHED() __builtin_amdgcn_sched_barrier(0)
#define RBAR() do { __builtin_amdgcn_s_barrier(); asm volatile("" ::: "memory"); } while (0)
#define BAR() do { SCHED(); RBAR(); SCHED(); } while (0)
__device__ __forceinline__ int fresh_lane() { int l; asm volatile("v_mbcnt_lo_u32_b32 %0, -1, 0\n\tv_mbcnt_hi_u32_b32 %0, -1, %0" : "=v"(l)); return l; }
typedef LAS char* lptr;
__device__ __forceinline__ lptr mk_l(unsigned a) { lptr p = (lptr)(size_t)a; asm volatile("" : "+v"(p)); return p; }
__device__ __forceinline__ bf16x8 lds_b128(lptr a) { return *(const LAS bf16x8*)a; }
__device__ __forceinline__ bf16x8 lds_tr2(lptr a0, lptr a1) {
  s16x4 lo = __builtin_amdgcn_ds_read_tr16_b64_v4i16((LAS s16x4*)a0);
  s16x4 hi = __builtin_amdgcn_ds_read_tr16_b64_v4i16((LAS s16x4*)a1);
  return __builtin_shufflevector(lo, hi, 0, 1, 2, 3, 4, 5, 6, 7); }
#define MFMA(a, b, c) asm volatile("v_mfma_f32_16x16x32_bf16 %0, %1, %2, %0" : "+v"(c) : "v"(a), "v"(b))
#define MFMA0(a, b, c) asm volatile("v_mfma_f32_16x16x32_bf16 %0, %1, %2, 0" : "=&v"(c) : "v"(a), "v"(b))
#define PIN(x) asm volatile("" : "+v"(x))
#define MFMA_DRAIN() do { asm volatile("s_nop 7\n\ts_nop 7" ::: "memory"); SCHED(); } while (0)
#define ZERO4 ((f32x4){0.f, 0.f, 0.f, 0.f})

#define ST_DECL
#define STAMP()
#define TS2()
#define ST_DUMP(ws, wid)

struct Args {
  const u16 *x, *lre, *lim, *bre, *bim, *cre, *cim, *ldt, *dsk, *wg, *wu, *wd, *gw;
  char* ws; float* out;
};

constexpr unsigned L_X = 0, L_W = 65536, L_BS = 131072, L_PRM = 151552  ,
                   L_WA = 153600  , L_FLG = 154624  , L_SINK = 154880, LDS_BYTES = 163840;

__device__ __forceinline__ f32x2 cmul(f32x2 a, f32x2 b) { return (f32x2){a.x * b.x - a.y * b.y, a.x * b.y + a.y * b.x}; }
__device__ __forceinline__ f32x2 cfma(f32x2 a, f32x2 b, f32x2 c) {
  return (f32x2){__builtin_fmaf(a.x, b.x, __builtin_fmaf(-a.y, b.y, c.x)), __builtin_fmaf(a.x, b.y, __builtin_fmaf(a.y, b.x, c.y))}; }
__device__ __forceinline__ void sw16(float v, float& r0, float& r1) { auto t = __builtin_amdgcn_permlane16_swap(__float_as_uint(v), __float_as_uint(v), false, false); r0 = __uint_as_float(t[0]); r1 = __uint_as_float(t[1]); }
__device__ __forceinline__ void sw32(float v, float& r0, float& r1) { auto t = __builtin_amdgcn_permlane32_swap(__float_as_uint(v), __float_as_uint(v), false, false); r0 = __uint_as_float(t[0]); r1 = __uint_as_float(t[1]); }

__device__ __forceinline__ void touch(const char* p) {
  asm volatile("s_mov_b32 m0, %0\n\ts_nop 0\n\tglobal_load_lds_dword %1, off" :: "s"(L_SINK), "v"(p) : "memory"); }
template <int GRP>
struct P1 {
  static constexpr int NW = GRP == 0 ? 4 : 3, NS = GRP == 0 ? 0 : 4;
  f32x4 ag[NW][4], au[NW][4], as[4][4];
  bf16x8 xa[4], wgf[NW], wuf[NW], bsf[4];
  lptr aX[2], aB[2], aW[NW];
  unsigned voffX, voffW, voffB;
  unsigned sX, sW, sB, ldsX, ldsW, ldsB;
  rsrc_t rx, rwg, rwu, rb;
  const char *pfp[2], *pfc; unsigned pfs[2]; int pfo[2], pfm[2], pfg;
  __device__ __forceinline__ void pull(int T, int k) {
    const bool ev = (T & (PER - 1)) == pfg;
    const int i = T + pfo[k];
    const char* p = pfp[k] + pfs[k] * (unsigned)(i < 0 ? 0 : i > pfm[k] ? pfm[k] : i);
    touch(ev ? p : pfc);
  }

  template <int BUF, int KS, class M> __device__ __forceinline__ void load(M&& mem) {
    mem(0);
#pragma unroll
    for (int m = 0; m < 4; ++m) xa[m] = lds_b128(aX[KS] + BUF * 32768 + m * 2048);
    mem(1);
#pragma unroll
    for (int i = 0; i < NS; ++i) bsf[i] = lds_b128(aB[KS] + BUF * 8192 + i * 2048);
    if (NS) mem(2);
#pragma unroll
    for (int n = 0; n < NW; ++n) {
      wgf[n] = lds_tr2(aW[n] + BUF * 32768 + KS * 8192, aW[n] + BUF * 32768 + KS * 8192 + 1024);
      wuf[n] = lds_tr2(aW[n] + BUF * 32768 + 16384 + KS * 8192, aW[n] + BUF * 32768 + 16384 + KS * 8192 + 1024);
      mem(n + (NS ? 3 : 2));
    }
  }
  template <int BUF, int KS> __device__ __forceinline__ void load() { load<BUF, KS>([](int) {}); }
  template <int BUF> __device__ __forceinline__ void dmaX1(int T, int s) { dma16(ldsX + BUF * 32768 + s * 2048, voffX, rx, sX + T * 128 + s * 8192); }
  template <int BUF, int HH> __device__ __forceinline__ void dmaW1(int T, int i) {
    dma16(ldsW + BUF * 32768 + (i & 1) * 16384 + HH * 8192 + (i >> 1) * 1024, voffW, (i & 1) ? rwu : rwg, sW + (unsigned)(T * 64 + HH * 32 + (i >> 1) * 4) * 7168u);
  }
  template <int BUF> __device__ __forceinline__ void dmaB1(int T, int pf) { dma16(ldsB + BUF * 8192 + pf * 4096, voffB, rb, sB + pf * 32768 + T * 128); }
  template <int NV> __device__ __forceinline__ void compute() {
    constexpr int NM = 4 * NS + 8 * NW;
    __builtin_amdgcn_s_setprio(1);
#pragma unroll
    for (int k = 0; k < NM; ++k) {
      if (k == NM - EB && NV != -2) { __builtin_amdgcn_s_setprio(2); if (NV >= 0) WAIT_VN(NV < 0 ? 0 : NV); RBAR(); }
      const int f = k >> 2, m = (f & 1) ? 3 - (k & 3) : (k & 3);
      if (f < NS) MFMA(xa[m], bsf[f], as[f][m]);
      else if (((f - NS) & 1) == 0) MFMA(wgf[(f - NS) >> 1], xa[m], ag[(f - NS) >> 1][m]);
      else MFMA(wuf[(f - NS) >> 1], xa[m], au[(f - NS) >> 1][m]);
    }
    if (EB == 0 && NV != -2) { if (NV >= 0) WAIT_VN(NV < 0 ? 0 : NV); RBAR(); }
    __builtin_amdgcn_s_setprio(0);
    SCHED();
  }
  template <int BUF> __device__ __forceinline__ void dmaX(int T) {
#pragma unroll
    for (int s = 0; s < 4; ++s) dma16(ldsX + BUF * 32768 + s * 2048, voffX, rx, sX + T * 128 + s * 8192);
  }
  template <int BUF, int HH> __device__ __forceinline__ void dmaW(int T) {
#pragma unroll
    for (int s = 0; s < 2; ++s) {
      const unsigned so = sW + (unsigned)(T * 64 + HH * 32 + s * 4) * 7168u;
      dma16(ldsW + BUF * 32768 + HH * 8192 + s * 1024, voffW, rwg, so);
      dma16(ldsW + BUF * 32768 + 16384 + HH * 8192 + s * 1024, voffW, rwu, so);
    }
  }
  template <int BUF> __device__ __forceinline__ void dmaB(int T) {
#pragma unroll
    for (int pf = 0; pf < 2; ++pf) dma16(ldsB + BUF * 8192 + pf * 4096, voffB, rb, sB + pf * 32768 + T * 128);
  }
};

template <int BUF> __device__ __forceinline__ void p1_tile(P1<0>& w, int T) {
  w.template compute<8>();
  w.template load<BUF, 1>([&](int i) { if (i < 4) w.template dmaW1<BUF ^ 1, 1>(T + 1, i); else w.pull(T, i - 4); }); WAIT_L0(); BAR();
  w.template compute<6>();
  w.template load<BUF ^ 1, 0>([&](int i) { if (i < 4) w.template dmaX1<BUF>(T + 2, i); else w.template dmaB1<BUF>(T + 2, i - 4); }); WAIT_L0(); BAR();
}
template <int BUF> __device__ __forceinline__ void p1_tile(P1<1>& w, int T) {
  w.template load<BUF, 0>([&](int i) { if (i >= SO && i < SO + 4) w.template dmaX1<BUF ^ 1>(T + 1, i - SO); }); WAIT_L0(); BAR();
  w.template compute<-1>();
  w.template load<BUF, 1>([&](int i) { if (i >= SO && i < SO + 4) w.template dmaW1<BUF, 0>(T + 2, i - SO); }); WAIT_V(4); WAIT_L0(); BAR();
  w.template compute<-1>();
}

constexpr int NSTEP = 22;
struct P2 {
  f32x4 af[4][4], as[4][4];
  bf16x8 ha[2][4], bb[2][4];
  lptr aH, aW[4], aC[2];
  unsigned voffH, voffW, voffC;
  unsigned sH, sHs, sW, sC, ldsA, ldsB, kk, pj[4];
  rsrc_t rh, rw, rc;
  template <int ST> __device__ __forceinline__ unsigned kf() const {
    constexpr int e = 2 * (ST - 8), k0 = ST < 8 ? 7 * (ST >> 1) + 3 * (ST & 1) + 1 : 7 * (e / 3) + 3 * (e % 3), k1 = ST < 8 ? k0 + 1 : 7 * ((e + 1) / 3) + 3 * ((e + 1) % 3);
    return k0 + kk * (k1 - k0);
  }
  template <int ST, int GA> __device__ __forceinline__ void dmaA(int j) {
    const unsigned so = ST < 14 ? sH + kf<ST>() * 8192u : sHs + (ST - 14) * 16384u;
    dma16_dev(ldsA + GA * 49152 + (ST % 3) * 16384 + j * 1024, voffH, rh, so + GA * (NKF * 8192u) + pj[j]);
  }
  template <int ST> __device__ __forceinline__ void dmaB(int s) {
    const unsigned l = ldsB + (ST % 3) * 16384 + s * 2048;
    if (ST >= 14) dma16(l, voffC, rc, sC + (ST - 14) * 128u + s * 32768u);
    else dma16(l, voffW, rw, sW + kf<ST>() * 65536u + s * 16384u);
  }
  template <int ST, int GA> __device__ __forceinline__ void stageA() {
#pragma unroll
    for (int j = 0; j < 4; ++j) dmaA<ST, GA>(j);
  }
  template <int ST> __device__ __forceinline__ void stageB() {
#pragma unroll
    for (int s = 0; s < 4; ++s) dmaB<ST>(s);
  }
  template <int ST, int G> __device__ __forceinline__ void load() {
    constexpr int SL = (ST % 3) * 16384;
#pragma unroll
    for (int k2 = 0; k2 < 2; ++k2) {
#pragma unroll
      for (int n = 0; n < 4; ++n) {
        const int i = k2 * 4 + n;
        if constexpr (G == 0) { if constexpr (ST + 2 < NSTEP) { if (i < 4) dmaB<ST + 2>(i); else if (i < 6) dmaA<ST + 2, 1>(i - 4); } }
        else { if constexpr (ST + 2 < NSTEP) { if (i < 2) dmaA<ST + 2, 1>(i + 2); } if constexpr (ST + 3 < NSTEP) { if (i >= 2 && i < 6) dmaA<ST + 3, 0>(i - 2); } }
        if (ST >= 14) bb[k2][n] = lds_b128(aC[k2] + SL + n * 2048);
        else bb[k2][n] = lds_tr2(aW[n] + SL + k2 * 8192, aW[n] + SL + k2 * 8192 + 4096);
        ha[k2][n] = lds_b128(aH + SL + k2 * 8192 + n * 1024);
      }
    }
  }
  template <int ST, int NV> __device__ __forceinline__ void compute() {
    __builtin_amdgcn_s_setprio(1);
#pragma unroll
    for (int k = 0; k < 32; ++k) {
      if (k == 32 - EB) { __builtin_amdgcn_s_setprio(2); WAIT_VN(NV); RBAR(); }
      const int k2 = k >> 4, n = (k >> 2) & 3, m = (n & 1) ? 3 - (k & 3) : (k & 3);
      f32x4& c = ST >= 14 ? as[m][n] : af[m][n];
      if ((ST == 0 || ST == 14) && k2 == 0) MFMA0(bb[k2][n], ha[k2][m], c); else MFMA(bb[k2][n], ha[k2][m], c);
    }
    if (EB == 0) { WAIT_VN(NV); RBAR(); }
    __builtin_amdgcn_s_setprio(0);
    SCHED();
  }
};
template <int I, int N, class F> __device__ __forceinline__ void static_for(F&& f) {
  if constexpr (I < N) { f(std::integral_constant<int, I>{}); static_for<I + 1, N>(f); }
}

__device__ __forceinline__ void arrive(char* ws, int lane, unsigned flag_off, int ctr, unsigned epoch) {
  if (lane == 0) {
    const unsigned old = __hip_atomic_fetch_add((LAS unsigned*)mk_l(L_FLG + 16 + ctr * 4), 1u, __ATOMIC_RELAXED, __HIP_MEMORY_SCOPE_WORKGROUP);
    if (old == 3u) __hip_atomic_store((GAS unsigned*)(ws + flag_off), epoch, RLX_AGENT);
  }
}
__device__ __forceinline__ void wait_flags(const char* ws, unsigned off, unsigned epoch) {
_Pragma("nounroll")
  for (unsigned spins = 0; spins < SPIN_CAP; ++spins) {
    if (__all(__hip_atomic_load((const GAS unsigned*)(ws + off), RLX_AGENT) == epoch)) break;
    __builtin_amdgcn_s_sleep(1);
  }
  asm volatile("" ::: "memory");
}

template <int GRP>
__device__ __forceinline__ void body(const Args& A, const int wid, const char* code, const unsigned code_len) {
  __builtin_amdgcn_s_waitcnt(0x0F70);
  ST_DECL STAMP();
  const int xcd = blockIdx.x & 7, idx = blockIdx.x >> 3, c = xcd * 4 + (idx >> 3), rch = idx & 7;
  const int kq = xcd >> 1, tt = 2 * rch + GRP, dt = (xcd & 1) * 4 + (idx >> 3), tile = rch * 8 + dt;
  const unsigned flb = (unsigned)WS_FL + rch * 1024;
  const int wr = wid & 3, wm = wr >> 1, wn = wr & 1;
  int lane = fresh_lane();
  unsigned epoch;
  P2 q;
  u32x4 hq[14]; unsigned ho[3];
  const rsrc_t rws = mk_rsrc(A.ws, (unsigned)WS_END);
  {
  P1<GRP> w;
  constexpr int NW = P1<GRP>::NW, NS = P1<GRP>::NS;
  int r = lane & 15, g = lane >> 4, qq = (lane & 15) >> 2, pp = lane & 3;
  unsigned ep0, plr = 0, pli = 0, pld = 0;
  asm volatile("global_load_dword %0, %1, %2 sc1" : "=v"(ep0) : "v"((unsigned)WS_EP), "s"(A.ws) : "memory");
  if (GRP == 1) {
    const unsigned po = (c * 32 + (lane & 31)) * 2;
    asm volatile("global_load_ushort %0, %1, %2" : "=v"(plr) : "v"(po), "s"(A.lre) : "memory");
    asm volatile("global_load_ushort %0, %1, %2" : "=v"(pli) : "v"(po), "s"(A.lim) : "memory");
    asm volatile("global_load_ushort %0, %1, %2" : "=v"(pld) : "v"(po), "s"(A.ldt) : "memory");
  }
  w.rx = mk_rsrc(A.x, L_ * D_ * 2); w.rwg = mk_rsrc(A.wg, D_ * F_ * 2); w.rwu = mk_rsrc(A.wu, D_ * F_ * 2);
  w.rb = mk_rsrc((wr >> 1) ? A.bim : A.bre, P_ * D_ * 2);
  {
    const unsigned sw8 = ((lane & 7) ^ (((wr & 1) * 4 + (lane >> 4)) & 7)) * 16;
    w.voffX = ((lane >> 5) * 16 + ((lane >> 3) & 3)) * 2048 + sw8;
    w.voffB = (lane >> 3) * 2048 + sw8;
    w.voffW = (lane >> 4) * 7168 + ((((lane & 15) >> 1) ^ ((lane >> 4) | ((wr & 1) << 2))) * 32) + (lane & 1) * 16;
    w.sX = (unsigned)(rch * 256 + (GRP * 2 + (wr >> 1)) * 64 + (wr & 1) * 32) * 2048u;
    w.ldsX = L_X + (GRP * 16 + (wr >> 1) * 8 + (wr & 1)) * 1024;
    const int ii = (wr & 1) * 2 + (wr >> 1) * 4;
    w.sW = (unsigned)(ii * 4) * 7168u + (unsigned)((224 * c) & ~63);
    w.ldsW = L_W + ii * 1024;
    w.sB = (unsigned)(c * 32 + (wr & 1) * 8) * 2048u;
    w.ldsB = L_BS + wr * 1024;
  }
  if (GRP == 0) { w.template dmaX<0>(0); w.template dmaB<0>(0); w.template dmaW<0, 1>(0); w.template dmaX<1>(1); w.template dmaB<1>(1); }
  else { w.template dmaW<0, 0>(0); w.template dmaX<0>(0); w.template dmaW<1, 0>(1); }
  if (GRP == 0) {
    {
      const char* wbase = (const char*)((wr & 1) ? A.wu : A.wg) + (blockIdx.x * 14 + (wr >> 1) * 7) * 128;
      const char* rbase = wr < 2 ? (const char*)A.x + (blockIdx.x * 8 + wr * 4) * 2048 : (const char*)(wr == 2 ? A.bre : A.bim) + blockIdx.x * 4 * 2048;
      const int l2 = lane < 7 * (PFD - 2) ? lane : 0;
      touch(lane < 56 ? wbase + (size_t)(2 + l2 / 7) * 458752 + (l2 % 7) * 128 : rbase + ((lane - 56) >> 1) * 2048 + (2 + (lane & 1)) * 128);
    }
    constexpr int NG = 256 / PER;
    w.pfg = idx & (PER - 1);
    const unsigned rk = (blockIdx.x & 7) + (idx / PER) * 8, gw = rk * 4 + wr;
    const unsigned co = ((idx * 4 + wr) * 4 + (lane & 3)) * 128u;
    w.pfc = lane < 4 ? code + (co < code_len ? co : 0u) : (const char*)((idx & 4) ? A.dsk : A.gw) + ((idx & 3) * 4 + wr) * 128;
    constexpr int LW = 7 * PER, LX = 8 * PER;
    if (lane < LW) { w.pfp[0] = (const char*)((wr & 1) ? A.wu : A.wg) + ((rk * 2 + (wr >> 1)) * LW + lane) * 128; w.pfs[0] = 458752u; w.pfo[0] = PFD; w.pfm[0] = 15; }
    else {
      const int l = lane < LW + LX ? lane - LW : 0;
      const char* rb_ = wr < 2 ? (const char*)A.x + (rk * 2 + wr) * (LX / 2) * 2048 : (const char*)(wr == 2 ? A.bre : A.bim) + rk * (LX / 2) * 2048;
      w.pfp[0] = rb_ + (l >> 1) * 2048 + (l & 1) * 128; w.pfs[0] = 256u; w.pfo[0] = 2; w.pfm[0] = 7;
    }
    constexpr int LD = 4 * PER;
    if (lane < LD) { w.pfp[1] = (const char*)A.wd + gw * (LD * 128u) + lane * 128; w.pfs[1] = 524288u; w.pfo[1] = 0; w.pfm[1] = 13; }
    else { const int l = lane < 2 * LD ? lane - LD : 0;
      w.pfp[1] = (const char*)(l < LD / 2 ? A.cre : A.cim) + gw * (LD * 64u) + (l & (LD / 2 - 1)) * 128; w.pfs[1] = 262144u; w.pfo[1] = -6; w.pfm[1] = 7; }
    static_assert(LW + LX <= 64 && NG * PER == 256, "");
    touch(w.pfc);
  }
  {
#pragma unroll
    for (int ks = 0; ks < 2; ++ks) {
      const unsigned ch = ((g + 4 * ks) ^ ((r >> 1) & 7)) << 4;
      w.aX[ks] = mk_l(L_X + (wr * 64 + r) * 128 + ch);
      if (NS) w.aB[ks] = mk_l(L_BS + r * 128 + ch);
    }
    const unsigned swz = qq | ((g & 1) << 2);
#pragma unroll
    for (int n = 0; n < NW; ++n) {
      unsigned col;
      if (GRP == 0) col = (((2 + n) ^ swz) << 5) + pp * 8;
      else if (n < 2) col = (((((c & 1) ? 6 : 0) + n) ^ swz) << 5) + pp * 8;
      else col = (((((c & 1) ? 0 : 6) + (pp >> 1)) ^ swz) << 5) + (c & 1) * 16 + (pp & 1) * 8;
      w.aW[n] = mk_l(L_W + (8 * g + qq) * 256 + col);
    }
  }
#pragma unroll
  for (int m = 0; m < 4; ++m) {
#pragma unroll
    for (int n = 0; n < NW; ++n) { w.ag[n][m] = ZERO4; w.au[n][m] = ZERO4; PIN(w.ag[n][m]); PIN(w.au[n][m]); }
#pragma unroll
    for (int i = 0; i < NS; ++i) { w.as[i][m] = ZERO4; PIN(w.as[i][m]); }
  }
  if (GRP == 0) asm volatile("s_waitcnt vmcnt(18)" : "+v"(ep0) :: "memory");
  else asm volatile("s_waitcnt vmcnt(12)" : "+v"(ep0), "+v"(plr), "+v"(pli), "+v"(pld) :: "memory");
  epoch = __builtin_amdgcn_readfirstlane(ep0) + 1u;
  if (GRP == 1) {
    if (wr == 0) {
      const float lr = bf2f(plr), li = bf2f(pli);
      const float dt_ = bf2f(pk_bf16(__expf(bf2f(pld)), 0.f) & 0xffffu);
      const float e = __expf(dt_ * lr), rev = __builtin_amdgcn_fractf(dt_ * li * 0.15915494309189535f);
      const f32x2 lam = {e * __builtin_amdgcn_cosf(rev), e * __builtin_amdgcn_sinf(rev)};
      const float nr = lam.x - 1.f, rden = 1.f / (lr * lr + li * li);
      const f32x2 l2 = cmul(lam, lam);
      f32x2 l16 = cmul(l2, l2); l16 = cmul(l16, l16); l16 = cmul(l16, l16);
      const f32x2 l32 = cmul(l16, l16), l64 = cmul(l32, l32), l128 = cmul(l64, l64), l256 = cmul(l128, l128);
      const lptr pb = mk_l(L_PRM + (lane & 31) * 64);
      *(LAS f32x4*)(pb) = (f32x4){lam.x, lam.y, (nr * lr + lam.y * li) * rden, (lam.y * lr - nr * li) * rden};
      *(LAS f32x4*)(pb + 16) = (f32x4){l16.x, l16.y, l32.x, l32.y};
      *(LAS f32x4*)(pb + 32) = (f32x4){l64.x, l64.y, l256.x, l256.y};

    } else if (wr == 1) {
      *(LAS unsigned*)mk_l(L_FLG + (lane & 7) * 4) = 0u;
    }
    WAIT_V(4);
    if (wr == 2) {
      __hip_atomic_store((GAS u64*)(A.ws + WS_AG) + (c * 8 + rch) * 64 + lane, (u64)(epoch - 1u) << 32, RLX_AGENT);
      if (lane < 6) __hip_atomic_store((GAS unsigned*)(A.ws + flb + lane * 128 + c * 4), epoch - 1u, RLX_AGENT);
      else if (lane >= 32 && (lane & 3)) __hip_atomic_store((GAS unsigned*)(A.ws + WS_XF + (tile * 4 + (kq ^ (lane & 3))) * 128) + (lane - 32), epoch - 1u, RLX_AGENT);
    }
  } else WAIT_V(12);
  WAIT_L0(); BAR();
  if (GRP == 0) { w.template load<0, 0>(); WAIT_L0(); }
  BAR();
  STAMP();
  for (int T = 0; T < 14; T += 2) {
    TS2(); p1_tile<0>(w, T); TS2();
    p1_tile<1>(w, T + 1);
  }
  STAMP();
  lane = fresh_lane();
  q.rh = mk_rsrc(A.ws + WS_H, (unsigned)H_BYTES); q.rw = mk_rsrc(A.wd, F_ * D_ * 2); q.rc = mk_rsrc((kq >> 1) ? A.cim : A.cre, D_ * P_ * 2);
  q.voffW = (lane >> 4) * 2048 + ((((lane & 15) >> 1) ^ (((wr & 1) << 2) | (lane >> 4))) * 32) + (lane & 1) * 16;
  {
    const int i0 = (wr & 1) + (wr >> 1) * 8;
    q.kk = wr >> 1;
    q.sH = (unsigned)(2 * rch * NKF + 28 * kq) * 8192u + (wr & 1) * 4096u;
    q.sHs = (unsigned)(2 * rch * NKF + 112 + 16 * kq + (wr >> 1)) * 8192u + (wr & 1) * 4096u;
    q.sW = (unsigned)(28 * kq * 32 + (wr & 1) * 4) * 2048u + dt * 256u;
    q.sC = (unsigned)(dt * 128 + i0 * 8) * 2048u + (kq & 1) * 1024u;
#pragma unroll
    for (int j = 0; j < 4; ++j) q.pj[j] = (unsigned)(j ^ kq) << 10;
    q.ldsA = 49152 + wr * 4096; q.ldsB = i0 * 1024;
  }
  u32x4 gx[4], gg[4];
  const int tg = rch * 256 + c * 8 + wr * 2 + (lane >> 5);
  if constexpr (GRP == 0) {
    w.template compute<8>();
    w.template load<0, 1>(); w.template dmaW<1, 1>(15);
#pragma unroll
    for (int i = 0; i < 4; ++i) gx[i] = *(const u32x4*)(A.x + tg * D_ + i * 256 + (lane & 31) * 8);
    WAIT_L0(); BAR();
    w.template compute<8>();
    w.template load<1, 0>(); q.template stageB<0>();
#pragma unroll
    for (int i = 0; i < 4; ++i) gg[i] = *(const u32x4*)(A.gw + i * 256 + (lane & 31) * 8);
    WAIT_L0(); BAR();
    w.template compute<12>();
    w.template load<1, 1>();
    {
      typedef __bf16 bf2_t __attribute__((ext_vector_type(2)));
      float s0 = 0.f, s1 = 0.f;
#pragma unroll
      for (int i = 0; i < 4; ++i)
#pragma unroll
        for (int j = 0; j < 4; j += 2) {
          const unsigned a0 = gx[i][j], b0 = gg[i][j], a1 = gx[i][j + 1], b1 = gg[i][j + 1];
          s0 = __builtin_amdgcn_fdot2_f32_bf16(__builtin_bit_cast(bf2_t, a0), __builtin_bit_cast(bf2_t, b0), s0, false);
          s1 = __builtin_amdgcn_fdot2_f32_bf16(__builtin_bit_cast(bf2_t, a1), __builtin_bit_cast(bf2_t, b1), s1, false);
        }
      float s = s0 + s1;
      s += dpp<0x128, 0xf>(0.f, s); s += dpp<0x124, 0xf>(0.f, s); s += dpp<0x122, 0xf>(0.f, s); s += dpp<0x121, 0xf>(0.f, s);
      float t0, t1; sw16(s, t0, t1); s = t0 + t1;
      if ((lane & 31) == 0) __hip_atomic_store((GAS unsigned*)(A.ws + WS_G) + tg, __float_as_uint(1.f / (1.f + __expf(-s))), RLX_AGENT);
    }
    WAIT_L0(); BAR();
    w.template compute<-2>();
    __builtin_amdgcn_s_setprio(3);
  } else {
    w.template load<0, 0>(); w.template dmaX<1>(15); WAIT_L0(); BAR();
    w.template compute<-1>();
    w.template load<0, 1>(); WAIT_V(0); WAIT_L0(); BAR();
    w.template compute<-1>();
  }
  lane = fresh_lane(); r = lane & 15; g = lane >> 4; qq = (lane & 15) >> 2; pp = lane & 3;
  const unsigned hb = (unsigned)WS_H + (unsigned)(2 * rch + (wr >> 1)) * NKF * 8192u + (wr & 1) * 4096u;
  const int kf0 = 7 * (c >> 1) + 3 * (c & 1);
  q.voffH = lane * 16;
#define FFN_H(n, m, h) pk_bf16(relu(w.ag[n][m][2 * (h)]) * w.au[n][m][2 * (h)], relu(w.ag[n][m][2 * (h) + 1]) * w.au[n][m][2 * (h) + 1])
  if constexpr (GRP == 0) {
    MFMA_DRAIN();
    STAMP();
#pragma unroll
    for (int f = 0; f < 2; ++f)
#pragma unroll
      for (int m = 0; m < 4; ++m) {
        __builtin_amdgcn_raw_buffer_store_b128((u32x4){FFN_H(2 * f, m, 0), FFN_H(2 * f, m, 1), FFN_H(2 * f + 1, m, 0), FFN_H(2 * f + 1, m, 1)}, rws,
                                               lane * 16 + m * 1024 + hb + (kf0 + 1 + f) * 8192u, 0, 16);
        SCHED();
      }
    STAMP();
    WAIT_V(0); arrive(A.ws, lane, flb + (c >> 3) * 128 + c * 4, 0, epoch);
    __builtin_amdgcn_s_setprio(0);
    BAR();
    STAMP();
    wait_flags(A.ws, flb + kq * 160 + (lane & 7) * 4, epoch);
    STAMP();
    q.template stageA<0, 0>(); q.template stageA<0, 1>(); q.template stageA<1, 0>();
    STAMP();
  } else {
    bf16x8 xb[4], bs1[4], wg1[3], wu1[3];
#pragma unroll
    for (int m = 0; m < 4; ++m) { w.xa[m] = lds_b128(w.aX[0] + 32768 + m * 2048); xb[m] = lds_b128(w.aX[1] + 32768 + m * 2048); }
#pragma unroll
    for (int i = 0; i < 4; ++i) { w.bsf[i] = lds_b128(w.aB[0] + 8192 + i * 2048); bs1[i] = lds_b128(w.aB[1] + 8192 + i * 2048); }
    w.wgf[0] = lds_tr2(w.aW[0] + 32768, w.aW[0] + 32768 + 1024); w.wuf[0] = lds_tr2(w.aW[0] + 49152, w.aW[0] + 49152 + 1024);
    q.template stageB<1>(); WAIT_L0(); BAR();
    __builtin_amdgcn_s_setprio(1);
#pragma unroll
    for (int i = 0; i < 4; ++i)
#pragma unroll
      for (int m = 0; m < 4; ++m) MFMA(w.xa[m], w.bsf[i], w.as[i][m]);
#pragma unroll
    for (int i = 0; i < 4; ++i)
#pragma unroll
      for (int m = 0; m < 4; ++m) MFMA(xb[m], bs1[i], w.as[i][m]);
#pragma unroll
    for (int m = 0; m < 4; ++m) MFMA(w.wgf[0], w.xa[m], w.ag[0][m]);
#pragma unroll
    for (int m = 0; m < 4; ++m) MFMA(w.wuf[0], w.xa[m], w.au[0][m]);
    __builtin_amdgcn_s_setprio(0);
    BAR();
    STAMP();
    f32x4 pl[2], pm[2];
    f32x2 l64[2], cin[2], sw[2];
#pragma unroll
    for (int pf = 0; pf < 2; ++pf) {
      const lptr pb = mk_l(L_PRM + (pf * 16 + r) * 64);
      pl[pf] = *(const LAS f32x4*)pb; pm[pf] = *(const LAS f32x4*)(pb + 16);
    }
#pragma unroll
    for (int n = 1; n < 3; ++n) { w.wgf[n] = lds_tr2(w.aW[n] + 32768, w.aW[n] + 32768 + 1024); w.wuf[n] = lds_tr2(w.aW[n] + 49152, w.aW[n] + 49152 + 1024); }
    SCHED();
    f32x2 agg[2];
    {
      f32x2 e2[2];
#pragma unroll
      for (int k = 0; k < 16; ++k)
#pragma unroll
        for (int pf = 0; pf < 2; ++pf) {
          const f32x2 u = {w.as[2 * pf][k >> 2][k & 3], w.as[2 * pf + 1][k >> 2][k & 3]};
          e2[pf] = k ? cfma((f32x2){pl[pf][0], pl[pf][1]}, e2[pf], u) : u;
        }
#pragma unroll
      for (int pf = 0; pf < 2; ++pf) {
        const f32x2 e = e2[pf];
        const f32x2 l16 = {pm[pf][0], pm[pf][1]}, l32 = {pm[pf][2], pm[pf][3]};
        float a0, a1, b0, b1; sw16(e.x, a0, a1); sw16(e.y, b0, b1);
        const f32x2 r0 = {a0, b0};
        const f32x2 xx = cfma(l16, r0, (f32x2){a1, b1});
        float c0, c1, d0, d1; sw32(xx.x, c0, c1); sw32(xx.y, d0, d1);
        const f32x2 s0 = {c0, d0};
        agg[pf] = cfma(l32, s0, (f32x2){c1, d1});
        const f32x2 c3 = cfma(l16, s0, r0);
        cin[pf] = g == 0 ? (f32x2){0.f, 0.f} : g == 1 ? r0 : g == 2 ? s0 : c3;
      }
    }
    f32x2 aggq = (g & 1) ? agg[1] : agg[0]; PIN(aggq); PIN(cin[0]); PIN(cin[1]);
    if (wr < 3) {
      if (g < 2) *(LAS f32x2*)mk_l(L_WA + ((g * 16 + r) * 4 + wr) * 8) = aggq;
      WAIT_L0();
      *(volatile LAS unsigned*)mk_l(L_FLG + wr * 4) = 1u;
    }
    SCHED();
#pragma unroll
    for (int n = 0; n < 3; ++n) { wg1[n] = lds_tr2(w.aW[n] + 40960, w.aW[n] + 40960 + 1024); wu1[n] = lds_tr2(w.aW[n] + 57344, w.aW[n] + 57344 + 1024); }
    if (wr == 3) {
      const lptr fp = mk_l(L_FLG);
_Pragma("nounroll")
      for (unsigned spins = 0; spins < SPIN_CAP; ++spins) {
        const u32x4 f = *(volatile LAS u32x4*)fp;
        if (__builtin_amdgcn_readfirstlane(f[0] & f[1] & f[2])) break;
      }
      asm volatile("" ::: "memory");
      const lptr ap = mk_l(L_WA + ((g & 1) * 16 + r) * 32);
      const f32x4 a01 = *(const LAS f32x4*)ap; const f32x2 a2 = *(const LAS f32x2*)(ap + 16);
      const f32x2 lw = *(const LAS f32x2*)mk_l(L_PRM + ((g & 1) * 16 + r) * 64 + 32);
      const f32x2 t = cfma(lw, cfma(lw, cfma(lw, (f32x2){a01[0], a01[1]}, (f32x2){a01[2], a01[3]}), a2), aggq);
      if (rch < 7) __hip_atomic_store((GAS u64*)(A.ws + WS_AG) + (c * 8 + rch) * 64 + lane, ((u64)epoch << 32) | __float_as_uint((g & 2) ? t.y : t.x), RLX_AGENT);
    }
    WAIT_L0(); SCHED();
    STAMP();
    __builtin_amdgcn_s_setprio(1);
#pragma unroll
    for (int n = 1; n < 3; ++n) {
#pragma unroll
      for (int m = 0; m < 4; ++m) MFMA(w.wgf[n], w.xa[m], w.ag[n][m]);
#pragma unroll
      for (int m = 0; m < 4; ++m) MFMA(w.wuf[n], w.xa[m], w.au[n][m]);
    }
#pragma unroll
    for (int n = 0; n < 3; ++n) {
#pragma unroll
      for (int m = 0; m < 4; ++m) MFMA(wg1[n], xb[m], w.ag[n][m]);
#pragma unroll
      for (int m = 0; m < 4; ++m) MFMA(wu1[n], xb[m], w.au[n][m]);
    }
    BAR();
    __builtin_amdgcn_s_setprio(2);
    lane = fresh_lane(); r = lane & 15; g = lane >> 4; qq = (lane & 15) >> 2; pp = lane & 3;
    q.voffH = lane * 16;
    const GAS u64* agp = (const GAS u64*)(A.ws + WS_AG) + c * 8 * 64 + lane;
    u64 gv_[7];
#pragma unroll
    for (int cc = 0; cc < 7; ++cc) gv_[cc] = __hip_atomic_load(agp + cc * 64, RLX_AGENT);
    STAMP();
    f32x2 coef[2], l256q;
    {
      l256q = *(const LAS f32x2*)mk_l(L_PRM + ((g & 1) * 16 + r) * 64 + 40);
#pragma unroll
      for (int pf = 0; pf < 2; ++pf) {
        const lptr pb = mk_l(L_PRM + (pf * 16 + r) * 64);
        pl[pf] = *(const LAS f32x4*)pb; pm[pf] = *(const LAS f32x4*)(pb + 16); l64[pf] = *(const LAS f32x2*)(pb + 32); coef[pf] = (f32x2){pl[pf][2], pl[pf][3]};
        const lptr ap = mk_l(L_WA + (pf * 16 + r) * 32);
        const f32x4 a01 = *(const LAS f32x4*)ap; const f32x2 a2 = *(const LAS f32x2*)(ap + 16);
        sw[pf] = wr > 0 ? (f32x2){a01[0], a01[1]} : (f32x2){0.f, 0.f};
        if (wr > 1) sw[pf] = cfma(l64[pf], sw[pf], (f32x2){a01[2], a01[3]});
        if (wr > 2) sw[pf] = cfma(l64[pf], sw[pf], a2);
      }
    }
#pragma unroll
    for (int pf = 0; pf < 2; ++pf)
#pragma unroll
      for (int m = 0; m < 4; ++m) {
        const f32x4 re = w.as[2 * pf][m], im = w.as[2 * pf + 1][m];
        w.as[2 * pf][m] = re * coef[pf].x - im * coef[pf].y; w.as[2 * pf + 1][m] = -(re * coef[pf].y) - im * coef[pf].x;
        PIN(w.as[2 * pf][m]); PIN(w.as[2 * pf + 1][m]);
      }
    f32x2 ca[2], cm[2];
#pragma unroll
    for (int pf = 0; pf < 2; ++pf) {
      const f32x2 l16 = {pm[pf][0], pm[pf][1]}, l32 = {pm[pf][2], pm[pf][3]};
      f32x2 pw = (wr & 1) ? l64[pf] : (f32x2){1.f, 0.f};
      if (wr & 2) pw = cmul(pw, cmul(l64[pf], l64[pf]));
      f32x2 pq = (g & 1) ? l16 : (f32x2){1.f, 0.f};
      if (g & 2) pq = cmul(pq, l32);
      ca[pf] = cmul(coef[pf], cfma(pq, sw[pf], cin[pf])); cm[pf] = cmul(coef[pf], cmul(pq, pw));
      PIN(ca[pf]); PIN(cm[pf]);
    }
    const unsigned trb = wr == 0 ? 32768u : wr == 1 ? 81920u : 131072u + (wr - 2) * 9216u;
    const lptr tb = mk_l(trb + r * 144 + g * 32), rb = mk_l(trb + (8 * g + qq) * 144 + pp * 32);
    STAMP();
_Pragma("nounroll")
    for (unsigned spins = 0; spins < SPIN_CAP; ++spins) {
      bool ok = true;
#pragma unroll
      for (int cc = 0; cc < 7; ++cc) ok &= (cc >= rch) || (unsigned)(gv_[cc] >> 32) == epoch;
      if (__all(ok)) break;
      __builtin_amdgcn_s_sleep(1);
#pragma unroll
      for (int cc = 0; cc < 7; ++cc) gv_[cc] = __hip_atomic_load(agp + cc * 64, RLX_AGENT);
    }
    STAMP();
    f32x2 cb = {0.f, 0.f};
#pragma unroll
    for (int cc = 0; cc < 7; ++cc) {
      float are, aim; sw32(__uint_as_float((unsigned)gv_[cc]), are, aim);
      if (cc < rch) cb = cfma(l256q, cb, (f32x2){are, aim});
    }
    f32x2 e[2];
    { float a0, a1, b0, b1; sw16(cb.x, a0, a1); sw16(cb.y, b0, b1);
      e[0] = cfma(cm[0], (f32x2){a0, b0}, ca[0]); e[1] = cfma(cm[1], (f32x2){a1, b1}, ca[1]); e[0].y = -e[0].y; e[1].y = -e[1].y; }
    unsigned hr[2][8], hi[2][8];
#pragma unroll
    for (int k = 0; k < 8; ++k)
#pragma unroll
      for (int pf = 0; pf < 2; ++pf) {
        const f32x2 lc = {pl[pf][0], -pl[pf][1]};
        const f32x2 h0 = cfma(lc, e[pf], (f32x2){w.as[2 * pf][k >> 1][(k & 1) * 2], w.as[2 * pf + 1][k >> 1][(k & 1) * 2]});
        e[pf] = cfma(lc, h0, (f32x2){w.as[2 * pf][k >> 1][(k & 1) * 2 + 1], w.as[2 * pf + 1][k >> 1][(k & 1) * 2 + 1]});
        hr[pf][k] = pk_bf16(h0.x, e[pf].x); hi[pf][k] = pk_bf16(h0.y, e[pf].y);
      }
#pragma unroll
    for (int ri = 0; ri < 2; ++ri)
#pragma unroll
      for (int pf = 0; pf < 2; ++pf) {
        const unsigned* h = ri ? hi[pf] : hr[pf];
        *(LAS u32x4*)(tb + ri * 4608 + pf * 2304) = (u32x4){h[0], h[1], h[2], h[3]};
        *(LAS u32x4*)(tb + ri * 4608 + pf * 2304 + 16) = (u32x4){h[4], h[5], h[6], h[7]};
      }
    WAIT_L0();
#pragma unroll
    for (int i = 0; i < 8; ++i) hq[6 + i] = __builtin_bit_cast(u32x4, lds_tr2(rb + (i >> 2) * 4608 + (i & 3) * 8, rb + (i >> 2) * 4608 + 4 * 144 + (i & 3) * 8));
    WAIT_L0();
    STAMP(); TS2();
    WAIT_V(0); BAR();
    STAMP();
    q.template stageA<1, 1>(); q.template stageA<2, 0>();
#pragma unroll
    for (int m = 0; m < 4; ++m) { hq[m] = (u32x4){FFN_H(0, m, 0), FFN_H(0, m, 1), FFN_H(1, m, 0), FFN_H(1, m, 1)}; PIN(hq[m]); }
#pragma unroll
    for (int m = 0; m < 4; m += 2) {
      const auto s0 = __builtin_amdgcn_permlane32_swap(FFN_H(2, m, 0), FFN_H(2, m + 1, 0), false, false);
      const auto s1 = __builtin_amdgcn_permlane32_swap(FFN_H(2, m, 1), FFN_H(2, m + 1, 1), false, false);
      hq[4 + m / 2] = (u32x4){s0[0], s1[0], s0[1], s1[1]}; PIN(hq[4 + m / 2]);
    }
    ho[0] = lane * 16 + hb + (kf0 + ((c & 1) ? 3 : 0)) * 8192u;
    ho[1] = (lane & 31) * 16 + (lane >> 5) * 1024 + (c & 1) * 512 + hb + (kf0 + ((c & 1) ? 0 : 3)) * 8192u;
    ho[2] = lane * 16 + hb + (112 + c) * 8192u;
  }
  }
  if (GRP == 0) { STAMP(); TS2(); }
  constexpr int G = GRP;
  int r = lane & 15, g = lane >> 4;
  {
    const int qq = (lane & 15) >> 2, pp = lane & 3;
    q.voffC = (lane >> 3) * 2048 + (((lane & 7) ^ (((wr & 1) * 4 + (lane >> 4)) & 7)) * 16);
    q.aH = mk_l(49152 + G * 49152 + wm * 4096 + lane * 16);
    const unsigned swz = 4 * (g & 1) + qq;
#pragma unroll
    for (int n = 0; n < 4; ++n) q.aW[n] = mk_l((4 * g + qq) * 256 + (((wn * 4 + n) ^ swz) << 5) + pp * 8);
#pragma unroll
    for (int k2 = 0; k2 < 2; ++k2) q.aC[k2] = mk_l((wn * 64 + r) * 128 + (((g + 4 * k2) ^ ((r >> 1) & 7)) << 4));
  }
  unsigned fl = 0u;
#define FLAG_LOAD(off) asm volatile("global_load_dword %0, %1, %2 sc1" : "+v"(fl) : "v"(off), "s"(A.ws) : "memory")
#define FLAG_CHECK(off) do { PIN(fl); if (!__all(fl == epoch)) wait_flags(A.ws, off, epoch); } while (0)
#define HQ_ST(i) __builtin_amdgcn_raw_buffer_store_b128(hq[i], rws, (i) < 4 ? ho[0] + (i) * 1024 : (i) < 6 ? ho[1] + ((i) - 4) * 2048 : ho[2] + (((i) - 6) >> 2) * 262144 + (((i) - 6) & 3) * 1024, 0, 16)
#define XTRA(st) (((st) == 13 ? 4 : 0) + (TCH && ((st) == 6 || (st) == 11 || (st) == 15 || (st) == 16) ? 1 : 0))
  float gv[4];
  u32x2 xv[4], dv; float gr[4];
  const int t0 = tt * 128 + wm * 64 + kq * 4, dc0 = dt * 128 + wn * 64;
  auto side = [&](auto ST_) {
    constexpr int ST = decltype(ST_)::value;
    if constexpr (ST == 13) {
      const int l = fresh_lane();
      const unsigned go = (unsigned)WS_G + (tt * 128 + wm * 64 + ((l & 15) >> 2) * 16 + (l & 3)) * 4;
#pragma unroll
      for (int j = 0; j < 4; ++j) asm volatile("global_load_dword %0, %1, %2 sc1" : "=v"(gv[j]) : "v"(go + (q.pj[j] >> 6)), "s"(A.ws) : "memory");
    }
    if constexpr (TCH && (ST == 6 || ST == 11 || ST == 16)) {
      const char* pc; asm volatile("s_getpc_b64 %0" : "=s"(pc));
      const unsigned o = (unsigned)(pc - code) + 2048u + (wr * 16 + (fresh_lane() & 15)) * 128u;
      touch(code + (o < code_len ? o : 0u));
    }
    if constexpr (TCH && ST == 15) {
      const int l = fresh_lane(), j = (l & 3) ? (l & 3) : 1;
      const char* p = l < 16 ? A.ws + WS_XB + ((l & 4) ? (tile * 4 + kq) * 3 + (j - 1) : (tile * 4 + (kq ^ j)) * 3 + (j - 1)) * 16384u + wid * 2048
                    : l < 32 ? (const char*)(A.out + (size_t)(t0 + ((l & 15) >> 2) * 16 + (l & 3)) * D_ + dc0)
                    : l < 48 ? (const char*)(A.x + (t0 + ((l & 15) >> 2) * 16 + (l & 3)) * D_ + dc0) : l < 56 ? (const char*)(A.dsk + dc0) : A.ws + WS_XF + tile * 512;
      asm volatile("s_mov_b32 m0, %0\n\ts_nop 0\n\tglobal_load_lds_dword %1, off sc1" :: "s"(L_SINK), "v"(p) : "memory");
    }
    if constexpr (ST >= 16 && ST < 20) {
      constexpr int m = ST - 16;
      PIN(gv[m]);
#pragma unroll
      for (int n = 0; n < 4; ++n) { q.af[m][n] *= 1.f - gv[m]; PIN(q.af[m][n]); }
    }
    if constexpr (ST == NSTEP - 1) {
      const int l = fresh_lane();
      dv = *(const u32x2*)(A.dsk + dc0 + (l & 15) * 4);
#pragma unroll
      for (int i = 0; i < 4; ++i) {
        xv[i] = *(const u32x2*)(A.x + (t0 + i * 16 + (l >> 4)) * D_ + dc0 + (l & 15) * 4);
        gr[i] = __int_as_float(__builtin_amdgcn_ds_bpermute((4 * i + (l >> 4)) * 4, __float_as_int(gv[0])));
      }
#pragma unroll
      for (int j = 0; j < 4; ++j) { gv[j] *= 2.f; PIN(gv[j]); }
    }
  };
  __builtin_amdgcn_s_setprio(0);
  if constexpr (G == 0) {
    const unsigned f2 = flb + 512 + (8 * kq + (lane & 7)) * 4, f3 = flb + 640 + (lane & 31) * 4;
    WAIT_V(8); BAR(); STAMP();
    q.template load<0, 0>(); WAIT_V(10); WAIT_L0(); BAR();
    static_for<0, NSTEP - 1>([&](auto ST_) {
      constexpr int ST = decltype(ST_)::value;
      constexpr int prev = (ST + 2 < NSTEP ? 6 : 0) + (ST == 9 ? 1 : 0) + XTRA(ST);
      q.template compute<ST, prev>();
      if constexpr (ST == 10) {
        FLAG_CHECK(f3);
        if (blockIdx.x == 255 && wr == 0 && lane == 0) __hip_atomic_store((GAS unsigned*)(A.ws + WS_EP), epoch, RLX_AGENT);
      }
      if constexpr (ST + 1 == 5) FLAG_LOAD(f2);
      q.template load<ST + 1, 0>();
      if constexpr (ST + 1 == 5) { WAIT_V(6); FLAG_CHECK(f2); }
      if constexpr (ST + 1 == 9) FLAG_LOAD(f3);
      side(std::integral_constant<int, ST + 1>{});
      WAIT_L0(); BAR(); TS2();
      if constexpr (ST == 1 || ST == 13) STAMP();
    });
  } else {
    HQ_ST(0); HQ_ST(1); HQ_ST(2);
    BAR();
    static_for<0, NSTEP>([&](auto ST_) {
      constexpr int ST = decltype(ST_)::value;
      constexpr int nst = ST == 0 ? 3 : ST <= 4 ? 2 : 0;
      if constexpr (ST == 2) arrive(A.ws, lane, flb + 512 + c * 4, 1, epoch);
      if constexpr (ST == 6) arrive(A.ws, lane, flb + 640 + c * 4, 2, epoch);
      q.template load<ST, 1>();
      if constexpr (ST == 0) { HQ_ST(3); HQ_ST(4); HQ_ST(5); }
      if constexpr (ST >= 1 && ST <= 4) { HQ_ST(4 + 2 * ST); HQ_ST(5 + 2 * ST); }
      side(ST_);
      WAIT_L0(); BAR();
      if constexpr (ST + 1 < NSTEP) {
        q.template compute<ST, (ST + 2 < NSTEP ? 2 : 0) + (ST + 3 < NSTEP ? 4 : 0) + nst + XTRA(ST)>(); TS2();
        if constexpr (ST == 1 || ST == 13) STAMP();
      }
    });
  }
  lane = fresh_lane(); r = lane & 15; g = lane >> 4;
  f32x4 o[4];
  {
    unsigned xo[3], pk[4];
#pragma unroll
    for (int j = 1; j < 4; ++j) xo[j - 1] = (unsigned)WS_XB + ((tile * 4 + (kq ^ j)) * 3 + (j - 1)) * 16384u + wid * 2048 + lane * 16;
    __builtin_amdgcn_s_setprio(1);
#pragma unroll
    for (int k = 0; k < 19; ++k) {
      if (k < 16) { const int m = ((k >> 2) + 1) & 3, n = k & 3; MFMA(q.bb[0][n], q.ha[0][m], q.as[m][n]); MFMA(q.bb[1][n], q.ha[1][m], q.as[m][n]); }
      if (k == 3 || k >= 16) asm volatile("s_nop 3");
      if (G == 0 && k == 14) RBAR();
      SCHED();
      if (k >= 3) {
        const int m = (((k - 3) >> 2) + 1) & 3, n = (k - 3) & 3;
        f32x4 v;
#pragma unroll
        for (int j = 0; j < 4; ++j) v[j] = __builtin_fmaf(q.as[m][n][j], gv[m], q.af[m][n][j]);
        if (m == 0) o[n] = v;
        else {
          pk[(n & 1) * 2] = pk_bf16(v[0], v[1]); pk[(n & 1) * 2 + 1] = pk_bf16(v[2], v[3]);
          if (n & 1) __builtin_amdgcn_raw_buffer_store_b128((u32x4){pk[0], pk[1], pk[2], pk[3]}, rws, xo[m - 1] + (n >> 1) * 1024, 0, 16);
        }
      }
      SCHED();
    }
    __builtin_amdgcn_s_setprio(0);
  }
  STAMP();
  WAIT_V(0);
  if (lane >= 1 && lane < 4) __hip_atomic_store((GAS unsigned*)(A.ws + WS_XF + (tile * 4 + (kq ^ lane)) * 128 + wid * 16) + lane, epoch, RLX_AGENT);
  STAMP();
  f32x4 sk[4];
  {
    const f32x4 df = {bf2f(dv[0] & 0xffffu), bfhi(dv[0]), bf2f(dv[1] & 0xffffu), bfhi(dv[1])};
#pragma unroll
    for (int i = 0; i < 4; ++i) { sk[i] = (f32x4){bf2f(xv[i][0] & 0xffffu), bfhi(xv[i][0]), bf2f(xv[i][1] & 0xffffu), bfhi(xv[i][1])} * df * gr[i]; PIN(sk[i]); }
  }
  const lptr tw = mk_l(49152 + G * 49152 + wr * 4096 + r * 256), tr_ = mk_l(49152 + G * 49152 + wr * 4096 + g * 256);
  wait_flags(A.ws, (unsigned)WS_XF + (tile * 4 + kq) * 128 + wid * 16 + (1 + (lane & 3) % 3) * 4, epoch);
  STAMP();
  {
    u32x4 pv[6];
    const unsigned xi = (unsigned)WS_XB + (tile * 4 + kq) * 3 * 16384u + wid * 2048 + lane * 16;
#pragma unroll
    for (int i = 0; i < 6; ++i) pv[i] = __builtin_amdgcn_raw_buffer_load_b128(rws, xi + (i >> 1) * 16384 + (i & 1) * 1024, 0, 16);
#pragma unroll
    for (int i = 0; i < 6; ++i)
#pragma unroll
      for (int h = 0; h < 2; ++h) {
        const unsigned a0 = pv[i][2 * h], a1 = pv[i][2 * h + 1];
        o[(i & 1) * 2 + h] += (f32x4){bf2f(a0 & 0xffffu), bfhi(a0), bf2f(a1 & 0xffffu), bfhi(a1)};
      }
  }
#pragma unroll
  for (int n = 0; n < 4; ++n) *(LAS f32x4*)(tw + (((n * 4 + g) ^ (r & 7)) << 4)) = o[n];
  WAIT_L0();
#pragma unroll
  for (int i = 0; i < 4; ++i) {
    const f32x4 v = *(const LAS f32x4*)(tr_ + i * 1024 + ((r ^ ((4 * i + g) & 7)) << 4));
    *(f32x4*)(A.out + (size_t)(t0 + i * 16 + g) * D_ + dc0 + r * 4) = v + sk[i];
  }
  STAMP();
  ST_DUMP(A.ws, wid);
  if (GRP == 0) asm volatile(".Lgm_end0:"); else asm volatile(".Lgm_end1:");
  __builtin_amdgcn_endpgm();
  __builtin_unreachable();
}

__global__ __launch_bounds__(512, 2) void fused_kernel(Args A) {
  const int wid = __builtin_amdgcn_readfirstlane(threadIdx.x >> 6);
  const char* code; unsigned code_len;
  asm volatile("s_getpc_b64 %0\n.Lgm_pc:\n\ts_mov_b32 %1, .Lgm_end0-.Lgm_pc\n\ts_max_u32 %1, %1, .Lgm_end1-.Lgm_pc" : "=s"(code), "=s"(code_len) :: "scc");
  if (wid < 4) body<0>(A, wid, code, code_len); else body<1>(A, wid, code, code_len);
}

extern "C" void kernel_launch(void* const* d_in, const int* in_sizes, int n_in, void* d_out, int out_size, void* d_ws, size_t ws_size,
                              hipStream_t stream) {
  Args a; a.x = (const u16*)d_in[0]; a.lre = (const u16*)d_in[1]; a.lim = (const u16*)d_in[2]; a.bre = (const u16*)d_in[3];
  a.bim = (const u16*)d_in[4]; a.cre = (const u16*)d_in[5]; a.cim = (const u16*)d_in[6]; a.ldt = (const u16*)d_in[7]; a.dsk = (const u16*)d_in[8];
  a.wg = (const u16*)d_in[10]; a.wu = (const u16*)d_in[11]; a.wd = (const u16*)d_in[12]; a.gw = (const u16*)d_in[13];
  a.ws = (char*)d_ws; a.out = (float*)d_out;
  (void)hipFuncSetAttribute((const void*)fused_kernel, hipFuncAttributeMaxDynamicSharedMemorySize, LDS_BYTES);
  fused_kernel<<<256, 512, LDS_BYTES, stream>>>(a);
}
```

```cpp
#include <hip/hip_runtime.h>
#include <hip/hip_bf16.h>
#include <type_traits>
#define SO 1
#define EB 0
#define PFD 4

typedef unsigned short u16;
typedef short bf16x8 __attribute__((ext_vector_type(8)));
typedef short s16x4 __attribute__((ext_vector_type(4)));
typedef float f32x4 __attribute__((ext_vector_type(4)));
typedef float f32x2 __attribute__((ext_vector_type(2)));
typedef unsigned u32x4 __attribute__((ext_vector_type(4)));
typedef unsigned u32x2 __attribute__((ext_vector_type(2)));
typedef unsigned long long u64;
#define LAS __attribute__((address_space(3)))
#define GAS __attribute__((address_space(1)))
#define RLX_AGENT __ATOMIC_RELAXED, __HIP_MEMORY_SCOPE_AGENT
typedef __amdgpu_buffer_rsrc_t rsrc_t;

constexpr int L_ = 2048, D_ = 1024, P_ = 1024, F_ = 3584;
constexpr int NKF = 176;
constexpr size_t WS_H = 0;
constexpr size_t H_BYTES = (size_t)16 * NKF * 8 * 1024;
constexpr size_t WS_XB = WS_H + H_BYTES;
constexpr size_t XB_BYTES = (size_t)64 * 4 * 3 * 16384;
constexpr size_t WS_G = WS_XB + XB_BYTES;
constexpr size_t WS_AG = WS_G + 8192;
constexpr size_t WS_EP = WS_AG + (size_t)32 * 8 * 64 * 8;
constexpr size_t WS_FL = WS_EP + 512;
constexpr size_t WS_XF = WS_FL + 8192;
constexpr size_t WS_END = WS_XF + 256 * 128;
constexpr unsigned SPIN_CAP = 1u << 20;

__device__ __forceinline__ float bf2f(unsigned b) { return __uint_as_float(b << 16); }
__device__ __forceinline__ float bfhi(unsigned b) { return __uint_as_float(b & 0xffff0000u); }
__device__ __forceinline__ unsigned pk_bf16(float lo, float hi) {
  typedef __bf16 bf2_t __attribute__((ext_vector_type(2)));
  return __builtin_bit_cast(unsigned, __builtin_convertvector((f32x2){lo, hi}, bf2_t)); }
__device__ __forceinline__ float relu(float v) { const int b = __float_as_int(v); return __int_as_float(b > 0 ? b : 0); }
template <int CTRL, int BANKS> __device__ __forceinline__ float dpp(float old, float src) {
  return __int_as_float(__builtin_amdgcn_update_dpp(__float_as_int(old), __float_as_int(src), CTRL, 0xf, BANKS, false)); }
__device__ __forceinline__ rsrc_t mk_rsrc(const void* p, unsigned bytes) {
  return __builtin_amdgcn_make_buffer_rsrc((void*)p, (short)0, (int)bytes, 0x00020000); }
__device__ __forceinline__ void dma16(unsigned ldsaddr, unsigned voff, rsrc_t srd, unsigned soff) {
  asm volatile("s_mov_b32 m0, %0\n\ts_nop 0\n\tbuffer_load_dwordx4 %1, %2, %3 offen lds"
               :: "s"(ldsaddr), "v"(voff), "s"(srd), "s"(soff) : "memory"); }
__device__ __forceinline__ void dma16_dev(unsigned ldsaddr, unsigned voff, rsrc_t srd, unsigned soff) {
  asm volatile("s_mov_b32 m0, %0\n\ts_nop 0\n\tbuffer_load_dwordx4 %1, %2, %3 offen sc1 lds"
               :: "s"(ldsaddr), "v"(voff), "s"(srd), "s"(soff) : "memory"); }
__device__ __forceinline__ void dma4(unsigned ldsaddr, unsigned voff, rsrc_t srd, unsigned soff) {
  asm volatile("s_mov_b32 m0, %0\n\ts_nop 0\n\tbuffer_load_dword %1, %2, %3 offen lds"
               :: "s"(ldsaddr), "v"(voff), "s"(srd), "s"(soff) : "memory"); }
#define WAIT_V(n) asm volatile("s_waitcnt vmcnt(" #n ")" ::: "memory")
#define WAIT_VN(n) asm volatile("s_waitcnt vmcnt(%0)" :: "i"(n) : "memory")
#define WAIT_L0() do { __builtin_amdgcn_s_waitcnt(0xC07F); asm volatile("" ::: "memory"); } while (0)
#define SCHED() __builtin_amdgcn_sched_barrier(0)
#define RBAR() do { __builtin_amdgcn_s_barrier(); asm volatile("" ::: "memory"); } while (0)
#define BAR() do { SCHED(); RBAR(); SCHED(); } while (0)
__device__ __forceinline__ int fresh_lane() { int l; asm volatile("v_mbcnt_lo_u32_b32 %0, -1, 0\n\tv_mbcnt_hi_u32_b32 %0, -1, %0" : "=v"(l)); return l; }
typedef LAS char* lptr;
__device__ __forceinline__ lptr mk_l(unsigned a) { lptr p = (lptr)(size_t)a; asm volatile("" : "+v"(p)); return p; }
__device__ __forceinline__ bf16x8 lds_b128(lptr a) { return *(const LAS bf16x8*)a; }
__device__ __forceinline__ bf16x8 lds_tr2(lptr a0, lptr a1) {
  s16x4 lo = __builtin_amdgcn_ds_read_tr16_b64_v4i16((LAS s16x4*)a0);
  s16x4 hi = __builtin_amdgcn_ds_read_tr16_b64_v4i16((LAS s16x4*)a1);
  return __builtin_shufflevector(lo, hi, 0, 1, 2, 3, 4, 5, 6, 7); }
#define MFMA(a, b, c) asm volatile("v_mfma_f32_16x16x32_bf16 %0, %1, %2, %0" : "+v"(c) : "v"(a), "v"(b))
#define MFMA0(a, b, c) asm volatile("v_mfma_f32_16x16x32_bf16 %0, %1, %2, 0" : "=&v"(c) : "v"(a), "v"(b))
#define PIN(x) asm volatile("" : "+v"(x))
#define MFMA_DRAIN() do { asm volatile("s_nop 7\n\ts_nop 7" ::: "memory"); SCHED(); } while (0)
#define ZERO4 ((f32x4){0.f, 0.f, 0.f, 0.f})

#define ST_DECL
#define STAMP()
#define TS2()
#define ST_DUMP(ws, wid)

struct Args {
  const u16 *x, *lre, *lim, *bre, *bim, *cre, *cim, *ldt, *dsk, *wg, *wu, *wd, *gw;
  char* ws; float* out;
};

constexpr unsigned L_X = 0, L_W = 65536, L_BS = 131072, L_PRM = 151552  ,
                   L_WA = 153600  , L_FLG = 154624  , L_SINK = 154880, L_TAB = 155136  , LDS_BYTES = 163840;

__device__ __forceinline__ int tj(int j) { return (j & 1) * 8 + ((j >> 1) & 1); }
__device__ __forceinline__ f32x2 cmul(f32x2 a, f32x2 b) { return (f32x2){a.x * b.x - a.y * b.y, a.x * b.y + a.y * b.x}; }
__device__ __forceinline__ f32x2 cfma(f32x2 a, f32x2 b, f32x2 c) {
  return (f32x2){__builtin_fmaf(a.x, b.x, __builtin_fmaf(-a.y, b.y, c.x)), __builtin_fmaf(a.x, b.y, __builtin_fmaf(a.y, b.x, c.y))}; }
__device__ __forceinline__ void sw16(float v, float& r0, float& r1) { auto t = __builtin_amdgcn_permlane16_swap(__float_as_uint(v), __float_as_uint(v), false, false); r0 = __uint_as_float(t[0]); r1 = __uint_as_float(t[1]); }
__device__ __forceinline__ void sw32(float v, float& r0, float& r1) { auto t = __builtin_amdgcn_permlane32_swap(__float_as_uint(v), __float_as_uint(v), false, false); r0 = __uint_as_float(t[0]); r1 = __uint_as_float(t[1]); }

__device__ __forceinline__ void touch(const char* p) {
  asm volatile("s_mov_b32 m0, %0\n\ts_nop 0\n\tglobal_load_lds_dword %1, off" :: "s"(L_SINK), "v"(p) : "memory"); }
template <int GRP>
struct P1 {
  static constexpr int NW = GRP == 0 ? 4 : 3, NS = GRP == 0 ? 0 : 4;
  f32x4 ag[NW][4], au[NW][4], as[4][4];
  bf16x8 xa[4], wgf[NW], wuf[NW], bsf[4];
  lptr aX[2], aB[2], aW[NW];
  unsigned voffX, voffW, voffB;
  unsigned sX, sW, sB, ldsX, ldsW, ldsB;
  rsrc_t rx, rwg, rwu, rb;
  const char* pfp; unsigned pfs; int pfo, pfm;
  __device__ __forceinline__ void pull(int T) { const int i = T + pfo; touch(pfp + pfs * (unsigned)(i < 0 ? 0 : i > pfm ? pfm : i)); }

  template <int BUF, int KS, class M> __device__ __forceinline__ void load(M&& mem) {
    mem(0);
#pragma unroll
    for (int m = 0; m < 4; ++m) xa[m] = lds_b128(aX[KS] + BUF * 32768 + m * 2048);
    mem(1);
#pragma unroll
    for (int i = 0; i < NS; ++i) bsf[i] = lds_b128(aB[KS] + BUF * 8192 + i * 2048);
    if (NS) mem(2);
#pragma unroll
    for (int n = 0; n < NW; ++n) {
      wgf[n] = lds_tr2(aW[n] + BUF * 32768 + KS * 8192, aW[n] + BUF * 32768 + KS * 8192 + 1024);
      wuf[n] = lds_tr2(aW[n] + BUF * 32768 + 16384 + KS * 8192, aW[n] + BUF * 32768 + 16384 + KS * 8192 + 1024);
      mem(n + (NS ? 3 : 2));
    }
  }
  template <int BUF, int KS> __device__ __forceinline__ void load() { load<BUF, KS>([](int) {}); }
  template <int BUF> __device__ __forceinline__ void dmaX1(int T, int s) { dma16(ldsX + BUF * 32768 + s * 2048, voffX, rx, sX + T * 128 + s * 4096); }
  template <int BUF, int HH> __device__ __forceinline__ void dmaW1(int T, int i) {
    dma16(ldsW + BUF * 32768 + (i & 1) * 16384 + HH * 8192 + (i >> 1) * 1024, voffW, (i & 1) ? rwu : rwg, sW + (unsigned)(T * 64 + HH * 32 + (i >> 1) * 4) * 7168u);
  }
  template <int BUF> __device__ __forceinline__ void dmaB1(int T, int pf) { dma16(ldsB + BUF * 8192 + pf * 4096, voffB, rb, sB + pf * 32768 + T * 128); }
  template <int NV, bool FIRST = false> __device__ __forceinline__ void compute() {
    constexpr int NM = 4 * NS + 8 * NW;
    __builtin_amdgcn_s_setprio(1);
#pragma unroll
    for (int k = 0; k < NM; ++k) {
      if (k == NM - EB && NV != -2) { __builtin_amdgcn_s_setprio(2); if (NV >= 0) WAIT_VN(NV < 0 ? 0 : NV); RBAR(); }
      const int f = k >> 2, m = (f & 1) ? 3 - (k & 3) : (k & 3);
      const int n = f < NS ? 0 : (f - NS) >> 1;
      const bf16x8& a = f < NS ? xa[m] : ((f - NS) & 1) ? wuf[n] : wgf[n];
      const bf16x8& b = f < NS ? bsf[f & 3] : xa[m];
      f32x4& c = f < NS ? as[f & 3][m] : ((f - NS) & 1) ? au[n][m] : ag[n][m];
      if (FIRST) MFMA0(a, b, c); else MFMA(a, b, c);
    }
    if (EB == 0 && NV != -2) { if (NV >= 0) WAIT_VN(NV < 0 ? 0 : NV); RBAR(); }
    __builtin_amdgcn_s_setprio(0);
    SCHED();
  }
  template <int BUF> __device__ __forceinline__ void dmaX(int T) {
#pragma unroll
    for (int s = 0; s < 4; ++s) dma16(ldsX + BUF * 32768 + s * 2048, voffX, rx, sX + T * 128 + s * 4096);
  }
  template <int BUF, int HH> __device__ __forceinline__ void dmaW(int T) {
#pragma unroll
    for (int s = 0; s < 2; ++s) {
      const unsigned so = sW + (unsigned)(T * 64 + HH * 32 + s * 4) * 7168u;
      dma16(ldsW + BUF * 32768 + HH * 8192 + s * 1024, voffW, rwg, so);
      dma16(ldsW + BUF * 32768 + 16384 + HH * 8192 + s * 1024, voffW, rwu, so);
    }
  }
  template <int BUF> __device__ __forceinline__ void dmaB(int T) {
#pragma unroll
    for (int pf = 0; pf < 2; ++pf) dma16(ldsB + BUF * 8192 + pf * 4096, voffB, rb, sB + pf * 32768 + T * 128);
  }
};

template <int BUF> __device__ __forceinline__ void p1_tile(P1<0>& w, int T) {
  w.template compute<7>();
  w.template load<BUF, 1>([&](int i) { if (i < 4) w.template dmaW1<BUF ^ 1, 1>(T + 1, i); else if (i == 4) w.pull(T); }); WAIT_L0(); BAR();
  w.template compute<5>();
  w.template load<BUF ^ 1, 0>([&](int i) { if (i < 4) w.template dmaX1<BUF>(T + 2, i); else w.template dmaB1<BUF>(T + 2, i - 4); }); WAIT_L0(); BAR();
}
template <int BUF> __device__ __forceinline__ void p1_tile(P1<1>& w, int T) {
  w.template load<BUF, 0>([&](int i) { if (i >= SO && i < SO + 4) w.template dmaX1<BUF ^ 1>(T + 1, i - SO); }); WAIT_L0(); BAR();
  w.template compute<-1>();
  w.template load<BUF, 1>([&](int i) { if (i >= SO && i < SO + 4) w.template dmaW1<BUF, 0>(T + 2, i - SO); }); WAIT_V(4); WAIT_L0(); BAR();
  w.template compute<-1>();
}

constexpr int NSTEP = 22;
struct P2 {
  f32x4 af[4][4], as[4][4];
  bf16x8 ha[2][4], bb[2][4];
  lptr aH, aW[4], aC[2];
  unsigned voffH, voffW, voffC;
  unsigned sH, sHs, sW, sC, ldsA, ldsB, kk, pj[4];
  rsrc_t rh, rw, rc;
  template <int ST> __device__ __forceinline__ unsigned kf() const {
    constexpr int e = 2 * (ST - 8), k0 = ST < 8 ? 7 * (ST >> 1) + 3 * (ST & 1) + 1 : 7 * (e / 3) + 3 * (e % 3), k1 = ST < 8 ? k0 + 1 : 7 * ((e + 1) / 3) + 3 * ((e + 1) % 3);
    return k0 + kk * (k1 - k0);
  }
  template <int ST, int GA> __device__ __forceinline__ void dmaA(int j) {
    const unsigned so = ST < 14 ? sH + kf<ST>() * 8192u : sHs + (ST - 14) * 16384u;
    dma16_dev(ldsA + GA * 49152 + (ST % 3) * 16384 + j * 1024, voffH, rh, so + GA * (NKF * 8192u) + pj[j]);
  }
  template <int ST> __device__ __forceinline__ void dmaB(int s) {
    const unsigned l = ldsB + (ST % 3) * 16384 + s * 2048;
    if (ST >= 14) dma16(l, voffC, rc, sC + (ST - 14) * 128u + s * 32768u);
    else dma16(l, voffW, rw, sW + kf<ST>() * 65536u + s * 16384u);
  }
  template <int ST, int GA> __device__ __forceinline__ void stageA() {
#pragma unroll
    for (int j = 0; j < 4; ++j) dmaA<ST, GA>(j);
  }
  template <int ST> __device__ __forceinline__ void stageB() {
#pragma unroll
    for (int s = 0; s < 4; ++s) dmaB<ST>(s);
  }
  template <int ST, int G> __device__ __forceinline__ void load() {
    constexpr int SL = (ST % 3) * 16384;
#pragma unroll
    for (int k2 = 0; k2 < 2; ++k2) {
#pragma unroll
      for (int n = 0; n < 4; ++n) {
        const int i = k2 * 4 + n;
        if constexpr (G == 0) { if constexpr (ST + 2 < NSTEP) { if (i < 4) dmaB<ST + 2>(i); else if (i < 6) dmaA<ST + 2, 1>(i - 4); } }
        else { if constexpr (ST + 2 < NSTEP) { if (i < 2) dmaA<ST + 2, 1>(i + 2); } if constexpr (ST + 3 < NSTEP) { if (i >= 2 && i < 6) dmaA<ST + 3, 0>(i - 2); } }
        if (ST >= 14) bb[k2][n] = lds_b128(aC[k2] + SL + n * 2048);
        else bb[k2][n] = lds_tr2(aW[n] + SL + k2 * 8192, aW[n] + SL + k2 * 8192 + 4096);
        ha[k2][n] = lds_b128(aH + SL + k2 * 8192 + n * 1024);
      }
    }
  }
  template <int ST, int NV> __device__ __forceinline__ void compute() {
    __builtin_amdgcn_s_setprio(1);
#pragma unroll
    for (int k = 0; k < 32; ++k) {
      if (k == 32 - EB) { __builtin_amdgcn_s_setprio(2); WAIT_VN(NV); RBAR(); }
      const int k2 = k >> 4, n = (k >> 2) & 3, m = (n & 1) ? 3 - (k & 3) : (k & 3);
      f32x4& c = ST >= 14 ? as[m][n] : af[m][n];
      if ((ST == 0 || ST == 14) && k2 == 0) MFMA0(bb[k2][n], ha[k2][m], c); else MFMA(bb[k2][n], ha[k2][m], c);
    }
    if (EB == 0) { WAIT_VN(NV); RBAR(); }
    __builtin_amdgcn_s_setprio(0);
    SCHED();
  }
};
template <int I, int N, class F> __device__ __forceinline__ void static_for(F&& f) {
  if constexpr (I < N) { f(std::integral_constant<int, I>{}); static_for<I + 1, N>(f); }
}

__device__ __forceinline__ void arrive(char* ws, int lane, unsigned flag_off, int ctr, unsigned epoch) {
  if (lane == 0) {
    const unsigned old = __hip_atomic_fetch_add((LAS unsigned*)mk_l(L_FLG + 16 + ctr * 4), 1u, __ATOMIC_RELAXED, __HIP_MEMORY_SCOPE_WORKGROUP);
    if (old == 3u) __hip_atomic_store((GAS unsigned*)(ws + flag_off), epoch, RLX_AGENT);
  }
}
__device__ __forceinline__ void wait_flags(const char* ws, unsigned off, unsigned epoch) {
_Pragma("nounroll")
  for (unsigned spins = 0; spins < SPIN_CAP; ++spins) {
    if (__all(__hip_atomic_load((const GAS unsigned*)(ws + off), RLX_AGENT) == epoch)) break;
    __builtin_amdgcn_s_sleep(1);
  }
  asm volatile("" ::: "memory");
}

struct Pre { unsigned voffX, voffW, voffB, sX, ldsX, sW, ldsW, sB, ldsB; };
__device__ __forceinline__ Pre mk_pre(int lane, int wid, int bid) {
  Pre pre;
  const int wr = wid & 3, G = wid >> 2, idx = bid >> 3, c = (bid & 7) * 4 + (idx >> 3), rch = idx & 7;
  const unsigned sw8 = ((lane & 7) ^ (((wr & 1) * 4 + (lane >> 4)) & 7)) * 16;
  pre.voffX = ((lane >> 5) * 16 + tj(lane >> 3)) * 2048 + sw8;
  pre.voffB = (lane >> 3) * 2048 + sw8;
  pre.voffW = (lane >> 4) * 7168 + ((((lane & 15) >> 1) ^ ((lane >> 4) | ((wr & 1) << 2))) * 32) + (lane & 1) * 16;
  pre.sX = (unsigned)(rch * 256 + (G * 2 + (wr >> 1)) * 64 + (wr & 1) * 32) * 2048u;
  pre.ldsX = L_X + (G * 16 + (wr >> 1) * 8 + (wr & 1)) * 1024;
  const int ii = (wr & 1) * 2 + (wr >> 1) * 4;
  pre.sW = (unsigned)(ii * 4) * 7168u + (unsigned)((224 * c) & ~63);
  pre.ldsW = L_W + ii * 1024;
  pre.sB = (unsigned)(c * 32 + (wr & 1) * 8) * 2048u;
  pre.ldsB = L_BS + wr * 1024;
  return pre;
}
template <int GRP>
__device__ __forceinline__ void body(const Args& A, int wid, const char* code, const unsigned code_len, const u64 t00) {
  asm volatile("" : "+s"(wid));
  ST_DECL STAMP();
  const int xcd = blockIdx.x & 7, idx = blockIdx.x >> 3, c = xcd * 4 + (idx >> 3), rch = idx & 7;
  const int kq = xcd >> 1, tt = 2 * rch + GRP, dt = (xcd & 1) * 4 + (idx >> 3), tile = rch * 8 + dt;
  const unsigned flb = (unsigned)WS_FL + rch * 1024;
  const int wr = wid & 3, wm = wr >> 1, wn = wr & 1;
  int lane = fresh_lane();
  unsigned epoch;
  P2 q;
  u32x4 hq[14]; unsigned ho[3];
  const rsrc_t rws = mk_rsrc(A.ws, (unsigned)WS_END);
  {
  P1<GRP> w;
  constexpr int NW = P1<GRP>::NW, NS = P1<GRP>::NS;
  int r = lane & 15, g = lane >> 4, qq = (lane & 15) >> 2, pp = lane & 3;
  const Pre pre = mk_pre(lane, wid, blockIdx.x);
  w.rx = mk_rsrc(A.x, L_ * D_ * 2); w.rwg = mk_rsrc(A.wg, D_ * F_ * 2); w.rwu = mk_rsrc(A.wu, D_ * F_ * 2);
  w.rb = mk_rsrc((wr >> 1) ? A.bim : A.bre, P_ * D_ * 2);
  w.voffX = pre.voffX; w.voffB = pre.voffB; w.voffW = pre.voffW;
  w.sX = pre.sX; w.ldsX = pre.ldsX; w.sW = pre.sW; w.ldsW = pre.ldsW; w.sB = pre.sB; w.ldsB = pre.ldsB;
  const char* wbase = (const char*)((wr & 1) ? A.wu : A.wg) + (blockIdx.x * 14 + (wr >> 1) * 7) * 128;
  const char* rbase = wr < 2 ? (const char*)A.x + (blockIdx.x * 8 + wr * 4) * 2048 : (const char*)(wr == 2 ? A.bre : A.bim) + blockIdx.x * 4 * 2048;
  if (GRP == 1) {
    w.template dmaW<0, 1>(0); w.template dmaW<1, 0>(1);
    const unsigned co = ((idx * 4 + wr) * 4 + (lane & 3)) * 128u;
    touch(lane < 16 ? wbase + (2 + (lane >> 3)) * 458752u + ((lane & 7) < 7 ? (lane & 7) : 0) * 128 : lane < 24 ? rbase + ((lane - 16) >> 1) * 2048 + (2 + (lane & 1)) * 128
                    : code + (co < code_len ? co : 0u));
  }
  {
#pragma unroll
    for (int ks = 0; ks < 2; ++ks) {
      const unsigned ch = ((g + 4 * ks) ^ ((r >> 1) & 7)) << 4;
      w.aX[ks] = mk_l(L_X + (wr * 64 + r) * 128 + ch);
      if (NS) w.aB[ks] = mk_l(L_BS + r * 128 + ch);
    }
    const unsigned swz = qq | ((g & 1) << 2);
#pragma unroll
    for (int n = 0; n < NW; ++n) {
      unsigned col;
      if (GRP == 0) col = (((2 + n) ^ swz) << 5) + pp * 8;
      else if (n < 2) col = (((((c & 1) ? 6 : 0) + n) ^ swz) << 5) + pp * 8;
      else col = (((((c & 1) ? 0 : 6) + (pp >> 1)) ^ swz) << 5) + (c & 1) * 16 + (pp & 1) * 8;
      w.aW[n] = mk_l(L_W + (8 * g + qq) * 256 + col);
    }
  }
  if constexpr (GRP == 0) {
    WAIT_V(0); BAR();
    w.template load<0, 0>([&](int i) { if (i < 4) w.template dmaX1<1>(1, i); else w.template dmaB1<1>(1, i - 4); });
    unsigned ep0, plr, pli, pld;
    {
      const unsigned po = (c * 32 + (lane & 31)) * 2;
      asm volatile("global_load_dword %0, %1, %2 sc1" : "=v"(ep0) : "v"((unsigned)WS_EP), "s"(A.ws) : "memory");
      asm volatile("global_load_ushort %0, %1, %2" : "=v"(plr) : "v"(po), "s"(A.lre) : "memory");
      asm volatile("global_load_ushort %0, %1, %2" : "=v"(pli) : "v"(po), "s"(A.lim) : "memory");
      asm volatile("global_load_ushort %0, %1, %2" : "=v"(pld) : "v"(po), "s"(A.ldt) : "memory");
    }
    WAIT_L0(); BAR();
    STAMP(); TS2();
    w.template compute<-1, true>();
    w.template load<0, 1>([&](int i) { if (i < 4) w.template dmaW1<1, 1>(1, i); });
    {
      const unsigned gw = blockIdx.x * 4 + wr, co = ((idx * 4 + wr) * 4 + (lane & 3)) * 128u;
      const int l = lane < 28 ? lane : 27;
      if (l < 7) { w.pfp = wbase + l * 128; w.pfs = 458752u; w.pfo = PFD; w.pfm = 15; }
      else if (l < 15) { w.pfp = rbase + ((l - 7) >> 1) * 2048 + ((l - 7) & 1) * 128; w.pfs = 256u; w.pfo = 2; w.pfm = 7; }
      else if (l < 19) { w.pfp = (const char*)A.wd + gw * 512u + (l - 15) * 128; w.pfs = 524288u; w.pfo = 0; w.pfm = 13; }
      else if (l < 23) { w.pfp = (const char*)(l < 21 ? A.cre : A.cim) + gw * 256u + ((l - 19) & 1) * 128; w.pfs = 262144u; w.pfo = -6; w.pfm = 7; }
      else { w.pfp = l < 27 ? code + (co < code_len ? co : 0u) : (const char*)((idx & 4) ? A.dsk : A.gw) + ((idx & 3) * 4 + wr) * 128; w.pfs = 0u; w.pfo = 0; w.pfm = 0; }
      w.pull(0);
    }
    WAIT_L0(); BAR();
    w.template compute<5>();
    w.template load<1, 0>([&](int i) { if (i < 4) w.template dmaX1<0>(2, i); else w.template dmaB1<0>(2, i - 4); });
    PIN(ep0); PIN(plr); PIN(pli); PIN(pld);
    epoch = __builtin_amdgcn_readfirstlane(ep0) + 1u;
    f32x2 lam, l8;
    {
      const float lr = bf2f(plr), li = bf2f(pli);
      const float dt_ = bf2f(pk_bf16(__expf(bf2f(pld)), 0.f) & 0xffffu);
      const float e = __expf(dt_ * lr), rev = __builtin_amdgcn_fractf(dt_ * li * 0.15915494309189535f);
      lam = (f32x2){e * __builtin_amdgcn_cosf(rev), -e * __builtin_amdgcn_sinf(rev)};
      const float nr = lam.x - 1.f, rden = 1.f / (lr * lr + li * li);
      if (wr == 0) *(LAS f32x4*)mk_l(L_PRM + (lane & 31) * 64) = (f32x4){lam.x, lam.y, (nr * lr - lam.y * li) * rden, (-lam.y * lr - nr * li) * rden};
      else if (wr == 1) *(LAS unsigned*)mk_l(L_FLG + (lane & 7) * 4) = (lane & 7) == 3 ? epoch : 0u;
      l8 = cmul(lam, lam); l8 = cmul(l8, l8); l8 = cmul(l8, l8); PIN(l8);
    }
    WAIT_L0(); BAR(); TS2();
    w.template compute<7>();
    if (wr == 2) {
      __hip_atomic_store((GAS u64*)(A.ws + WS_AG) + (c * 8 + rch) * 64 + lane, (u64)(epoch - 1u) << 32, RLX_AGENT);
      if (lane < 6) __hip_atomic_store((GAS unsigned*)(A.ws + flb + lane * 128 + c * 4), epoch - 1u, RLX_AGENT);
      else if (lane >= 32 && (lane & 3)) __hip_atomic_store((GAS unsigned*)(A.ws + WS_XF + (tile * 4 + (kq ^ (lane & 3))) * 128) + (lane - 32), epoch - 1u, RLX_AGENT);
    }
    w.template load<1, 1>([&](int i) { if (i < 4) w.template dmaW1<0, 1>(2, i); else if (i == 4) w.pull(1); });
    {
      const f32x2 l16 = cmul(l8, l8), l32 = cmul(l16, l16), l64 = cmul(l32, l32), l128 = cmul(l64, l64), l256 = cmul(l128, l128);
      if (wr == 0) {
        const lptr pb = mk_l(L_PRM + (lane & 31) * 64);
        *(LAS f32x4*)(pb + 16) = (f32x4){l16.x, l16.y, l32.x, l32.y};
        *(LAS f32x4*)(pb + 32) = (f32x4){l8.x, l8.y, l64.x, l64.y};
        *(LAS f32x2*)(pb + 48) = l256;
      }
      f32x2 pw = (wr & 1) ? l64 : (f32x2){1.f, 0.f}; if (wr & 2) pw = cmul(pw, l128);
      const f32x2 q0 = (lane & 32) ? l32 : (f32x2){1.f, 0.f}, q1 = cmul(q0, l16), c0 = cmul(q0, pw), c1 = cmul(q1, pw);
      const lptr tp = mk_l(L_TAB + wr * 2048 + (lane >> 5) * 1024 + (lane & 31) * 16);
      *(LAS f32x4*)tp = (f32x4){q0.x, q0.y, c0.x, c0.y}; *(LAS f32x4*)(tp + 512) = (f32x4){q1.x, q1.y, c1.x, c1.y};
    }
    WAIT_L0(); BAR();
    w.template compute<5>();
    w.template load<0, 0>([&](int i) { if (i < 4) w.template dmaX1<1>(3, i); else w.template dmaB1<1>(3, i - 4); }); WAIT_L0(); BAR();
  } else {
    WAIT_V(9); BAR(); BAR();
    STAMP(); TS2();
    w.template load<0, 0>([&](int i) { if (i >= SO && i < SO + 4) w.template dmaX1<1>(1, i - SO); }); WAIT_V(9); WAIT_L0(); BAR();
    w.template compute<-1, true>();
    w.template load<0, 1>([&](int i) { if (i >= SO && i < SO + 4) w.template dmaW1<0, 0>(2, i - SO); }); WAIT_V(4); WAIT_L0(); BAR();
    w.template compute<-1>(); TS2();
    p1_tile<1>(w, 1);
  }
  for (int T = 2; T < 14; T += 2) {
    TS2(); p1_tile<0>(w, T); TS2();
    p1_tile<1>(w, T + 1);
  }
  if (GRP == 1) epoch = __builtin_amdgcn_readfirstlane(*(const LAS unsigned*)mk_l(L_FLG + 12));
  STAMP();
  lane = fresh_lane();
  q.rh = mk_rsrc(A.ws + WS_H, (unsigned)H_BYTES); q.rw = mk_rsrc(A.wd, F_ * D_ * 2); q.rc = mk_rsrc((kq >> 1) ? A.cim : A.cre, D_ * P_ * 2);
  q.voffW = (lane >> 4) * 2048 + ((((lane & 15) >> 1) ^ (((wr & 1) << 2) | (lane >> 4))) * 32) + (lane & 1) * 16;
  {
    const int i0 = (wr & 1) + (wr >> 1) * 8;
    q.kk = wr >> 1;
    q.sH = (unsigned)(2 * rch * NKF + 28 * kq) * 8192u + (wr & 1) * 4096u;
    q.sHs = (unsigned)(2 * rch * NKF + 112 + 16 * kq + (wr >> 1)) * 8192u + (wr & 1) * 4096u;
    q.sW = (unsigned)(28 * kq * 32 + (wr & 1) * 4) * 2048u + dt * 256u;
    q.sC = (unsigned)(dt * 128 + i0 * 8) * 2048u + (kq & 1) * 1024u;
#pragma unroll
    for (int j = 0; j < 4; ++j) q.pj[j] = (unsigned)(j ^ kq) << 10;
    q.ldsA = 49152 + wr * 4096; q.ldsB = i0 * 1024;
  }
  u32x4 gx[4], gg[4];
  const int tg = rch * 256 + c * 8 + wr * 2 + (lane >> 5);
  if constexpr (GRP == 0) {
    w.template compute<7>();
    w.template load<0, 1>(); w.template dmaW<1, 1>(15);
#pragma unroll
    for (int i = 0; i < 4; ++i) gx[i] = *(const u32x4*)(A.x + tg * D_ + i * 256 + (lane & 31) * 8);
    WAIT_L0(); BAR();
    w.template compute<8>();
    w.template load<1, 0>(); q.template stageB<0>();
#pragma unroll
    for (int i = 0; i < 4; ++i) gg[i] = *(const u32x4*)(A.gw + i * 256 + (lane & 31) * 8);
    WAIT_L0(); BAR();
    w.template compute<12>();
    w.template load<1, 1>();
    {
      typedef __bf16 bf2_t __attribute__((ext_vector_type(2)));
      float s0 = 0.f, s1 = 0.f;
#pragma unroll
      for (int i = 0; i < 4; ++i)
#pragma unroll
        for (int j = 0; j < 4; j += 2) {
          const unsigned a0 = gx[i][j], b0 = gg[i][j], a1 = gx[i][j + 1], b1 = gg[i][j + 1];
          s0 = __builtin_amdgcn_fdot2_f32_bf16(__builtin_bit_cast(bf2_t, a0), __builtin_bit_cast(bf2_t, b0), s0, false);
          s1 = __builtin_amdgcn_fdot2_f32_bf16(__builtin_bit_cast(bf2_t, a1), __builtin_bit_cast(bf2_t, b1), s1, false);
        }
      float s = s0 + s1;
      s += dpp<0x128, 0xf>(0.f, s); s += dpp<0x124, 0xf>(0.f, s); s += dpp<0x122, 0xf>(0.f, s); s += dpp<0x121, 0xf>(0.f, s);
      float t0, t1; sw16(s, t0, t1); s = t0 + t1;
      if ((lane & 31) == 0) __hip_atomic_store((GAS unsigned*)(A.ws + WS_G) + tg, __float_as_uint(1.f / (1.f + __expf(-s))), RLX_AGENT);
    }
    WAIT_L0(); BAR();
    w.template compute<-2>();
    __builtin_amdgcn_s_setprio(3);
  } else {
    w.template load<0, 0>(); w.template dmaX<1>(15); WAIT_L0(); BAR();
    w.template compute<-1>();
    w.template load<0, 1>(); WAIT_V(0); WAIT_L0(); BAR();
    w.template compute<-1>();
  }
  lane = fresh_lane(); r = lane & 15; g = lane >> 4; qq = (lane & 15) >> 2; pp = lane & 3;
  const unsigned hb = (unsigned)WS_H + (unsigned)(2 * rch + (wr >> 1)) * NKF * 8192u + (wr & 1) * 4096u;
  const int kf0 = 7 * (c >> 1) + 3 * (c & 1);
  q.voffH = lane * 16;
#define FFN_H(n, m, h) pk_bf16(relu(w.ag[n][m][2 * (h)]) * w.au[n][m][2 * (h)], relu(w.ag[n][m][2 * (h) + 1]) * w.au[n][m][2 * (h) + 1])
#define PACK_S() do { _Pragma("unroll") for (int m = 0; m < 4; ++m) { hq[m] = (u32x4){FFN_H(0, m, 0), FFN_H(0, m, 1), FFN_H(1, m, 0), FFN_H(1, m, 1)}; PIN(hq[m]); } \
    _Pragma("unroll") for (int m = 0; m < 4; m += 2) {     \
      const auto s0 = __builtin_amdgcn_permlane32_swap(FFN_H(2, m, 0), FFN_H(2, m + 1, 0), false, false); \
      const auto s1 = __builtin_amdgcn_permlane32_swap(FFN_H(2, m, 1), FFN_H(2, m + 1, 1), false, false); \
      hq[4 + m / 2] = (u32x4){s0[0], s1[0], s0[1], s1[1]}; PIN(hq[4 + m / 2]); } \
    ho[0] = lane * 16 + hb + (kf0 + ((c & 1) ? 3 : 0)) * 8192u; \
    ho[1] = (lane & 31) * 16 + (lane >> 5) * 1024 + (c & 1) * 512 + hb + (kf0 + ((c & 1) ? 0 : 3)) * 8192u; \
    ho[2] = lane * 16 + hb + (112 + c) * 8192u; } while (0)
  if constexpr (GRP == 0) {
    MFMA_DRAIN();
    STAMP();
#pragma unroll
    for (int f = 0; f < 2; ++f)
#pragma unroll
      for (int m = 0; m < 4; ++m) {
        __builtin_amdgcn_raw_buffer_store_b128((u32x4){FFN_H(2 * f, m, 0), FFN_H(2 * f, m, 1), FFN_H(2 * f + 1, m, 0), FFN_H(2 * f + 1, m, 1)}, rws,
                                               lane * 16 + m * 1024 + hb + (kf0 + 1 + f) * 8192u, 0, 16);
        SCHED();
      }
    STAMP();
    WAIT_V(0); arrive(A.ws, lane, flb + (c >> 3) * 128 + c * 4, 0, epoch);
    __builtin_amdgcn_s_setprio(0);
    BAR();
    STAMP();
    wait_flags(A.ws, flb + kq * 160 + (lane & 7) * 4, epoch);
    STAMP();
    q.template stageA<0, 0>(); q.template stageA<0, 1>(); q.template stageA<1, 0>();
    STAMP();
  } else {
    bf16x8 xb[4], bs1[4], wg1[3], wu1[3];
#pragma unroll
    for (int m = 0; m < 4; ++m) { w.xa[m] = lds_b128(w.aX[0] + 32768 + m * 2048); xb[m] = lds_b128(w.aX[1] + 32768 + m * 2048); }
#pragma unroll
    for (int i = 0; i < 4; ++i) { w.bsf[i] = lds_b128(w.aB[0] + 8192 + i * 2048); bs1[i] = lds_b128(w.aB[1] + 8192 + i * 2048); }
    w.wgf[0] = lds_tr2(w.aW[0] + 32768, w.aW[0] + 32768 + 1024); w.wuf[0] = lds_tr2(w.aW[0] + 49152, w.aW[0] + 49152 + 1024);
    q.template stageB<1>(); WAIT_L0(); BAR();
    __builtin_amdgcn_s_setprio(1);
#pragma unroll
    for (int i = 0; i < 4; ++i)
#pragma unroll
      for (int m = 0; m < 4; ++m) MFMA(w.xa[m], w.bsf[i], w.as[i][m]);
#pragma unroll
    for (int i = 0; i < 4; ++i)
#pragma unroll
      for (int m = 0; m < 4; ++m) MFMA(xb[m], bs1[i], w.as[i][m]);
#pragma unroll
    for (int m = 0; m < 4; ++m) MFMA(w.wgf[0], w.xa[m], w.ag[0][m]);
#pragma unroll
    for (int m = 0; m < 4; ++m) MFMA(w.wuf[0], w.xa[m], w.au[0][m]);
    __builtin_amdgcn_s_setprio(0);
    BAR();
    STAMP();
    f32x4 pl[2], pm[2];
    f32x2 l8[2], cin[2], ea[2], agg[2];
#pragma unroll
    for (int pf = 0; pf < 2; ++pf) {
      const lptr pb = mk_l(L_PRM + (pf * 16 + r) * 64);
      pl[pf] = *(const LAS f32x4*)pb; pm[pf] = *(const LAS f32x4*)(pb + 16); l8[pf] = *(const LAS f32x2*)(pb + 32);
    }
#pragma unroll
    for (int n = 1; n < 3; ++n) { w.wgf[n] = lds_tr2(w.aW[n] + 32768, w.aW[n] + 32768 + 1024); w.wuf[n] = lds_tr2(w.aW[n] + 49152, w.aW[n] + 49152 + 1024); }
    SCHED();
#define PAIR(v, k) ((k) & 1 ? (f32x2){(v)[(k) >> 1][2], (v)[(k) >> 1][3]} : (f32x2){(v)[(k) >> 1][0], (v)[(k) >> 1][1]})
#define CSTEP(P, Q, pf, k) do { const f32x2 lx_ = {pl[pf][0], pl[pf][0]}, ly_ = {pl[pf][1], pl[pf][1]}; \
      const f32x2 a_ = __builtin_elementwise_fma(-ly_, Q, PAIR(w.as[2 * (pf)], k)), b_ = __builtin_elementwise_fma(ly_, P, PAIR(w.as[2 * (pf) + 1], k)); \
      P = __builtin_elementwise_fma(lx_, P, a_); Q = __builtin_elementwise_fma(lx_, Q, b_); } while (0)
    {
#pragma unroll
      for (int pf = 0; pf < 2; ++pf)
#pragma unroll
        for (int m = 0; m < 4; ++m) {
          const f32x4 re = w.as[2 * pf][m], im = w.as[2 * pf + 1][m];
          w.as[2 * pf][m] = re * pl[pf][2] - im * pl[pf][3]; w.as[2 * pf + 1][m] = -(re * pl[pf][3]) - im * pl[pf][2];
          PIN(w.as[2 * pf][m]); PIN(w.as[2 * pf + 1][m]);
        }
      f32x2 P[2], Q[2];
#pragma unroll
      for (int k = 0; k < 8; ++k)
#pragma unroll
        for (int pf = 0; pf < 2; ++pf) {
          if (k == 0) { P[pf] = PAIR(w.as[2 * pf], 0); Q[pf] = PAIR(w.as[2 * pf + 1], 0); } else CSTEP(P[pf], Q[pf], pf, k);
        }
#pragma unroll
      for (int pf = 0; pf < 2; ++pf) {
        ea[pf] = (f32x2){P[pf].x, Q[pf].x};
        const f32x2 e = cfma(l8[pf], ea[pf], (f32x2){P[pf].y, Q[pf].y});
        const f32x2 l16 = {pm[pf][0], pm[pf][1]}, l32 = {pm[pf][2], pm[pf][3]};
        float a0, a1, b0, b1; sw16(e.x, a0, a1); sw16(e.y, b0, b1);
        const f32x2 r0 = {a0, b0};
        const f32x2 xx = cfma(l16, r0, (f32x2){a1, b1});
        float c0, c1, d0, d1; sw32(xx.x, c0, c1); sw32(xx.y, d0, d1);
        const f32x2 s0 = {c0, d0};
        agg[pf] = cfma(l32, s0, (f32x2){c1, d1});
        const f32x2 c3 = cfma(l16, s0, r0);
        cin[pf] = g == 0 ? (f32x2){0.f, 0.f} : g == 1 ? r0 : g == 2 ? s0 : c3;
        PIN(ea[pf]);
      }
    }
    f32x2 aggq = (g & 1) ? agg[1] : agg[0]; PIN(aggq); PIN(cin[0]); PIN(cin[1]);
    if (wr < 3) {
      if (g < 2) *(LAS f32x2*)mk_l(L_WA + ((g * 16 + r) * 4 + wr) * 8) = aggq;
      WAIT_L0();
      *(volatile LAS unsigned*)mk_l(L_FLG + wr * 4) = 1u;
    }
    SCHED();
#pragma unroll
    for (int n = 0; n < 3; ++n) { wg1[n] = lds_tr2(w.aW[n] + 40960, w.aW[n] + 40960 + 1024); wu1[n] = lds_tr2(w.aW[n] + 57344, w.aW[n] + 57344 + 1024); }
    if (wr == 3) {
      const lptr fp = mk_l(L_FLG);
_Pragma("nounroll")
      for (unsigned spins = 0; spins < SPIN_CAP; ++spins) {
        const u32x4 f = *(volatile LAS u32x4*)fp;
        if (__builtin_amdgcn_readfirstlane(f[0] & f[1] & f[2])) break;
      }
      asm volatile("" ::: "memory");
      const lptr ap = mk_l(L_WA + ((g & 1) * 16 + r) * 32);
      const f32x4 a01 = *(const LAS f32x4*)ap; const f32x2 a2 = *(const LAS f32x2*)(ap + 16);
      const f32x2 lw = *(const LAS f32x2*)mk_l(L_PRM + ((g & 1) * 16 + r) * 64 + 40);
      const f32x2 t = cfma(lw, cfma(lw, cfma(lw, (f32x2){a01[0], a01[1]}, (f32x2){a01[2], a01[3]}), a2), aggq);
      if (rch < 7) __hip_atomic_store((GAS u64*)(A.ws + WS_AG) + (c * 8 + rch) * 64 + lane, ((u64)epoch << 32) | __float_as_uint((g & 2) ? t.y : t.x), RLX_AGENT);
    }
    WAIT_L0(); SCHED();
    STAMP();
    __builtin_amdgcn_s_setprio(1);
#pragma unroll
    for (int n = 1; n < 3; ++n) {
#pragma unroll
      for (int m = 0; m < 4; ++m) MFMA(w.wgf[n], w.xa[m], w.ag[n][m]);
#pragma unroll
      for (int m = 0; m < 4; ++m) MFMA(w.wuf[n], w.xa[m], w.au[n][m]);
    }
#pragma unroll
    for (int n = 0; n < 3; ++n) {
#pragma unroll
      for (int m = 0; m < 4; ++m) MFMA(wg1[n], xb[m], w.ag[n][m]);
#pragma unroll
      for (int m = 0; m < 4; ++m) MFMA(wu1[n], xb[m], w.au[n][m]);
    }
    BAR();
    __builtin_amdgcn_s_setprio(2);
    lane = fresh_lane(); r = lane & 15; g = lane >> 4; qq = (lane & 15) >> 2; pp = lane & 3;
    q.voffH = lane * 16;
    const GAS u64* agp = (const GAS u64*)(A.ws + WS_AG) + c * 8 * 64 + lane;
    u64 gv_[7];
#pragma unroll
    for (int cc = 0; cc < 7; ++cc) gv_[cc] = __hip_atomic_load(agp + cc * 64, RLX_AGENT);
    STAMP();
    f32x2 car[2], cai[2], cmr[2], cmi[2];
    const f32x2 l256q = *(const LAS f32x2*)mk_l(L_PRM + ((g & 1) * 16 + r) * 64 + 48);
#pragma unroll
    for (int pf = 0; pf < 2; ++pf) {
      const lptr pb = mk_l(L_PRM + (pf * 16 + r) * 64), ap = mk_l(L_WA + (pf * 16 + r) * 32);
      pl[pf] = *(const LAS f32x4*)pb;
      const f32x4 p2 = *(const LAS f32x4*)(pb + 32), tb4 = *(const LAS f32x4*)mk_l(L_TAB + wr * 2048 + g * 512 + (pf * 16 + r) * 16);
      const f32x4 a01 = *(const LAS f32x4*)ap; const f32x2 a2 = *(const LAS f32x2*)(ap + 16);
      const f32x2 l8_ = {p2[0], p2[1]}, l64 = {p2[2], p2[3]};
      f32x2 sw = wr > 0 ? (f32x2){a01[0], a01[1]} : (f32x2){0.f, 0.f};
      if (wr > 1) sw = cfma(l64, sw, (f32x2){a01[2], a01[3]});
      if (wr > 2) sw = cfma(l64, sw, a2);
      const f32x2 ca = cfma((f32x2){tb4[0], tb4[1]}, sw, cin[pf]), cb_ = cfma(l8_, ca, ea[pf]), cm = {tb4[2], tb4[3]}, cn = cmul(l8_, cm);
      car[pf] = (f32x2){ca.x, cb_.x}; cai[pf] = (f32x2){ca.y, cb_.y}; cmr[pf] = (f32x2){cm.x, cn.x}; cmi[pf] = (f32x2){cm.y, cn.y};
      PIN(car[pf]); PIN(cai[pf]); PIN(cmr[pf]); PIN(cmi[pf]);
    }
    PACK_S();
    const unsigned trb = wr == 0 ? 32768u : wr == 1 ? 81920u : 131072u + (wr - 2) * 9216u;
    const lptr tb = mk_l(trb + r * 144 + g * 32), rb = mk_l(trb + (8 * g + qq) * 144 + pp * 32);
    STAMP();
_Pragma("nounroll")
    for (unsigned spins = 0; spins < SPIN_CAP; ++spins) {
      bool ok = true;
#pragma unroll
      for (int cc = 0; cc < 7; ++cc) ok &= (cc >= rch) || (unsigned)(gv_[cc] >> 32) == epoch;
      if (__all(ok)) break;
      __builtin_amdgcn_s_sleep(1);
#pragma unroll
      for (int cc = 0; cc < 7; ++cc) gv_[cc] = __hip_atomic_load(agp + cc * 64, RLX_AGENT);
    }
    STAMP();
    f32x2 cb = {0.f, 0.f};
#pragma unroll
    for (int cc = 0; cc < 7; ++cc) {
      float are, aim; sw32(__uint_as_float((unsigned)gv_[cc]), are, aim);
      if (cc < rch) cb = cfma(l256q, cb, (f32x2){are, aim});
    }
    unsigned hr[2][8], hi[2][8];
    {
      float cx[2], cy[2]; sw16(cb.x, cx[0], cx[1]); sw16(cb.y, cy[0], cy[1]);
      f32x2 P[2], Q[2];
#pragma unroll
      for (int pf = 0; pf < 2; ++pf) { P[pf] = car[pf] + cmr[pf] * cx[pf] - cmi[pf] * cy[pf]; Q[pf] = cai[pf] + cmr[pf] * cy[pf] + cmi[pf] * cx[pf]; }
#pragma unroll
      for (int k = 0; k < 8; ++k)
#pragma unroll
        for (int pf = 0; pf < 2; ++pf) { CSTEP(P[pf], Q[pf], pf, k); hr[pf][k] = pk_bf16(P[pf].x, P[pf].y); hi[pf][k] = pk_bf16(Q[pf].x, Q[pf].y); }
    }
#pragma unroll
    for (int ri = 0; ri < 2; ++ri)
#pragma unroll
      for (int pf = 0; pf < 2; ++pf) {
        const unsigned* h = ri ? hi[pf] : hr[pf];
        *(LAS u32x4*)(tb + ri * 4608 + pf * 2304) = (u32x4){h[0], h[1], h[2], h[3]};
        *(LAS u32x4*)(tb + ri * 4608 + pf * 2304 + 16) = (u32x4){h[4], h[5], h[6], h[7]};
      }
    WAIT_L0();
#pragma unroll
    for (int i = 0; i < 8; ++i) hq[6 + i] = __builtin_bit_cast(u32x4, lds_tr2(rb + (i >> 2) * 4608 + (i & 3) * 8, rb + (i >> 2) * 4608 + 4 * 144 + (i & 3) * 8));
    WAIT_L0();
    STAMP(); TS2();
    WAIT_V(0); BAR();
    STAMP();
    q.template stageA<1, 1>(); q.template stageA<2, 0>();
  }
  }
  if (GRP == 0) { STAMP(); TS2(); }
  constexpr int G = GRP;
  int r = lane & 15, g = lane >> 4;
  {
    const int qq = (lane & 15) >> 2, pp = lane & 3;
    q.voffC = (lane >> 3) * 2048 + (((lane & 7) ^ (((wr & 1) * 4 + (lane >> 4)) & 7)) * 16);
    q.aH = mk_l(49152 + G * 49152 + wm * 4096 + lane * 16);
    const unsigned swz = 4 * (g & 1) + qq;
#pragma unroll
    for (int n = 0; n < 4; ++n) q.aW[n] = mk_l((4 * g + qq) * 256 + (((wn * 4 + n) ^ swz) << 5) + pp * 8);
#pragma unroll
    for (int k2 = 0; k2 < 2; ++k2) q.aC[k2] = mk_l((wn * 64 + r) * 128 + (((g + 4 * k2) ^ ((r >> 1) & 7)) << 4));
  }
  unsigned fl = 0u;
#define FLAG_LOAD(off) asm volatile("global_load_dword %0, %1, %2 sc1" : "+v"(fl) : "v"(off), "s"(A.ws) : "memory")
#define FLAG_CHECK(off) do { PIN(fl); if (!__all(fl == epoch)) wait_flags(A.ws, off, epoch); } while (0)
#define HQ_ST(i) __builtin_amdgcn_raw_buffer_store_b128(hq[i], rws, (i) < 4 ? ho[0] + (i) * 1024 : (i) < 6 ? ho[1] + ((i) - 4) * 2048 : ho[2] + (((i) - 6) >> 2) * 262144 + (((i) - 6) & 3) * 1024, 0, 16)
#define XTRA(st) ((st) == 13 ? 4 : 0)
  float gv[4];
  u32x2 xv[4], dv; float gr[4];
  const int t0 = tt * 128 + wm * 64 + kq * 2, dc0 = dt * 128 + wn * 64;
  auto side = [&](auto ST_) {
    constexpr int ST = decltype(ST_)::value;
    if constexpr (ST == 13) {
      const int l = fresh_lane();
      const unsigned go = (unsigned)WS_G + (tt * 128 + wm * 64 + ((l & 15) >> 2) * 16 + tj(l)) * 4;
#pragma unroll
      for (int j = 0; j < 4; ++j) asm volatile("global_load_dword %0, %1, %2 sc1" : "=v"(gv[j]) : "v"(go + (q.pj[j] >> 7)), "s"(A.ws) : "memory");
    }
    if constexpr (ST >= 16 && ST < 20) {
      constexpr int m = ST - 16;
      PIN(gv[m]);
#pragma unroll
      for (int n = 0; n < 4; ++n) { q.af[m][n] *= 1.f - gv[m]; PIN(q.af[m][n]); }
    }
    if constexpr (ST == NSTEP - 1) {
      const int l = fresh_lane();
      dv = *(const u32x2*)(A.dsk + dc0 + (l & 15) * 4);
#pragma unroll
      for (int i = 0; i < 4; ++i) {
        xv[i] = *(const u32x2*)(A.x + (t0 + i * 16 + tj(l >> 4)) * D_ + dc0 + (l & 15) * 4);
        gr[i] = __int_as_float(__builtin_amdgcn_ds_bpermute((4 * i + (l >> 4)) * 4, __float_as_int(gv[0])));
      }
#pragma unroll
      for (int j = 0; j < 4; ++j) { gv[j] *= 2.f; PIN(gv[j]); }
    }
  };
  __builtin_amdgcn_s_setprio(0);
  if constexpr (G == 0) {
    const unsigned f2 = flb + 512 + (8 * kq + (lane & 7)) * 4, f3 = flb + 640 + (lane & 31) * 4;
    WAIT_V(8); BAR(); STAMP();
    q.template load<0, 0>(); WAIT_V(10); WAIT_L0(); BAR();
    static_for<0, NSTEP - 1>([&](auto ST_) {
      constexpr int ST = decltype(ST_)::value;
      constexpr int prev = (ST + 2 < NSTEP ? 6 : 0) + (ST == 9 ? 1 : 0) + XTRA(ST);
      q.template compute<ST, prev>();
      if constexpr (ST == 10) {
        FLAG_CHECK(f3);
        if (blockIdx.x == 255 && wr == 0 && lane == 0) __hip_atomic_store((GAS unsigned*)(A.ws + WS_EP), epoch, RLX_AGENT);
      }
      if constexpr (ST + 1 == 5) FLAG_LOAD(f2);
      q.template load<ST + 1, 0>();
      if constexpr (ST + 1 == 5) { WAIT_V(6); FLAG_CHECK(f2); }
      if constexpr (ST + 1 == 9) FLAG_LOAD(f3);
      side(std::integral_constant<int, ST + 1>{});
      WAIT_L0(); BAR(); TS2();
      if constexpr (ST == 1 || ST == 13) STAMP();
    });
  } else {
    HQ_ST(0); HQ_ST(1); HQ_ST(2);
    BAR();
    static_for<0, NSTEP>([&](auto ST_) {
      constexpr int ST = decltype(ST_)::value;
      constexpr int nst = ST == 0 ? 3 : ST <= 4 ? 2 : 0;
      if constexpr (ST == 2) arrive(A.ws, lane, flb + 512 + c * 4, 1, epoch);
      if constexpr (ST == 6) arrive(A.ws, lane, flb + 640 + c * 4, 2, epoch);
      q.template load<ST, 1>();
      if constexpr (ST == 0) { HQ_ST(3); HQ_ST(4); HQ_ST(5); }
      if constexpr (ST >= 1 && ST <= 4) { HQ_ST(4 + 2 * ST); HQ_ST(5 + 2 * ST); }
      side(ST_);
      WAIT_L0(); BAR();
      if constexpr (ST + 1 < NSTEP) {
        q.template compute<ST, (ST + 2 < NSTEP ? 2 : 0) + (ST + 3 < NSTEP ? 4 : 0) + nst + XTRA(ST)>(); TS2();
        if constexpr (ST == 1 || ST == 13) STAMP();
      }
    });
  }
  lane = fresh_lane(); r = lane & 15; g = lane >> 4;
  f32x4 o[4];
  {
    unsigned xo[3], pk[4];
#pragma unroll
    for (int j = 1; j < 4; ++j) xo[j - 1] = (unsigned)WS_XB + ((tile * 4 + (kq ^ j)) * 3 + (j - 1)) * 16384u + wid * 2048 + lane * 16;
    __builtin_amdgcn_s_setprio(1);
#pragma unroll
    for (int k = 0; k < 19; ++k) {
      if (k < 16) { const int m = ((k >> 2) + 1) & 3, n = k & 3; MFMA(q.bb[0][n], q.ha[0][m], q.as[m][n]); MFMA(q.bb[1][n], q.ha[1][m], q.as[m][n]); }
      if (k == 3 || k >= 16) asm volatile("s_nop 3");
      if (G == 0 && k == 14) RBAR();
      SCHED();
      if (k >= 3) {
        const int m = (((k - 3) >> 2) + 1) & 3, n = (k - 3) & 3;
        f32x4 v;
#pragma unroll
        for (int j = 0; j < 4; ++j) v[j] = __builtin_fmaf(q.as[m][n][j], gv[m], q.af[m][n][j]);
        if (m == 0) o[n] = v;
        else {
          pk[(n & 1) * 2] = pk_bf16(v[0], v[1]); pk[(n & 1) * 2 + 1] = pk_bf16(v[2], v[3]);
          if (n & 1) __builtin_amdgcn_raw_buffer_store_b128((u32x4){pk[0], pk[1], pk[2], pk[3]}, rws, xo[m - 1] + (n >> 1) * 1024, 0, 16);
        }
      }
      SCHED();
    }
    __builtin_amdgcn_s_setprio(0);
  }
  STAMP();
  WAIT_V(0);
  if (lane >= 1 && lane < 4) __hip_atomic_store((GAS unsigned*)(A.ws + WS_XF + (tile * 4 + (kq ^ lane)) * 128 + wid * 16) + lane, epoch, RLX_AGENT);
  STAMP();
  f32x4 sk[4];
  {
    const f32x4 df = {bf2f(dv[0] & 0xffffu), bfhi(dv[0]), bf2f(dv[1] & 0xffffu), bfhi(dv[1])};
#pragma unroll
    for (int i = 0; i < 4; ++i) { sk[i] = (f32x4){bf2f(xv[i][0] & 0xffffu), bfhi(xv[i][0]), bf2f(xv[i][1] & 0xffffu), bfhi(xv[i][1])} * df * gr[i]; PIN(sk[i]); }
  }
  const lptr tw = mk_l(49152 + G * 49152 + wr * 4096 + r * 256), tr_ = mk_l(49152 + G * 49152 + wr * 4096 + g * 256);
  wait_flags(A.ws, (unsigned)WS_XF + (tile * 4 + kq) * 128 + wid * 16 + (1 + (lane & 3) % 3) * 4, epoch);
  STAMP();
  {
    u32x4 pv[6];
    const unsigned xi = (unsigned)WS_XB + (tile * 4 + kq) * 3 * 16384u + wid * 2048 + lane * 16;
#pragma unroll
    for (int i = 0; i < 6; ++i) pv[i] = __builtin_amdgcn_raw_buffer_load_b128(rws, xi + (i >> 1) * 16384 + (i & 1) * 1024, 0, 16);
#pragma unroll
    for (int i = 0; i < 6; ++i)
#pragma unroll
      for (int h = 0; h < 2; ++h) {
        const unsigned a0 = pv[i][2 * h], a1 = pv[i][2 * h + 1];
        o[(i & 1) * 2 + h] += (f32x4){bf2f(a0 & 0xffffu), bfhi(a0), bf2f(a1 & 0xffffu), bfhi(a1)};
      }
  }
#pragma unroll
  for (int n = 0; n < 4; ++n) *(LAS f32x4*)(tw + (((n * 4 + g) ^ (r & 7)) << 4)) = o[n];
  WAIT_L0();
#pragma unroll
  for (int i = 0; i < 4; ++i) {
    const f32x4 v = *(const LAS f32x4*)(tr_ + i * 1024 + ((r ^ ((4 * i + g) & 7)) << 4));
    *(f32x4*)(A.out + (size_t)(t0 + i * 16 + tj(g)) * D_ + dc0 + r * 4) = v + sk[i];
  }
  STAMP();
  ST_DUMP(A.ws, wid);
  if (GRP == 0) asm volatile(".Lgm_end0:"); else asm volatile(".Lgm_end1:");
  __builtin_amdgcn_endpgm();
  __builtin_unreachable();
}

__global__ __launch_bounds__(512, 2) void fused_kernel(Args A) {
  const u64 t00 = 0;
  const int wid = __builtin_amdgcn_readfirstlane(threadIdx.x >> 6);
  {
    const Pre pre = mk_pre(threadIdx.x & 63, wid, blockIdx.x);
    const int G = wid >> 2;
    const rsrc_t rx = mk_rsrc(A.x, L_ * D_ * 2);
#pragma unroll
    for (int s = 0; s < 4; ++s) dma16(pre.ldsX + s * 2048, pre.voffX, rx, pre.sX + s * 4096);
    dma16(pre.ldsW + G * 1024, pre.voffW, mk_rsrc(A.wg, D_ * F_ * 2), pre.sW + G * 28672u);
    dma16(pre.ldsW + 16384 + G * 1024, pre.voffW, mk_rsrc(A.wu, D_ * F_ * 2), pre.sW + G * 28672u);
    dma16(pre.ldsB + G * 4096, pre.voffB, mk_rsrc((wid & 2) ? A.bim : A.bre, P_ * D_ * 2), pre.sB + G * 32768);
  }
  const char* code; unsigned code_len;
  asm volatile("s_getpc_b64 %0\n.Lgm_pc:\n\ts_mov_b32 %1, .Lgm_end0-.Lgm_pc\n\ts_max_u32 %1, %1, .Lgm_end1-.Lgm_pc" : "=s"(code), "=s"(code_len) :: "scc");
  if (wid < 4) body<0>(A, wid, code, code_len, t00); else body<1>(A, wid, code, code_len, t00);
}

extern "C" void kernel_launch(void* const* d_in, const int* in_sizes, int n_in, void* d_out, int out_size, void* d_ws, size_t ws_size,
                              hipStream_t stream) {
  Args a; a.x = (const u16*)d_in[0]; a.lre = (const u16*)d_in[1]; a.lim = (const u16*)d_in[2]; a.bre = (const u16*)d_in[3];
  a.bim = (const u16*)d_in[4]; a.cre = (const u16*)d_in[5]; a.cim = (const u16*)d_in[6]; a.ldt = (const u16*)d_in[7]; a.dsk = (const u16*)d_in[8];
  a.wg = (const u16*)d_in[10]; a.wu = (const u16*)d_in[11]; a.wd = (const u16*)d_in[12]; a.gw = (const u16*)d_in[13];
  a.ws = (char*)d_ws; a.out = (float*)d_out;
  (void)hipFuncSetAttribute((const void*)fused_kernel, hipFuncAttributeMaxDynamicSharedMemorySize, LDS_BYTES);
  fused_kernel<<<256, 512, LDS_BYTES, stream>>>(a);
}
```

```cpp
#include <hip/hip_runtime.h>
#include <hip/hip_bf16.h>
#include <type_traits>
#define SO 1
#define EB 0
#define PFD 4

typedef unsigned short u16;
typedef short bf16x8 __attribute__((ext_vector_type(8)));
typedef short s16x4 __attribute__((ext_vector_type(4)));
typedef float f32x4 __attribute__((ext_vector_type(4)));
typedef float f32x2 __attribute__((ext_vector_type(2)));
typedef unsigned u32x4 __attribute__((ext_vector_type(4)));
typedef unsigned u32x2 __attribute__((ext_vector_type(2)));
typedef unsigned long long u64;
#define LAS __attribute__((address_space(3)))
#define GAS __attribute__((address_space(1)))
#define RLX_AGENT __ATOMIC_RELAXED, __HIP_MEMORY_SCOPE_AGENT
typedef __amdgpu_buffer_rsrc_t rsrc_t;

constexpr int L_ = 2048, D_ = 1024, P_ = 1024, F_ = 3584;
constexpr int NKF = 176;
constexpr size_t WS_H = 0;
constexpr size_t H_BYTES = (size_t)16 * NKF * 8 * 1024;
constexpr size_t WS_XB = WS_H + H_BYTES;
constexpr size_t XB_BYTES = (size_t)64 * 4 * 3 * 16384;
constexpr size_t WS_G = WS_XB + XB_BYTES;
constexpr size_t WS_AG = WS_G + 8192;
constexpr size_t WS_EP = WS_AG + (size_t)32 * 8 * 64 * 8;
constexpr size_t WS_FL = WS_EP + 512;
constexpr size_t WS_XF = WS_FL + 8192;
constexpr size_t WS_END = WS_XF + 256 * 128;
constexpr unsigned SPIN_CAP = 1u << 20;

__device__ __forceinline__ float bf2f(unsigned b) { return __uint_as_float(b << 16); }
__device__ __forceinline__ float bfhi(unsigned b) { return __uint_as_float(b & 0xffff0000u); }
__device__ __forceinline__ unsigned pk_bf16(float lo, float hi) {
  typedef __bf16 bf2_t __attribute__((ext_vector_type(2)));
  return __builtin_bit_cast(unsigned, __builtin_convertvector((f32x2){lo, hi}, bf2_t)); }
__device__ __forceinline__ float relu(float v) { const int b = __float_as_int(v); return __int_as_float(b > 0 ? b : 0); }
template <int CTRL, int BANKS> __device__ __forceinline__ float dpp(float old, float src) {
  return __int_as_float(__builtin_amdgcn_update_dpp(__float_as_int(old), __float_as_int(src), CTRL, 0xf, BANKS, false)); }
__device__ __forceinline__ rsrc_t mk_rsrc(const void* p, unsigned bytes) {
  return __builtin_amdgcn_make_buffer_rsrc((void*)p, (short)0, (int)bytes, 0x00020000); }
__device__ __forceinline__ void dma16(unsigned ldsaddr, unsigned voff, rsrc_t srd, unsigned soff) {
  asm volatile("s_mov_b32 m0, %0\n\ts_nop 0\n\tbuffer_load_dwordx4 %1, %2, %3 offen lds"
               :: "s"(ldsaddr), "v"(voff), "s"(srd), "s"(soff) : "memory"); }
__device__ __forceinline__ void dma16_dev(unsigned ldsaddr, unsigned voff, rsrc_t srd, unsigned soff) {
  asm volatile("s_mov_b32 m0, %0\n\ts_nop 0\n\tbuffer_load_dwordx4 %1, %2, %3 offen sc1 lds"
               :: "s"(ldsaddr), "v"(voff), "s"(srd), "s"(soff) : "memory"); }
__device__ __forceinline__ void dma4(unsigned ldsaddr, unsigned voff, rsrc_t srd, unsigned soff) {
  asm volatile("s_mov_b32 m0, %0\n\ts_nop 0\n\tbuffer_load_dword %1, %2, %3 offen lds"
               :: "s"(ldsaddr), "v"(voff), "s"(srd), "s"(soff) : "memory"); }
#define WAIT_V(n) asm volatile("s_waitcnt vmcnt(" #n ")" ::: "memory")
#define WAIT_VN(n) asm volatile("s_waitcnt vmcnt(%0)" :: "i"(n) : "memory")
#define WAIT_L0() do { __builtin_amdgcn_s_waitcnt(0xC07F); asm volatile("" ::: "memory"); } while (0)
#define SCHED() __builtin_amdgcn_sched_barrier(0)
#define RBAR() do { __builtin_amdgcn_s_barrier(); asm volatile("" ::: "memory"); } while (0)
#define BAR() do { SCHED(); RBAR(); SCHED(); } while (0)
__device__ __forceinline__ int fresh_lane() { int l; asm volatile("v_mbcnt_lo_u32_b32 %0, -1, 0\n\tv_mbcnt_hi_u32_b32 %0, -1, %0" : "=v"(l)); return l; }
typedef LAS char* lptr;
__device__ __forceinline__ lptr mk_l(unsigned a) { lptr p = (lptr)(size_t)a; asm volatile("" : "+v"(p)); return p; }
__device__ __forceinline__ bf16x8 lds_b128(lptr a) { return *(const LAS bf16x8*)a; }
__device__ __forceinline__ bf16x8 lds_tr2(lptr a0, lptr a1) {
  s16x4 lo = __builtin_amdgcn_ds_read_tr16_b64_v4i16((LAS s16x4*)a0);
  s16x4 hi = __builtin_amdgcn_ds_read_tr16_b64_v4i16((LAS s16x4*)a1);
  return __builtin_shufflevector(lo, hi, 0, 1, 2, 3, 4, 5, 6, 7); }
#define MFMA(a, b, c) asm volatile("v_mfma_f32_16x16x32_bf16 %0, %1, %2, %0" : "+v"(c) : "v"(a), "v"(b))
#define MFMA0(a, b, c) asm volatile("v_mfma_f32_16x16x32_bf16 %0, %1, %2, 0" : "=&v"(c) : "v"(a), "v"(b))
#define PIN(x) asm volatile("" : "+v"(x))
#define MFMA_DRAIN() do { asm volatile("s_nop 7\n\ts_nop 7" ::: "memory"); SCHED(); } while (0)
#define ZERO4 ((f32x4){0.f, 0.f, 0.f, 0.f})

#define ST_DECL
#define STAMP()
#define TS2()
#define ST_DUMP(ws, wid)

struct Args {
  const u16 *x, *lre, *lim, *bre, *bim, *cre, *cim, *ldt, *dsk, *wg, *wu, *wd, *gw;
  char* ws; float* out;
};

constexpr unsigned L_X = 0, L_W = 65536, L_BS = 131072, L_PRM = 151552  ,
                   L_WA = 153600  , L_FLG = 154624  , L_SINK = 154880, L_TAB = 155136  , LDS_BYTES = 163840;

__device__ __forceinline__ int tj(int j) { return (j & 1) * 8 + ((j >> 1) & 1); }
__device__ __forceinline__ f32x2 cmul(f32x2 a, f32x2 b) { return (f32x2){a.x * b.x - a.y * b.y, a.x * b.y + a.y * b.x}; }
__device__ __forceinline__ f32x2 cfma(f32x2 a, f32x2 b, f32x2 c) {
  return (f32x2){__builtin_fmaf(a.x, b.x, __builtin_fmaf(-a.y, b.y, c.x)), __builtin_fmaf(a.x, b.y, __builtin_fmaf(a.y, b.x, c.y))}; }
__device__ __forceinline__ void sw16(float v, float& r0, float& r1) { auto t = __builtin_amdgcn_permlane16_swap(__float_as_uint(v), __float_as_uint(v), false, false); r0 = __uint_as_float(t[0]); r1 = __uint_as_float(t[1]); }
__device__ __forceinline__ void sw32(float v, float& r0, float& r1) { auto t = __builtin_amdgcn_permlane32_swap(__float_as_uint(v), __float_as_uint(v), false, false); r0 = __uint_as_float(t[0]); r1 = __uint_as_float(t[1]); }

__device__ __forceinline__ void touch(const char* p) {
  asm volatile("s_mov_b32 m0, %0\n\ts_nop 0\n\tglobal_load_lds_dword %1, off" :: "s"(L_SINK), "v"(p) : "memory"); }
template <int GRP>
struct P1 {
  static constexpr int NW = GRP == 0 ? 4 : 3, NS = GRP == 0 ? 0 : 4;
  f32x4 ag[NW][4], au[NW][4], as[4][4];
  bf16x8 xa[4], wgf[NW], wuf[NW], bsf[4];
  lptr aX[2], aB[2], aW[NW];
  unsigned voffX, voffW, voffB;
  unsigned sX, sW, sB, ldsX, ldsW, ldsB;
  rsrc_t rx, rwg, rwu, rb;
  const char* pfp; unsigned pfs; int pfo, pfm;
  __device__ __forceinline__ void pull(int T) { const int i = T + pfo; touch(pfp + pfs * (unsigned)(i < 0 ? 0 : i > pfm ? pfm : i)); }

  template <int BUF, int KS, class M> __device__ __forceinline__ void load(M&& mem) {
    mem(0);
#pragma unroll
    for (int m = 0; m < 4; ++m) xa[m] = lds_b128(aX[KS] + BUF * 32768 + m * 2048);
    mem(1);
#pragma unroll
    for (int i = 0; i < NS; ++i) bsf[i] = lds_b128(aB[KS] + BUF * 8192 + i * 2048);
    if (NS) mem(2);
#pragma unroll
    for (int n = 0; n < NW; ++n) {
      wgf[n] = lds_tr2(aW[n] + BUF * 32768 + KS * 8192, aW[n] + BUF * 32768 + KS * 8192 + 1024);
      wuf[n] = lds_tr2(aW[n] + BUF * 32768 + 16384 + KS * 8192, aW[n] + BUF * 32768 + 16384 + KS * 8192 + 1024);
      mem(n + (NS ? 3 : 2));
    }
  }
  template <int BUF, int KS> __device__ __forceinline__ void load() { load<BUF, KS>([](int) {}); }
  template <int BUF> __device__ __forceinline__ void dmaX1(int T, int s) { dma16(ldsX + BUF * 32768 + s * 2048, voffX, rx, sX + T * 128 + s * 4096); }
  template <int BUF, int HH> __device__ __forceinline__ void dmaW1(int T, int i) {
    dma16(ldsW + BUF * 32768 + (i & 1) * 16384 + HH * 8192 + (i >> 1) * 1024, voffW, (i & 1) ? rwu : rwg, sW + (unsigned)(T * 64 + HH * 32 + (i >> 1) * 4) * 7168u);
  }
  template <int BUF> __device__ __forceinline__ void dmaB1(int T, int pf) { dma16(ldsB + BUF * 8192 + pf * 4096, voffB, rb, sB + pf * 32768 + T * 128); }
  template <int NV, bool FIRST = false> __device__ __forceinline__ void compute() {
    constexpr int NM = 4 * NS + 8 * NW;
    __builtin_amdgcn_s_setprio(1);
#pragma unroll
    for (int k = 0; k < NM; ++k) {
      if (k == NM - EB && NV != -2) { __builtin_amdgcn_s_setprio(2); if (NV >= 0) WAIT_VN(NV < 0 ? 0 : NV); RBAR(); }
      const int f = k >> 2, m = (f & 1) ? 3 - (k & 3) : (k & 3);
      const int n = f < NS ? 0 : (f - NS) >> 1;
      const bf16x8& a = f < NS ? xa[m] : ((f - NS) & 1) ? wuf[n] : wgf[n];
      const bf16x8& b = f < NS ? bsf[f & 3] : xa[m];
      f32x4& c = f < NS ? as[f & 3][m] : ((f - NS) & 1) ? au[n][m] : ag[n][m];
      if (FIRST) MFMA0(a, b, c); else MFMA(a, b, c);
    }
    if (EB == 0 && NV != -2) { if (NV >= 0) WAIT_VN(NV < 0 ? 0 : NV); RBAR(); }
    __builtin_amdgcn_s_setprio(0);
    SCHED();
  }
  template <int BUF> __device__ __forceinline__ void dmaX(int T) {
#pragma unroll
    for (int s = 0; s < 4; ++s) dma16(ldsX + BUF * 32768 + s * 2048, voffX, rx, sX + T * 128 + s * 4096);
  }
  template <int BUF, int HH> __device__ __forceinline__ void dmaW(int T) {
#pragma unroll
    for (int s = 0; s < 2; ++s) {
      const unsigned so = sW + (unsigned)(T * 64 + HH * 32 + s * 4) * 7168u;
      dma16(ldsW + BUF * 32768 + HH * 8192 + s * 1024, voffW, rwg, so);
      dma16(ldsW + BUF * 32768 + 16384 + HH * 8192 + s * 1024, voffW, rwu, so);
    }
  }
  template <int BUF> __device__ __forceinline__ void dmaB(int T) {
#pragma unroll
    for (int pf = 0; pf < 2; ++pf) dma16(ldsB + BUF * 8192 + pf * 4096, voffB, rb, sB + pf * 32768 + T * 128);
  }
};

template <int BUF> __device__ __forceinline__ void p1_tile(P1<0>& w, int T) {
  w.template compute<7>();
  w.template load<BUF, 1>([&](int i) { if (i < 4) w.template dmaW1<BUF ^ 1, 1>(T + 1, i); else if (i == 4) w.pull(T); }); WAIT_L0(); BAR();
  w.template compute<5>();
  w.template load<BUF ^ 1, 0>([&](int i) { if (i < 4) w.template dmaX1<BUF>(T + 2, i); else w.template dmaB1<BUF>(T + 2, i - 4); }); WAIT_L0(); BAR();
}
template <int BUF> __device__ __forceinline__ void p1_tile(P1<1>& w, int T) {
  w.template load<BUF, 0>([&](int i) { if (i >= SO && i < SO + 4) w.template dmaX1<BUF ^ 1>(T + 1, i - SO); }); WAIT_L0(); BAR();
  w.template compute<-1>();
  w.template load<BUF, 1>([&](int i) { if (i >= SO && i < SO + 4) w.template dmaW1<BUF, 0>(T + 2, i - SO); }); WAIT_V(4); WAIT_L0(); BAR();
  w.template compute<-1>();
}

constexpr int NSTEP = 22;
struct P2 {
  f32x4 af[4][4], as[4][4];
  bf16x8 ha[2][4], bb[2][4];
  lptr aH, aS[2], aS2[2], aW[4], aC[2];
  unsigned voffH, voffW, voffC;
  unsigned sH, sHs, sW, sC, ldsA, ldsB, kk, pj[4], ps;
  rsrc_t rh, rw, rc;
  template <int ST> __device__ __forceinline__ unsigned kf() const {
    constexpr int e = 2 * (ST - 8), k0 = ST < 8 ? 7 * (ST >> 1) + 3 * (ST & 1) + 1 : 7 * (e / 3) + 3 * (e % 3), k1 = ST < 8 ? k0 + 1 : 7 * ((e + 1) / 3) + 3 * ((e + 1) % 3);
    return k0 + kk * (k1 - k0);
  }
  template <int ST, int GA> __device__ __forceinline__ void dmaA(int j) {
    const unsigned so = ST < 14 ? sH + kf<ST>() * 8192u : sHs + (ST - 14) * 16384u;
    dma16_dev(ldsA + GA * 49152 + (ST % 3) * 16384 + j * 1024, voffH, rh, so + GA * (NKF * 8192u) + (ST < 14 ? pj[j] : (unsigned)(j << 10) ^ ps));
  }
  template <int ST> __device__ __forceinline__ void dmaB(int s) {
    const unsigned l = ldsB + (ST % 3) * 16384 + s * 2048;
    if (ST >= 14) dma16(l, voffC, rc, sC + (ST - 14) * 128u + s * 32768u);
    else dma16(l, voffW, rw, sW + kf<ST>() * 65536u + s * 16384u);
  }
  template <int ST, int GA> __device__ __forceinline__ void stageA() {
#pragma unroll
    for (int j = 0; j < 4; ++j) dmaA<ST, GA>(j);
  }
  template <int ST> __device__ __forceinline__ void stageB() {
#pragma unroll
    for (int s = 0; s < 4; ++s) dmaB<ST>(s);
  }
  template <int ST, int G> __device__ __forceinline__ void load() {
    constexpr int SL = (ST % 3) * 16384;
#pragma unroll
    for (int k2 = 0; k2 < 2; ++k2) {
#pragma unroll
      for (int n = 0; n < 4; ++n) {
        const int i = k2 * 4 + n;
        if constexpr (G == 0) { if constexpr (ST + 2 < NSTEP) { if (i < 4) dmaB<ST + 2>(i); else if (i < 6) dmaA<ST + 2, 1>(i - 4); } }
        else { if constexpr (ST + 2 < NSTEP) { if (i < 2) dmaA<ST + 2, 1>(i + 2); } if constexpr (ST + 3 < NSTEP) { if (i >= 2 && i < 6) dmaA<ST + 3, 0>(i - 2); } }
        if (ST >= 14) bb[k2][n] = lds_b128(aC[k2] + SL + n * 2048);
        else bb[k2][n] = lds_tr2(aW[n] + SL + k2 * 8192, aW[n] + SL + k2 * 8192 + 4096);
        if (ST >= 14) ha[k2][n] = lds_tr2(aS[n & 1] + SL + k2 * 8192 + (n >> 1) * 1024, aS2[n & 1] + SL + k2 * 8192 + (n >> 1) * 1024);
        else ha[k2][n] = lds_b128(aH + SL + k2 * 8192 + n * 1024);
      }
    }
  }
  template <int ST, int NV> __device__ __forceinline__ void compute() {
    __builtin_amdgcn_s_setprio(1);
#pragma unroll
    for (int k = 0; k < 32; ++k) {
      if (k == 32 - EB) { __builtin_amdgcn_s_setprio(2); WAIT_VN(NV); RBAR(); }
      const int k2 = k >> 4, n = (k >> 2) & 3, m = (n & 1) ? 3 - (k & 3) : (k & 3);
      f32x4& c = ST >= 14 ? as[m][n] : af[m][n];
      if ((ST == 0 || ST == 14) && k2 == 0) MFMA0(bb[k2][n], ha[k2][m], c); else MFMA(bb[k2][n], ha[k2][m], c);
    }
    if (EB == 0) { WAIT_VN(NV); RBAR(); }
    __builtin_amdgcn_s_setprio(0);
    SCHED();
  }
};
template <int I, int N, class F> __device__ __forceinline__ void static_for(F&& f) {
  if constexpr (I < N) { f(std::integral_constant<int, I>{}); static_for<I + 1, N>(f); }
}

__device__ __forceinline__ void arrive(char* ws, int lane, unsigned flag_off, int ctr, unsigned epoch) {
  if (lane == 0) {
    const unsigned old = __hip_atomic_fetch_add((LAS unsigned*)mk_l(L_FLG + 16 + ctr * 4), 1u, __ATOMIC_RELAXED, __HIP_MEMORY_SCOPE_WORKGROUP);
    if (old == 3u) __hip_atomic_store((GAS unsigned*)(ws + flag_off), epoch, RLX_AGENT);
  }
}
__device__ __forceinline__ void wait_flags(const char* ws, unsigned off, unsigned epoch) {
_Pragma("nounroll")
  for (unsigned spins = 0; spins < SPIN_CAP; ++spins) {
    if (__all(__hip_atomic_load((const GAS unsigned*)(ws + off), RLX_AGENT) == epoch)) break;
    __builtin_amdgcn_s_sleep(1);
  }
  asm volatile("" ::: "memory");
}

struct Pre { unsigned voffX, voffW, voffB, sX, ldsX, sW, ldsW, sB, ldsB; };
__device__ __forceinline__ Pre mk_pre(int lane, int wid, int bid) {
  Pre pre;
  const int wr = wid & 3, G = wid >> 2, idx = bid >> 3, c = (bid & 7) * 4 + (idx >> 3), rch = idx & 7;
  const unsigned sw8 = ((lane & 7) ^ (((wr & 1) * 4 + (lane >> 4)) & 7)) * 16;
  pre.voffX = ((lane >> 5) * 16 + tj(lane >> 3)) * 2048 + sw8;
  pre.voffB = (lane >> 3) * 2048 + sw8;
  pre.voffW = (lane >> 4) * 7168 + ((((lane & 15) >> 1) ^ ((lane >> 4) | ((wr & 1) << 2))) * 32) + (lane & 1) * 16;
  pre.sX = (unsigned)(rch * 256 + (G * 2 + (wr >> 1)) * 64 + (wr & 1) * 32) * 2048u;
  pre.ldsX = L_X + (G * 16 + (wr >> 1) * 8 + (wr & 1)) * 1024;
  const int ii = (wr & 1) * 2 + (wr >> 1) * 4;
  pre.sW = (unsigned)(ii * 4) * 7168u + (unsigned)((224 * c) & ~63);
  pre.ldsW = L_W + ii * 1024;
  pre.sB = (unsigned)(c * 32 + (wr & 1) * 8) * 2048u;
  pre.ldsB = L_BS + wr * 1024;
  return pre;
}
template <int GRP>
__device__ __forceinline__ void body(const Args& A, int wid, const char* code, const unsigned code_len, const u64 t00) {
  asm volatile("" : "+s"(wid));
  ST_DECL STAMP();
  const int xcd = blockIdx.x & 7, idx = blockIdx.x >> 3, c = xcd * 4 + (idx >> 3), rch = idx & 7;
  const int kq = xcd >> 1, tt = 2 * rch + GRP, dt = (xcd & 1) * 4 + (idx >> 3), tile = rch * 8 + dt;
  const unsigned flb = (unsigned)WS_FL + rch * 1024;
  const int wr = wid & 3, wm = wr >> 1, wn = wr & 1;
  int lane = fresh_lane();
  unsigned epoch;
  P2 q;
  u32x4 hq[14]; unsigned ho[3];
  const rsrc_t rws = mk_rsrc(A.ws, (unsigned)WS_END);
  {
  P1<GRP> w;
  constexpr int NW = P1<GRP>::NW, NS = P1<GRP>::NS;
  int r = lane & 15, g = lane >> 4, qq = (lane & 15) >> 2, pp = lane & 3;
  const Pre pre = mk_pre(lane, wid, blockIdx.x);
  w.rx = mk_rsrc(A.x, L_ * D_ * 2); w.rwg = mk_rsrc(A.wg, D_ * F_ * 2); w.rwu = mk_rsrc(A.wu, D_ * F_ * 2);
  w.rb = mk_rsrc((wr >> 1) ? A.bim : A.bre, P_ * D_ * 2);
  w.voffX = pre.voffX; w.voffB = pre.voffB; w.voffW = pre.voffW;
  w.sX = pre.sX; w.ldsX = pre.ldsX; w.sW = pre.sW; w.ldsW = pre.ldsW; w.sB = pre.sB; w.ldsB = pre.ldsB;
  const char* wbase = (const char*)((wr & 1) ? A.wu : A.wg) + (idx * 2 + (wr >> 1)) * 7168 + xcd * 896;
  const char* rbase = wr < 2 ? (const char*)A.x + (blockIdx.x * 8 + wr * 4) * 2048 : (const char*)(wr == 2 ? A.bre : A.bim) + (xcd * 128 + idx * 4) * 2048;
  if (GRP == 1) {
    w.template dmaW<0, 1>(0); w.template dmaW<1, 0>(1);
    const unsigned co = ((idx * 4 + wr) * 4 + (lane & 3)) * 128u;
    touch(lane < 16 ? wbase + (2 + (lane >> 3)) * 458752u + ((lane & 7) < 7 ? (lane & 7) : 0) * 128 : lane < 24 ? rbase + ((lane - 16) >> 1) * 2048 + (2 + (lane & 1)) * 128
                    : code + (co < code_len ? co : 0u));
  }
  {
#pragma unroll
    for (int ks = 0; ks < 2; ++ks) {
      const unsigned ch = ((g + 4 * ks) ^ ((r >> 1) & 7)) << 4;
      w.aX[ks] = mk_l(L_X + (wr * 64 + r) * 128 + ch);
      if (NS) w.aB[ks] = mk_l(L_BS + r * 128 + ch);
    }
    const unsigned swz = qq | ((g & 1) << 2);
#pragma unroll
    for (int n = 0; n < NW; ++n) {
      unsigned col;
      if (GRP == 0) col = (((2 + n) ^ swz) << 5) + pp * 8;
      else if (n < 2) col = (((((c & 1) ? 6 : 0) + n) ^ swz) << 5) + pp * 8;
      else col = (((((c & 1) ? 0 : 6) + (pp >> 1)) ^ swz) << 5) + (c & 1) * 16 + (pp & 1) * 8;
      w.aW[n] = mk_l(L_W + (8 * g + qq) * 256 + col);
    }
  }
  if constexpr (GRP == 0) {
    WAIT_V(0); BAR();
    w.template load<0, 0>([&](int i) { if (i < 4) w.template dmaX1<1>(1, i); else w.template dmaB1<1>(1, i - 4); });
    unsigned ep0, plr, pli, pld;
    {
      const unsigned po = (c * 32 + (lane & 31)) * 2;
      asm volatile("global_load_dword %0, %1, %2 sc1" : "=v"(ep0) : "v"((unsigned)WS_EP), "s"(A.ws) : "memory");
      asm volatile("global_load_ushort %0, %1, %2" : "=v"(plr) : "v"(po), "s"(A.lre) : "memory");
      asm volatile("global_load_ushort %0, %1, %2" : "=v"(pli) : "v"(po), "s"(A.lim) : "memory");
      asm volatile("global_load_ushort %0, %1, %2" : "=v"(pld) : "v"(po), "s"(A.ldt) : "memory");
    }
    WAIT_L0(); BAR();
    STAMP(); TS2();
    w.template compute<-1, true>();
    w.template load<0, 1>([&](int i) { if (i < 4) w.template dmaW1<1, 1>(1, i); });
    {
      const unsigned j = idx * 4 + wr, co = (j * 4 + (lane & 3)) * 128u;
      const int l = lane < 32 ? lane : 31;
      if (l < 7) { w.pfp = wbase + l * 128; w.pfs = 458752u; w.pfo = PFD; w.pfm = 15; }
      else if (l < 15) { w.pfp = rbase + ((l - 7) >> 1) * 2048 + ((l - 7) & 1) * 128; w.pfs = 256u; w.pfo = 2; w.pfm = 7; }
      else if (l < 23) { w.pfp = (const char*)A.wd + (kq * 896 + j) * 2048u + (xcd & 1) * 1024 + (l - 15) * 128; w.pfs = 262144u; w.pfo = -7; w.pfm = 6; }
      else if (l < 27) { w.pfp = (const char*)((kq >> 1) ? A.cim : A.cre) + ((xcd & 1) * 512 + (j >> 1)) * 2048u + (kq & 1) * 1024 + ((j & 1) * 4 + l - 23) * 128; w.pfs = 131072u; w.pfo = 0; w.pfm = 7; }
      else { w.pfp = l < 31 ? code + (co < code_len ? co : 0u) : (const char*)((idx & 4) ? A.dsk : A.gw) + ((idx & 3) * 4 + wr) * 128; w.pfs = 0u; w.pfo = 0; w.pfm = 0; }
      w.pull(0);
    }
    WAIT_L0(); BAR();
    w.template compute<5>();
    w.template load<1, 0>([&](int i) { if (i < 4) w.template dmaX1<0>(2, i); else w.template dmaB1<0>(2, i - 4); });
    PIN(ep0); PIN(plr); PIN(pli); PIN(pld);
    epoch = __builtin_amdgcn_readfirstlane(ep0) + 1u;
    f32x2 lam, l8, l256; f32x4 p0, p1, p2, t0_, t1_;
    {
      const float lr = bf2f(plr), li = bf2f(pli);
      const float dt_ = bf2f(pk_bf16(__expf(bf2f(pld)), 0.f) & 0xffffu);
      const float e = __expf(dt_ * lr), rev = __builtin_amdgcn_fractf(dt_ * li * 0.15915494309189535f);
      lam = (f32x2){e * __builtin_amdgcn_cosf(rev), -e * __builtin_amdgcn_sinf(rev)};
      const float nr = lam.x - 1.f, rden = 1.f / (lr * lr + li * li);
      p0 = (f32x4){lam.x, lam.y, (nr * lr - lam.y * li) * rden, (-lam.y * lr - nr * li) * rden}; PIN(p0);
      l8 = cmul(lam, lam); l8 = cmul(l8, l8); l8 = cmul(l8, l8); PIN(l8);
    }
    WAIT_L0(); BAR(); TS2();
    w.template compute<7>();
    if (wr == 2) {
      __hip_atomic_store((GAS u64*)(A.ws + WS_AG) + (c * 8 + rch) * 64 + lane, (u64)(epoch - 1u) << 32, RLX_AGENT);
      if (lane < 6) __hip_atomic_store((GAS unsigned*)(A.ws + flb + lane * 128 + c * 4), epoch - 1u, RLX_AGENT);
      else if (lane >= 32 && (lane & 3)) __hip_atomic_store((GAS unsigned*)(A.ws + WS_XF + (tile * 4 + (kq ^ (lane & 3))) * 128) + (lane - 32), epoch - 1u, RLX_AGENT);
    }
    w.template load<1, 1>([&](int i) { if (i < 4) w.template dmaW1<0, 1>(2, i); else if (i == 4) w.pull(1); });
    {
      const f32x2 l16 = cmul(l8, l8), l32 = cmul(l16, l16), l64 = cmul(l32, l32), l128 = cmul(l64, l64);
      l256 = cmul(l128, l128); p1 = (f32x4){l16.x, l16.y, l32.x, l32.y}; p2 = (f32x4){l8.x, l8.y, l64.x, l64.y};
      f32x2 pw = (wr & 1) ? l64 : (f32x2){1.f, 0.f}; if (wr & 2) pw = cmul(pw, l128);
      const f32x2 q0 = (lane & 32) ? l32 : (f32x2){1.f, 0.f}, q1 = cmul(q0, l16), c0 = cmul(q0, pw), c1 = cmul(q1, pw);
      t0_ = (f32x4){q0.x, q0.y, c0.x, c0.y}; t1_ = (f32x4){q1.x, q1.y, c1.x, c1.y};
      PIN(l256); PIN(p1); PIN(p2); PIN(t0_); PIN(t1_);
    }
    WAIT_L0(); BAR();
    w.template compute<5>();
    {
      const lptr tp = mk_l(L_TAB + wr * 2048 + (lane >> 5) * 1024 + (lane & 31) * 16);
      *(LAS f32x4*)tp = t0_; *(LAS f32x4*)(tp + 512) = t1_;
      if (wr == 0) {
        const lptr pb = mk_l(L_PRM + (lane & 31) * 64);
        *(LAS f32x4*)pb = p0; *(LAS f32x4*)(pb + 16) = p1; *(LAS f32x4*)(pb + 32) = p2; *(LAS f32x2*)(pb + 48) = l256;
      } else if (wr == 1) *(LAS unsigned*)mk_l(L_FLG + (lane & 7) * 4) = (lane & 7) == 3 ? epoch : 0u;
    }
    w.template load<0, 0>([&](int i) { if (i < 4) w.template dmaX1<1>(3, i); else w.template dmaB1<1>(3, i - 4); }); WAIT_L0(); BAR();
  } else {
    WAIT_V(9); BAR(); BAR();
    STAMP(); TS2();
    w.template load<0, 0>([&](int i) { if (i >= SO && i < SO + 4) w.template dmaX1<1>(1, i - SO); }); WAIT_V(9); WAIT_L0(); BAR();
    w.template compute<-1, true>();
    w.template load<0, 1>([&](int i) { if (i >= SO && i < SO + 4) w.template dmaW1<0, 0>(2, i - SO); }); WAIT_V(4); WAIT_L0(); BAR();
    w.template compute<-1>(); TS2();
    p1_tile<1>(w, 1);
  }
  for (int T = 2; T < 14; T += 2) {
    TS2(); p1_tile<0>(w, T); TS2();
    p1_tile<1>(w, T + 1);
  }
  if (GRP == 1) epoch = __builtin_amdgcn_readfirstlane(*(const LAS unsigned*)mk_l(L_FLG + 12));
  STAMP();
  lane = fresh_lane();
  q.rh = mk_rsrc(A.ws + WS_H, (unsigned)H_BYTES); q.rw = mk_rsrc(A.wd, F_ * D_ * 2); q.rc = mk_rsrc((kq >> 1) ? A.cim : A.cre, D_ * P_ * 2);
  q.voffW = (lane >> 4) * 2048 + ((((lane & 15) >> 1) ^ (((wr & 1) << 2) | (lane >> 4))) * 32) + (lane & 1) * 16;
  {
    const int i0 = (wr & 1) + (wr >> 1) * 8;
    q.kk = wr >> 1;
    q.sH = (unsigned)(2 * rch * NKF + 28 * kq) * 8192u + (wr & 1) * 4096u;
    q.sHs = (unsigned)(2 * rch * NKF + 112 + 16 * kq + (wr >> 1)) * 8192u + (wr & 1) * 4096u;
    q.sW = (unsigned)(28 * kq * 32 + (wr & 1) * 4) * 2048u + dt * 256u;
    q.sC = (unsigned)(dt * 128 + i0 * 8) * 2048u + (kq & 1) * 1024u;
#pragma unroll
    for (int j = 0; j < 4; ++j) q.pj[j] = (unsigned)(j ^ kq) << 10;
    q.ps = (unsigned)(kq >> 1) << 10;
    q.ldsA = 49152 + wr * 4096; q.ldsB = i0 * 1024;
  }
  u32x4 gx[4], gg[4];
  const int tg = rch * 256 + c * 8 + wr * 2 + (lane >> 5);
  if constexpr (GRP == 0) {
    w.template compute<7>();
    w.template load<0, 1>(); w.template dmaW<1, 1>(15);
#pragma unroll
    for (int i = 0; i < 4; ++i) gx[i] = *(const u32x4*)(A.x + tg * D_ + i * 256 + (lane & 31) * 8);
    WAIT_L0(); BAR();
    w.template compute<8>();
    w.template load<1, 0>(); q.template stageB<0>();
#pragma unroll
    for (int i = 0; i < 4; ++i) gg[i] = *(const u32x4*)(A.gw + i * 256 + (lane & 31) * 8);
    WAIT_L0(); BAR();
    w.template compute<12>();
    w.template load<1, 1>();
    {
      typedef __bf16 bf2_t __attribute__((ext_vector_type(2)));
      float s0 = 0.f, s1 = 0.f;
#pragma unroll
      for (int i = 0; i < 4; ++i)
#pragma unroll
        for (int j = 0; j < 4; j += 2) {
          const unsigned a0 = gx[i][j], b0 = gg[i][j], a1 = gx[i][j + 1], b1 = gg[i][j + 1];
          s0 = __builtin_amdgcn_fdot2_f32_bf16(__builtin_bit_cast(bf2_t, a0), __builtin_bit_cast(bf2_t, b0), s0, false);
          s1 = __builtin_amdgcn_fdot2_f32_bf16(__builtin_bit_cast(bf2_t, a1), __builtin_bit_cast(bf2_t, b1), s1, false);
        }
      float s = s0 + s1;
      s += dpp<0x128, 0xf>(0.f, s); s += dpp<0x124, 0xf>(0.f, s); s += dpp<0x122, 0xf>(0.f, s); s += dpp<0x121, 0xf>(0.f, s);
      float t0, t1; sw16(s, t0, t1); s = t0 + t1;
      if ((lane & 31) == 0) __hip_atomic_store((GAS unsigned*)(A.ws + WS_G) + tg, __float_as_uint(1.f / (1.f + __expf(-s))), RLX_AGENT);
    }
    WAIT_L0(); BAR();
    w.template compute<-2>();
    __builtin_amdgcn_s_setprio(3);
  } else {
    w.template load<0, 0>(); w.template dmaX<1>(15); WAIT_L0(); BAR();
    w.template compute<-1>();
    w.template load<0, 1>(); WAIT_V(0); WAIT_L0(); BAR();
    w.template compute<-1>();
  }
  lane = fresh_lane(); r = lane & 15; g = lane >> 4; qq = (lane & 15) >> 2; pp = lane & 3;
  const unsigned hb = (unsigned)WS_H + (unsigned)(2 * rch + (wr >> 1)) * NKF * 8192u + (wr & 1) * 4096u;
  const int kf0 = 7 * (c >> 1) + 3 * (c & 1);
  q.voffH = lane * 16;
#define FFN_H(n, m, h) pk_bf16(relu(w.ag[n][m][2 * (h)]) * w.au[n][m][2 * (h)], relu(w.ag[n][m][2 * (h) + 1]) * w.au[n][m][2 * (h) + 1])
#define PACK_S() do { _Pragma("unroll") for (int m = 0; m < 4; ++m) { hq[m] = (u32x4){FFN_H(0, m, 0), FFN_H(0, m, 1), FFN_H(1, m, 0), FFN_H(1, m, 1)}; PIN(hq[m]); } \
    _Pragma("unroll") for (int m = 0; m < 4; m += 2) {     \
      const auto s0 = __builtin_amdgcn_permlane32_swap(FFN_H(2, m, 0), FFN_H(2, m + 1, 0), false, false); \
      const auto s1 = __builtin_amdgcn_permlane32_swap(FFN_H(2, m, 1), FFN_H(2, m + 1, 1), false, false); \
      hq[4 + m / 2] = (u32x4){s0[0], s1[0], s0[1], s1[1]}; PIN(hq[4 + m / 2]); } \
    ho[0] = lane * 16 + hb + (kf0 + ((c & 1) ? 3 : 0)) * 8192u; \
    ho[1] = (lane & 31) * 16 + (lane >> 5) * 1024 + (c & 1) * 512 + hb + (kf0 + ((c & 1) ? 0 : 3)) * 8192u; \
    ho[2] = (lane ^ ((lane >> 5) << 2)) * 16 + hb + (112 + c) * 8192u; } while (0)
  if constexpr (GRP == 0) {
    MFMA_DRAIN();
    STAMP();
#pragma unroll
    for (int f = 0; f < 2; ++f)
#pragma unroll
      for (int m = 0; m < 4; ++m) {
        __builtin_amdgcn_raw_buffer_store_b128((u32x4){FFN_H(2 * f, m, 0), FFN_H(2 * f, m, 1), FFN_H(2 * f + 1, m, 0), FFN_H(2 * f + 1, m, 1)}, rws,
                                               lane * 16 + m * 1024 + hb + (kf0 + 1 + f) * 8192u, 0, 16);
        SCHED();
      }
    STAMP();
    WAIT_V(0); arrive(A.ws, lane, flb + (c >> 3) * 128 + c * 4, 0, epoch);
    __builtin_amdgcn_s_setprio(0);
    BAR();
    STAMP();
    wait_flags(A.ws, flb + kq * 160 + (lane & 7) * 4, epoch);
    STAMP();
    q.template stageA<0, 0>(); q.template stageA<0, 1>(); q.template stageA<1, 0>();
    STAMP();
  } else {
    bf16x8 xb[4], bs1[4], wg1[3], wu1[3];
#pragma unroll
    for (int m = 0; m < 4; ++m) { w.xa[m] = lds_b128(w.aX[0] + 32768 + m * 2048); xb[m] = lds_b128(w.aX[1] + 32768 + m * 2048); }
#pragma unroll
    for (int i = 0; i < 4; ++i) { w.bsf[i] = lds_b128(w.aB[0] + 8192 + i * 2048); bs1[i] = lds_b128(w.aB[1] + 8192 + i * 2048); }
    w.wgf[0] = lds_tr2(w.aW[0] + 32768, w.aW[0] + 32768 + 1024); w.wuf[0] = lds_tr2(w.aW[0] + 49152, w.aW[0] + 49152 + 1024);
    q.template stageB<1>(); WAIT_L0(); BAR();
    __builtin_amdgcn_s_setprio(1);
#pragma unroll
    for (int i = 0; i < 4; ++i)
#pragma unroll
      for (int m = 0; m < 4; ++m) MFMA(w.xa[m], w.bsf[i], w.as[i][m]);
#pragma unroll
    for (int i = 0; i < 4; ++i)
#pragma unroll
      for (int m = 0; m < 4; ++m) MFMA(xb[m], bs1[i], w.as[i][m]);
#pragma unroll
    for (int m = 0; m < 4; ++m) MFMA(w.wgf[0], w.xa[m], w.ag[0][m]);
#pragma unroll
    for (int m = 0; m < 4; ++m) MFMA(w.wuf[0], w.xa[m], w.au[0][m]);
    __builtin_amdgcn_s_setprio(0);
    BAR();
    STAMP();
    f32x4 pl[2], pm[2];
    f32x2 l8[2], cin[2], ea[2], agg[2];
#pragma unroll
    for (int pf = 0; pf < 2; ++pf) {
      const lptr pb = mk_l(L_PRM + (pf * 16 + r) * 64);
      pl[pf] = *(const LAS f32x4*)pb; pm[pf] = *(const LAS f32x4*)(pb + 16); l8[pf] = *(const LAS f32x2*)(pb + 32);
    }
#pragma unroll
    for (int n = 1; n < 3; ++n) { w.wgf[n] = lds_tr2(w.aW[n] + 32768, w.aW[n] + 32768 + 1024); w.wuf[n] = lds_tr2(w.aW[n] + 49152, w.aW[n] + 49152 + 1024); }
    SCHED();
#define PAIR(v, k) ((k) & 1 ? (f32x2){(v)[(k) >> 1][2], (v)[(k) >> 1][3]} : (f32x2){(v)[(k) >> 1][0], (v)[(k) >> 1][1]})
#define CSTEP(P, Q, pf, k) do { const f32x2 lx_ = {pl[pf][0], pl[pf][0]}, ly_ = {pl[pf][1], pl[pf][1]}; \
      const f32x2 a_ = __builtin_elementwise_fma(-ly_, Q, PAIR(w.as[2 * (pf)], k)), b_ = __builtin_elementwise_fma(ly_, P, PAIR(w.as[2 * (pf) + 1], k)); \
      P = __builtin_elementwise_fma(lx_, P, a_); Q = __builtin_elementwise_fma(lx_, Q, b_); } while (0)
    {
#pragma unroll
      for (int pf = 0; pf < 2; ++pf)
#pragma unroll
        for (int m = 0; m < 4; ++m) {
          const f32x4 re = w.as[2 * pf][m], im = w.as[2 * pf + 1][m];
          w.as[2 * pf][m] = re * pl[pf][2] - im * pl[pf][3]; w.as[2 * pf + 1][m] = -(re * pl[pf][3]) - im * pl[pf][2];
          PIN(w.as[2 * pf][m]); PIN(w.as[2 * pf + 1][m]);
        }
      f32x2 P[2], Q[2];
#pragma unroll
      for (int k = 0; k < 8; ++k)
#pragma unroll
        for (int pf = 0; pf < 2; ++pf) {
          if (k == 0) { P[pf] = PAIR(w.as[2 * pf], 0); Q[pf] = PAIR(w.as[2 * pf + 1], 0); } else CSTEP(P[pf], Q[pf], pf, k);
        }
#pragma unroll
      for (int pf = 0; pf < 2; ++pf) {
        ea[pf] = (f32x2){P[pf].x, Q[pf].x};
        const f32x2 e = cfma(l8[pf], ea[pf], (f32x2){P[pf].y, Q[pf].y});
        const f32x2 l16 = {pm[pf][0], pm[pf][1]}, l32 = {pm[pf][2], pm[pf][3]};
        float a0, a1, b0, b1; sw16(e.x, a0, a1); sw16(e.y, b0, b1);
        const f32x2 r0 = {a0, b0};
        const f32x2 xx = cfma(l16, r0, (f32x2){a1, b1});
        float c0, c1, d0, d1; sw32(xx.x, c0, c1); sw32(xx.y, d0, d1);
        const f32x2 s0 = {c0, d0};
        agg[pf] = cfma(l32, s0, (f32x2){c1, d1});
        const f32x2 c3 = cfma(l16, s0, r0);
        cin[pf] = g == 0 ? (f32x2){0.f, 0.f} : g == 1 ? r0 : g == 2 ? s0 : c3;
        PIN(ea[pf]);
      }
    }
    f32x2 aggq = (g & 1) ? agg[1] : agg[0]; PIN(aggq); PIN(cin[0]); PIN(cin[1]);
    if (wr < 3) {
      if (g < 2) *(LAS f32x2*)mk_l(L_WA + ((g * 16 + r) * 4 + wr) * 8) = aggq;
      WAIT_L0();
      *(volatile LAS unsigned*)mk_l(L_FLG + wr * 4) = 1u;
    }
    SCHED();
#pragma unroll
    for (int n = 0; n < 3; ++n) { wg1[n] = lds_tr2(w.aW[n] + 40960, w.aW[n] + 40960 + 1024); wu1[n] = lds_tr2(w.aW[n] + 57344, w.aW[n] + 57344 + 1024); }
    if (wr == 3) {
      const lptr fp = mk_l(L_FLG);
_Pragma("nounroll")
      for (unsigned spins = 0; spins < SPIN_CAP; ++spins) {
        const u32x4 f = *(volatile LAS u32x4*)fp;
        if (__builtin_amdgcn_readfirstlane(f[0] & f[1] & f[2])) break;
      }
      asm volatile("" ::: "memory");
      const lptr ap = mk_l(L_WA + ((g & 1) * 16 + r) * 32);
      const f32x4 a01 = *(const LAS f32x4*)ap; const f32x2 a2 = *(const LAS f32x2*)(ap + 16);
      const f32x2 lw = *(const LAS f32x2*)mk_l(L_PRM + ((g & 1) * 16 + r) * 64 + 40);
      const f32x2 t = cfma(lw, cfma(lw, cfma(lw, (f32x2){a01[0], a01[1]}, (f32x2){a01[2], a01[3]}), a2), aggq);
      if (rch < 7) __hip_atomic_store((GAS u64*)(A.ws + WS_AG) + (c * 8 + rch) * 64 + lane, ((u64)epoch << 32) | __float_as_uint((g & 2) ? t.y : t.x), RLX_AGENT);
    }
    WAIT_L0(); SCHED();
    STAMP();
    __builtin_amdgcn_s_setprio(1);
#pragma unroll
    for (int n = 1; n < 3; ++n) {
#pragma unroll
      for (int m = 0; m < 4; ++m) MFMA(w.wgf[n], w.xa[m], w.ag[n][m]);
#pragma unroll
      for (int m = 0; m < 4; ++m) MFMA(w.wuf[n], w.xa[m], w.au[n][m]);
    }
#pragma unroll
    for (int n = 0; n < 3; ++n) {
#pragma unroll
      for (int m = 0; m < 4; ++m) MFMA(wg1[n], xb[m], w.ag[n][m]);
#pragma unroll
      for (int m = 0; m < 4; ++m) MFMA(wu1[n], xb[m], w.au[n][m]);
    }
    BAR();
    __builtin_amdgcn_s_setprio(2);
    lane = fresh_lane(); r = lane & 15; g = lane >> 4; qq = (lane & 15) >> 2; pp = lane & 3;
    q.voffH = lane * 16;
    const GAS u64* agp = (const GAS u64*)(A.ws + WS_AG) + c * 8 * 64 + lane;
    u64 gv_[7];
#pragma unroll
    for (int cc = 0; cc < 7; ++cc) gv_[cc] = __hip_atomic_load(agp + cc * 64, RLX_AGENT);
    STAMP();
    f32x2 car[2], cai[2], cmr[2], cmi[2];
    const f32x2 l256q = *(const LAS f32x2*)mk_l(L_PRM + ((g & 1) * 16 + r) * 64 + 48);
#pragma unroll
    for (int pf = 0; pf < 2; ++pf) {
      const lptr pb = mk_l(L_PRM + (pf * 16 + r) * 64), ap = mk_l(L_WA + (pf * 16 + r) * 32);
      pl[pf] = *(const LAS f32x4*)pb;
      const f32x4 p2 = *(const LAS f32x4*)(pb + 32), tb4 = *(const LAS f32x4*)mk_l(L_TAB + wr * 2048 + g * 512 + (pf * 16 + r) * 16);
      const f32x4 a01 = *(const LAS f32x4*)ap; const f32x2 a2 = *(const LAS f32x2*)(ap + 16);
      const f32x2 l8_ = {p2[0], p2[1]}, l64 = {p2[2], p2[3]};
      f32x2 sw = wr > 0 ? (f32x2){a01[0], a01[1]} : (f32x2){0.f, 0.f};
      if (wr > 1) sw = cfma(l64, sw, (f32x2){a01[2], a01[3]});
      if (wr > 2) sw = cfma(l64, sw, a2);
      const f32x2 ca = cfma((f32x2){tb4[0], tb4[1]}, sw, cin[pf]), cb_ = cfma(l8_, ca, ea[pf]), cm = {tb4[2], tb4[3]}, cn = cmul(l8_, cm);
      car[pf] = (f32x2){ca.x, cb_.x}; cai[pf] = (f32x2){ca.y, cb_.y}; cmr[pf] = (f32x2){cm.x, cn.x}; cmi[pf] = (f32x2){cm.y, cn.y};
      PIN(car[pf]); PIN(cai[pf]); PIN(cmr[pf]); PIN(cmi[pf]);
    }
    PACK_S();
    STAMP();
_Pragma("nounroll")
    for (unsigned spins = 0; spins < SPIN_CAP; ++spins) {
      bool ok = true;
#pragma unroll
      for (int cc = 0; cc < 7; ++cc) ok &= (cc >= rch) || (unsigned)(gv_[cc] >> 32) == epoch;
      if (__all(ok)) break;
      __builtin_amdgcn_s_sleep(1);
#pragma unroll
      for (int cc = 0; cc < 7; ++cc) gv_[cc] = __hip_atomic_load(agp + cc * 64, RLX_AGENT);
    }
    STAMP();
    f32x2 cb = {0.f, 0.f};
#pragma unroll
    for (int cc = 0; cc < 7; ++cc) {
      float are, aim; sw32(__uint_as_float((unsigned)gv_[cc]), are, aim);
      if (cc < rch) cb = cfma(l256q, cb, (f32x2){are, aim});
    }
    unsigned hr[2][8], hi[2][8];
    {
      float cx[2], cy[2]; sw16(cb.x, cx[0], cx[1]); sw16(cb.y, cy[0], cy[1]);
      f32x2 P[2], Q[2];
#pragma unroll
      for (int pf = 0; pf < 2; ++pf) { P[pf] = car[pf] + cmr[pf] * cx[pf] - cmi[pf] * cy[pf]; Q[pf] = cai[pf] + cmr[pf] * cy[pf] + cmi[pf] * cx[pf]; }
#pragma unroll
      for (int k = 0; k < 8; ++k)
#pragma unroll
        for (int pf = 0; pf < 2; ++pf) { CSTEP(P[pf], Q[pf], pf, k); hr[pf][k] = pk_bf16(P[pf].x, P[pf].y); hi[pf][k] = pk_bf16(Q[pf].x, Q[pf].y); }
    }
#pragma unroll
    for (int i = 0; i < 8; ++i) {
      const unsigned* h = (i >> 2) ? hi[(i >> 1) & 1] : hr[(i >> 1) & 1];
      hq[6 + i] = (u32x4){h[(i & 1) * 4], h[(i & 1) * 4 + 1], h[(i & 1) * 4 + 2], h[(i & 1) * 4 + 3]}; PIN(hq[6 + i]);
    }
    STAMP(); TS2();
    WAIT_V(0); BAR();
    STAMP();
    q.template stageA<1, 1>(); q.template stageA<2, 0>();
  }
  }
  if (GRP == 0) { STAMP(); TS2(); }
  constexpr int G = GRP;
  int r = lane & 15, g = lane >> 4;
  {
    const int qq = (lane & 15) >> 2, pp = lane & 3;
    q.voffC = (lane >> 3) * 2048 + (((lane & 7) ^ (((wr & 1) * 4 + (lane >> 4)) & 7)) * 16);
    q.aH = mk_l(49152 + G * 49152 + wm * 4096 + lane * 16);
    const unsigned swz = 4 * (g & 1) + qq;
#pragma unroll
    for (int n = 0; n < 4; ++n) q.aW[n] = mk_l((4 * g + qq) * 256 + (((wn * 4 + n) ^ swz) << 5) + pp * 8);
  }
  unsigned fl = 0u;
#define FLAG_LOAD(off) asm volatile("global_load_dword %0, %1, %2 sc1" : "+v"(fl) : "v"(off), "s"(A.ws) : "memory")
#define FLAG_CHECK(off) do { PIN(fl); if (!__all(fl == epoch)) wait_flags(A.ws, off, epoch); } while (0)
#define HQ_ST(i) __builtin_amdgcn_raw_buffer_store_b128(hq[i], rws, (i) < 4 ? ho[0] + (i) * 1024 : (i) < 6 ? ho[1] + ((i) - 4) * 2048 : ho[2] + (((i) - 6) >> 2) * 262144 + (((i) - 6) & 3) * 1024, 0, 16)
#define XTRA(st) ((st) == 13 ? 4 : 0)
  float gv[4];
  u32x2 xv[4], dv; float gr[4];
  const int t0 = tt * 128 + wm * 64 + kq * 2, dc0 = dt * 128 + wn * 64;
  auto side = [&](auto ST_) {
    constexpr int ST = decltype(ST_)::value;
    if constexpr (ST == 13) {
      const int l = fresh_lane(), g_ = l >> 4, q_ = (l & 15) >> 2, p_ = l & 3;
#pragma unroll
      for (int e = 0; e < 2; ++e) {
        const unsigned b = 49152 + G * 49152 + wm * 4096 + (g_ >> 1) * 2048 + p_ * 256 + 8 * (g_ & 1) * 16 + q_ * 16 + (e ^ (kq & 1)) * 8;
        q.aS[e] = mk_l(b + ((p_ >> 1) << 6)); q.aS2[e] = mk_l(b + (((p_ >> 1) ^ 1) << 6));
        q.aC[e] = mk_l((wn * 64 + (l & 15)) * 128 + (((g_ + 4 * e) ^ (((l & 15) >> 1) & 7)) << 4));
      }
    }
    if constexpr (ST == 13) {
      const int l = fresh_lane();
      const unsigned go = (unsigned)WS_G + (tt * 128 + wm * 64 + ((l & 15) >> 2) * 16 + tj(l)) * 4;
#pragma unroll
      for (int j = 0; j < 4; ++j) asm volatile("global_load_dword %0, %1, %2 sc1" : "=v"(gv[j]) : "v"(go + (q.pj[j] >> 7)), "s"(A.ws) : "memory");
    }
    if constexpr (ST >= 16 && ST < 20) {
      constexpr int m = ST - 16;
      PIN(gv[m]);
#pragma unroll
      for (int n = 0; n < 4; ++n) { q.af[m][n] *= 1.f - gv[m]; PIN(q.af[m][n]); }
    }
    if constexpr (ST == NSTEP - 1) {
      const int l = fresh_lane();
      dv = *(const u32x2*)(A.dsk + dc0 + (l & 15) * 4);
#pragma unroll
      for (int i = 0; i < 4; ++i) {
        xv[i] = *(const u32x2*)(A.x + (t0 + i * 16 + tj(l >> 4)) * D_ + dc0 + (l & 15) * 4);
        gr[i] = __int_as_float(__builtin_amdgcn_ds_bpermute((4 * i + (l >> 4)) * 4, __float_as_int(gv[0])));
      }
#pragma unroll
      for (int j = 0; j < 4; ++j) { gv[j] *= 2.f; PIN(gv[j]); }
    }
  };
  __builtin_amdgcn_s_setprio(0);
  if constexpr (G == 0) {
    const unsigned f2 = flb + 512 + (8 * kq + (lane & 7)) * 4, f3 = flb + 640 + (lane & 31) * 4;
    WAIT_V(8); BAR(); STAMP();
    q.template load<0, 0>(); WAIT_V(10); WAIT_L0(); BAR();
    static_for<0, NSTEP - 1>([&](auto ST_) {
      constexpr int ST = decltype(ST_)::value;
      constexpr int prev = (ST + 2 < NSTEP ? 6 : 0) + (ST == 4 || ST == 10 ? 1 : 0) + XTRA(ST);
      q.template compute<ST, prev>();
      q.template load<ST + 1, 0>();
      if constexpr (ST + 1 == 4) FLAG_LOAD(f2);
      if constexpr (ST + 1 == 5) { WAIT_V(6); FLAG_CHECK(f2); }
      if constexpr (ST + 1 == 10) FLAG_LOAD(f3);
      if constexpr (ST + 1 == 11) { WAIT_V(6); FLAG_CHECK(f3); }
      if constexpr (ST + 1 == 20) { if (blockIdx.x == 255 && wr == 0 && lane == 0) __hip_atomic_store((GAS unsigned*)(A.ws + WS_EP), epoch, RLX_AGENT); }
      side(std::integral_constant<int, ST + 1>{});
      WAIT_L0(); BAR(); TS2();
      if constexpr (ST == 1 || ST == 13) STAMP();
    });
  } else {
    HQ_ST(0); HQ_ST(1); HQ_ST(2);
    BAR();
    static_for<0, NSTEP>([&](auto ST_) {
      constexpr int ST = decltype(ST_)::value;
      constexpr int nst = ST == 0 ? 3 : ST <= 4 ? 2 : 0;
      if constexpr (ST == 2 || ST == 6) {
        if (wr == 0 && lane == 0) __hip_atomic_store((GAS unsigned*)(A.ws + flb + (ST == 2 ? 512 : 640) + c * 4), epoch, RLX_AGENT);
      }
      q.template load<ST, 1>();
      if constexpr (ST == 0) { HQ_ST(3); HQ_ST(4); HQ_ST(5); }
      if constexpr (ST >= 1 && ST <= 4) { HQ_ST(4 + 2 * ST); HQ_ST(5 + 2 * ST); }
      side(ST_);
      WAIT_L0(); BAR();
      if constexpr (ST + 1 < NSTEP) {
        q.template compute<ST, (ST + 2 < NSTEP ? 2 : 0) + (ST + 3 < NSTEP ? 4 : 0) + nst + XTRA(ST)>(); TS2();
        if constexpr (ST == 1 || ST == 13) STAMP();
      }
    });
  }
  lane = fresh_lane(); r = lane & 15; g = lane >> 4;
  f32x4 o[4];
  {
    unsigned xo[3], pk[4];
#pragma unroll
    for (int j = 1; j < 4; ++j) xo[j - 1] = (unsigned)WS_XB + ((tile * 4 + (kq ^ j)) * 3 + (j - 1)) * 16384u + wid * 2048 + lane * 16;
    __builtin_amdgcn_s_setprio(1);
#pragma unroll
    for (int k = 0; k < 19; ++k) {
      if (k < 16) { const int m = ((k >> 2) + 1) & 3, n = k & 3; MFMA(q.bb[0][n], q.ha[0][m], q.as[m][n]); MFMA(q.bb[1][n], q.ha[1][m], q.as[m][n]); }
      if (k == 3 || k >= 16) asm volatile("s_nop 3");
      if (G == 0 && k == 14) RBAR();
      SCHED();
      if (k >= 3) {
        const int m = (((k - 3) >> 2) + 1) & 3, n = (k - 3) & 3;
        f32x4 v;
#pragma unroll
        for (int j = 0; j < 4; ++j) v[j] = __builtin_fmaf(q.as[m][n][j], gv[m], q.af[m][n][j]);
        if (m == 0) o[n] = v;
        else {
          pk[(n & 1) * 2] = pk_bf16(v[0], v[1]); pk[(n & 1) * 2 + 1] = pk_bf16(v[2], v[3]);
          if (n & 1) __builtin_amdgcn_raw_buffer_store_b128((u32x4){pk[0], pk[1], pk[2], pk[3]}, rws, xo[m - 1] + (n >> 1) * 1024, 0, 16);
        }
      }
      SCHED();
    }
    __builtin_amdgcn_s_setprio(0);
  }
  STAMP();
  WAIT_V(0);
  if (lane >= 1 && lane < 4) __hip_atomic_store((GAS unsigned*)(A.ws + WS_XF + (tile * 4 + (kq ^ lane)) * 128 + wid * 16) + lane, epoch, RLX_AGENT);
  STAMP();
  f32x4 sk[4];
  {
    const f32x4 df = {bf2f(dv[0] & 0xffffu), bfhi(dv[0]), bf2f(dv[1] & 0xffffu), bfhi(dv[1])};
#pragma unroll
    for (int i = 0; i < 4; ++i) { sk[i] = (f32x4){bf2f(xv[i][0] & 0xffffu), bfhi(xv[i][0]), bf2f(xv[i][1] & 0xffffu), bfhi(xv[i][1])} * df * gr[i]; PIN(sk[i]); }
  }
  const lptr tw = mk_l(49152 + G * 49152 + wr * 4096 + r * 256), tr_ = mk_l(49152 + G * 49152 + wr * 4096 + g * 256);
  wait_flags(A.ws, (unsigned)WS_XF + (tile * 4 + kq) * 128 + wid * 16 + (1 + (lane & 3) % 3) * 4, epoch);
  STAMP();
  {
    u32x4 pv[6];
    const unsigned xi = (unsigned)WS_XB + (tile * 4 + kq) * 3 * 16384u + wid * 2048 + lane * 16;
#pragma unroll
    for (int i = 0; i < 6; ++i) pv[i] = __builtin_amdgcn_raw_buffer_load_b128(rws, xi + (i >> 1) * 16384 + (i & 1) * 1024, 0, 16);
#pragma unroll
    for (int i = 0; i < 6; ++i)
#pragma unroll
      for (int h = 0; h < 2; ++h) {
        const unsigned a0 = pv[i][2 * h], a1 = pv[i][2 * h + 1];
        o[(i & 1) * 2 + h] += (f32x4){bf2f(a0 & 0xffffu), bfhi(a0), bf2f(a1 & 0xffffu), bfhi(a1)};
      }
  }
#pragma unroll
  for (int n = 0; n < 4; ++n) *(LAS f32x4*)(tw + (((n * 4 + g) ^ (r & 7)) << 4)) = o[n];
  WAIT_L0();
#pragma unroll
  for (int i = 0; i < 4; ++i) {
    const f32x4 v = *(const LAS f32x4*)(tr_ + i * 1024 + ((r ^ ((4 * i + g) & 7)) << 4));
    *(f32x4*)(A.out + (size_t)(t0 + i * 16 + tj(g)) * D_ + dc0 + r * 4) = v + sk[i];
  }
  STAMP();
  ST_DUMP(A.ws, wid);
  if (GRP == 0) asm volatile(".Lgm_end0:"); else asm volatile(".Lgm_end1:");
  __builtin_amdgcn_endpgm();
  __builtin_unreachable();
}

__global__ __launch_bounds__(512, 2) void fused_kernel(Args A) {
  const u64 t00 = 0;
  const int wid = __builtin_amdgcn_readfirstlane(threadIdx.x >> 6);
  {
    const Pre pre = mk_pre(threadIdx.x & 63, wid, blockIdx.x);
    const int G = wid >> 2;
    const rsrc_t rx = mk_rsrc(A.x, L_ * D_ * 2);
#pragma unroll
    for (int s = 0; s < 4; ++s) dma16(pre.ldsX + s * 2048, pre.voffX, rx, pre.sX + s * 4096);
    dma16(pre.ldsW + G * 1024, pre.voffW, mk_rsrc(A.wg, D_ * F_ * 2), pre.sW + G * 28672u);
    dma16(pre.ldsW + 16384 + G * 1024, pre.voffW, mk_rsrc(A.wu, D_ * F_ * 2), pre.sW + G * 28672u);
    dma16(pre.ldsB + G * 4096, pre.voffB, mk_rsrc((wid & 2) ? A.bim : A.bre, P_ * D_ * 2), pre.sB + G * 32768);
  }
  const char* code; unsigned code_len;
  asm volatile("s_getpc_b64 %0\n.Lgm_pc:\n\ts_mov_b32 %1, .Lgm_end0-.Lgm_pc\n\ts_max_u32 %1, %1, .Lgm_end1-.Lgm_pc" : "=s"(code), "=s"(code_len) :: "scc");
  if (wid < 4) body<0>(A, wid, code, code_len, t00); else body<1>(A, wid, code, code_len, t00);
}

extern "C" void kernel_launch(void* const* d_in, const int* in_sizes, int n_in, void* d_out, int out_size, void* d_ws, size_t ws_size,
                              hipStream_t stream) {
  Args a; a.x = (const u16*)d_in[0]; a.lre = (const u16*)d_in[1]; a.lim = (const u16*)d_in[2]; a.bre = (const u16*)d_in[3];
  a.bim = (const u16*)d_in[4]; a.cre = (const u16*)d_in[5]; a.cim = (const u16*)d_in[6]; a.ldt = (const u16*)d_in[7]; a.dsk = (const u16*)d_in[8];
  a.wg = (const u16*)d_in[10]; a.wu = (const u16*)d_in[11]; a.wd = (const u16*)d_in[12]; a.gw = (const u16*)d_in[13];
  a.ws = (char*)d_ws; a.out = (float*)d_out;
  (void)hipFuncSetAttribute((const void*)fused_kernel, hipFuncAttributeMaxDynamicSharedMemorySize, LDS_BYTES);
  fused_kernel<<<256, 512, LDS_BYTES, stream>>>(a);
}
```

```cpp
#include <hip/hip_runtime.h>
#include <hip/hip_bf16.h>
#include <type_traits>
#define MAP2 1
#define ORD2 1
#define DEC 1
#define MAP1 1
#define SQ 1
#define SO 1
#define EB 0
#define PFD 4

typedef unsigned short u16;
typedef short bf16x8 __attribute__((ext_vector_type(8)));
typedef short s16x4 __attribute__((ext_vector_type(4)));
typedef float f32x4 __attribute__((ext_vector_type(4)));
typedef float f32x2 __attribute__((ext_vector_type(2)));
typedef unsigned u32x4 __attribute__((ext_vector_type(4)));
typedef unsigned u32x2 __attribute__((ext_vector_type(2)));
typedef unsigned long long u64;
#define LAS __attribute__((address_space(3)))
#define GAS __attribute__((address_space(1)))
#define RLX_AGENT __ATOMIC_RELAXED, __HIP_MEMORY_SCOPE_AGENT
typedef __amdgpu_buffer_rsrc_t rsrc_t;

constexpr int L_ = 2048, D_ = 1024, P_ = 1024, F_ = 3584;
constexpr int NKF = 176;
constexpr size_t WS_H = 0;
constexpr size_t H_BYTES = (size_t)16 * NKF * 8 * 1024;
constexpr size_t WS_XB = WS_H + H_BYTES;
constexpr size_t XB_BYTES = (size_t)64 * 4 * 3 * 16384;
constexpr size_t WS_G = WS_XB + XB_BYTES;
constexpr size_t WS_AG = WS_G + 8192;
constexpr size_t WS_EP = WS_AG + (size_t)32 * 8 * 64 * 8;
constexpr size_t WS_FL = WS_EP + 512;
constexpr size_t WS_XF = WS_FL + 8192;
constexpr size_t WS_END = WS_XF + 256 * 128;
constexpr unsigned SPIN_CAP = 1u << 20;

__device__ __forceinline__ float bf2f(unsigned b) { return __uint_as_float(b << 16); }
__device__ __forceinline__ float bfhi(unsigned b) { return __uint_as_float(b & 0xffff0000u); }
__device__ __forceinline__ unsigned pk_bf16(float lo, float hi) {
  typedef __bf16 bf2_t __attribute__((ext_vector_type(2)));
  return __builtin_bit_cast(unsigned, __builtin_convertvector((f32x2){lo, hi}, bf2_t)); }
__device__ __forceinline__ float relu(float v) { const int b = __float_as_int(v); return __int_as_float(b > 0 ? b : 0); }
template <int CTRL, int BANKS> __device__ __forceinline__ float dpp(float old, float src) {
  return __int_as_float(__builtin_amdgcn_update_dpp(__float_as_int(old), __float_as_int(src), CTRL, 0xf, BANKS, false)); }
__device__ __forceinline__ rsrc_t mk_rsrc(const void* p, unsigned bytes) {
  return __builtin_amdgcn_make_buffer_rsrc((void*)p, (short)0, (int)bytes, 0x00020000); }
__device__ __forceinline__ void dma16(unsigned ldsaddr, unsigned voff, rsrc_t srd, unsigned soff) {
  asm volatile("s_mov_b32 m0, %0\n\ts_nop 0\n\tbuffer_load_dwordx4 %1, %2, %3 offen lds"
               :: "s"(ldsaddr), "v"(voff), "s"(srd), "s"(soff) : "memory"); }
__device__ __forceinline__ void dma16_dev(unsigned ldsaddr, unsigned voff, rsrc_t srd, unsigned soff) {
  asm volatile("s_mov_b32 m0, %0\n\ts_nop 0\n\tbuffer_load_dwordx4 %1, %2, %3 offen sc1 lds"
               :: "s"(ldsaddr), "v"(voff), "s"(srd), "s"(soff) : "memory"); }
__device__ __forceinline__ void dma4(unsigned ldsaddr, unsigned voff, rsrc_t srd, unsigned soff) {
  asm volatile("s_mov_b32 m0, %0\n\ts_nop 0\n\tbuffer_load_dword %1, %2, %3 offen lds"
               :: "s"(ldsaddr), "v"(voff), "s"(srd), "s"(soff) : "memory"); }
#define WAIT_V(n) asm volatile("s_waitcnt vmcnt(" #n ")" ::: "memory")
#define WAIT_VN(n) asm volatile("s_waitcnt vmcnt(%0)" :: "i"(n) : "memory")
#define WAIT_L0() do { __builtin_amdgcn_s_waitcnt(0xC07F); asm volatile("" ::: "memory"); } while (0)
#define SCHED() __builtin_amdgcn_sched_barrier(0)
#define RBAR() do { __builtin_amdgcn_s_barrier(); asm volatile("" ::: "memory"); } while (0)
#define BAR() do { SCHED(); RBAR(); SCHED(); } while (0)
__device__ __forceinline__ int fresh_lane() { int l; asm volatile("v_mbcnt_lo_u32_b32 %0, -1, 0\n\tv_mbcnt_hi_u32_b32 %0, -1, %0" : "=v"(l)); return l; }
typedef LAS char* lptr;
__device__ __forceinline__ lptr mk_l(unsigned a) { lptr p = (lptr)(size_t)a; asm volatile("" : "+v"(p)); return p; }
__device__ __forceinline__ bf16x8 lds_b128(lptr a) { return *(const LAS bf16x8*)a; }
__device__ __forceinline__ bf16x8 lds_tr2(lptr a0, lptr a1) {
  s16x4 lo = __builtin_amdgcn_ds_read_tr16_b64_v4i16((LAS s16x4*)a0);
  s16x4 hi = __builtin_amdgcn_ds_read_tr16_b64_v4i16((LAS s16x4*)a1);
  return __builtin_shufflevector(lo, hi, 0, 1, 2, 3, 4, 5, 6, 7); }
#define MFMA(a, b, c) asm volatile("v_mfma_f32_16x16x32_bf16 %0, %1, %2, %0" : "+v"(c) : "v"(a), "v"(b))
#define MFMA0(a, b, c) asm volatile("v_mfma_f32_16x16x32_bf16 %0, %1, %2, 0" : "=&v"(c) : "v"(a), "v"(b))
#define PIN(x) asm volatile("" : "+v"(x))
#define MFMA_DRAIN() do { asm volatile("s_nop 7\n\ts_nop 7" ::: "memory"); SCHED(); } while (0)
#define ZERO4 ((f32x4){0.f, 0.f, 0.f, 0.f})

#define ST_DECL
#define STAMP()
#define TS2()
#define ST_DUMP(ws, wid)

struct Args {
  const u16 *x, *lre, *lim, *bre, *bim, *cre, *cim, *ldt, *dsk, *wg, *wu, *wd, *gw;
  char* ws; float* out;
};

constexpr unsigned L_X = 0, L_W = 65536, L_BS = 131072, L_PRM = 151552  ,
                   L_WA = 153600  , L_FLG = 154624  , L_SINK = 154880, L_TAB = 155136  , LDS_BYTES = 163840;

__device__ __forceinline__ int tj(int j) { return (j & 1) * 8 + ((j >> 1) & 1); }
__device__ __forceinline__ f32x2 cmul(f32x2 a, f32x2 b) { return (f32x2){a.x * b.x - a.y * b.y, a.x * b.y + a.y * b.x}; }
__device__ __forceinline__ f32x2 cfma(f32x2 a, f32x2 b, f32x2 c) {
  return (f32x2){__builtin_fmaf(a.x, b.x, __builtin_fmaf(-a.y, b.y, c.x)), __builtin_fmaf(a.x, b.y, __builtin_fmaf(a.y, b.x, c.y))}; }
__device__ __forceinline__ void sw16(float v, float& r0, float& r1) { auto t = __builtin_amdgcn_permlane16_swap(__float_as_uint(v), __float_as_uint(v), false, false); r0 = __uint_as_float(t[0]); r1 = __uint_as_float(t[1]); }
__device__ __forceinline__ void sw32(float v, float& r0, float& r1) { auto t = __builtin_amdgcn_permlane32_swap(__float_as_uint(v), __float_as_uint(v), false, false); r0 = __uint_as_float(t[0]); r1 = __uint_as_float(t[1]); }

__device__ __forceinline__ void touch(const char* p) {
  asm volatile("s_mov_b32 m0, %0\n\ts_nop 0\n\tglobal_load_lds_dword %1, off" :: "s"(L_SINK), "v"(p) : "memory"); }
template <int GRP>
struct P1 {
  static constexpr int NW = GRP == 0 ? 4 : 3, NS = GRP == 0 ? 0 : 4;
  f32x4 ag[NW][4], au[NW][4], as[4][4];
  bf16x8 xa[4], wgf[NW], wuf[NW], bsf[4];
  lptr aX[2], aB[2], aW[NW];
  unsigned voffX, voffW, voffB;
  unsigned sX, sW, sB, ldsX, ldsW, ldsB;
  rsrc_t rx, rwg, rwu, rb;
  const char* pfp; unsigned pfs; int pfo, pfm;
  __device__ __forceinline__ void pull(int T) { const int i = T + pfo; touch(pfp + pfs * (unsigned)(i < 0 ? 0 : i > pfm ? pfm : i)); }

  template <int BUF, int KS, class M> __device__ __forceinline__ void load(M&& mem) {
    mem(0);
#pragma unroll
    for (int m = 0; m < 4; ++m) xa[m] = lds_b128(aX[KS] + BUF * 32768 + m * 2048);
    mem(1);
#pragma unroll
    for (int i = 0; i < NS; ++i) bsf[i] = lds_b128(aB[KS] + BUF * 8192 + i * 2048);
    if (NS) mem(2);
#pragma unroll
    for (int n = 0; n < NW; ++n) {
      wgf[n] = lds_tr2(aW[n] + BUF * 32768 + KS * 8192, aW[n] + BUF * 32768 + KS * 8192 + 1024);
      wuf[n] = lds_tr2(aW[n] + BUF * 32768 + 16384 + KS * 8192, aW[n] + BUF * 32768 + 16384 + KS * 8192 + 1024);
      mem(n + (NS ? 3 : 2));
    }
  }
  template <int BUF, int KS> __device__ __forceinline__ void load() { load<BUF, KS>([](int) {}); }
  template <int BUF> __device__ __forceinline__ void dmaX1(int T, int s) { dma16(ldsX + BUF * 32768 + s * 2048, voffX, rx, sX + T * 128 + s * 4096); }
  template <int BUF, int HH> __device__ __forceinline__ void dmaW1(int T, int i) {
    dma16(ldsW + BUF * 32768 + (i & 1) * 16384 + HH * 8192 + (i >> 1) * 1024, voffW, (i & 1) ? rwu : rwg, sW + (unsigned)(T * 64 + HH * 32 + (i >> 1) * 4) * 7168u);
  }
  template <int BUF> __device__ __forceinline__ void dmaB1(int T, int pf) { dma16(ldsB + BUF * 8192 + pf * 4096, voffB, rb, sB + pf * 32768 + T * 128); }
  template <int NV, bool FIRST = false> __device__ __forceinline__ void compute() {
    constexpr int NM = 4 * NS + 8 * NW;
    __builtin_amdgcn_s_setprio(1);
#pragma unroll
    for (int k = 0; k < NM; ++k) {
      if (k == NM - EB && NV != -2) { __builtin_amdgcn_s_setprio(2); if (NV >= 0) WAIT_VN(NV < 0 ? 0 : NV); RBAR(); }
      const int f = k >> 2, m = (f & 1) ? 3 - (k & 3) : (k & 3);
      const int n = f < NS ? 0 : (f - NS) >> 1;
      const bf16x8& a = f < NS ? xa[m] : ((f - NS) & 1) ? wuf[n] : wgf[n];
      const bf16x8& b = f < NS ? bsf[f & 3] : xa[m];
      f32x4& c = f < NS ? as[f & 3][m] : ((f - NS) & 1) ? au[n][m] : ag[n][m];
      if (FIRST) MFMA0(a, b, c); else MFMA(a, b, c);
    }
    if (EB == 0 && NV != -2) { if (NV >= 0) WAIT_VN(NV < 0 ? 0 : NV); RBAR(); }
    __builtin_amdgcn_s_setprio(0);
    SCHED();
  }
  template <int BUF> __device__ __forceinline__ void dmaX(int T) {
#pragma unroll
    for (int s = 0; s < 4; ++s) dma16(ldsX + BUF * 32768 + s * 2048, voffX, rx, sX + T * 128 + s * 4096);
  }
  template <int BUF, int HH> __device__ __forceinline__ void dmaW(int T) {
#pragma unroll
    for (int s = 0; s < 2; ++s) {
      const unsigned so = sW + (unsigned)(T * 64 + HH * 32 + s * 4) * 7168u;
      dma16(ldsW + BUF * 32768 + HH * 8192 + s * 1024, voffW, rwg, so);
      dma16(ldsW + BUF * 32768 + 16384 + HH * 8192 + s * 1024, voffW, rwu, so);
    }
  }
  template <int BUF> __device__ __forceinline__ void dmaB(int T) {
#pragma unroll
    for (int pf = 0; pf < 2; ++pf) dma16(ldsB + BUF * 8192 + pf * 4096, voffB, rb, sB + pf * 32768 + T * 128);
  }
};

template <int BUF> __device__ __forceinline__ void p1_tile(P1<0>& w, int T) {
  w.template compute<7>();
  w.template load<BUF, 1>([&](int i) { if (i < 4) w.template dmaW1<BUF ^ 1, 1>(T + 1, i); else if (i == 4) w.pull(T); }); WAIT_L0(); BAR();
  w.template compute<5>();
  w.template load<BUF ^ 1, 0>([&](int i) { if (i < 4) w.template dmaX1<BUF>(T + 2, i); else w.template dmaB1<BUF>(T + 2, i - 4); }); WAIT_L0(); BAR();
}
template <int BUF> __device__ __forceinline__ void p1_tile(P1<1>& w, int T) {
  w.template load<BUF, 0>([&](int i) { if (i >= SO && i < SO + 4) w.template dmaX1<BUF ^ 1>(T + 1, i - SO); }); WAIT_L0(); BAR();
  w.template compute<-1>();
  w.template load<BUF, 1>([&](int i) { if (i >= SO && i < SO + 4) w.template dmaW1<BUF, 0>(T + 2, i - SO); }); WAIT_V(4); WAIT_L0(); BAR();
  w.template compute<-1>();
}

constexpr int NSTEP = 22;
struct P2 {
  f32x4 af[4][4], as[4][4];
  bf16x8 ha[2][4], bb[2][4];
  lptr aH, aS[2], aS2[2], aW[4], aC[2];
  unsigned voffH, voffW, voffC;
  unsigned sH, sHs, sW, sC, ldsA, ldsB, kk, pj[4], ps;
  rsrc_t rh, rw, rc, rc2_;
  template <int ST> __device__ __forceinline__ unsigned kf() const {
    constexpr int e = 2 * (ST - 8), k0 = ST < 8 ? 7 * (ST >> 1) + 3 * (ST & 1) + 1 : 7 * (e / 3) + 3 * (e % 3), k1 = ST < 8 ? k0 + 1 : 7 * ((e + 1) / 3) + 3 * ((e + 1) % 3);
    return k0 + kk * (k1 - k0);
  }
  template <int ST, int GA> __device__ __forceinline__ void dmaA(int j) {
    const unsigned so = ST < 14 ? sH + kf<ST>() * 8192u : sHs + ((ST - 14) & 3) * 16384u + ((ST - 14) >> 2) * 262144u;
    dma16_dev(ldsA + GA * 49152 + (ST % 3) * 16384 + j * 1024, voffH, rh, so + GA * (NKF * 8192u) + (ST < 14 ? pj[j] : (unsigned)(j << 10) ^ ps));
  }
  template <int ST> __device__ __forceinline__ void dmaB(int s) {
    const unsigned l = ldsB + (ST % 3) * 16384 + s * 2048;
    if (ST >= 14) dma16(l, voffC, ST < 18 ? rc : rc2_, sC + ((ST - 14) & 3) * 128u + s * 32768u);
    else dma16(l, voffW, rw, sW + kf<ST>() * 65536u + s * 16384u);
  }
  template <int ST, int GA> __device__ __forceinline__ void stageA() {
#pragma unroll
    for (int j = 0; j < 4; ++j) dmaA<ST, GA>(j);
  }
  template <int ST> __device__ __forceinline__ void stageB() {
#pragma unroll
    for (int s = 0; s < 4; ++s) dmaB<ST>(s);
  }
  template <int ST, int G> __device__ __forceinline__ void load() {
    constexpr int SL = (ST % 3) * 16384;
#pragma unroll
    for (int k2 = 0; k2 < 2; ++k2) {
#pragma unroll
      for (int n = 0; n < 4; ++n) {
        const int i = k2 * 4 + n;
        if constexpr (G == 0) { if constexpr (ST + 2 < NSTEP) { if (i < 4) dmaB<ST + 2>(i); else if (i < 6) dmaA<ST + 2, 1>(i - 4); } }
        else { if constexpr (ST + 2 < NSTEP) { if (i < 2) dmaA<ST + 2, 1>(i + 2); } if constexpr (ST + 3 < NSTEP) { if (i >= 2 && i < 6) dmaA<ST + 3, 0>(i - 2); } }
        if (ST >= 14) bb[k2][n] = lds_b128(aC[k2] + SL + n * 2048);
        else bb[k2][n] = lds_tr2(aW[n] + SL + k2 * 8192, aW[n] + SL + k2 * 8192 + 4096);
        if (ST >= 14) ha[k2][n] = lds_tr2(aS[n & 1] + SL + k2 * 8192 + (n >> 1) * 1024, aS2[n & 1] + SL + k2 * 8192 + (n >> 1) * 1024);
        else ha[k2][n] = lds_b128(aH + SL + k2 * 8192 + n * 1024);
      }
    }
  }
  template <int ST, int NV> __device__ __forceinline__ void compute() {
    __builtin_amdgcn_s_setprio(1);
#pragma unroll
    for (int k = 0; k < 32; ++k) {
      if (k == 32 - EB) { __builtin_amdgcn_s_setprio(2); WAIT_VN(NV); RBAR(); }
      const int k2 = k >> 4, m = (k >> 2) & 3, n = (m & 1) ? 3 - (k & 3) : (k & 3);
      f32x4& c = ST >= 14 ? as[m][n] : af[m][n];
      if ((ST == 0 || ST == 14) && k2 == 0) MFMA0(bb[k2][n], ha[k2][m], c); else MFMA(bb[k2][n], ha[k2][m], c);
    }
    if (EB == 0) { WAIT_VN(NV); RBAR(); }
    __builtin_amdgcn_s_setprio(0);
    SCHED();
  }
};
template <int I, int N, class F> __device__ __forceinline__ void static_for(F&& f) {
  if constexpr (I < N) { f(std::integral_constant<int, I>{}); static_for<I + 1, N>(f); }
}

__device__ __forceinline__ void arrive(char* ws, int lane, unsigned flag_off, int ctr, unsigned epoch) {
  if (lane == 0) {
    const unsigned old = __hip_atomic_fetch_add((LAS unsigned*)mk_l(L_FLG + 16 + ctr * 4), 1u, __ATOMIC_RELAXED, __HIP_MEMORY_SCOPE_WORKGROUP);
    if (old == 3u) __hip_atomic_store((GAS unsigned*)(ws + flag_off), epoch, RLX_AGENT);
  }
}
__device__ __forceinline__ void lds_post(int ctr) {
  if (fresh_lane() == 0) __hip_atomic_fetch_add((LAS unsigned*)mk_l(L_FLG + 16 + ctr * 4), 1u, __ATOMIC_RELAXED, __HIP_MEMORY_SCOPE_WORKGROUP);
}
__device__ __forceinline__ void lds_wait(int ctr) {
  const lptr p = mk_l(L_FLG + 16 + ctr * 4);
_Pragma("nounroll")
  for (unsigned spins = 0; spins < (SPIN_CAP << 6); ++spins) {
    if (__builtin_amdgcn_readfirstlane(*(volatile LAS unsigned*)p) == 4u) break;
    __builtin_amdgcn_s_sleep(1);
  }
  asm volatile("" ::: "memory");
}
__device__ __forceinline__ void wait_flags(const char* ws, unsigned off, unsigned epoch) {
_Pragma("nounroll")
  for (unsigned spins = 0; spins < SPIN_CAP; ++spins) {
    if (__all(__hip_atomic_load((const GAS unsigned*)(ws + off), RLX_AGENT) == epoch)) break;
    __builtin_amdgcn_s_sleep(1);
  }
  asm volatile("" ::: "memory");
}

struct Pre { unsigned voffX, voffW, voffB, sX, ldsX, sW, ldsW, sB, ldsB; };
__device__ __forceinline__ Pre mk_pre(int lane, int wid, int bid) {
  Pre pre;
  const int wr = wid & 3, G = wid >> 2, idx = bid >> 3, c = ((bid & 7) >> 1) * 8 + (idx & 7), rch = (bid & 1) * 4 + (idx >> 3);
  const unsigned sw8 = ((lane & 7) ^ (((wr & 1) * 4 + (lane >> 4)) & 7)) * 16;
  pre.voffX = ((lane >> 5) * 16 + tj(lane >> 3)) * 2048 + sw8;
  pre.voffB = (lane >> 3) * 2048 + sw8;
  pre.voffW = (lane >> 4) * 7168 + ((((lane & 15) >> 1) ^ ((lane >> 4) | ((wr & 1) << 2))) * 32) + (lane & 1) * 16;
  pre.sX = (unsigned)(rch * 256 + (G * 2 + (wr >> 1)) * 64 + (wr & 1) * 32) * 2048u;
  pre.ldsX = L_X + (G * 16 + (wr >> 1) * 8 + (wr & 1)) * 1024;
  const int ii = (wr & 1) * 2 + (wr >> 1) * 4;
  pre.sW = (unsigned)(ii * 4) * 7168u + (unsigned)((224 * c) & ~63);
  pre.ldsW = L_W + ii * 1024;
  pre.sB = (unsigned)(c * 32 + (wr & 1) * 8) * 2048u;
  pre.ldsB = L_BS + wr * 1024;
  return pre;
}
template <int GRP>
__device__ __forceinline__ void body(const Args& A, int wid, const char* code, const unsigned code_len, const u64 t00) {
  asm volatile("" : "+s"(wid));
  ST_DECL STAMP();
  const int xcd = blockIdx.x & 7, idx = blockIdx.x >> 3, c = (xcd >> 1) * 8 + (idx & 7), rch = (xcd & 1) * 4 + (idx >> 3);
  const int xrow = (xcd & 1) * 1024 + (xcd >> 1) * 256 + idx * 8;
  const int kq = xcd >> 1, rc2 = (xcd & 1) * 4 + (idx >> 3), dt = idx & 7;
  const int tt = 2 * rc2 + GRP, tile = rc2 * 8 + dt;
  const unsigned flb = (unsigned)WS_FL + rch * 1024, flc = (unsigned)WS_FL + rc2 * 1024;
  const int wr = wid & 3, wm = wr >> 1, wn = wr & 1;
  int lane = fresh_lane();
  unsigned epoch;
  P2 q;
  u32x4 hq[14]; unsigned ho[3];
  const rsrc_t rws = mk_rsrc(A.ws, (unsigned)WS_END);
  {
  P1<GRP> w;
  constexpr int NW = P1<GRP>::NW, NS = P1<GRP>::NS;
  int r = lane & 15, g = lane >> 4, qq = (lane & 15) >> 2, pp = lane & 3;
  const Pre pre = mk_pre(lane, wid, blockIdx.x);
  w.rx = mk_rsrc(A.x, L_ * D_ * 2); w.rwg = mk_rsrc(A.wg, D_ * F_ * 2); w.rwu = mk_rsrc(A.wu, D_ * F_ * 2);
  w.rb = mk_rsrc((wr >> 1) ? A.bim : A.bre, P_ * D_ * 2);
  w.voffX = pre.voffX; w.voffB = pre.voffB; w.voffW = pre.voffW;
  w.sX = pre.sX; w.ldsX = pre.ldsX; w.sW = pre.sW; w.ldsW = pre.ldsW; w.sB = pre.sB; w.ldsB = pre.ldsB;
  const char* wbase = (const char*)((wr & 1) ? A.wu : A.wg) + (idx * 2 + (wr >> 1)) * 7168 + xcd * 896;
  const char* rbase = wr < 2 ? (const char*)A.x + (xrow + wr * 4) * 2048 : (const char*)(wr == 2 ? A.bre : A.bim) + (xcd * 128 + idx * 4) * 2048;
  if (GRP == 1) {
    w.template dmaW<0, 1>(0); w.template dmaW<1, 0>(1);
    const unsigned co = ((idx * 4 + wr) * 4 + (lane & 3)) * 128u;
    touch(lane < 16 ? wbase + (2 + (lane >> 3)) * 458752u + ((lane & 7) < 7 ? (lane & 7) : 0) * 128 : lane < 24 ? rbase + ((lane - 16) >> 1) * 2048 + (2 + (lane & 1)) * 128
                    : code + (co < code_len ? co : 0u));
  }
  {
#pragma unroll
    for (int ks = 0; ks < 2; ++ks) {
      const unsigned ch = ((g + 4 * ks) ^ ((r >> 1) & 7)) << 4;
      w.aX[ks] = mk_l(L_X + (wr * 64 + r) * 128 + ch);
      if (NS) w.aB[ks] = mk_l(L_BS + r * 128 + ch);
    }
    const unsigned swz = qq | ((g & 1) << 2);
#pragma unroll
    for (int n = 0; n < NW; ++n) {
      unsigned col;
      if (GRP == 0) col = (((2 + n) ^ swz) << 5) + pp * 8;
      else if (n < 2) col = (((((c & 1) ? 6 : 0) + n) ^ swz) << 5) + pp * 8;
      else col = (((((c & 1) ? 0 : 6) + (pp >> 1)) ^ swz) << 5) + (c & 1) * 16 + (pp & 1) * 8;
      w.aW[n] = mk_l(L_W + (8 * g + qq) * 256 + col);
    }
  }
  if constexpr (GRP == 0) {
    WAIT_V(0); BAR();
    w.template load<0, 0>([&](int i) { if (i < 4) w.template dmaX1<1>(1, i); else w.template dmaB1<1>(1, i - 4); });
    unsigned ep0, plr, pli, pld;
    {
      const unsigned po = (c * 32 + (lane & 31)) * 2;
      asm volatile("global_load_dword %0, %1, %2 sc1" : "=v"(ep0) : "v"((unsigned)WS_EP), "s"(A.ws) : "memory");
      asm volatile("global_load_ushort %0, %1, %2" : "=v"(plr) : "v"(po), "s"(A.lre) : "memory");
      asm volatile("global_load_ushort %0, %1, %2" : "=v"(pli) : "v"(po), "s"(A.lim) : "memory");
      asm volatile("global_load_ushort %0, %1, %2" : "=v"(pld) : "v"(po), "s"(A.ldt) : "memory");
    }
    WAIT_L0(); BAR();
    STAMP(); TS2();
    w.template compute<-1, true>();
    w.template load<0, 1>([&](int i) { if (i < 4) w.template dmaW1<1, 1>(1, i); });
    {
      const unsigned j = idx * 4 + wr, co = (j * 4 + (lane & 3)) * 128u;
      const int l = lane < 32 ? lane : 31;
      if (l < 7) { w.pfp = wbase + l * 128; w.pfs = 458752u; w.pfo = PFD; w.pfm = 15; }
      else if (l < 15) { w.pfp = rbase + ((l - 7) >> 1) * 2048 + ((l - 7) & 1) * 128; w.pfs = 256u; w.pfo = 2; w.pfm = 7; }
      else if (l < 23) { w.pfp = (const char*)A.wd + (kq * 896 + j) * 2048u + (xcd & 1) * 1024 + (l - 15) * 128; w.pfs = 262144u; w.pfo = -7; w.pfm = 6; }
      else if (l < 27) { w.pfp = (const char*)((j & 1) ? A.cim : A.cre) + ((xcd & 1) * 512 + (j >> 1)) * 2048u + kq * 512 + (l - 23) * 128; w.pfs = 131072u; w.pfo = 0; w.pfm = 7; }
      else { w.pfp = l < 31 ? code + (co < code_len ? co : 0u) : (const char*)((idx & 4) ? A.dsk : A.gw) + ((idx & 3) * 4 + wr) * 128; w.pfs = 0u; w.pfo = 0; w.pfm = 0; }
      w.pull(0);
    }
    WAIT_L0(); BAR();
    w.template compute<5>();
    w.template load<1, 0>([&](int i) { if (i < 4) w.template dmaX1<0>(2, i); else w.template dmaB1<0>(2, i - 4); });
    PIN(ep0); PIN(plr); PIN(pli); PIN(pld);
    epoch = __builtin_amdgcn_readfirstlane(ep0) + 1u;
    f32x2 lam, l8, l256; f32x4 p0, p1, p2, t0_, t1_;
    {
      const float lr = bf2f(plr), li = bf2f(pli);
      const float dt_ = bf2f(pk_bf16(__expf(bf2f(pld)), 0.f) & 0xffffu);
      const float e = __expf(dt_ * lr), rev = __builtin_amdgcn_fractf(dt_ * li * 0.15915494309189535f);
      lam = (f32x2){e * __builtin_amdgcn_cosf(rev), -e * __builtin_amdgcn_sinf(rev)};
      const float nr = lam.x - 1.f, rden = 1.f / (lr * lr + li * li);
      p0 = (f32x4){lam.x, lam.y, (nr * lr - lam.y * li) * rden, (-lam.y * lr - nr * li) * rden}; PIN(p0);
      l8 = cmul(lam, lam); l8 = cmul(l8, l8); l8 = cmul(l8, l8); PIN(l8);
    }
    WAIT_L0(); BAR(); TS2();
    w.template compute<7>();
    if (wr == 2) {
      __hip_atomic_store((GAS u64*)(A.ws + WS_AG) + (c * 8 + rch) * 64 + lane, (u64)(epoch - 1u) << 32, RLX_AGENT);
      if (lane < 6) __hip_atomic_store((GAS unsigned*)(A.ws + flb + lane * 128 + c * 4), epoch - 1u, RLX_AGENT);
      else if (lane >= 32 && (lane & 3)) __hip_atomic_store((GAS unsigned*)(A.ws + WS_XF + (tile * 4 + (kq ^ (lane & 3))) * 128) + (lane - 32), epoch - 1u, RLX_AGENT);
    }
    w.template load<1, 1>([&](int i) { if (i < 4) w.template dmaW1<0, 1>(2, i); else if (i == 4) w.pull(1); });
    {
      const f32x2 l16 = cmul(l8, l8), l32 = cmul(l16, l16), l64 = cmul(l32, l32), l128 = cmul(l64, l64);
      l256 = cmul(l128, l128); p1 = (f32x4){l16.x, l16.y, l32.x, l32.y}; p2 = (f32x4){l8.x, l8.y, l64.x, l64.y};
      f32x2 pw = (wr & 1) ? l64 : (f32x2){1.f, 0.f}; if (wr & 2) pw = cmul(pw, l128);
      const f32x2 q0 = (lane & 32) ? l32 : (f32x2){1.f, 0.f}, q1 = cmul(q0, l16), c0 = cmul(q0, pw), c1 = cmul(q1, pw);
      t0_ = (f32x4){q0.x, q0.y, c0.x, c0.y}; t1_ = (f32x4){q1.x, q1.y, c1.x, c1.y};
      PIN(l256); PIN(p1); PIN(p2); PIN(t0_); PIN(t1_);
    }
    WAIT_L0(); BAR();
    w.template compute<5>();
    {
      const lptr tp = mk_l(L_TAB + wr * 2048 + (lane >> 5) * 1024 + (lane & 31) * 16);
      *(LAS f32x4*)tp = t0_; *(LAS f32x4*)(tp + 512) = t1_;
      if (wr == 0) {
        const lptr pb = mk_l(L_PRM + (lane & 31) * 64);
        *(LAS f32x4*)pb = p0; *(LAS f32x4*)(pb + 16) = p1; *(LAS f32x4*)(pb + 32) = p2; *(LAS f32x2*)(pb + 48) = l256;
      } else if (wr == 1) *(LAS unsigned*)mk_l(L_FLG + (lane & 7) * 4) = (lane & 7) == 3 ? epoch : 0u;
    }
    w.template load<0, 0>([&](int i) { if (i < 4) w.template dmaX1<1>(3, i); else w.template dmaB1<1>(3, i - 4); }); WAIT_L0(); BAR();
  } else {
    WAIT_V(9); BAR(); BAR();
    STAMP(); TS2();
    w.template load<0, 0>([&](int i) { if (i >= SO && i < SO + 4) w.template dmaX1<1>(1, i - SO); }); WAIT_V(9); WAIT_L0(); BAR();
    w.template compute<-1, true>();
    w.template load<0, 1>([&](int i) { if (i >= SO && i < SO + 4) w.template dmaW1<0, 0>(2, i - SO); }); WAIT_V(4); WAIT_L0(); BAR();
    w.template compute<-1>(); TS2();
    p1_tile<1>(w, 1);
  }
  for (int T = 2; T < 14; T += 2) {
    TS2(); p1_tile<0>(w, T); TS2();
    p1_tile<1>(w, T + 1);
  }
  if (GRP == 1) epoch = __builtin_amdgcn_readfirstlane(*(const LAS unsigned*)mk_l(L_FLG + 12));
  STAMP();
  lane = fresh_lane();
  q.rh = mk_rsrc(A.ws + WS_H, (unsigned)H_BYTES); q.rw = mk_rsrc(A.wd, F_ * D_ * 2); q.rc = mk_rsrc((kq >> 1) ? A.cim : A.cre, D_ * P_ * 2);
  q.voffW = (lane >> 4) * 2048 + ((((lane & 15) >> 1) ^ (((wr & 1) << 2) | (lane >> 4))) * 32) + (lane & 1) * 16;
  {
    const int i0 = (wr & 1) + (wr >> 1) * 8;
    q.kk = wr >> 1;
    q.sH = (unsigned)(2 * rc2 * NKF + 28 * kq) * 8192u + (wr & 1) * 4096u;
    q.sHs = (unsigned)(2 * rc2 * NKF + 112 + 16 * kq + (wr >> 1)) * 8192u + (wr & 1) * 4096u;
    q.sW = (unsigned)(28 * kq * 32 + (wr & 1) * 4) * 2048u + dt * 256u;
    q.sC = (unsigned)(dt * 128 + i0 * 8) * 2048u + (kq & 1) * 1024u;
#pragma unroll
    for (int j = 0; j < 4; ++j) q.pj[j] = (unsigned)(j ^ kq) << 10;
    q.rc = mk_rsrc(A.cre, D_ * P_ * 2); q.rc2_ = mk_rsrc(A.cim, D_ * P_ * 2);
    q.sHs = (unsigned)(2 * rc2 * NKF + 112 + 8 * kq + (wr >> 1)) * 8192u + (wr & 1) * 4096u;
    q.sC = (unsigned)(dt * 128 + i0 * 8) * 2048u + kq * 512u;
    q.ps = (unsigned)(kq >> 1) << 10;
    q.ldsA = 49152 + wr * 4096; q.ldsB = i0 * 1024;
  }
  u32x4 gx[4], gg[4];
  const int tg = rch * 256 + c * 8 + wr * 2 + (lane >> 5);
  if constexpr (GRP == 0) {
    w.template compute<7>();
    w.template load<0, 1>(); w.template dmaW<1, 1>(15);
#pragma unroll
    for (int i = 0; i < 4; ++i) gx[i] = *(const u32x4*)(A.x + tg * D_ + i * 256 + (lane & 31) * 8);
    WAIT_L0(); BAR();
    w.template compute<8>();
    w.template load<1, 0>(); q.template stageB<0>();
#pragma unroll
    for (int i = 0; i < 4; ++i) gg[i] = *(const u32x4*)(A.gw + i * 256 + (lane & 31) * 8);
    WAIT_L0(); BAR();
    w.template compute<12>();
    w.template load<1, 1>();
    {
      typedef __bf16 bf2_t __attribute__((ext_vector_type(2)));
      float s0 = 0.f, s1 = 0.f;
#pragma unroll
      for (int i = 0; i < 4; ++i)
#pragma unroll
        for (int j = 0; j < 4; j += 2) {
          const unsigned a0 = gx[i][j], b0 = gg[i][j], a1 = gx[i][j + 1], b1 = gg[i][j + 1];
          s0 = __builtin_amdgcn_fdot2_f32_bf16(__builtin_bit_cast(bf2_t, a0), __builtin_bit_cast(bf2_t, b0), s0, false);
          s1 = __builtin_amdgcn_fdot2_f32_bf16(__builtin_bit_cast(bf2_t, a1), __builtin_bit_cast(bf2_t, b1), s1, false);
        }
      float s = s0 + s1;
      s += dpp<0x128, 0xf>(0.f, s); s += dpp<0x124, 0xf>(0.f, s); s += dpp<0x122, 0xf>(0.f, s); s += dpp<0x121, 0xf>(0.f, s);
      float t0, t1; sw16(s, t0, t1); s = t0 + t1;
      if ((lane & 31) == 0) __hip_atomic_store((GAS unsigned*)(A.ws + WS_G) + tg, __float_as_uint(1.f / (1.f + __expf(-s))), RLX_AGENT);
    }
    WAIT_L0(); BAR();
    w.template compute<-2>();
    __builtin_amdgcn_s_setprio(3);
  } else {
    w.template load<0, 0>(); w.template dmaX<1>(15); WAIT_L0(); BAR();
    w.template compute<-1>();
    w.template load<0, 1>(); WAIT_V(0); WAIT_L0(); BAR();
    w.template compute<-1>();
  }
  lane = fresh_lane(); r = lane & 15; g = lane >> 4; qq = (lane & 15) >> 2; pp = lane & 3;
  const unsigned hb = (unsigned)WS_H + (unsigned)(2 * rch + (wr >> 1)) * NKF * 8192u + (wr & 1) * 4096u;
  const int kf0 = 7 * (c >> 1) + 3 * (c & 1);
  q.voffH = lane * 16;
#define FFN_H(n, m, h) pk_bf16(relu(w.ag[n][m][2 * (h)]) * w.au[n][m][2 * (h)], relu(w.ag[n][m][2 * (h) + 1]) * w.au[n][m][2 * (h) + 1])
#define PACK_S() do { _Pragma("unroll") for (int m = 0; m < 4; ++m) { hq[m] = (u32x4){FFN_H(0, m, 0), FFN_H(0, m, 1), FFN_H(1, m, 0), FFN_H(1, m, 1)}; PIN(hq[m]); } \
    _Pragma("unroll") for (int m = 0; m < 4; m += 2) {     \
      const auto s0 = __builtin_amdgcn_permlane32_swap(FFN_H(2, m, 0), FFN_H(2, m + 1, 0), false, false); \
      const auto s1 = __builtin_amdgcn_permlane32_swap(FFN_H(2, m, 1), FFN_H(2, m + 1, 1), false, false); \
      hq[4 + m / 2] = (u32x4){s0[0], s1[0], s0[1], s1[1]}; PIN(hq[4 + m / 2]); } \
    ho[0] = lane * 16 + hb + (kf0 + ((c & 1) ? 3 : 0)) * 8192u; \
    ho[1] = (lane & 31) * 16 + (lane >> 5) * 1024 + (c & 1) * 512 + hb + (kf0 + ((c & 1) ? 0 : 3)) * 8192u; \
    ho[2] = (lane ^ ((lane >> 5) << 2)) * 16 + hb + (112 + c) * 8192u; } while (0)
  if constexpr (GRP == 0) {
    MFMA_DRAIN();
    STAMP();
#pragma unroll
    for (int f = 0; f < 2; ++f)
#pragma unroll
      for (int m = 0; m < 4; ++m) {
        __builtin_amdgcn_raw_buffer_store_b128((u32x4){FFN_H(2 * f, m, 0), FFN_H(2 * f, m, 1), FFN_H(2 * f + 1, m, 0), FFN_H(2 * f + 1, m, 1)}, rws,
                                               lane * 16 + m * 1024 + hb + (kf0 + 1 + f) * 8192u, 0, 16);
        SCHED();
      }
    STAMP();
    WAIT_V(0); arrive(A.ws, lane, flb + (c >> 3) * 128 + c * 4, 0, epoch);
    __builtin_amdgcn_s_setprio(0);
    BAR();
    STAMP();
    wait_flags(A.ws, flc + kq * 160 + (lane & 7) * 4, epoch);
    STAMP();
    q.template stageA<0, 0>(); q.template stageA<0, 1>(); q.template stageA<1, 0>();
    q.template stageA<1, 1>(); q.template stageA<2, 0>();
    STAMP();
  } else {
    bf16x8 xb[4], bs1[4], wg1[3], wu1[3];
#pragma unroll
    for (int m = 0; m < 4; ++m) { w.xa[m] = lds_b128(w.aX[0] + 32768 + m * 2048); xb[m] = lds_b128(w.aX[1] + 32768 + m * 2048); }
#pragma unroll
    for (int i = 0; i < 4; ++i) { w.bsf[i] = lds_b128(w.aB[0] + 8192 + i * 2048); bs1[i] = lds_b128(w.aB[1] + 8192 + i * 2048); }
    w.wgf[0] = lds_tr2(w.aW[0] + 32768, w.aW[0] + 32768 + 1024); w.wuf[0] = lds_tr2(w.aW[0] + 49152, w.aW[0] + 49152 + 1024);
    q.template stageB<1>(); WAIT_L0(); BAR();
    __builtin_amdgcn_s_setprio(1);
#pragma unroll
    for (int i = 0; i < 4; ++i)
#pragma unroll
      for (int m = 0; m < 4; ++m) MFMA(w.xa[m], w.bsf[i], w.as[i][m]);
#pragma unroll
    for (int i = 0; i < 4; ++i)
#pragma unroll
      for (int m = 0; m < 4; ++m) MFMA(xb[m], bs1[i], w.as[i][m]);
#pragma unroll
    for (int m = 0; m < 4; ++m) MFMA(w.wgf[0], w.xa[m], w.ag[0][m]);
#pragma unroll
    for (int m = 0; m < 4; ++m) MFMA(w.wuf[0], w.xa[m], w.au[0][m]);
    __builtin_amdgcn_s_setprio(0);
    BAR();
    STAMP();
    f32x4 pl[2], pm[2];
    f32x2 l8[2], cin[2], ea[2], agg[2];
#pragma unroll
    for (int pf = 0; pf < 2; ++pf) {
      const lptr pb = mk_l(L_PRM + (pf * 16 + r) * 64);
      pl[pf] = *(const LAS f32x4*)pb; pm[pf] = *(const LAS f32x4*)(pb + 16); l8[pf] = *(const LAS f32x2*)(pb + 32);
    }
#pragma unroll
    for (int n = 1; n < 3; ++n) { w.wgf[n] = lds_tr2(w.aW[n] + 32768, w.aW[n] + 32768 + 1024); w.wuf[n] = lds_tr2(w.aW[n] + 49152, w.aW[n] + 49152 + 1024); }
    SCHED();
#define PAIR(v, k) ((k) & 1 ? (f32x2){(v)[(k) >> 1][2], (v)[(k) >> 1][3]} : (f32x2){(v)[(k) >> 1][0], (v)[(k) >> 1][1]})
#define CSTEP(P, Q, pf, k) do { const f32x2 lx_ = {pl[pf][0], pl[pf][0]}, ly_ = {pl[pf][1], pl[pf][1]}; \
      const f32x2 a_ = __builtin_elementwise_fma(-ly_, Q, PAIR(w.as[2 * (pf)], k)), b_ = __builtin_elementwise_fma(ly_, P, PAIR(w.as[2 * (pf) + 1], k)); \
      P = __builtin_elementwise_fma(lx_, P, a_); Q = __builtin_elementwise_fma(lx_, Q, b_); } while (0)
    {
#pragma unroll
      for (int pf = 0; pf < 2; ++pf)
#pragma unroll
        for (int m = 0; m < 4; ++m) {
          const f32x4 re = w.as[2 * pf][m], im = w.as[2 * pf + 1][m];
          w.as[2 * pf][m] = re * pl[pf][2] - im * pl[pf][3]; w.as[2 * pf + 1][m] = -(re * pl[pf][3]) - im * pl[pf][2];
          PIN(w.as[2 * pf][m]); PIN(w.as[2 * pf + 1][m]);
        }
      f32x2 P[2], Q[2];
#pragma unroll
      for (int k = 0; k < 8; ++k)
#pragma unroll
        for (int pf = 0; pf < 2; ++pf) {
          if (k == 0) { P[pf] = PAIR(w.as[2 * pf], 0); Q[pf] = PAIR(w.as[2 * pf + 1], 0); } else CSTEP(P[pf], Q[pf], pf, k);
        }
#pragma unroll
      for (int pf = 0; pf < 2; ++pf) {
        ea[pf] = (f32x2){P[pf].x, Q[pf].x};
        const f32x2 e = cfma(l8[pf], ea[pf], (f32x2){P[pf].y, Q[pf].y});
        const f32x2 l16 = {pm[pf][0], pm[pf][1]}, l32 = {pm[pf][2], pm[pf][3]};
        float a0, a1, b0, b1; sw16(e.x, a0, a1); sw16(e.y, b0, b1);
        const f32x2 r0 = {a0, b0};
        const f32x2 xx = cfma(l16, r0, (f32x2){a1, b1});
        float c0, c1, d0, d1; sw32(xx.x, c0, c1); sw32(xx.y, d0, d1);
        const f32x2 s0 = {c0, d0};
        agg[pf] = cfma(l32, s0, (f32x2){c1, d1});
        const f32x2 c3 = cfma(l16, s0, r0);
        cin[pf] = g == 0 ? (f32x2){0.f, 0.f} : g == 1 ? r0 : g == 2 ? s0 : c3;
        PIN(ea[pf]);
      }
    }
    f32x2 aggq = (g & 1) ? agg[1] : agg[0]; PIN(aggq); PIN(cin[0]); PIN(cin[1]);
    if (wr < 3) {
      if (g < 2) *(LAS f32x2*)mk_l(L_WA + ((g * 16 + r) * 4 + wr) * 8) = aggq;
      WAIT_L0();
      *(volatile LAS unsigned*)mk_l(L_FLG + wr * 4) = 1u;
    }
    SCHED();
#pragma unroll
    for (int n = 0; n < 3; ++n) { wg1[n] = lds_tr2(w.aW[n] + 40960, w.aW[n] + 40960 + 1024); wu1[n] = lds_tr2(w.aW[n] + 57344, w.aW[n] + 57344 + 1024); }
    if (wr == 3) {
      const lptr fp = mk_l(L_FLG);
_Pragma("nounroll")
      for (unsigned spins = 0; spins < SPIN_CAP; ++spins) {
        const u32x4 f = *(volatile LAS u32x4*)fp;
        if (__builtin_amdgcn_readfirstlane(f[0] & f[1] & f[2])) break;
      }
      asm volatile("" ::: "memory");
      const lptr ap = mk_l(L_WA + ((g & 1) * 16 + r) * 32);
      const f32x4 a01 = *(const LAS f32x4*)ap; const f32x2 a2 = *(const LAS f32x2*)(ap + 16);
      const f32x2 lw = *(const LAS f32x2*)mk_l(L_PRM + ((g & 1) * 16 + r) * 64 + 40);
      const f32x2 t = cfma(lw, cfma(lw, cfma(lw, (f32x2){a01[0], a01[1]}, (f32x2){a01[2], a01[3]}), a2), aggq);
      if (rch < 7) __hip_atomic_store((GAS u64*)(A.ws + WS_AG) + (c * 8 + rch) * 64 + lane, ((u64)epoch << 32) | __float_as_uint((g & 2) ? t.y : t.x), RLX_AGENT);
    }
    WAIT_L0(); SCHED();
    STAMP();
    __builtin_amdgcn_s_setprio(1);
#pragma unroll
    for (int n = 1; n < 3; ++n) {
#pragma unroll
      for (int m = 0; m < 4; ++m) MFMA(w.wgf[n], w.xa[m], w.ag[n][m]);
#pragma unroll
      for (int m = 0; m < 4; ++m) MFMA(w.wuf[n], w.xa[m], w.au[n][m]);
    }
#pragma unroll
    for (int n = 0; n < 3; ++n) {
#pragma unroll
      for (int m = 0; m < 4; ++m) MFMA(wg1[n], xb[m], w.ag[n][m]);
#pragma unroll
      for (int m = 0; m < 4; ++m) MFMA(wu1[n], xb[m], w.au[n][m]);
    }
    BAR();
    __builtin_amdgcn_s_setprio(2);
    lane = fresh_lane(); r = lane & 15; g = lane >> 4; qq = (lane & 15) >> 2; pp = lane & 3;
    q.voffH = lane * 16;
    const GAS u64* agp = (const GAS u64*)(A.ws + WS_AG) + c * 8 * 64 + lane;
    u64 gv_[7];
#pragma unroll
    for (int cc = 0; cc < 7; ++cc) gv_[cc] = __hip_atomic_load(agp + cc * 64, RLX_AGENT);
    STAMP();
    f32x2 car[2], cai[2], cmr[2], cmi[2];
    const f32x2 l256q = *(const LAS f32x2*)mk_l(L_PRM + ((g & 1) * 16 + r) * 64 + 48);
#pragma unroll
    for (int pf = 0; pf < 2; ++pf) {
      const lptr pb = mk_l(L_PRM + (pf * 16 + r) * 64), ap = mk_l(L_WA + (pf * 16 + r) * 32);
      pl[pf] = *(const LAS f32x4*)pb;
      const f32x4 p2 = *(const LAS f32x4*)(pb + 32), tb4 = *(const LAS f32x4*)mk_l(L_TAB + wr * 2048 + g * 512 + (pf * 16 + r) * 16);
      const f32x4 a01 = *(const LAS f32x4*)ap; const f32x2 a2 = *(const LAS f32x2*)(ap + 16);
      const f32x2 l8_ = {p2[0], p2[1]}, l64 = {p2[2], p2[3]};
      f32x2 sw = wr > 0 ? (f32x2){a01[0], a01[1]} : (f32x2){0.f, 0.f};
      if (wr > 1) sw = cfma(l64, sw, (f32x2){a01[2], a01[3]});
      if (wr > 2) sw = cfma(l64, sw, a2);
      const f32x2 ca = cfma((f32x2){tb4[0], tb4[1]}, sw, cin[pf]), cb_ = cfma(l8_, ca, ea[pf]), cm = {tb4[2], tb4[3]}, cn = cmul(l8_, cm);
      car[pf] = (f32x2){ca.x, cb_.x}; cai[pf] = (f32x2){ca.y, cb_.y}; cmr[pf] = (f32x2){cm.x, cn.x}; cmi[pf] = (f32x2){cm.y, cn.y};
      PIN(car[pf]); PIN(cai[pf]); PIN(cmr[pf]); PIN(cmi[pf]);
    }
    PACK_S();
    STAMP();
_Pragma("nounroll")
    for (unsigned spins = 0; spins < SPIN_CAP; ++spins) {
      bool ok = true;
#pragma unroll
      for (int cc = 0; cc < 7; ++cc) ok &= (cc >= rch) || (unsigned)(gv_[cc] >> 32) == epoch;
      if (__all(ok)) break;
      __builtin_amdgcn_s_sleep(1);
#pragma unroll
      for (int cc = 0; cc < 7; ++cc) gv_[cc] = __hip_atomic_load(agp + cc * 64, RLX_AGENT);
    }
    STAMP();
    f32x2 cb = {0.f, 0.f};
#pragma unroll
    for (int cc = 0; cc < 7; ++cc) {
      float are, aim; sw32(__uint_as_float((unsigned)gv_[cc]), are, aim);
      if (cc < rch) cb = cfma(l256q, cb, (f32x2){are, aim});
    }
    unsigned hr[2][8], hi[2][8];
    {
      float cx[2], cy[2]; sw16(cb.x, cx[0], cx[1]); sw16(cb.y, cy[0], cy[1]);
      f32x2 P[2], Q[2];
#pragma unroll
      for (int pf = 0; pf < 2; ++pf) { P[pf] = car[pf] + cmr[pf] * cx[pf] - cmi[pf] * cy[pf]; Q[pf] = cai[pf] + cmr[pf] * cy[pf] + cmi[pf] * cx[pf]; }
#pragma unroll
      for (int k = 0; k < 8; ++k)
#pragma unroll
        for (int pf = 0; pf < 2; ++pf) { CSTEP(P[pf], Q[pf], pf, k); hr[pf][k] = pk_bf16(P[pf].x, P[pf].y); hi[pf][k] = pk_bf16(Q[pf].x, Q[pf].y); }
    }
#pragma unroll
    for (int i = 0; i < 8; ++i) {
      const unsigned* h = (i >> 2) ? hi[(i >> 1) & 1] : hr[(i >> 1) & 1];
      hq[6 + i] = (u32x4){h[(i & 1) * 4], h[(i & 1) * 4 + 1], h[(i & 1) * 4 + 2], h[(i & 1) * 4 + 3]}; PIN(hq[6 + i]);
    }
    STAMP(); TS2();
    WAIT_V(0); lds_wait(1);
    STAMP();
  }
  }
  if (GRP == 0) { STAMP(); TS2(); }
  constexpr int G = GRP;
  int r = lane & 15, g = lane >> 4;
  {
    const int qq = (lane & 15) >> 2, pp = lane & 3;
    q.voffC = (lane >> 3) * 2048 + (((lane & 7) ^ (((wr & 1) * 4 + (lane >> 4)) & 7)) * 16);
    q.aH = mk_l(49152 + G * 49152 + wm * 4096 + lane * 16);
    const unsigned swz = 4 * (g & 1) + qq;
#pragma unroll
    for (int n = 0; n < 4; ++n) q.aW[n] = mk_l((4 * g + qq) * 256 + (((wn * 4 + n) ^ swz) << 5) + pp * 8);
  }
  unsigned fl = 0u;
#define FLAG_LOAD(off) asm volatile("global_load_dword %0, %1, %2 sc1" : "+v"(fl) : "v"(off), "s"(A.ws) : "memory")
#define FLAG_CHECK(off) do { PIN(fl); if (!__all(fl == epoch)) wait_flags(A.ws, off, epoch); } while (0)
#define HQ_ST(i) __builtin_amdgcn_raw_buffer_store_b128(hq[i], rws, (i) < 4 ? ho[0] + (i) * 1024 : (i) < 6 ? ho[1] + ((i) - 4) * 2048 : ho[2] + (((i) - 6) >> 2) * 262144 + (((i) - 6) & 3) * 1024, 0, 16)
#define XTRA(st) ((st) == 13 ? 4 : 0)
  float gv[4];
  u32x2 xv[4], dv; float gr[4];
  const int t0 = tt * 128 + wm * 64 + kq * 2, dc0 = dt * 128 + wn * 64;
  auto side = [&](auto ST_) {
    constexpr int ST = decltype(ST_)::value;
    if constexpr (ST == 13) {
      const int l = fresh_lane(), g_ = l >> 4, q_ = (l & 15) >> 2, p_ = l & 3;
#pragma unroll
      for (int e = 0; e < 2; ++e) {
        const unsigned b = 49152 + G * 49152 + wm * 4096 + (g_ >> 1) * 2048 + p_ * 256 + 8 * (g_ & 1) * 16 + q_ * 16 + (e ^ (kq & 1)) * 8;
        q.aS[e] = mk_l(b + ((p_ >> 1) << 6)); q.aS2[e] = mk_l(b + (((p_ >> 1) ^ 1) << 6));
        q.aC[e] = mk_l((wn * 64 + (l & 15)) * 128 + (((g_ + 4 * e) ^ (((l & 15) >> 1) & 7)) << 4));
      }
    }
    if constexpr (ST == 13) {
      const int l = fresh_lane();
      const unsigned go = (unsigned)WS_G + (tt * 128 + wm * 64 + ((l & 15) >> 2) * 16 + tj(l)) * 4;
#pragma unroll
      for (int j = 0; j < 4; ++j) asm volatile("global_load_dword %0, %1, %2 sc1" : "=v"(gv[j]) : "v"(go + (q.pj[j] >> 7)), "s"(A.ws) : "memory");
    }
    if constexpr (ST >= 16 && ST < 20) {
      constexpr int m = ST - 16;
      PIN(gv[m]);
#pragma unroll
      for (int n = 0; n < 4; ++n) { q.af[m][n] *= 1.f - gv[m]; PIN(q.af[m][n]); }
    }
    if constexpr (ST == NSTEP - 1) {
      const int l = fresh_lane();
      dv = *(const u32x2*)(A.dsk + dc0 + (l & 15) * 4);
#pragma unroll
      for (int i = 0; i < 4; ++i) {
        xv[i] = *(const u32x2*)(A.x + (t0 + i * 16 + tj(l >> 4)) * D_ + dc0 + (l & 15) * 4);
        gr[i] = __int_as_float(__builtin_amdgcn_ds_bpermute((4 * i + (l >> 4)) * 4, __float_as_int(gv[0])));
      }
#pragma unroll
      for (int j = 0; j < 4; ++j) { gv[j] *= 2.f; PIN(gv[j]); }
    }
  };
  __builtin_amdgcn_s_setprio(0);
  if constexpr (G == 0) {
    const unsigned f2 = flc + 512 + (8 * kq + (lane & 7)) * 4, f3 = flc + 640 + (lane & 31) * 4;
    WAIT_V(16); lds_post(1); lds_wait(1); STAMP();
    q.template load<0, 0>(); WAIT_V(18); WAIT_L0(); lds_post(2); SCHED();
    static_for<0, NSTEP - 1>([&](auto ST_) {
      constexpr int ST = decltype(ST_)::value;
      constexpr int prev = (ST + 2 < NSTEP ? 6 : 0) + (ST == 4 || ST == 10 ? 1 : 0) + XTRA(ST) + (DEC && ST == 0 ? 8 : 0);
      q.template compute<ST, prev>();
      q.template load<ST + 1, 0>();
      if constexpr (DEC && ST == 0) WAIT_V(16);
      if constexpr (ST + 1 == 4) FLAG_LOAD(f2);
      if constexpr (ST + 1 == 5) { WAIT_V(6); FLAG_CHECK(f2); }
      if constexpr (ST + 1 == 10) FLAG_LOAD(f3);
      if constexpr (ST + 1 == 11) { WAIT_V(6); FLAG_CHECK(f3); }
      if constexpr (ST + 1 == 20) { if (blockIdx.x == 255 && wr == 0 && lane == 0) __hip_atomic_store((GAS unsigned*)(A.ws + WS_EP), epoch, RLX_AGENT); }
      side(std::integral_constant<int, ST + 1>{});
      WAIT_L0(); BAR(); TS2();
      if constexpr (ST == 1 || ST == 13) STAMP();
    });
  } else {
    HQ_ST(0); HQ_ST(1); HQ_ST(2);
    lds_wait(2);
    static_for<0, NSTEP>([&](auto ST_) {
      constexpr int ST = decltype(ST_)::value;
      constexpr int nst = ST == 0 ? 3 : ST <= 4 ? 2 : 0;
      if constexpr (ST == 2 || ST == 6) {
        if (wr == 0 && lane == 0) __hip_atomic_store((GAS unsigned*)(A.ws + flb + (ST == 2 ? 512 : 640) + c * 4), epoch, RLX_AGENT);
      }
      q.template load<ST, 1>();
      if constexpr (ST == 0) { HQ_ST(3); HQ_ST(4); HQ_ST(5); }
      if constexpr (ST >= 1 && ST <= 4) { HQ_ST(4 + 2 * ST); HQ_ST(5 + 2 * ST); }
      side(ST_);
      WAIT_L0(); BAR();
      if constexpr (ST + 1 < NSTEP) {
        q.template compute<ST, (ST + 2 < NSTEP ? 2 : 0) + (ST + 3 < NSTEP ? 4 : 0) + nst + XTRA(ST)>(); TS2();
        if constexpr (ST == 1 || ST == 13) STAMP();
      }
    });
  }
  lane = fresh_lane(); r = lane & 15; g = lane >> 4;
  f32x4 o[4];
  {
    unsigned xo[3], pk[4];
#pragma unroll
    for (int j = 1; j < 4; ++j) xo[j - 1] = (unsigned)WS_XB + ((tile * 4 + (kq ^ j)) * 3 + (j - 1)) * 16384u + wid * 2048 + lane * 16;
    __builtin_amdgcn_s_setprio(1);
#pragma unroll
    for (int k = 0; k < 19; ++k) {
      if (k < 16) { const int m = ((k >> 2) + 1) & 3, n = k & 3; MFMA(q.bb[0][n], q.ha[0][m], q.as[m][n]); MFMA(q.bb[1][n], q.ha[1][m], q.as[m][n]); }
      if (k == 3 || k >= 16) asm volatile("s_nop 3");
      if (G == 0 && k == 14) RBAR();
      SCHED();
      if (k >= 3) {
        const int m = (((k - 3) >> 2) + 1) & 3, n = (k - 3) & 3;
        f32x4 v;
#pragma unroll
        for (int j = 0; j < 4; ++j) v[j] = __builtin_fmaf(q.as[m][n][j], gv[m], q.af[m][n][j]);
        if (m == 0) o[n] = v;
        else {
          pk[(n & 1) * 2] = pk_bf16(v[0], v[1]); pk[(n & 1) * 2 + 1] = pk_bf16(v[2], v[3]);
          if (n & 1) __builtin_amdgcn_raw_buffer_store_b128((u32x4){pk[0], pk[1], pk[2], pk[3]}, rws, xo[m - 1] + (n >> 1) * 1024, 0, 16);
        }
      }
      SCHED();
    }
    __builtin_amdgcn_s_setprio(0);
  }
  STAMP();
  WAIT_V(0);
  if (lane >= 1 && lane < 4) __hip_atomic_store((GAS unsigned*)(A.ws + WS_XF + (tile * 4 + (kq ^ lane)) * 128 + wid * 16) + lane, epoch, RLX_AGENT);
  STAMP();
  f32x4 sk[4];
  {
    const f32x4 df = {bf2f(dv[0] & 0xffffu), bfhi(dv[0]), bf2f(dv[1] & 0xffffu), bfhi(dv[1])};
#pragma unroll
    for (int i = 0; i < 4; ++i) { sk[i] = (f32x4){bf2f(xv[i][0] & 0xffffu), bfhi(xv[i][0]), bf2f(xv[i][1] & 0xffffu), bfhi(xv[i][1])} * df * gr[i]; PIN(sk[i]); }
  }
  const lptr tw = mk_l(49152 + G * 49152 + wr * 4096 + r * 256), tr_ = mk_l(49152 + G * 49152 + wr * 4096 + g * 256);
  wait_flags(A.ws, (unsigned)WS_XF + (tile * 4 + kq) * 128 + wid * 16 + (1 + (lane & 3) % 3) * 4, epoch);
  STAMP();
  {
    u32x4 pv[6];
    const unsigned xi = (unsigned)WS_XB + (tile * 4 + kq) * 3 * 16384u + wid * 2048 + lane * 16;
#pragma unroll
    for (int i = 0; i < 6; ++i) pv[i] = __builtin_amdgcn_raw_buffer_load_b128(rws, xi + (i >> 1) * 16384 + (i & 1) * 1024, 0, 16);
#pragma unroll
    for (int i = 0; i < 6; ++i)
#pragma unroll
      for (int h = 0; h < 2; ++h) {
        const unsigned a0 = pv[i][2 * h], a1 = pv[i][2 * h + 1];
        o[(i & 1) * 2 + h] += (f32x4){bf2f(a0 & 0xffffu), bfhi(a0), bf2f(a1 & 0xffffu), bfhi(a1)};
      }
  }
#pragma unroll
  for (int n = 0; n < 4; ++n) *(LAS f32x4*)(tw + (((n * 4 + g) ^ (r & 7)) << 4)) = o[n];
  WAIT_L0();
#pragma unroll
  for (int i = 0; i < 4; ++i) {
    const f32x4 v = *(const LAS f32x4*)(tr_ + i * 1024 + ((r ^ ((4 * i + g) & 7)) << 4));
    __builtin_amdgcn_raw_buffer_store_b128(__builtin_bit_cast(u32x4, v + sk[i]), mk_rsrc(A.out, L_ * D_ * 4), ((t0 + i * 16 + tj(g)) * D_ + dc0 + r * 4) * 4, 0, 17);
  }
  STAMP();
  ST_DUMP(A.ws, wid);
  if (GRP == 0) asm volatile(".Lgm_end0:"); else asm volatile(".Lgm_end1:");
  __builtin_amdgcn_endpgm();
  __builtin_unreachable();
}

__global__ __launch_bounds__(512, 2) void fused_kernel(Args A) {
  const u64 t00 = 0;
  const int wid = __builtin_amdgcn_readfirstlane(threadIdx.x >> 6);
  {
    const Pre pre = mk_pre(threadIdx.x & 63, wid, blockIdx.x);
    const int G = wid >> 2;
    const rsrc_t rx = mk_rsrc(A.x, L_ * D_ * 2);
#pragma unroll
    for (int s = 0; s < 4; ++s) dma16(pre.ldsX + s * 2048, pre.voffX, rx, pre.sX + s * 4096);
    dma16(pre.ldsW + G * 1024, pre.voffW, mk_rsrc(A.wg, D_ * F_ * 2), pre.sW + G * 28672u);
    dma16(pre.ldsW + 16384 + G * 1024, pre.voffW, mk_rsrc(A.wu, D_ * F_ * 2), pre.sW + G * 28672u);
    dma16(pre.ldsB + G * 4096, pre.voffB, mk_rsrc((wid & 2) ? A.bim : A.bre, P_ * D_ * 2), pre.sB + G * 32768);
  }
  const char* code; unsigned code_len;
  asm volatile("s_getpc_b64 %0\n.Lgm_pc:\n\ts_mov_b32 %1, .Lgm_end0-.Lgm_pc\n\ts_max_u32 %1, %1, .Lgm_end1-.Lgm_pc" : "=s"(code), "=s"(code_len) :: "scc");
  if (wid < 4) body<0>(A, wid, code, code_len, t00); else body<1>(A, wid, code, code_len, t00);
}

extern "C" void kernel_launch(void* const* d_in, const int* in_sizes, int n_in, void* d_out, int out_size, void* d_ws, size_t ws_size,
                              hipStream_t stream) {
  Args a; a.x = (const u16*)d_in[0]; a.lre = (const u16*)d_in[1]; a.lim = (const u16*)d_in[2]; a.bre = (const u16*)d_in[3];
  a.bim = (const u16*)d_in[4]; a.cre = (const u16*)d_in[5]; a.cim = (const u16*)d_in[6]; a.ldt = (const u16*)d_in[7]; a.dsk = (const u16*)d_in[8];
  a.wg = (const u16*)d_in[10]; a.wu = (const u16*)d_in[11]; a.wd = (const u16*)d_in[12]; a.gw = (const u16*)d_in[13];
  a.ws = (char*)d_ws; a.out = (float*)d_out;
  (void)hipFuncSetAttribute((const void*)fused_kernel, hipFuncAttributeMaxDynamicSharedMemorySize, LDS_BYTES);
  fused_kernel<<<256, 512, LDS_BYTES, stream>>>(a);
}
```

```cpp
#include <hip/hip_runtime.h>
#include <hip/hip_bf16.h>
#include <type_traits>
#define MAP2 1
#define ORD2 1
#define DEC 1
#define MAP1 1
#define SQ 1
#define SO 1
#define EB 0
#define PFD 4

typedef unsigned short u16;
typedef short bf16x8 __attribute__((ext_vector_type(8)));
typedef short s16x4 __attribute__((ext_vector_type(4)));
typedef float f32x4 __attribute__((ext_vector_type(4)));
typedef float f32x2 __attribute__((ext_vector_type(2)));
typedef unsigned u32x4 __attribute__((ext_vector_type(4)));
typedef unsigned u32x2 __attribute__((ext_vector_type(2)));
typedef unsigned long long u64;
#define LAS __attribute__((address_space(3)))
#define GAS __attribute__((address_space(1)))
#define RLX_AGENT __ATOMIC_RELAXED, __HIP_MEMORY_SCOPE_AGENT
typedef __amdgpu_buffer_rsrc_t rsrc_t;

constexpr int L_ = 2048, D_ = 1024, P_ = 1024, F_ = 3584;
constexpr int NKF = 176;
constexpr size_t WS_H = 0;
constexpr size_t H_BYTES = (size_t)16 * NKF * 8 * 1024;
constexpr size_t WS_XB = WS_H + H_BYTES;
constexpr size_t XB_BYTES = (size_t)64 * 4 * 3 * 16384;
constexpr size_t WS_G = WS_XB + XB_BYTES;
constexpr size_t WS_AG = WS_G + 8192;
constexpr size_t WS_EP = WS_AG + (size_t)32 * 8 * 64 * 8;
constexpr size_t WS_FL = WS_EP + 512;
constexpr size_t WS_XF = WS_FL + 8192;
constexpr size_t WS_END = WS_XF + 256 * 128;
constexpr unsigned SPIN_CAP = 1u << 20;

__device__ __forceinline__ float bf2f(unsigned b) { return __uint_as_float(b << 16); }
__device__ __forceinline__ float bfhi(unsigned b) { return __uint_as_float(b & 0xffff0000u); }
__device__ __forceinline__ unsigned pk_bf16(float lo, float hi) {
  typedef __bf16 bf2_t __attribute__((ext_vector_type(2)));
  return __builtin_bit_cast(unsigned, __builtin_convertvector((f32x2){lo, hi}, bf2_t)); }
__device__ __forceinline__ float relu(float v) { const int b = __float_as_int(v); return __int_as_float(b > 0 ? b : 0); }
template <int CTRL, int BANKS> __device__ __forceinline__ float dpp(float old, float src) {
  return __int_as_float(__builtin_amdgcn_update_dpp(__float_as_int(old), __float_as_int(src), CTRL, 0xf, BANKS, false)); }
__device__ __forceinline__ rsrc_t mk_rsrc(const void* p, unsigned bytes) {
  return __builtin_amdgcn_make_buffer_rsrc((void*)p, (short)0, (int)bytes, 0x00020000); }
__device__ __forceinline__ void dma16(unsigned ldsaddr, unsigned voff, rsrc_t srd, unsigned soff) {
  asm volatile("s_mov_b32 m0, %0\n\ts_nop 0\n\tbuffer_load_dwordx4 %1, %2, %3 offen lds"
               :: "s"(ldsaddr), "v"(voff), "s"(srd), "s"(soff) : "memory"); }
__device__ __forceinline__ void dma16_dev(unsigned ldsaddr, unsigned voff, rsrc_t srd, unsigned soff) {
  asm volatile("s_mov_b32 m0, %0\n\ts_nop 0\n\tbuffer_load_dwordx4 %1, %2, %3 offen sc1 lds"
               :: "s"(ldsaddr), "v"(voff), "s"(srd), "s"(soff) : "memory"); }
__device__ __forceinline__ void dma4(unsigned ldsaddr, unsigned voff, rsrc_t srd, unsigned soff) {
  asm volatile("s_mov_b32 m0, %0\n\ts_nop 0\n\tbuffer_load_dword %1, %2, %3 offen lds"
               :: "s"(ldsaddr), "v"(voff), "s"(srd), "s"(soff) : "memory"); }
#define WAIT_V(n) asm volatile("s_waitcnt vmcnt(" #n ")" ::: "memory")
#define WAIT_VN(n) asm volatile("s_waitcnt vmcnt(%0)" :: "i"(n) : "memory")
#define WAIT_L0() do { __builtin_amdgcn_s_waitcnt(0xC07F); asm volatile("" ::: "memory"); } while (0)
#define SCHED() __builtin_amdgcn_sched_barrier(0)
#define RBAR() do { __builtin_amdgcn_s_barrier(); asm volatile("" ::: "memory"); } while (0)
#define BAR() do { SCHED(); RBAR(); SCHED(); } while (0)
__device__ __forceinline__ int fresh_lane() { int l; asm volatile("v_mbcnt_lo_u32_b32 %0, -1, 0\n\tv_mbcnt_hi_u32_b32 %0, -1, %0" : "=v"(l)); return l; }
typedef LAS char* lptr;
__device__ __forceinline__ lptr mk_l(unsigned a) { lptr p = (lptr)(size_t)a; asm volatile("" : "+v"(p)); return p; }
__device__ __forceinline__ bf16x8 lds_b128(lptr a) { return *(const LAS bf16x8*)a; }
__device__ __forceinline__ bf16x8 lds_tr2(lptr a0, lptr a1) {
  s16x4 lo = __builtin_amdgcn_ds_read_tr16_b64_v4i16((LAS s16x4*)a0);
  s16x4 hi = __builtin_amdgcn_ds_read_tr16_b64_v4i16((LAS s16x4*)a1);
  return __builtin_shufflevector(lo, hi, 0, 1, 2, 3, 4, 5, 6, 7); }
#define MFMA(a, b, c) asm volatile("v_mfma_f32_16x16x32_bf16 %0, %1, %2, %0" : "+v"(c) : "v"(a), "v"(b))
#define MFMA0(a, b, c) asm volatile("v_mfma_f32_16x16x32_bf16 %0, %1, %2, 0" : "=&v"(c) : "v"(a), "v"(b))
#define PIN(x) asm volatile("" : "+v"(x))
#define MFMA_DRAIN() do { asm volatile("s_nop 7\n\ts_nop 7" ::: "memory"); SCHED(); } while (0)
#define ZERO4 ((f32x4){0.f, 0.f, 0.f, 0.f})

#define ST_DECL
#define STAMP()
#define TS2()
#define ST_DUMP(ws, wid)

struct Args {
  const u16 *x, *lre, *lim, *bre, *bim, *cre, *cim, *ldt, *dsk, *wg, *wu, *wd, *gw;
  char* ws; float* out;
};

constexpr unsigned L_X = 0, L_W = 65536, L_BS = 131072, L_PRM = 151552  ,
                   L_WA = 153600  , L_FLG = 154624  , L_SINK = 154880, L_TAB = 155136  , LDS_BYTES = 163840;

__device__ __forceinline__ int tj(int j) { return (j & 1) * 8 + ((j >> 1) & 1); }
__device__ __forceinline__ f32x2 cmul(f32x2 a, f32x2 b) { return (f32x2){a.x * b.x - a.y * b.y, a.x * b.y + a.y * b.x}; }
__device__ __forceinline__ f32x2 cfma(f32x2 a, f32x2 b, f32x2 c) {
  return (f32x2){__builtin_fmaf(a.x, b.x, __builtin_fmaf(-a.y, b.y, c.x)), __builtin_fmaf(a.x, b.y, __builtin_fmaf(a.y, b.x, c.y))}; }
__device__ __forceinline__ void sw16(float v, float& r0, float& r1) { auto t = __builtin_amdgcn_permlane16_swap(__float_as_uint(v), __float_as_uint(v), false, false); r0 = __uint_as_float(t[0]); r1 = __uint_as_float(t[1]); }
__device__ __forceinline__ void sw32(float v, float& r0, float& r1) { auto t = __builtin_amdgcn_permlane32_swap(__float_as_uint(v), __float_as_uint(v), false, false); r0 = __uint_as_float(t[0]); r1 = __uint_as_float(t[1]); }

__device__ __forceinline__ void touch(const char* p) {
  asm volatile("s_mov_b32 m0, %0\n\ts_nop 0\n\tglobal_load_lds_dword %1, off" :: "s"(L_SINK), "v"(p) : "memory"); }
template <int GRP>
struct P1 {
  static constexpr int NW = GRP == 0 ? 4 : 3, NS = GRP == 0 ? 0 : 4;
  f32x4 ag[NW][4], au[NW][4], as[4][4];
  bf16x8 xa[4], wgf[NW], wuf[NW], bsf[4];
  lptr aX[2], aB[2], aW[NW];
  unsigned voffX, voffW, voffB;
  unsigned sX, sW, sB, ldsX, ldsW, ldsB;
  rsrc_t rx, rwg, rwu, rb;
  const char* pfp; unsigned pfs; int pfo, pfm;
  __device__ __forceinline__ void pull(int T) { const int i = T + pfo; touch(pfp + pfs * (unsigned)(i < 0 ? 0 : i > pfm ? pfm : i)); }

  template <int BUF, int KS, class M> __device__ __forceinline__ void load(M&& mem) {
    mem(0);
#pragma unroll
    for (int m = 0; m < 4; ++m) xa[m] = lds_b128(aX[KS] + BUF * 32768 + m * 2048);
    mem(1);
#pragma unroll
    for (int i = 0; i < NS; ++i) bsf[i] = lds_b128(aB[KS] + BUF * 8192 + i * 2048);
    if (NS) mem(2);
#pragma unroll
    for (int n = 0; n < NW; ++n) {
      wgf[n] = lds_tr2(aW[n] + BUF * 32768 + KS * 8192, aW[n] + BUF * 32768 + KS * 8192 + 1024);
      wuf[n] = lds_tr2(aW[n] + BUF * 32768 + 16384 + KS * 8192, aW[n] + BUF * 32768 + 16384 + KS * 8192 + 1024);
      mem(n + (NS ? 3 : 2));
    }
  }
  template <int BUF, int KS> __device__ __forceinline__ void load() { load<BUF, KS>([](int) {}); }
  template <int BUF> __device__ __forceinline__ void dmaX1(int T, int s) { dma16(ldsX + BUF * 32768 + s * 2048, voffX, rx, sX + T * 128 + s * 4096); }
  template <int BUF, int HH> __device__ __forceinline__ void dmaW1(int T, int i) {
    dma16(ldsW + BUF * 32768 + (i & 1) * 16384 + HH * 8192 + (i >> 1) * 1024, voffW, (i & 1) ? rwu : rwg, sW + (unsigned)(T * 64 + HH * 32 + (i >> 1) * 4) * 7168u);
  }
  template <int BUF> __device__ __forceinline__ void dmaB1(int T, int pf) { dma16(ldsB + BUF * 8192 + pf * 4096, voffB, rb, sB + pf * 32768 + T * 128); }
  template <int NV, bool FIRST = false> __device__ __forceinline__ void compute() {
    constexpr int NM = 4 * NS + 8 * NW;
    __builtin_amdgcn_s_setprio(1);
#pragma unroll
    for (int k = 0; k < NM; ++k) {
      if (k == NM - EB && NV != -2) { __builtin_amdgcn_s_setprio(2); if (NV >= 0) WAIT_VN(NV < 0 ? 0 : NV); RBAR(); }
      const int f = k >> 2, m = (f & 1) ? 3 - (k & 3) : (k & 3);
      const int n = f < NS ? 0 : (f - NS) >> 1;
      const bf16x8& a = f < NS ? xa[m] : ((f - NS) & 1) ? wuf[n] : wgf[n];
      const bf16x8& b = f < NS ? bsf[f & 3] : xa[m];
      f32x4& c = f < NS ? as[f & 3][m] : ((f - NS) & 1) ? au[n][m] : ag[n][m];
      if (FIRST) MFMA0(a, b, c); else MFMA(a, b, c);
    }
    if (EB == 0 && NV != -2) { if (NV >= 0) WAIT_VN(NV < 0 ? 0 : NV); RBAR(); }
    __builtin_amdgcn_s_setprio(0);
    SCHED();
  }
  template <int BUF> __device__ __forceinline__ void dmaX(int T) {
#pragma unroll
    for (int s = 0; s < 4; ++s) dma16(ldsX + BUF * 32768 + s * 2048, voffX, rx, sX + T * 128 + s * 4096);
  }
  template <int BUF, int HH> __device__ __forceinline__ void dmaW(int T) {
#pragma unroll
    for (int s = 0; s < 2; ++s) {
      const unsigned so = sW + (unsigned)(T * 64 + HH * 32 + s * 4) * 7168u;
      dma16(ldsW + BUF * 32768 + HH * 8192 + s * 1024, voffW, rwg, so);
      dma16(ldsW + BUF * 32768 + 16384 + HH * 8192 + s * 1024, voffW, rwu, so);
    }
  }
  template <int BUF> __device__ __forceinline__ void dmaB(int T) {
#pragma unroll
    for (int pf = 0; pf < 2; ++pf) dma16(ldsB + BUF * 8192 + pf * 4096, voffB, rb, sB + pf * 32768 + T * 128);
  }
};

template <int BUF> __device__ __forceinline__ void p1_tile(P1<0>& w, int T) {
  w.template compute<7>();
  w.template load<BUF, 1>([&](int i) { if (i < 4) w.template dmaW1<BUF ^ 1, 1>(T + 1, i); else if (i == 4) w.pull(T); }); WAIT_L0(); BAR();
  w.template compute<5>();
  w.template load<BUF ^ 1, 0>([&](int i) { if (i < 4) w.template dmaX1<BUF>(T + 2, i); else w.template dmaB1<BUF>(T + 2, i - 4); }); WAIT_L0(); BAR();
}
template <int BUF> __device__ __forceinline__ void p1_tile(P1<1>& w, int T) {
  w.template load<BUF, 0>([&](int i) { if (i >= SO && i < SO + 4) w.template dmaX1<BUF ^ 1>(T + 1, i - SO); }); WAIT_L0(); BAR();
  w.template compute<-1>();
  w.template load<BUF, 1>([&](int i) { if (i >= SO && i < SO + 4) w.template dmaW1<BUF, 0>(T + 2, i - SO); }); WAIT_V(4); WAIT_L0(); BAR();
  w.template compute<-1>();
}

constexpr int NSTEP = 22;
struct P2 {
  f32x4 af[4][4], as[4][4];
  bf16x8 ha[2][4], bb[2][4];
  lptr aH, aS[2], aS2[2], aW[4], aC[2];
  unsigned voffH, voffW, voffC;
  unsigned sH, sHs, sW, sC, ldsA, ldsB, kk, pj[4], ps;
  rsrc_t rh, rw, rc, rc2_;
  template <int ST> __device__ __forceinline__ unsigned kf() const {
    constexpr int e = 2 * (ST - 8), k0 = ST < 8 ? 7 * (ST >> 1) + 3 * (ST & 1) + 1 : 7 * (e / 3) + 3 * (e % 3), k1 = ST < 8 ? k0 + 1 : 7 * ((e + 1) / 3) + 3 * ((e + 1) % 3);
    return k0 + kk * (k1 - k0);
  }
  template <int ST, int GA> __device__ __forceinline__ void dmaA(int j) {
    const unsigned so = ST < 14 ? sH + kf<ST>() * 8192u : sHs + ((ST - 14) & 3) * 16384u + ((ST - 14) >> 2) * 262144u;
    dma16_dev(ldsA + GA * 49152 + (ST % 3) * 16384 + j * 1024, voffH, rh, so + GA * (NKF * 8192u) + (ST < 14 ? pj[j] : (unsigned)(j << 10) ^ ps));
  }
  template <int ST> __device__ __forceinline__ void dmaB(int s) {
    const unsigned l = ldsB + (ST % 3) * 16384 + s * 2048;
    if (ST >= 14) dma16(l, voffC, ST < 18 ? rc : rc2_, sC + ((ST - 14) & 3) * 128u + s * 32768u);
    else dma16(l, voffW, rw, sW + kf<ST>() * 65536u + s * 16384u);
  }
  template <int ST, int GA> __device__ __forceinline__ void stageA() {
#pragma unroll
    for (int j = 0; j < 4; ++j) dmaA<ST, GA>(j);
  }
  template <int ST> __device__ __forceinline__ void stageB() {
#pragma unroll
    for (int s = 0; s < 4; ++s) dmaB<ST>(s);
  }
  template <int ST, int G> __device__ __forceinline__ void load() {
    constexpr int SL = (ST % 3) * 16384;
#pragma unroll
    for (int k2 = 0; k2 < 2; ++k2) {
#pragma unroll
      for (int n = 0; n < 4; ++n) {
        const int i = k2 * 4 + n;
        if constexpr (G == 0) { if constexpr (ST + 2 < NSTEP) { if (i < 4) dmaB<ST + 2>(i); else if (i < 6) dmaA<ST + 2, 1>(i - 4); } }
        else { if constexpr (ST + 2 < NSTEP) { if (i < 2) dmaA<ST + 2, 1>(i + 2); } if constexpr (ST + 3 < NSTEP) { if (i >= 2 && i < 6) dmaA<ST + 3, 0>(i - 2); } }
        if (ST >= 14) bb[k2][n] = lds_b128(aC[k2] + SL + n * 2048);
        else bb[k2][n] = lds_tr2(aW[n] + SL + k2 * 8192, aW[n] + SL + k2 * 8192 + 4096);
        if (ST >= 14) ha[k2][n] = lds_tr2(aS[n & 1] + SL + k2 * 8192 + (n >> 1) * 1024, aS2[n & 1] + SL + k2 * 8192 + (n >> 1) * 1024);
        else ha[k2][n] = lds_b128(aH + SL + k2 * 8192 + n * 1024);
      }
    }
  }
  template <int ST, int NV> __device__ __forceinline__ void compute() {
    __builtin_amdgcn_s_setprio(1);
#pragma unroll
    for (int k = 0; k < 32; ++k) {
      if (k == 32 - EB) { __builtin_amdgcn_s_setprio(2); WAIT_VN(NV); RBAR(); }
      const int k2 = k >> 4, m = (k >> 2) & 3, n = (m & 1) ? 3 - (k & 3) : (k & 3);
      f32x4& c = ST >= 14 ? as[m][n] : af[m][n];
      if ((ST == 0 || ST == 14) && k2 == 0) MFMA0(bb[k2][n], ha[k2][m], c); else MFMA(bb[k2][n], ha[k2][m], c);
    }
    if (EB == 0) { WAIT_VN(NV); RBAR(); }
    __builtin_amdgcn_s_setprio(0);
    SCHED();
  }
};
template <int I, int N, class F> __device__ __forceinline__ void static_for(F&& f) {
  if constexpr (I < N) { f(std::integral_constant<int, I>{}); static_for<I + 1, N>(f); }
}

__device__ __forceinline__ void arrive(char* ws, int lane, unsigned flag_off, int ctr, unsigned epoch) {
  if (lane == 0) {
    const unsigned old = __hip_atomic_fetch_add((LAS unsigned*)mk_l(L_FLG + 16 + ctr * 4), 1u, __ATOMIC_RELAXED, __HIP_MEMORY_SCOPE_WORKGROUP);
    if (old == 3u) __hip_atomic_store((GAS unsigned*)(ws + flag_off), epoch, RLX_AGENT);
  }
}
__device__ __forceinline__ void lds_post(int ctr) {
  if (fresh_lane() == 0) __hip_atomic_fetch_add((LAS unsigned*)mk_l(L_FLG + 16 + ctr * 4), 1u, __ATOMIC_RELAXED, __HIP_MEMORY_SCOPE_WORKGROUP);
}
__device__ __forceinline__ void lds_wait(int ctr) {
  const lptr p = mk_l(L_FLG + 16 + ctr * 4);
_Pragma("nounroll")
  for (unsigned spins = 0; spins < (SPIN_CAP << 6); ++spins) {
    if (__builtin_amdgcn_readfirstlane(*(volatile LAS unsigned*)p) == 4u) break;
    __builtin_amdgcn_s_sleep(1);
  }
  asm volatile("" ::: "memory");
}
__device__ __forceinline__ void wait_flags(const char* ws, unsigned off, unsigned epoch) {
_Pragma("nounroll")
  for (unsigned spins = 0; spins < SPIN_CAP; ++spins) {
    if (__all(__hip_atomic_load((const GAS unsigned*)(ws + off), RLX_AGENT) == epoch)) break;
    __builtin_amdgcn_s_sleep(1);
  }
  asm volatile("" ::: "memory");
}

struct Pre { unsigned voffX, voffW, voffB, sX, ldsX, sW, ldsW, sB, ldsB; };
__device__ __forceinline__ Pre mk_pre(int lane, int wid, int bid) {
  Pre pre;
  const int wr = wid & 3, G = wid >> 2, idx = bid >> 3, c = ((bid & 7) >> 1) * 8 + (idx & 7), rch = (bid & 1) * 4 + (idx >> 3);
  const unsigned sw8 = ((lane & 7) ^ (((wr & 1) * 4 + (lane >> 4)) & 7)) * 16;
  pre.voffX = ((lane >> 5) * 16 + tj(lane >> 3)) * 2048 + sw8;
  pre.voffB = (lane >> 3) * 2048 + sw8;
  pre.voffW = (lane >> 4) * 7168 + ((((lane & 15) >> 1) ^ ((lane >> 4) | ((wr & 1) << 2))) * 32) + (lane & 1) * 16;
  pre.sX = (unsigned)(rch * 256 + (G * 2 + (wr >> 1)) * 64 + (wr & 1) * 32) * 2048u;
  pre.ldsX = L_X + (G * 16 + (wr >> 1) * 8 + (wr & 1)) * 1024;
  const int ii = (wr & 1) * 2 + (wr >> 1) * 4;
  pre.sW = (unsigned)(ii * 4) * 7168u + (unsigned)((224 * c) & ~63);
  pre.ldsW = L_W + ii * 1024;
  pre.sB = (unsigned)(c * 32 + (wr & 1) * 8) * 2048u;
  pre.ldsB = L_BS + wr * 1024;
  return pre;
}
template <int GRP>
__device__ __forceinline__ void body(const Args& A, int wid, const char* code, const unsigned code_len, const u64 t00) {
  asm volatile("" : "+s"(wid));
  ST_DECL STAMP();
  const int xcd = blockIdx.x & 7, idx = blockIdx.x >> 3, c = (xcd >> 1) * 8 + (idx & 7), rch = (xcd & 1) * 4 + (idx >> 3);
  const int xrow = (xcd & 1) * 1024 + (xcd >> 1) * 256 + idx * 8;
  const int kq = xcd >> 1, rc2 = (xcd & 1) * 4 + (idx >> 3), dt = idx & 7;
  const int tt = 2 * rc2 + GRP, tile = rc2 * 8 + dt;
  const unsigned flb = (unsigned)WS_FL + rch * 1024, flc = (unsigned)WS_FL + rc2 * 1024;
  const int wr = wid & 3, wm = wr >> 1, wn = wr & 1;
  int lane = fresh_lane();
  unsigned epoch;
  P2 q;
  u32x4 hq[14]; unsigned ho[3];
  const rsrc_t rws = mk_rsrc(A.ws, (unsigned)WS_END);
  {
  P1<GRP> w;
  constexpr int NW = P1<GRP>::NW, NS = P1<GRP>::NS;
  int r = lane & 15, g = lane >> 4, qq = (lane & 15) >> 2, pp = lane & 3;
  const Pre pre = mk_pre(lane, wid, blockIdx.x);
  w.rx = mk_rsrc(A.x, L_ * D_ * 2); w.rwg = mk_rsrc(A.wg, D_ * F_ * 2); w.rwu = mk_rsrc(A.wu, D_ * F_ * 2);
  w.rb = mk_rsrc((wr >> 1) ? A.bim : A.bre, P_ * D_ * 2);
  w.voffX = pre.voffX; w.voffB = pre.voffB; w.voffW = pre.voffW;
  w.sX = pre.sX; w.ldsX = pre.ldsX; w.sW = pre.sW; w.ldsW = pre.ldsW; w.sB = pre.sB; w.ldsB = pre.ldsB;
  const char* wbase = (const char*)((wr & 1) ? A.wu : A.wg) + (idx * 2 + (wr >> 1)) * 7168 + xcd * 896;
  const char* rbase = wr < 2 ? (const char*)A.x + (xrow + wr * 4) * 2048 : (const char*)(wr == 2 ? A.bre : A.bim) + (xcd * 128 + idx * 4) * 2048;
  if (GRP == 1) {
    w.template dmaW<0, 1>(0); w.template dmaW<1, 0>(1);
    const unsigned co = ((idx * 4 + wr) * 4 + (lane & 3)) * 128u;
    touch(lane < 16 ? wbase + (2 + (lane >> 3)) * 458752u + ((lane & 7) < 7 ? (lane & 7) : 0) * 128 : lane < 24 ? rbase + ((lane - 16) >> 1) * 2048 + (2 + (lane & 1)) * 128
                    : code + (co < code_len ? co : 0u));
  }
  {
#pragma unroll
    for (int ks = 0; ks < 2; ++ks) {
      const unsigned ch = ((g + 4 * ks) ^ ((r >> 1) & 7)) << 4;
      w.aX[ks] = mk_l(L_X + (wr * 64 + r) * 128 + ch);
      if (NS) w.aB[ks] = mk_l(L_BS + r * 128 + ch);
    }
    const unsigned swz = qq | ((g & 1) << 2);
#pragma unroll
    for (int n = 0; n < NW; ++n) {
      unsigned col;
      if (GRP == 0) col = (((2 + n) ^ swz) << 5) + pp * 8;
      else if (n < 2) col = (((((c & 1) ? 6 : 0) + n) ^ swz) << 5) + pp * 8;
      else col = (((((c & 1) ? 0 : 6) + (pp >> 1)) ^ swz) << 5) + (c & 1) * 16 + (pp & 1) * 8;
      w.aW[n] = mk_l(L_W + (8 * g + qq) * 256 + col);
    }
  }
  if constexpr (GRP == 0) {
    WAIT_V(0); BAR();
    w.template load<0, 0>([&](int i) { if (i < 4) w.template dmaX1<1>(1, i); else w.template dmaB1<1>(1, i - 4); });
    unsigned ep0, plr, pli, pld;
    {
      const unsigned po = (c * 32 + (lane & 31)) * 2;
      asm volatile("global_load_dword %0, %1, %2 sc1" : "=v"(ep0) : "v"((unsigned)WS_EP), "s"(A.ws) : "memory");
      asm volatile("global_load_ushort %0, %1, %2" : "=v"(plr) : "v"(po), "s"(A.lre) : "memory");
      asm volatile("global_load_ushort %0, %1, %2" : "=v"(pli) : "v"(po), "s"(A.lim) : "memory");
      asm volatile("global_load_ushort %0, %1, %2" : "=v"(pld) : "v"(po), "s"(A.ldt) : "memory");
    }
    WAIT_L0(); BAR();
    STAMP(); TS2();
    w.template compute<-1, true>();
    w.template load<0, 1>([&](int i) { if (i < 4) w.template dmaW1<1, 1>(1, i); });
    {
      const unsigned j = idx * 4 + wr, co = (j * 4 + (lane & 3)) * 128u;
      const int l = lane < 32 ? lane : 31;
      if (l < 7) { w.pfp = wbase + l * 128; w.pfs = 458752u; w.pfo = PFD; w.pfm = 15; }
      else if (l < 15) { w.pfp = rbase + ((l - 7) >> 1) * 2048 + ((l - 7) & 1) * 128; w.pfs = 256u; w.pfo = 2; w.pfm = 7; }
      else if (l < 23) { w.pfp = (const char*)A.wd + (kq * 896 + j) * 2048u + (xcd & 1) * 1024 + (l - 15) * 128; w.pfs = 262144u; w.pfo = -7; w.pfm = 6; }
      else if (l < 27) { w.pfp = (const char*)((j & 1) ? A.cim : A.cre) + ((xcd & 1) * 512 + (j >> 1)) * 2048u + kq * 512 + (l - 23) * 128; w.pfs = 131072u; w.pfo = 0; w.pfm = 7; }
      else { w.pfp = l < 31 ? code + (co < code_len ? co : 0u) : (const char*)((idx & 4) ? A.dsk : A.gw) + ((idx & 3) * 4 + wr) * 128; w.pfs = 0u; w.pfo = 0; w.pfm = 0; }
      w.pull(0);
    }
    WAIT_L0(); BAR();
    w.template compute<5>();
    w.template load<1, 0>([&](int i) { if (i < 4) w.template dmaX1<0>(2, i); else w.template dmaB1<0>(2, i - 4); });
    PIN(ep0); PIN(plr); PIN(pli); PIN(pld);
    epoch = __builtin_amdgcn_readfirstlane(ep0) + 1u;
    f32x2 lam, l8, l256; f32x4 p0, p1, p2, t0_, t1_;
    {
      const float lr = bf2f(plr), li = bf2f(pli);
      const float dt_ = bf2f(pk_bf16(__expf(bf2f(pld)), 0.f) & 0xffffu);
      const float e = __expf(dt_ * lr), rev = __builtin_amdgcn_fractf(dt_ * li * 0.15915494309189535f);
      lam = (f32x2){e * __builtin_amdgcn_cosf(rev), -e * __builtin_amdgcn_sinf(rev)};
      const float nr = lam.x - 1.f, rden = 1.f / (lr * lr + li * li);
      p0 = (f32x4){lam.x, lam.y, (nr * lr - lam.y * li) * rden, (-lam.y * lr - nr * li) * rden}; PIN(p0);
      l8 = cmul(lam, lam); l8 = cmul(l8, l8); l8 = cmul(l8, l8); PIN(l8);
    }
    WAIT_L0(); BAR(); TS2();
    w.template compute<7>();
    if (wr == 2) {
      __hip_atomic_store((GAS u64*)(A.ws + WS_AG) + (c * 8 + 7 - rch) * 64 + lane, (u64)(epoch - 1u) << 32, RLX_AGENT);
      if (lane < 6) __hip_atomic_store((GAS unsigned*)(A.ws + flb + lane * 128 + c * 4), epoch - 1u, RLX_AGENT);
      else if (lane >= 32 && (lane & 3)) __hip_atomic_store((GAS unsigned*)(A.ws + WS_XF + (tile * 4 + (kq ^ (lane & 3))) * 128) + (lane - 32), epoch - 1u, RLX_AGENT);
    }
    w.template load<1, 1>([&](int i) { if (i < 4) w.template dmaW1<0, 1>(2, i); else if (i == 4) w.pull(1); });
    {
      const f32x2 l16 = cmul(l8, l8), l32 = cmul(l16, l16), l64 = cmul(l32, l32), l128 = cmul(l64, l64);
      l256 = cmul(l128, l128); p1 = (f32x4){l16.x, l16.y, l32.x, l32.y}; p2 = (f32x4){l8.x, l8.y, l64.x, l64.y};
      f32x2 pw = (wr & 1) ? l64 : (f32x2){1.f, 0.f}; if (wr & 2) pw = cmul(pw, l128);
      const f32x2 q0 = (lane & 32) ? l32 : (f32x2){1.f, 0.f}, q1 = cmul(q0, l16), c0 = cmul(q0, pw), c1 = cmul(q1, pw);
      t0_ = (f32x4){q0.x, q0.y, c0.x, c0.y}; t1_ = (f32x4){q1.x, q1.y, c1.x, c1.y};
      PIN(l256); PIN(p1); PIN(p2); PIN(t0_); PIN(t1_);
    }
    WAIT_L0(); BAR();
    w.template compute<5>();
    {
      const lptr tp = mk_l(L_TAB + wr * 2048 + (lane >> 5) * 1024 + (lane & 31) * 16);
      *(LAS f32x4*)tp = t0_; *(LAS f32x4*)(tp + 512) = t1_;
      if (wr == 0) {
        const lptr pb = mk_l(L_PRM + (lane & 31) * 64);
        *(LAS f32x4*)pb = p0; *(LAS f32x4*)(pb + 16) = p1; *(LAS f32x4*)(pb + 32) = p2; *(LAS f32x2*)(pb + 48) = l256;
      } else if (wr == 1) *(LAS unsigned*)mk_l(L_FLG + (lane & 7) * 4) = (lane & 7) == 3 ? epoch : 0u;
    }
    w.template load<0, 0>([&](int i) { if (i < 4) w.template dmaX1<1>(3, i); else w.template dmaB1<1>(3, i - 4); }); WAIT_L0(); BAR();
  } else {
    WAIT_V(9); BAR(); BAR();
    STAMP(); TS2();
    w.template load<0, 0>([&](int i) { if (i >= SO && i < SO + 4) w.template dmaX1<1>(1, i - SO); }); WAIT_V(9); WAIT_L0(); BAR();
    w.template compute<-1, true>();
    w.template load<0, 1>([&](int i) { if (i >= SO && i < SO + 4) w.template dmaW1<0, 0>(2, i - SO); }); WAIT_V(4); WAIT_L0(); BAR();
    w.template compute<-1>(); TS2();
    p1_tile<1>(w, 1);
  }
  for (int T = 2; T < 14; T += 2) {
    TS2(); p1_tile<0>(w, T); TS2();
    p1_tile<1>(w, T + 1);
  }
  if (GRP == 1) epoch = __builtin_amdgcn_readfirstlane(*(const LAS unsigned*)mk_l(L_FLG + 12));
  STAMP();
  lane = fresh_lane();
  q.rh = mk_rsrc(A.ws + WS_H, (unsigned)H_BYTES); q.rw = mk_rsrc(A.wd, F_ * D_ * 2); q.rc = mk_rsrc((kq >> 1) ? A.cim : A.cre, D_ * P_ * 2);
  q.voffW = (lane >> 4) * 2048 + ((((lane & 15) >> 1) ^ (((wr & 1) << 2) | (lane >> 4))) * 32) + (lane & 1) * 16;
  {
    const int i0 = (wr & 1) + (wr >> 1) * 8;
    q.kk = wr >> 1;
    q.sH = (unsigned)(2 * rc2 * NKF + 28 * kq) * 8192u + (wr & 1) * 4096u;
    q.sHs = (unsigned)(2 * rc2 * NKF + 112 + 16 * kq + (wr >> 1)) * 8192u + (wr & 1) * 4096u;
    q.sW = (unsigned)(28 * kq * 32 + (wr & 1) * 4) * 2048u + dt * 256u;
    q.sC = (unsigned)(dt * 128 + i0 * 8) * 2048u + (kq & 1) * 1024u;
#pragma unroll
    for (int j = 0; j < 4; ++j) q.pj[j] = (unsigned)(j ^ kq) << 10;
    q.rc = mk_rsrc(A.cre, D_ * P_ * 2); q.rc2_ = mk_rsrc(A.cim, D_ * P_ * 2);
    q.sHs = (unsigned)(2 * rc2 * NKF + 112 + 8 * kq + (wr >> 1)) * 8192u + (wr & 1) * 4096u;
    q.sC = (unsigned)(dt * 128 + i0 * 8) * 2048u + kq * 512u;
    q.ps = (unsigned)(kq >> 1) << 10;
    q.ldsA = 49152 + wr * 4096; q.ldsB = i0 * 1024;
  }
  u32x4 gx[4], gg[4];
  const int tg = rch * 256 + c * 8 + wr * 2 + (lane >> 5);
  if constexpr (GRP == 0) {
    w.template compute<7>();
    w.template load<0, 1>(); w.template dmaW<1, 1>(15);
#pragma unroll
    for (int i = 0; i < 4; ++i) gx[i] = *(const u32x4*)(A.x + tg * D_ + i * 256 + (lane & 31) * 8);
    WAIT_L0(); BAR();
    w.template compute<8>();
    w.template load<1, 0>(); q.template stageB<0>();
#pragma unroll
    for (int i = 0; i < 4; ++i) gg[i] = *(const u32x4*)(A.gw + i * 256 + (lane & 31) * 8);
    WAIT_L0(); BAR();
    w.template compute<12>();
    w.template load<1, 1>();
    {
      typedef __bf16 bf2_t __attribute__((ext_vector_type(2)));
      float s0 = 0.f, s1 = 0.f;
#pragma unroll
      for (int i = 0; i < 4; ++i)
#pragma unroll
        for (int j = 0; j < 4; j += 2) {
          const unsigned a0 = gx[i][j], b0 = gg[i][j], a1 = gx[i][j + 1], b1 = gg[i][j + 1];
          s0 = __builtin_amdgcn_fdot2_f32_bf16(__builtin_bit_cast(bf2_t, a0), __builtin_bit_cast(bf2_t, b0), s0, false);
          s1 = __builtin_amdgcn_fdot2_f32_bf16(__builtin_bit_cast(bf2_t, a1), __builtin_bit_cast(bf2_t, b1), s1, false);
        }
      float s = s0 + s1;
      s += dpp<0x128, 0xf>(0.f, s); s += dpp<0x124, 0xf>(0.f, s); s += dpp<0x122, 0xf>(0.f, s); s += dpp<0x121, 0xf>(0.f, s);
      float t0, t1; sw16(s, t0, t1); s = t0 + t1;
      if ((lane & 31) == 0) __hip_atomic_store((GAS unsigned*)(A.ws + WS_G) + tg, __float_as_uint(1.f / (1.f + __expf(-s))), RLX_AGENT);
    }
    WAIT_L0(); BAR();
    w.template compute<-2>();
    __builtin_amdgcn_s_setprio(3);
  } else {
    w.template load<0, 0>(); w.template dmaX<1>(15); WAIT_L0(); BAR();
    w.template compute<-1>();
    w.template load<0, 1>(); WAIT_V(0); WAIT_L0(); BAR();
    w.template compute<-1>();
  }
  lane = fresh_lane(); r = lane & 15; g = lane >> 4; qq = (lane & 15) >> 2; pp = lane & 3;
  const unsigned hb = (unsigned)WS_H + (unsigned)(2 * rch + (wr >> 1)) * NKF * 8192u + (wr & 1) * 4096u;
  const int kf0 = 7 * (c >> 1) + 3 * (c & 1);
  q.voffH = lane * 16;
#define FFN_H(n, m, h) pk_bf16(relu(w.ag[n][m][2 * (h)]) * w.au[n][m][2 * (h)], relu(w.ag[n][m][2 * (h) + 1]) * w.au[n][m][2 * (h) + 1])
#define P2_ADDR() do { q.voffC = (lane >> 3) * 2048 + (((lane & 7) ^ (((wr & 1) * 4 + (lane >> 4)) & 7)) * 16); PIN(q.voffC); \
    q.aH = mk_l(49152 + GRP * 49152 + wm * 4096 + lane * 16); \
    _Pragma("unroll") for (int n = 0; n < 4; ++n) q.aW[n] = mk_l((4 * g + qq) * 256 + (((wn * 4 + n) ^ (4 * (g & 1) + qq)) << 5) + pp * 8); } while (0)
#define PACK_S() do { _Pragma("unroll") for (int m = 0; m < 4; ++m) { hq[m] = (u32x4){FFN_H(0, m, 0), FFN_H(0, m, 1), FFN_H(1, m, 0), FFN_H(1, m, 1)}; PIN(hq[m]); } \
    _Pragma("unroll") for (int m = 0; m < 4; m += 2) {     \
      const auto s0 = __builtin_amdgcn_permlane32_swap(FFN_H(2, m, 0), FFN_H(2, m + 1, 0), false, false); \
      const auto s1 = __builtin_amdgcn_permlane32_swap(FFN_H(2, m, 1), FFN_H(2, m + 1, 1), false, false); \
      hq[4 + m / 2] = (u32x4){s0[0], s1[0], s0[1], s1[1]}; PIN(hq[4 + m / 2]); } \
    ho[0] = lane * 16 + hb + (kf0 + ((c & 1) ? 3 : 0)) * 8192u; \
    ho[1] = (lane & 31) * 16 + (lane >> 5) * 1024 + (c & 1) * 512 + hb + (kf0 + ((c & 1) ? 0 : 3)) * 8192u; \
    ho[2] = (lane ^ ((lane >> 5) << 2)) * 16 + hb + (112 + c) * 8192u; } while (0)
  if constexpr (GRP == 0) {
    MFMA_DRAIN();
    STAMP();
#pragma unroll
    for (int f = 0; f < 2; ++f)
#pragma unroll
      for (int m = 0; m < 4; ++m) {
        __builtin_amdgcn_raw_buffer_store_b128((u32x4){FFN_H(2 * f, m, 0), FFN_H(2 * f, m, 1), FFN_H(2 * f + 1, m, 0), FFN_H(2 * f + 1, m, 1)}, rws,
                                               lane * 16 + m * 1024 + hb + (kf0 + 1 + f) * 8192u, 0, 16);
        SCHED();
      }
    STAMP();
    WAIT_V(0); arrive(A.ws, lane, flb + (c >> 3) * 128 + c * 4, 0, epoch);
    __builtin_amdgcn_s_setprio(0);
    BAR();
    STAMP();
    P2_ADDR();
    wait_flags(A.ws, flc + kq * 160 + (lane & 7) * 4, epoch);
    STAMP();
    q.template stageA<0, 0>(); q.template stageA<0, 1>(); q.template stageA<1, 0>();
    q.template stageA<1, 1>(); q.template stageA<2, 0>();
    STAMP();
  } else {
    bf16x8 xb[4], bs1[4], wg1[3], wu1[3];
#pragma unroll
    for (int m = 0; m < 4; ++m) { w.xa[m] = lds_b128(w.aX[0] + 32768 + m * 2048); xb[m] = lds_b128(w.aX[1] + 32768 + m * 2048); }
#pragma unroll
    for (int i = 0; i < 4; ++i) { w.bsf[i] = lds_b128(w.aB[0] + 8192 + i * 2048); bs1[i] = lds_b128(w.aB[1] + 8192 + i * 2048); }
    w.wgf[0] = lds_tr2(w.aW[0] + 32768, w.aW[0] + 32768 + 1024); w.wuf[0] = lds_tr2(w.aW[0] + 49152, w.aW[0] + 49152 + 1024);
    q.template stageB<1>(); WAIT_L0(); BAR();
    __builtin_amdgcn_s_setprio(1);
#pragma unroll
    for (int i = 0; i < 4; ++i)
#pragma unroll
      for (int m = 0; m < 4; ++m) MFMA(w.xa[m], w.bsf[i], w.as[i][m]);
#pragma unroll
    for (int i = 0; i < 4; ++i)
#pragma unroll
      for (int m = 0; m < 4; ++m) MFMA(xb[m], bs1[i], w.as[i][m]);
#pragma unroll
    for (int m = 0; m < 4; ++m) MFMA(w.wgf[0], w.xa[m], w.ag[0][m]);
#pragma unroll
    for (int m = 0; m < 4; ++m) MFMA(w.wuf[0], w.xa[m], w.au[0][m]);
    __builtin_amdgcn_s_setprio(0);
    BAR();
    STAMP();
    f32x4 pl[2], pm[2];
    f32x2 l8[2], cin[2], ea[2], agg[2];
#pragma unroll
    for (int pf = 0; pf < 2; ++pf) {
      const lptr pb = mk_l(L_PRM + (pf * 16 + r) * 64);
      pl[pf] = *(const LAS f32x4*)pb; pm[pf] = *(const LAS f32x4*)(pb + 16); l8[pf] = *(const LAS f32x2*)(pb + 32);
    }
#pragma unroll
    for (int n = 1; n < 3; ++n) { w.wgf[n] = lds_tr2(w.aW[n] + 32768, w.aW[n] + 32768 + 1024); w.wuf[n] = lds_tr2(w.aW[n] + 49152, w.aW[n] + 49152 + 1024); }
    SCHED();
#define PAIR(v, k) ((k) & 1 ? (f32x2){(v)[(k) >> 1][2], (v)[(k) >> 1][3]} : (f32x2){(v)[(k) >> 1][0], (v)[(k) >> 1][1]})
#define CSTEP(P, Q, pf, k) do { const f32x2 lx_ = {pl[pf][0], pl[pf][0]}, ly_ = {pl[pf][1], pl[pf][1]}; \
      const f32x2 a_ = __builtin_elementwise_fma(-ly_, Q, PAIR(w.as[2 * (pf)], k)), b_ = __builtin_elementwise_fma(ly_, P, PAIR(w.as[2 * (pf) + 1], k)); \
      P = __builtin_elementwise_fma(lx_, P, a_); Q = __builtin_elementwise_fma(lx_, Q, b_); } while (0)
    {
#pragma unroll
      for (int pf = 0; pf < 2; ++pf)
#pragma unroll
        for (int m = 0; m < 4; ++m) {
          const f32x4 re = w.as[2 * pf][m], im = w.as[2 * pf + 1][m];
          w.as[2 * pf][m] = re * pl[pf][2] - im * pl[pf][3]; w.as[2 * pf + 1][m] = -(re * pl[pf][3]) - im * pl[pf][2];
          PIN(w.as[2 * pf][m]); PIN(w.as[2 * pf + 1][m]);
        }
      f32x2 P[2], Q[2];
#pragma unroll
      for (int k = 0; k < 8; ++k)
#pragma unroll
        for (int pf = 0; pf < 2; ++pf) {
          if (k == 0) { P[pf] = PAIR(w.as[2 * pf], 0); Q[pf] = PAIR(w.as[2 * pf + 1], 0); } else CSTEP(P[pf], Q[pf], pf, k);
        }
#pragma unroll
      for (int pf = 0; pf < 2; ++pf) {
        ea[pf] = (f32x2){P[pf].x, Q[pf].x};
        const f32x2 e = cfma(l8[pf], ea[pf], (f32x2){P[pf].y, Q[pf].y});
        const f32x2 l16 = {pm[pf][0], pm[pf][1]}, l32 = {pm[pf][2], pm[pf][3]};
        float a0, a1, b0, b1; sw16(e.x, a0, a1); sw16(e.y, b0, b1);
        const f32x2 r0 = {a0, b0};
        const f32x2 xx = cfma(l16, r0, (f32x2){a1, b1});
        float c0, c1, d0, d1; sw32(xx.x, c0, c1); sw32(xx.y, d0, d1);
        const f32x2 s0 = {c0, d0};
        agg[pf] = cfma(l32, s0, (f32x2){c1, d1});
        const f32x2 c3 = cfma(l16, s0, r0);
        cin[pf] = g == 0 ? (f32x2){0.f, 0.f} : g == 1 ? r0 : g == 2 ? s0 : c3;
        PIN(ea[pf]);
      }
    }
    f32x2 aggq = (g & 1) ? agg[1] : agg[0]; PIN(aggq); PIN(cin[0]); PIN(cin[1]);
    if (wr < 3) {
      if (g < 2) *(LAS f32x2*)mk_l(L_WA + ((g * 16 + r) * 4 + wr) * 8) = aggq;
      WAIT_L0();
      *(volatile LAS unsigned*)mk_l(L_FLG + wr * 4) = 1u;
    }
    SCHED();
#pragma unroll
    for (int n = 0; n < 3; ++n) { wg1[n] = lds_tr2(w.aW[n] + 40960, w.aW[n] + 40960 + 1024); wu1[n] = lds_tr2(w.aW[n] + 57344, w.aW[n] + 57344 + 1024); }
    if (wr == 3) {
      const lptr fp = mk_l(L_FLG);
_Pragma("nounroll")
      for (unsigned spins = 0; spins < SPIN_CAP; ++spins) {
        const u32x4 f = *(volatile LAS u32x4*)fp;
        if (__builtin_amdgcn_readfirstlane(f[0] & f[1] & f[2])) break;
      }
      asm volatile("" ::: "memory");
      const lptr ap = mk_l(L_WA + ((g & 1) * 16 + r) * 32);
      const f32x4 a01 = *(const LAS f32x4*)ap; const f32x2 a2 = *(const LAS f32x2*)(ap + 16);
      const f32x2 lw = *(const LAS f32x2*)mk_l(L_PRM + ((g & 1) * 16 + r) * 64 + 40);
      const f32x2 t = cfma(lw, cfma(lw, cfma(lw, (f32x2){a01[0], a01[1]}, (f32x2){a01[2], a01[3]}), a2), aggq);
      if (rch < 7) __hip_atomic_store((GAS u64*)(A.ws + WS_AG) + (c * 8 + 7 - rch) * 64 + lane, ((u64)epoch << 32) | __float_as_uint((g & 2) ? t.y : t.x), RLX_AGENT);
    }
    WAIT_L0(); SCHED();
    STAMP();
    __builtin_amdgcn_s_setprio(1);
#pragma unroll
    for (int n = 1; n < 3; ++n) {
#pragma unroll
      for (int m = 0; m < 4; ++m) MFMA(w.wgf[n], w.xa[m], w.ag[n][m]);
#pragma unroll
      for (int m = 0; m < 4; ++m) MFMA(w.wuf[n], w.xa[m], w.au[n][m]);
    }
#pragma unroll
    for (int n = 0; n < 3; ++n) {
#pragma unroll
      for (int m = 0; m < 4; ++m) MFMA(wg1[n], xb[m], w.ag[n][m]);
#pragma unroll
      for (int m = 0; m < 4; ++m) MFMA(wu1[n], xb[m], w.au[n][m]);
    }
    BAR();
    __builtin_amdgcn_s_setprio(2);
    lane = fresh_lane(); r = lane & 15; g = lane >> 4; qq = (lane & 15) >> 2; pp = lane & 3;
    q.voffH = lane * 16;
    const rsrc_t rag = mk_rsrc(A.ws + WS_AG + c * 4096, 4096u);
    const unsigned ago = lane * 8 + (8 - rch) * 512;
    u32x2 gv_[7];
#pragma unroll
    for (int cc = 0; cc < 7; ++cc) gv_[cc] = __builtin_amdgcn_raw_buffer_load_b64(rag, ago + cc * 512, 0, 16);
    STAMP();
    f32x4 p2[2], tb4[2], a01[2]; f32x2 a2[2];
    const f32x2 l256q = *(const LAS f32x2*)mk_l(L_PRM + ((g & 1) * 16 + r) * 64 + 48);
    {
      const lptr pb = mk_l(L_PRM + r * 64), ap = mk_l(L_WA + r * 32), tp = mk_l(L_TAB + wr * 2048 + g * 512 + r * 16);
#pragma unroll
      for (int pf = 0; pf < 2; ++pf) {
        pl[pf] = *(const LAS f32x4*)(pb + pf * 1024); p2[pf] = *(const LAS f32x4*)(pb + pf * 1024 + 32); tb4[pf] = *(const LAS f32x4*)(tp + pf * 256);
        a01[pf] = *(const LAS f32x4*)(ap + pf * 512); a2[pf] = *(const LAS f32x2*)(ap + pf * 512 + 16);
      }
    }
    SCHED();
    PACK_S();
    P2_ADDR();
    PIN(ho[0]); PIN(ho[1]); PIN(ho[2]);
    SCHED();
    f32x2 car[2], cai[2], cmr[2], cmi[2];
#pragma unroll
    for (int pf = 0; pf < 2; ++pf) {
      const f32x2 l8_ = {p2[pf][0], p2[pf][1]}, l64 = {p2[pf][2], p2[pf][3]};
      f32x2 sw = wr > 0 ? (f32x2){a01[pf][0], a01[pf][1]} : (f32x2){0.f, 0.f};
      if (wr > 1) sw = cfma(l64, sw, (f32x2){a01[pf][2], a01[pf][3]});
      if (wr > 2) sw = cfma(l64, sw, a2[pf]);
      const f32x2 ca = cfma((f32x2){tb4[pf][0], tb4[pf][1]}, sw, cin[pf]), cb_ = cfma(l8_, ca, ea[pf]), cm = {tb4[pf][2], tb4[pf][3]}, cn = cmul(l8_, cm);
      car[pf] = (f32x2){ca.x, cb_.x}; cai[pf] = (f32x2){ca.y, cb_.y}; cmr[pf] = (f32x2){cm.x, cn.x}; cmi[pf] = (f32x2){cm.y, cn.y};
      PIN(car[pf]); PIN(cai[pf]); PIN(cmr[pf]); PIN(cmi[pf]);
    }
    STAMP();
_Pragma("nounroll")
    for (unsigned spins = 0; spins < SPIN_CAP; ++spins) {
      __builtin_amdgcn_s_waitcnt(0x0F70);
      unsigned bad = 0;
#pragma unroll
      for (int cc = 0; cc < 7; ++cc) bad |= gv_[cc][1] ^ (cc < rch ? epoch : 0u);
      if (__all(bad == 0u)) break;
      __builtin_amdgcn_s_sleep(1);
      asm volatile("" ::: "memory");
#pragma unroll
      for (int cc = 0; cc < 7; ++cc) gv_[cc] = __builtin_amdgcn_raw_buffer_load_b64(rag, ago + cc * 512, 0, 16);
    }
    __builtin_amdgcn_s_waitcnt(0x0F70);
    STAMP();
    f32x2 cb;
    {
      float are[7], aim[7];
#pragma unroll
      for (int cc = 0; cc < 7; ++cc) { const unsigned v = gv_[cc][0]; sw32(__uint_as_float(v), are[cc], aim[cc]); }
      cb = (f32x2){are[6], aim[6]};
#pragma unroll
      for (int cc = 5; cc >= 0; --cc) cb = cfma(l256q, cb, (f32x2){are[cc], aim[cc]});
    }
    unsigned hr[2][8], hi[2][8];
    {
      float cx[2], cy[2]; sw16(cb.x, cx[0], cx[1]); sw16(cb.y, cy[0], cy[1]);
      f32x2 P[2], Q[2];
#pragma unroll
      for (int pf = 0; pf < 2; ++pf) { P[pf] = car[pf] + cmr[pf] * cx[pf] - cmi[pf] * cy[pf]; Q[pf] = cai[pf] + cmr[pf] * cy[pf] + cmi[pf] * cx[pf]; }
#pragma unroll
      for (int k = 0; k < 8; ++k)
#pragma unroll
        for (int pf = 0; pf < 2; ++pf) { CSTEP(P[pf], Q[pf], pf, k); hr[pf][k] = pk_bf16(P[pf].x, P[pf].y); hi[pf][k] = pk_bf16(Q[pf].x, Q[pf].y); }
    }
#pragma unroll
    for (int i = 0; i < 8; ++i) {
      const unsigned* h = (i >> 2) ? hi[(i >> 1) & 1] : hr[(i >> 1) & 1];
      hq[6 + i] = (u32x4){h[(i & 1) * 4], h[(i & 1) * 4 + 1], h[(i & 1) * 4 + 2], h[(i & 1) * 4 + 3]}; PIN(hq[6 + i]);
    }
    STAMP(); TS2();
    WAIT_V(0); lds_wait(1);
    STAMP();
  }
  }
  if (GRP == 0) { STAMP(); TS2(); }
  constexpr int G = GRP;
  int r, g;
  unsigned fl = 0u;
#define FLAG_LOAD(off) asm volatile("global_load_dword %0, %1, %2 sc1" : "+v"(fl) : "v"(off), "s"(A.ws) : "memory")
#define FLAG_CHECK(off) do { PIN(fl); if (!__all(fl == epoch)) wait_flags(A.ws, off, epoch); } while (0)
#define HQ_ST(i) __builtin_amdgcn_raw_buffer_store_b128(hq[i], rws, (i) < 4 ? ho[0] + (i) * 1024 : (i) < 6 ? ho[1] + ((i) - 4) * 2048 : ho[2] + (((i) - 6) >> 2) * 262144 + (((i) - 6) & 3) * 1024, 0, 16)
#define XTRA(st) ((st) == 13 ? 4 : 0)
  float gv[4];
  u32x2 xv[4], dv; float gr[4];
  const int t0 = tt * 128 + wm * 64 + kq * 2, dc0 = dt * 128 + wn * 64;
  auto side = [&](auto ST_) {
    constexpr int ST = decltype(ST_)::value;
    if constexpr (ST == 13) {
      const int l = fresh_lane(), g_ = l >> 4, q_ = (l & 15) >> 2, p_ = l & 3;
#pragma unroll
      for (int e = 0; e < 2; ++e) {
        const unsigned b = 49152 + G * 49152 + wm * 4096 + (g_ >> 1) * 2048 + p_ * 256 + 8 * (g_ & 1) * 16 + q_ * 16 + (e ^ (kq & 1)) * 8;
        q.aS[e] = mk_l(b + ((p_ >> 1) << 6)); q.aS2[e] = mk_l(b + (((p_ >> 1) ^ 1) << 6));
        q.aC[e] = mk_l((wn * 64 + (l & 15)) * 128 + (((g_ + 4 * e) ^ (((l & 15) >> 1) & 7)) << 4));
      }
    }
    if constexpr (ST == 13) {
      const int l = fresh_lane();
      const unsigned go = (unsigned)WS_G + (tt * 128 + wm * 64 + ((l & 15) >> 2) * 16 + tj(l)) * 4;
#pragma unroll
      for (int j = 0; j < 4; ++j) asm volatile("global_load_dword %0, %1, %2 sc1" : "=v"(gv[j]) : "v"(go + (q.pj[j] >> 7)), "s"(A.ws) : "memory");
    }
    if constexpr (ST >= 16 && ST < 20) {
      constexpr int m = ST - 16;
      PIN(gv[m]);
#pragma unroll
      for (int n = 0; n < 4; ++n) { q.af[m][n] *= 1.f - gv[m]; PIN(q.af[m][n]); }
    }
    if constexpr (ST == NSTEP - 1) {
      const int l = fresh_lane();
      dv = *(const u32x2*)(A.dsk + dc0 + (l & 15) * 4);
#pragma unroll
      for (int i = 0; i < 4; ++i) {
        xv[i] = *(const u32x2*)(A.x + (t0 + i * 16 + tj(l >> 4)) * D_ + dc0 + (l & 15) * 4);
        gr[i] = __int_as_float(__builtin_amdgcn_ds_bpermute((4 * i + (l >> 4)) * 4, __float_as_int(gv[0])));
      }
#pragma unroll
      for (int j = 0; j < 4; ++j) { gv[j] *= 2.f; PIN(gv[j]); }
    }
  };
  __builtin_amdgcn_s_setprio(0);
  if constexpr (G == 0) {
    const unsigned f2 = flc + 512 + (8 * kq + (lane & 7)) * 4, f3 = flc + 640 + (lane & 31) * 4;
    WAIT_V(16); lds_post(1); lds_wait(1); STAMP();
    q.template load<0, 0>(); WAIT_V(18); WAIT_L0(); lds_post(2); SCHED();
    static_for<0, NSTEP - 1>([&](auto ST_) {
      constexpr int ST = decltype(ST_)::value;
      constexpr int prev = (ST + 2 < NSTEP ? 6 : 0) + (ST == 4 || ST == 10 ? 1 : 0) + XTRA(ST) + (DEC && ST == 0 ? 8 : 0);
      q.template compute<ST, prev>();
      q.template load<ST + 1, 0>();
      if constexpr (DEC && ST == 0) WAIT_V(16);
      if constexpr (ST + 1 == 4) FLAG_LOAD(f2);
      if constexpr (ST + 1 == 5) { WAIT_V(6); FLAG_CHECK(f2); }
      if constexpr (ST + 1 == 10) FLAG_LOAD(f3);
      if constexpr (ST + 1 == 11) { WAIT_V(6); FLAG_CHECK(f3); }
      if constexpr (ST + 1 == 20) { if (blockIdx.x == 255 && wr == 0 && lane == 0) __hip_atomic_store((GAS unsigned*)(A.ws + WS_EP), epoch, RLX_AGENT); }
      side(std::integral_constant<int, ST + 1>{});
      WAIT_L0(); BAR(); TS2();
      if constexpr (ST == 1 || ST == 13) STAMP();
    });
  } else {
    HQ_ST(0); HQ_ST(1); HQ_ST(2);
    lds_wait(2);
    static_for<0, NSTEP>([&](auto ST_) {
      constexpr int ST = decltype(ST_)::value;
      constexpr int nst = ST == 0 ? 3 : ST <= 4 ? 2 : 0;
      if constexpr (ST == 2 || ST == 6) {
        if (wr == 0 && lane == 0) __hip_atomic_store((GAS unsigned*)(A.ws + flb + (ST == 2 ? 512 : 640) + c * 4), epoch, RLX_AGENT);
      }
      q.template load<ST, 1>();
      if constexpr (ST == 0) { HQ_ST(3); HQ_ST(4); HQ_ST(5); }
      if constexpr (ST >= 1 && ST <= 4) { HQ_ST(4 + 2 * ST); HQ_ST(5 + 2 * ST); }
      side(ST_);
      WAIT_L0(); BAR();
      if constexpr (ST + 1 < NSTEP) {
        q.template compute<ST, (ST + 2 < NSTEP ? 2 : 0) + (ST + 3 < NSTEP ? 4 : 0) + nst + XTRA(ST)>(); TS2();
        if constexpr (ST == 1 || ST == 13) STAMP();
      }
    });
  }
  lane = fresh_lane(); r = lane & 15; g = lane >> 4;
  f32x4 o[4];
  {
    unsigned xo[3], pk[4];
#pragma unroll
    for (int j = 1; j < 4; ++j) xo[j - 1] = (unsigned)WS_XB + ((tile * 4 + (kq ^ j)) * 3 + (j - 1)) * 16384u + wid * 2048 + lane * 16;
    __builtin_amdgcn_s_setprio(1);
#pragma unroll
    for (int k = 0; k < 19; ++k) {
      if (k < 16) { const int m = ((k >> 2) + 1) & 3, n = k & 3; MFMA(q.bb[0][n], q.ha[0][m], q.as[m][n]); MFMA(q.bb[1][n], q.ha[1][m], q.as[m][n]); }
      if (k == 3 || k >= 16) asm volatile("s_nop 3");
      if (G == 0 && k == 14) RBAR();
      SCHED();
      if (k >= 3) {
        const int m = (((k - 3) >> 2) + 1) & 3, n = (k - 3) & 3;
        f32x4 v;
#pragma unroll
        for (int j = 0; j < 4; ++j) v[j] = __builtin_fmaf(q.as[m][n][j], gv[m], q.af[m][n][j]);
        if (m == 0) o[n] = v;
        else {
          pk[(n & 1) * 2] = pk_bf16(v[0], v[1]); pk[(n & 1) * 2 + 1] = pk_bf16(v[2], v[3]);
          if (n & 1) __builtin_amdgcn_raw_buffer_store_b128((u32x4){pk[0], pk[1], pk[2], pk[3]}, rws, xo[m - 1] + (n >> 1) * 1024, 0, 16);
        }
      }
      SCHED();
    }
    __builtin_amdgcn_s_setprio(0);
  }
  STAMP();
  WAIT_V(0);
  if (lane >= 1 && lane < 4) __hip_atomic_store((GAS unsigned*)(A.ws + WS_XF + (tile * 4 + (kq ^ lane)) * 128 + wid * 16) + lane, epoch, RLX_AGENT);
  STAMP();
  f32x4 sk[4];
  {
    const f32x4 df = {bf2f(dv[0] & 0xffffu), bfhi(dv[0]), bf2f(dv[1] & 0xffffu), bfhi(dv[1])};
#pragma unroll
    for (int i = 0; i < 4; ++i) { sk[i] = (f32x4){bf2f(xv[i][0] & 0xffffu), bfhi(xv[i][0]), bf2f(xv[i][1] & 0xffffu), bfhi(xv[i][1])} * df * gr[i]; PIN(sk[i]); }
  }
  const lptr tw = mk_l(49152 + G * 49152 + wr * 4096 + r * 256), tr_ = mk_l(49152 + G * 49152 + wr * 4096 + g * 256);
  wait_flags(A.ws, (unsigned)WS_XF + (tile * 4 + kq) * 128 + wid * 16 + (1 + (lane & 3) % 3) * 4, epoch);
  STAMP();
  {
    u32x4 pv[6];
    const unsigned xi = (unsigned)WS_XB + (tile * 4 + kq) * 3 * 16384u + wid * 2048 + lane * 16;
#pragma unroll
    for (int i = 0; i < 6; ++i) pv[i] = __builtin_amdgcn_raw_buffer_load_b128(rws, xi + (i >> 1) * 16384 + (i & 1) * 1024, 0, 16);
#pragma unroll
    for (int i = 0; i < 6; ++i)
#pragma unroll
      for (int h = 0; h < 2; ++h) {
        const unsigned a0 = pv[i][2 * h], a1 = pv[i][2 * h + 1];
        o[(i & 1) * 2 + h] += (f32x4){bf2f(a0 & 0xffffu), bfhi(a0), bf2f(a1 & 0xffffu), bfhi(a1)};
      }
  }
#pragma unroll
  for (int n = 0; n < 4; ++n) *(LAS f32x4*)(tw + (((n * 4 + g) ^ (r & 7)) << 4)) = o[n];
  WAIT_L0();
#pragma unroll
  for (int i = 0; i < 4; ++i) {
    const f32x4 v = *(const LAS f32x4*)(tr_ + i * 1024 + ((r ^ ((4 * i + g) & 7)) << 4));
    __builtin_amdgcn_raw_buffer_store_b128(__builtin_bit_cast(u32x4, v + sk[i]), mk_rsrc(A.out, L_ * D_ * 4), ((t0 + i * 16 + tj(g)) * D_ + dc0 + r * 4) * 4, 0, 17);
  }
  STAMP();
  ST_DUMP(A.ws, wid);
  if (GRP == 0) asm volatile(".Lgm_end0:"); else asm volatile(".Lgm_end1:");
  __builtin_amdgcn_endpgm();
  __builtin_unreachable();
}

__global__ __launch_bounds__(512, 2) void fused_kernel(Args A) {
  const u64 t00 = 0;
  const int wid = __builtin_amdgcn_readfirstlane(threadIdx.x >> 6);
  {
    const Pre pre = mk_pre(threadIdx.x & 63, wid, blockIdx.x);
    const int G = wid >> 2;
    const rsrc_t rx = mk_rsrc(A.x, L_ * D_ * 2);
#pragma unroll
    for (int s = 0; s < 4; ++s) dma16(pre.ldsX + s * 2048, pre.voffX, rx, pre.sX + s * 4096);
    dma16(pre.ldsW + G * 1024, pre.voffW, mk_rsrc(A.wg, D_ * F_ * 2), pre.sW + G * 28672u);
    dma16(pre.ldsW + 16384 + G * 1024, pre.voffW, mk_rsrc(A.wu, D_ * F_ * 2), pre.sW + G * 28672u);
    dma16(pre.ldsB + G * 4096, pre.voffB, mk_rsrc((wid & 2) ? A.bim : A.bre, P_ * D_ * 2), pre.sB + G * 32768);
  }
  const char* code; unsigned code_len;
  asm volatile("s_getpc_b64 %0\n.Lgm_pc:\n\ts_mov_b32 %1, .Lgm_end0-.Lgm_pc\n\ts_max_u32 %1, %1, .Lgm_end1-.Lgm_pc" : "=s"(code), "=s"(code_len) :: "scc");
  if (wid < 4) body<0>(A, wid, code, code_len, t00); else body<1>(A, wid, code, code_len, t00);
}

extern "C" void kernel_launch(void* const* d_in, const int* in_sizes, int n_in, void* d_out, int out_size, void* d_ws, size_t ws_size,
                              hipStream_t stream) {
  Args a; a.x = (const u16*)d_in[0]; a.lre = (const u16*)d_in[1]; a.lim = (const u16*)d_in[2]; a.bre = (const u16*)d_in[3];
  a.bim = (const u16*)d_in[4]; a.cre = (const u16*)d_in[5]; a.cim = (const u16*)d_in[6]; a.ldt = (const u16*)d_in[7]; a.dsk = (const u16*)d_in[8];
  a.wg = (const u16*)d_in[10]; a.wu = (const u16*)d_in[11]; a.wd = (const u16*)d_in[12]; a.gw = (const u16*)d_in[13];
  a.ws = (char*)d_ws; a.out = (float*)d_out;
  (void)hipFuncSetAttribute((const void*)fused_kernel, hipFuncAttributeMaxDynamicSharedMemorySize, LDS_BYTES);
  fused_kernel<<<256, 512, LDS_BYTES, stream>>>(a);
}
```

```cpp
#define XS 16
#include <hip/hip_runtime.h>
#include <hip/hip_bf16.h>
#include <type_traits>
#define MAP2 1
#define ORD2 1
#define MAP1 1
#define SQ 1
#define SO 1
#define PFD 4

typedef unsigned short u16;
typedef short bf16x8 __attribute__((ext_vector_type(8)));
typedef short s16x4 __attribute__((ext_vector_type(4)));
typedef float f32x4 __attribute__((ext_vector_type(4)));
typedef float f32x2 __attribute__((ext_vector_type(2)));
typedef unsigned u32x4 __attribute__((ext_vector_type(4)));
typedef unsigned u32x2 __attribute__((ext_vector_type(2)));
typedef unsigned long long u64;
#define LAS __attribute__((address_space(3)))
#define GAS __attribute__((address_space(1)))
#define RLX_AGENT __ATOMIC_RELAXED, __HIP_MEMORY_SCOPE_AGENT
typedef __amdgpu_buffer_rsrc_t rsrc_t;

constexpr int L_ = 2048, D_ = 1024, P_ = 1024, F_ = 3584;
constexpr int NKF = 176;
constexpr size_t WS_H = 0;
constexpr size_t H_BYTES = (size_t)16 * NKF * 8 * 1024;
constexpr size_t WS_XB = WS_H + H_BYTES;
constexpr size_t XB_BYTES = (size_t)64 * 4 * 3 * 16384;
constexpr size_t WS_G = WS_XB + XB_BYTES;
constexpr size_t WS_AG = WS_G + 8192;
constexpr size_t WS_EP = WS_AG + (size_t)32 * 8 * 64 * 8;
constexpr size_t WS_FL = WS_EP + 512;
constexpr size_t WS_XF = WS_FL + 8192;
constexpr size_t WS_END = WS_XF + 256 * 8 * XS;
constexpr unsigned SPIN_CAP = 1u << 20;

__device__ __forceinline__ float bf2f(unsigned b) { return __uint_as_float(b << 16); }
__device__ __forceinline__ float bfhi(unsigned b) { return __uint_as_float(b & 0xffff0000u); }
__device__ __forceinline__ unsigned pk_bf16(float lo, float hi) {
  typedef __bf16 bf2_t __attribute__((ext_vector_type(2)));
  return __builtin_bit_cast(unsigned, __builtin_convertvector((f32x2){lo, hi}, bf2_t)); }
__device__ __forceinline__ float relu(float v) { const int b = __float_as_int(v); return __int_as_float(b > 0 ? b : 0); }
template <int CTRL, int BANKS> __device__ __forceinline__ float dpp(float old, float src) {
  return __int_as_float(__builtin_amdgcn_update_dpp(__float_as_int(old), __float_as_int(src), CTRL, 0xf, BANKS, false)); }
__device__ __forceinline__ rsrc_t mk_rsrc(const void* p, unsigned bytes) {
  return __builtin_amdgcn_make_buffer_rsrc((void*)p, (short)0, (int)bytes, 0x00020000); }
__device__ __forceinline__ void dma16(unsigned ldsaddr, unsigned voff, rsrc_t srd, unsigned soff) {
  asm volatile("s_mov_b32 m0, %0\n\ts_nop 0\n\tbuffer_load_dwordx4 %1, %2, %3 offen lds"
               :: "s"(ldsaddr), "v"(voff), "s"(srd), "s"(soff) : "memory"); }
__device__ __forceinline__ void dma16_dev(unsigned ldsaddr, unsigned voff, rsrc_t srd, unsigned soff) {
  asm volatile("s_mov_b32 m0, %0\n\ts_nop 0\n\tbuffer_load_dwordx4 %1, %2, %3 offen sc1 lds"
               :: "s"(ldsaddr), "v"(voff), "s"(srd), "s"(soff) : "memory"); }
#define WAIT_V(n) asm volatile("s_waitcnt vmcnt(" #n ")" ::: "memory")
#define WAIT_VN(n) asm volatile("s_waitcnt vmcnt(%0)" :: "i"(n) : "memory")
#define WAIT_L0() do { __builtin_amdgcn_s_waitcnt(0xC07F); asm volatile("" ::: "memory"); } while (0)
#define SCHED() __builtin_amdgcn_sched_barrier(0)
#define RBAR() do { __builtin_amdgcn_s_barrier(); asm volatile("" ::: "memory"); } while (0)
#define BAR() do { SCHED(); RBAR(); SCHED(); } while (0)
__device__ __forceinline__ int fresh_lane() { int l; asm volatile("v_mbcnt_lo_u32_b32 %0, -1, 0\n\tv_mbcnt_hi_u32_b32 %0, -1, %0" : "=v"(l)); return l; }
typedef LAS char* lptr;
__device__ __forceinline__ lptr mk_l(unsigned a) { lptr p = (lptr)(size_t)a; asm volatile("" : "+v"(p)); return p; }
__device__ __forceinline__ bf16x8 lds_b128(lptr a) { return *(const LAS bf16x8*)a; }
__device__ __forceinline__ bf16x8 lds_tr2(lptr a0, lptr a1) {
  s16x4 lo = __builtin_amdgcn_ds_read_tr16_b64_v4i16((LAS s16x4*)a0);
  s16x4 hi = __builtin_amdgcn_ds_read_tr16_b64_v4i16((LAS s16x4*)a1);
  return __builtin_shufflevector(lo, hi, 0, 1, 2, 3, 4, 5, 6, 7); }
#define MFMA(a, b, c) asm volatile("v_mfma_f32_16x16x32_bf16 %0, %1, %2, %0" : "+v"(c) : "v"(a), "v"(b))
#define MFMA0(a, b, c) asm volatile("v_mfma_f32_16x16x32_bf16 %0, %1, %2, 0" : "=&v"(c) : "v"(a), "v"(b))
#define PIN(x) asm volatile("" : "+v"(x))
#define MFMA_DRAIN() do { asm volatile("s_nop 7\n\ts_nop 7" ::: "memory"); SCHED(); } while (0)

#define ST_DECL
#define STAMP()
#define TS2()
#define ST_DUMP(ws, wid)

struct Args {
  const u16 *x, *lre, *lim, *bre, *bim, *cre, *cim, *ldt, *dsk, *wg, *wu, *wd, *gw;
  char* ws; float* out;
};

constexpr unsigned L_X = 0, L_W = 65536, L_BS = 131072, L_PRM = 151552  ,
                   L_WA = 153600  , L_FLG = 154624  , L_SINK = 154880, L_TAB = 155136  , LDS_BYTES = 163840;

__device__ __forceinline__ int tj(int j) { return (j & 1) * 8 + ((j >> 1) & 1); }
__device__ __forceinline__ f32x2 cmul(f32x2 a, f32x2 b) { return (f32x2){a.x * b.x - a.y * b.y, a.x * b.y + a.y * b.x}; }
__device__ __forceinline__ f32x2 cfma(f32x2 a, f32x2 b, f32x2 c) {
  return (f32x2){__builtin_fmaf(a.x, b.x, __builtin_fmaf(-a.y, b.y, c.x)), __builtin_fmaf(a.x, b.y, __builtin_fmaf(a.y, b.x, c.y))}; }
__device__ __forceinline__ void sw16(float v, float& r0, float& r1) { auto t = __builtin_amdgcn_permlane16_swap(__float_as_uint(v), __float_as_uint(v), false, false); r0 = __uint_as_float(t[0]); r1 = __uint_as_float(t[1]); }
__device__ __forceinline__ void sw32(float v, float& r0, float& r1) { auto t = __builtin_amdgcn_permlane32_swap(__float_as_uint(v), __float_as_uint(v), false, false); r0 = __uint_as_float(t[0]); r1 = __uint_as_float(t[1]); }

__device__ __forceinline__ void touch(const char* p) {
  asm volatile("s_mov_b32 m0, %0\n\ts_nop 0\n\tglobal_load_lds_dword %1, off" :: "s"(L_SINK), "v"(p) : "memory"); }
template <int GRP>
struct P1 {
  static constexpr int NW = GRP == 0 ? 4 : 3, NS = GRP == 0 ? 0 : 4;
  f32x4 ag[NW][4], au[NW][4], as[4][4];
  bf16x8 xa[4], wgf[NW], wuf[NW], bsf[4];
  lptr aX[2], aB[2], aW[NW];
  unsigned voffX, voffW, voffB;
  unsigned sX, sW, sB, ldsX, ldsW, ldsB;
  rsrc_t rx, rwg, rwu, rb;
  const char* pfp; unsigned pfs; int pfo, pfm;
  __device__ __forceinline__ void pull(int T) { const int i = T + pfo; touch(pfp + pfs * (unsigned)(i < 0 ? 0 : i > pfm ? pfm : i)); }

  template <int BUF, int KS, class M> __device__ __forceinline__ void load(M&& mem) {
    mem(0);
#pragma unroll
    for (int m = 0; m < 4; ++m) xa[m] = lds_b128(aX[KS] + BUF * 32768 + m * 2048);
    mem(1);
#pragma unroll
    for (int i = 0; i < NS; ++i) bsf[i] = lds_b128(aB[KS] + BUF * 8192 + i * 2048);
    if (NS) mem(2);
#pragma unroll
    for (int n = 0; n < NW; ++n) {
      wgf[n] = lds_tr2(aW[n] + BUF * 32768 + KS * 8192, aW[n] + BUF * 32768 + KS * 8192 + 1024);
      wuf[n] = lds_tr2(aW[n] + BUF * 32768 + 16384 + KS * 8192, aW[n] + BUF * 32768 + 16384 + KS * 8192 + 1024);
      mem(n + (NS ? 3 : 2));
    }
  }
  template <int BUF, int KS> __device__ __forceinline__ void load() { load<BUF, KS>([](int) {}); }
  template <int BUF> __device__ __forceinline__ void dmaX1(int T, int s) { dma16(ldsX + BUF * 32768 + s * 2048, voffX, rx, sX + T * 128 + s * 4096); }
  template <int BUF, int HH> __device__ __forceinline__ void dmaW1(int T, int i) {
    dma16(ldsW + BUF * 32768 + (i & 1) * 16384 + HH * 8192 + (i >> 1) * 1024, voffW, (i & 1) ? rwu : rwg, sW + (unsigned)(T * 64 + HH * 32 + (i >> 1) * 4) * 7168u);
  }
  template <int BUF> __device__ __forceinline__ void dmaB1(int T, int pf) { dma16(ldsB + BUF * 8192 + pf * 4096, voffB, rb, sB + pf * 32768 + T * 128); }
  template <int NV, bool FIRST = false> __device__ __forceinline__ void compute() {
    constexpr int NM = 4 * NS + 8 * NW;
    __builtin_amdgcn_s_setprio(1);
#pragma unroll
    for (int k = 0; k < NM; ++k) {
      const int f = k >> 2, m = (f & 1) ? 3 - (k & 3) : (k & 3);
      const int n = f < NS ? 0 : (f - NS) >> 1;
      const bf16x8& a = f < NS ? xa[m] : ((f - NS) & 1) ? wuf[n] : wgf[n];
      const bf16x8& b = f < NS ? bsf[f & 3] : xa[m];
      f32x4& c = f < NS ? as[f & 3][m] : ((f - NS) & 1) ? au[n][m] : ag[n][m];
      if (FIRST) MFMA0(a, b, c); else MFMA(a, b, c);
    }
    if (NV != -2) { if (NV >= 0) WAIT_VN(NV < 0 ? 0 : NV); RBAR(); }
    __builtin_amdgcn_s_setprio(0);
    SCHED();
  }
  template <int BUF> __device__ __forceinline__ void dmaX(int T) {
#pragma unroll
    for (int s = 0; s < 4; ++s) dma16(ldsX + BUF * 32768 + s * 2048, voffX, rx, sX + T * 128 + s * 4096);
  }
  template <int BUF, int HH> __device__ __forceinline__ void dmaW(int T) {
#pragma unroll
    for (int s = 0; s < 2; ++s) {
      const unsigned so = sW + (unsigned)(T * 64 + HH * 32 + s * 4) * 7168u;
      dma16(ldsW + BUF * 32768 + HH * 8192 + s * 1024, voffW, rwg, so);
      dma16(ldsW + BUF * 32768 + 16384 + HH * 8192 + s * 1024, voffW, rwu, so);
    }
  }
  template <int BUF> __device__ __forceinline__ void dmaB(int T) {
#pragma unroll
    for (int pf = 0; pf < 2; ++pf) dma16(ldsB + BUF * 8192 + pf * 4096, voffB, rb, sB + pf * 32768 + T * 128);
  }
};

template <int BUF> __device__ __forceinline__ void p1_tile(P1<0>& w, int T) {
  w.template compute<7>();
  w.template load<BUF, 1>([&](int i) { if (i < 4) w.template dmaW1<BUF ^ 1, 1>(T + 1, i); else if (i == 4) w.pull(T); }); WAIT_L0(); BAR();
  w.template compute<5>();
  w.template load<BUF ^ 1, 0>([&](int i) { if (i < 4) w.template dmaX1<BUF>(T + 2, i); else w.template dmaB1<BUF>(T + 2, i - 4); }); WAIT_L0(); BAR();
}
template <int BUF> __device__ __forceinline__ void p1_tile(P1<1>& w, int T) {
  w.template load<BUF, 0>([&](int i) { if (i >= SO && i < SO + 4) w.template dmaX1<BUF ^ 1>(T + 1, i - SO); }); WAIT_L0(); BAR();
  w.template compute<-1>();
  w.template load<BUF, 1>([&](int i) { if (i >= SO && i < SO + 4) w.template dmaW1<BUF, 0>(T + 2, i - SO); }); WAIT_V(4); WAIT_L0(); BAR();
  w.template compute<-1>();
}

constexpr int NSTEP = 22;
struct P2 {
  f32x4 af[4][4], as[4][4];
  bf16x8 ha[2][4], bb[2][4];
  lptr aH, aS[2], aS2[2], aW[4], aC[2];
  unsigned voffH, voffW, voffC;
  unsigned sH, sHs, sW, sC, ldsA, ldsB, kk, pj[4], ps;
  rsrc_t rh, rw, rc, rc2_;
  template <int ST> __device__ __forceinline__ unsigned kf() const {
    constexpr int e = 2 * (ST - 8), k0 = ST < 8 ? 7 * (ST >> 1) + 3 * (ST & 1) + 1 : 7 * (e / 3) + 3 * (e % 3), k1 = ST < 8 ? k0 + 1 : 7 * ((e + 1) / 3) + 3 * ((e + 1) % 3);
    return k0 + kk * (k1 - k0);
  }
  template <int ST, int GA> __device__ __forceinline__ void dmaA(int j) {
    const unsigned so = ST < 14 ? sH + kf<ST>() * 8192u : sHs + ((ST - 14) & 3) * 16384u + ((ST - 14) >> 2) * 262144u;
    dma16_dev(ldsA + GA * 49152 + (ST % 3) * 16384 + j * 1024, voffH, rh, so + GA * (NKF * 8192u) + (ST < 14 ? pj[j] : (unsigned)(j << 10) ^ ps));
  }
  template <int ST> __device__ __forceinline__ void dmaB(int s) {
    const unsigned l = ldsB + (ST % 3) * 16384 + s * 2048;
    if (ST >= 14) dma16(l, voffC, ST < 18 ? rc : rc2_, sC + ((ST - 14) & 3) * 128u + s * 32768u);
    else dma16(l, voffW, rw, sW + kf<ST>() * 65536u + s * 16384u);
  }
  template <int ST, int GA> __device__ __forceinline__ void stageA() {
#pragma unroll
    for (int j = 0; j < 4; ++j) dmaA<ST, GA>(j);
  }
  template <int ST> __device__ __forceinline__ void stageB() {
#pragma unroll
    for (int s = 0; s < 4; ++s) dmaB<ST>(s);
  }
  template <int ST, int G> __device__ __forceinline__ void load() {
    constexpr int SL = (ST % 3) * 16384;
#pragma unroll
    for (int k2 = 0; k2 < 2; ++k2) {
#pragma unroll
      for (int n = 0; n < 4; ++n) {
        const int i = k2 * 4 + n;
        if constexpr (G == 0) { if constexpr (ST + 2 < NSTEP) { if (i < 4) dmaB<ST + 2>(i); else if (i < 6) dmaA<ST + 2, 1>(i - 4); } }
        else { if constexpr (ST + 2 < NSTEP) { if (i < 2) dmaA<ST + 2, 1>(i + 2); } if constexpr (ST + 3 < NSTEP) { if (i >= 2 && i < 6) dmaA<ST + 3, 0>(i - 2); } }
        if (ST >= 14) bb[k2][n] = lds_b128(aC[k2] + SL + n * 2048);
        else bb[k2][n] = lds_tr2(aW[n] + SL + k2 * 8192, aW[n] + SL + k2 * 8192 + 4096);
        if (ST >= 14) ha[k2][n] = lds_tr2(aS[n & 1] + SL + k2 * 8192 + (n >> 1) * 1024, aS2[n & 1] + SL + k2 * 8192 + (n >> 1) * 1024);
        else ha[k2][n] = lds_b128(aH + SL + k2 * 8192 + n * 1024);
      }
    }
  }
  template <int ST, int NV> __device__ __forceinline__ void compute() {
    __builtin_amdgcn_s_setprio(1);
#pragma unroll
    for (int k = 0; k < 32; ++k) {
      const int k2 = k >> 4, m = (k >> 2) & 3, n = (m & 1) ? 3 - (k & 3) : (k & 3);
      f32x4& c = ST >= 14 ? as[m][n] : af[m][n];
      if ((ST == 0 || ST == 14) && k2 == 0) MFMA0(bb[k2][n], ha[k2][m], c); else MFMA(bb[k2][n], ha[k2][m], c);
    }
    WAIT_VN(NV); RBAR();
    __builtin_amdgcn_s_setprio(0);
    SCHED();
  }
};
template <int I, int N, class F> __device__ __forceinline__ void static_for(F&& f) {
  if constexpr (I < N) { f(std::integral_constant<int, I>{}); static_for<I + 1, N>(f); }
}

__device__ __forceinline__ void arrive(char* ws, int lane, unsigned flag_off, int ctr, unsigned epoch) {
  if (lane == 0) {
    const unsigned old = __hip_atomic_fetch_add((LAS unsigned*)mk_l(L_FLG + 16 + ctr * 4), 1u, __ATOMIC_RELAXED, __HIP_MEMORY_SCOPE_WORKGROUP);
    if (old == 3u) __hip_atomic_store((GAS unsigned*)(ws + flag_off), epoch, RLX_AGENT);
  }
}
__device__ __forceinline__ void lds_post(int ctr) {
  if (fresh_lane() == 0) __hip_atomic_fetch_add((LAS unsigned*)mk_l(L_FLG + 16 + ctr * 4), 1u, __ATOMIC_RELAXED, __HIP_MEMORY_SCOPE_WORKGROUP);
}
__device__ __forceinline__ void lds_wait(int ctr, unsigned n = 4u) {
  const lptr p = mk_l(L_FLG + 16 + ctr * 4);
_Pragma("nounroll")
  for (unsigned spins = 0; spins < (SPIN_CAP << 6); ++spins) {
    if (__builtin_amdgcn_readfirstlane(*(volatile LAS unsigned*)p) >= n) break;
    __builtin_amdgcn_s_sleep(1);
  }
  asm volatile("" ::: "memory");
}
__device__ __forceinline__ void wait_flags(const char* ws, unsigned off, unsigned epoch) {
_Pragma("nounroll")
  for (unsigned spins = 0; spins < SPIN_CAP; ++spins) {
    if (__all(__hip_atomic_load((const GAS unsigned*)(ws + off), RLX_AGENT) == epoch)) break;
    __builtin_amdgcn_s_sleep(1);
  }
  asm volatile("" ::: "memory");
}

struct Pre { unsigned voffX, voffW, voffB, sX, ldsX, sW, ldsW, sB, ldsB; };
__device__ __forceinline__ Pre mk_pre(int lane, int wid, int bid) {
  Pre pre;
  const int wr = wid & 3, G = wid >> 2, idx = bid >> 3, c = ((bid & 7) >> 1) * 8 + (idx & 7), rch = (bid & 1) * 4 + (idx >> 3);
  const unsigned sw8 = ((lane & 7) ^ (((wr & 1) * 4 + (lane >> 4)) & 7)) * 16;
  pre.voffX = ((lane >> 5) * 16 + tj(lane >> 3)) * 2048 + sw8;
  pre.voffB = (lane >> 3) * 2048 + sw8;
  pre.voffW = (lane >> 4) * 7168 + ((((lane & 15) >> 1) ^ ((lane >> 4) | ((wr & 1) << 2))) * 32) + (lane & 1) * 16;
  pre.sX = (unsigned)(rch * 256 + (G * 2 + (wr >> 1)) * 64 + (wr & 1) * 32) * 2048u;
  pre.ldsX = L_X + (G * 16 + (wr >> 1) * 8 + (wr & 1)) * 1024;
  const int ii = (wr & 1) * 2 + (wr >> 1) * 4;
  pre.sW = (unsigned)(ii * 4) * 7168u + (unsigned)((224 * c) & ~63);
  pre.ldsW = L_W + ii * 1024;
  pre.sB = (unsigned)(c * 32 + (wr & 1) * 8) * 2048u;
  pre.ldsB = L_BS + wr * 1024;
  return pre;
}
template <int GRP>
__device__ __forceinline__ void body(const Args& A, int wid, const char* code, const unsigned code_len, const u64 t00) {
  asm volatile("" : "+s"(wid));
  ST_DECL STAMP();
  const int xcd = blockIdx.x & 7, idx = blockIdx.x >> 3, c = (xcd >> 1) * 8 + (idx & 7), rch = (xcd & 1) * 4 + (idx >> 3);
  const int xrow = (xcd & 1) * 1024 + (xcd >> 1) * 256 + idx * 8;
  const int kq = xcd >> 1, rc2 = (xcd & 1) * 4 + (idx >> 3), dt = idx & 7;
  const int tt = 2 * rc2 + GRP, tile = rc2 * 8 + dt;
  const unsigned flb = (unsigned)WS_FL + rch * 1024, flc = (unsigned)WS_FL + rc2 * 1024;
  const int wr = wid & 3, wm = wr >> 1, wn = wr & 1;
  int lane = fresh_lane();
  unsigned epoch;
  P2 q;
  u32x4 hq[14]; unsigned ho[3];
  const rsrc_t rws = mk_rsrc(A.ws, (unsigned)WS_END);
  {
  P1<GRP> w;
  constexpr int NW = P1<GRP>::NW, NS = P1<GRP>::NS;
  int r = lane & 15, g = lane >> 4, qq = (lane & 15) >> 2, pp = lane & 3;
  const Pre pre = mk_pre(lane, wid, blockIdx.x);
  w.rx = mk_rsrc(A.x, L_ * D_ * 2); w.rwg = mk_rsrc(A.wg, D_ * F_ * 2); w.rwu = mk_rsrc(A.wu, D_ * F_ * 2);
  w.rb = mk_rsrc((wr >> 1) ? A.bim : A.bre, P_ * D_ * 2);
  w.voffX = pre.voffX; w.voffB = pre.voffB; w.voffW = pre.voffW;
  w.sX = pre.sX; w.ldsX = pre.ldsX; w.sW = pre.sW; w.ldsW = pre.ldsW; w.sB = pre.sB; w.ldsB = pre.ldsB;
  const char* wbase = (const char*)((wr & 1) ? A.wu : A.wg) + (idx * 2 + (wr >> 1)) * 7168 + xcd * 896;
  const char* rbase = wr < 2 ? (const char*)A.x + (xrow + wr * 4) * 2048 : (const char*)(wr == 2 ? A.bre : A.bim) + (xcd * 128 + idx * 4) * 2048;
  if (GRP == 1) {
    w.template dmaW<0, 1>(0); w.template dmaW<1, 0>(1);
    const unsigned co = ((idx * 4 + wr) * 4 + (lane & 3)) * 128u;
    touch(lane < 16 ? wbase + (2 + (lane >> 3)) * 458752u + ((lane & 7) < 7 ? (lane & 7) : 0) * 128 : lane < 24 ? rbase + ((lane - 16) >> 1) * 2048 + (2 + (lane & 1)) * 128
                    : code + (co < code_len ? co : 0u));
  }
  {
#pragma unroll
    for (int ks = 0; ks < 2; ++ks) {
      const unsigned ch = ((g + 4 * ks) ^ ((r >> 1) & 7)) << 4;
      w.aX[ks] = mk_l(L_X + (wr * 64 + r) * 128 + ch);
      if (NS) w.aB[ks] = mk_l(L_BS + r * 128 + ch);
    }
    const unsigned swz = qq | ((g & 1) << 2);
#pragma unroll
    for (int n = 0; n < NW; ++n) {
      unsigned col;
      if (GRP == 0) col = (((2 + n) ^ swz) << 5) + pp * 8;
      else if (n < 2) col = (((((c & 1) ? 6 : 0) + n) ^ swz) << 5) + pp * 8;
      else col = (((((c & 1) ? 0 : 6) + (pp >> 1)) ^ swz) << 5) + (c & 1) * 16 + (pp & 1) * 8;
      w.aW[n] = mk_l(L_W + (8 * g + qq) * 256 + col);
    }
  }
  if constexpr (GRP == 0) {
    WAIT_V(0); BAR();
    w.template load<0, 0>([&](int i) { if (i < 4) w.template dmaX1<1>(1, i); else w.template dmaB1<1>(1, i - 4); });
    unsigned ep0, plr, pli, pld;
    {
      const unsigned po = (c * 32 + (lane & 31)) * 2;
      asm volatile("global_load_dword %0, %1, %2 sc1" : "=v"(ep0) : "v"((unsigned)WS_EP), "s"(A.ws) : "memory");
      asm volatile("global_load_ushort %0, %1, %2" : "=v"(plr) : "v"(po), "s"(A.lre) : "memory");
      asm volatile("global_load_ushort %0, %1, %2" : "=v"(pli) : "v"(po), "s"(A.lim) : "memory");
      asm volatile("global_load_ushort %0, %1, %2" : "=v"(pld) : "v"(po), "s"(A.ldt) : "memory");
    }
    WAIT_L0(); BAR();
    STAMP(); TS2();
    w.template compute<-1, true>();
    w.template load<0, 1>([&](int i) { if (i < 4) w.template dmaW1<1, 1>(1, i); });
    {
      const unsigned j = idx * 4 + wr, co = (j * 4 + (lane & 3)) * 128u;
      const int l = lane < 32 ? lane : 31;
      if (l < 7) { w.pfp = wbase + l * 128; w.pfs = 458752u; w.pfo = PFD; w.pfm = 15; }
      else if (l < 15) { w.pfp = rbase + ((l - 7) >> 1) * 2048 + ((l - 7) & 1) * 128; w.pfs = 256u; w.pfo = 2; w.pfm = 7; }
      else if (l < 23) { w.pfp = (const char*)A.wd + (kq * 896 + j) * 2048u + (xcd & 1) * 1024 + (l - 15) * 128; w.pfs = 262144u; w.pfo = -7; w.pfm = 6; }
      else if (l < 27) { w.pfp = (const char*)((j & 1) ? A.cim : A.cre) + ((xcd & 1) * 512 + (j >> 1)) * 2048u + kq * 512 + (l - 23) * 128; w.pfs = 131072u; w.pfo = 0; w.pfm = 7; }
      else { w.pfp = l < 31 ? code + (co < code_len ? co : 0u) : (const char*)((idx & 4) ? A.dsk : A.gw) + ((idx & 3) * 4 + wr) * 128; w.pfs = 0u; w.pfo = 0; w.pfm = 0; }
      w.pull(0);
    }
    WAIT_L0(); BAR();
    w.template compute<5>();
    w.template load<1, 0>([&](int i) { if (i < 4) w.template dmaX1<0>(2, i); else w.template dmaB1<0>(2, i - 4); });
    PIN(ep0); PIN(plr); PIN(pli); PIN(pld);
    epoch = __builtin_amdgcn_readfirstlane(ep0) + 1u;
    f32x2 lam, l8, l256; f32x4 p0, p1, p2, t0_, t1_;
    {
      const float lr = bf2f(plr), li = bf2f(pli);
      const float dt_ = bf2f(pk_bf16(__expf(bf2f(pld)), 0.f) & 0xffffu);
      const float e = __expf(dt_ * lr), rev = __builtin_amdgcn_fractf(dt_ * li * 0.15915494309189535f);
      lam = (f32x2){e * __builtin_amdgcn_cosf(rev), -e * __builtin_amdgcn_sinf(rev)};
      const float nr = lam.x - 1.f, rden = 1.f / (lr * lr + li * li);
      p0 = (f32x4){lam.x, lam.y, (nr * lr - lam.y * li) * rden, (-lam.y * lr - nr * li) * rden}; PIN(p0);
      l8 = cmul(lam, lam); l8 = cmul(l8, l8); l8 = cmul(l8, l8); PIN(l8);
    }
    WAIT_L0(); BAR(); TS2();
    w.template compute<7>();
    if (wr == 2) {
      __hip_atomic_store((GAS u64*)(A.ws + WS_AG) + (c * 8 + 7 - rch) * 64 + lane, (u64)(epoch - 1u) << 32, RLX_AGENT);
      if (lane < 6) __hip_atomic_store((GAS unsigned*)(A.ws + flb + lane * 128 + c * 4), epoch - 1u, RLX_AGENT);
      else if (lane >= 32 && (lane & 3)) __hip_atomic_store((GAS unsigned*)(A.ws + WS_XF + ((tile * 4 + (kq ^ (lane & 3))) * 8 + ((lane - 32) >> 2)) * XS) + (lane & 3), epoch - 1u, RLX_AGENT);
    }
    w.template load<1, 1>([&](int i) { if (i < 4) w.template dmaW1<0, 1>(2, i); else if (i == 4) w.pull(1); });
    {
      const f32x2 l16 = cmul(l8, l8), l32 = cmul(l16, l16), l64 = cmul(l32, l32), l128 = cmul(l64, l64);
      l256 = cmul(l128, l128); p1 = (f32x4){l16.x, l16.y, l32.x, l32.y}; p2 = (f32x4){l8.x, l8.y, l64.x, l64.y};
      f32x2 pw = (wr & 1) ? l64 : (f32x2){1.f, 0.f}; if (wr & 2) pw = cmul(pw, l128);
      const f32x2 q0 = (lane & 32) ? l32 : (f32x2){1.f, 0.f}, q1 = cmul(q0, l16), c0 = cmul(q0, pw), c1 = cmul(q1, pw);
      t0_ = (f32x4){q0.x, q0.y, c0.x, c0.y}; t1_ = (f32x4){q1.x, q1.y, c1.x, c1.y};
      PIN(l256); PIN(p1); PIN(p2); PIN(t0_); PIN(t1_);
    }
    WAIT_L0(); BAR();
    w.template compute<5>();
    {
      const lptr tp = mk_l(L_TAB + wr * 2048 + (lane >> 5) * 1024 + (lane & 31) * 16);
      *(LAS f32x4*)tp = t0_; *(LAS f32x4*)(tp + 512) = t1_;
      if (wr == 0) {
        const lptr pb = mk_l(L_PRM + (lane & 31) * 64);
        *(LAS f32x4*)pb = p0; *(LAS f32x4*)(pb + 16) = p1; *(LAS f32x4*)(pb + 32) = p2; *(LAS f32x2*)(pb + 48) = l256;
      } else if (wr == 1) *(LAS unsigned*)mk_l(L_FLG + (lane & 7) * 4) = (lane & 7) == 3 ? epoch : 0u;
    }
    w.template load<0, 0>([&](int i) { if (i < 4) w.template dmaX1<1>(3, i); else w.template dmaB1<1>(3, i - 4); }); WAIT_L0(); BAR();
  } else {
    WAIT_V(9); BAR(); BAR();
    STAMP(); TS2();
    w.template load<0, 0>([&](int i) { if (i >= SO && i < SO + 4) w.template dmaX1<1>(1, i - SO); }); WAIT_V(9); WAIT_L0(); BAR();
    w.template compute<-1, true>();
    w.template load<0, 1>([&](int i) { if (i >= SO && i < SO + 4) w.template dmaW1<0, 0>(2, i - SO); }); WAIT_V(4); WAIT_L0(); BAR();
    w.template compute<-1>(); TS2();
    p1_tile<1>(w, 1);
  }
  for (int T = 2; T < 14; T += 2) {
    TS2(); p1_tile<0>(w, T); TS2();
    p1_tile<1>(w, T + 1);
  }
  if (GRP == 1) epoch = __builtin_amdgcn_readfirstlane(*(const LAS unsigned*)mk_l(L_FLG + 12));
  STAMP();
  lane = fresh_lane();
  q.rh = mk_rsrc(A.ws + WS_H, (unsigned)H_BYTES); q.rw = mk_rsrc(A.wd, F_ * D_ * 2); q.rc = mk_rsrc((kq >> 1) ? A.cim : A.cre, D_ * P_ * 2);
  q.voffW = (lane >> 4) * 2048 + ((((lane & 15) >> 1) ^ (((wr & 1) << 2) | (lane >> 4))) * 32) + (lane & 1) * 16;
  {
    const int i0 = (wr & 1) + (wr >> 1) * 8;
    q.kk = wr >> 1;
    q.sH = (unsigned)(2 * rc2 * NKF + 28 * kq) * 8192u + (wr & 1) * 4096u;
    q.sHs = (unsigned)(2 * rc2 * NKF + 112 + 16 * kq + (wr >> 1)) * 8192u + (wr & 1) * 4096u;
    q.sW = (unsigned)(28 * kq * 32 + (wr & 1) * 4) * 2048u + dt * 256u;
    q.sC = (unsigned)(dt * 128 + i0 * 8) * 2048u + (kq & 1) * 1024u;
#pragma unroll
    for (int j = 0; j < 4; ++j) q.pj[j] = (unsigned)(j ^ kq) << 10;
    q.rc = mk_rsrc(A.cre, D_ * P_ * 2); q.rc2_ = mk_rsrc(A.cim, D_ * P_ * 2);
    q.sHs = (unsigned)(2 * rc2 * NKF + 112 + 8 * kq + (wr >> 1)) * 8192u + (wr & 1) * 4096u;
    q.sC = (unsigned)(dt * 128 + i0 * 8) * 2048u + kq * 512u;
    q.ps = (unsigned)(kq >> 1) << 10;
    q.ldsA = 49152 + wr * 4096; q.ldsB = i0 * 1024;
  }
  u32x4 gx[4], gg[4];
  const int tg = rch * 256 + c * 8 + wr * 2 + (lane >> 5);
  if constexpr (GRP == 0) {
    w.template compute<7>();
    w.template load<0, 1>(); w.template dmaW<1, 1>(15);
#pragma unroll
    for (int i = 0; i < 4; ++i) gx[i] = *(const u32x4*)(A.x + tg * D_ + i * 256 + (lane & 31) * 8);
    WAIT_L0(); BAR();
    w.template compute<8>();
    w.template load<1, 0>(); q.template stageB<0>();
#pragma unroll
    for (int i = 0; i < 4; ++i) gg[i] = *(const u32x4*)(A.gw + i * 256 + (lane & 31) * 8);
    WAIT_L0(); BAR();
    w.template compute<12>();
    w.template load<1, 1>();
    {
      typedef __bf16 bf2_t __attribute__((ext_vector_type(2)));
      float s0 = 0.f, s1 = 0.f;
#pragma unroll
      for (int i = 0; i < 4; ++i)
#pragma unroll
        for (int j = 0; j < 4; j += 2) {
          const unsigned a0 = gx[i][j], b0 = gg[i][j], a1 = gx[i][j + 1], b1 = gg[i][j + 1];
          s0 = __builtin_amdgcn_fdot2_f32_bf16(__builtin_bit_cast(bf2_t, a0), __builtin_bit_cast(bf2_t, b0), s0, false);
          s1 = __builtin_amdgcn_fdot2_f32_bf16(__builtin_bit_cast(bf2_t, a1), __builtin_bit_cast(bf2_t, b1), s1, false);
        }
      float s = s0 + s1;
      s += dpp<0x128, 0xf>(0.f, s); s += dpp<0x124, 0xf>(0.f, s); s += dpp<0x122, 0xf>(0.f, s); s += dpp<0x121, 0xf>(0.f, s);
      float t0, t1; sw16(s, t0, t1); s = t0 + t1;
      if ((lane & 31) == 0) __hip_atomic_store((GAS unsigned*)(A.ws + WS_G) + tg, __float_as_uint(1.f / (1.f + __expf(-s))), RLX_AGENT);
    }
    WAIT_L0(); BAR();
    w.template compute<-2>();
    __builtin_amdgcn_s_setprio(3);
  } else {
    w.template load<0, 0>(); w.template dmaX<1>(15); WAIT_L0(); BAR();
    w.template compute<-1>();
    w.template load<0, 1>(); WAIT_V(0); WAIT_L0(); BAR();
    w.template compute<-1>();
  }
  lane = fresh_lane(); r = lane & 15; g = lane >> 4; qq = (lane & 15) >> 2; pp = lane & 3;
  const unsigned hb = (unsigned)WS_H + (unsigned)(2 * rch + (wr >> 1)) * NKF * 8192u + (wr & 1) * 4096u;
  const int kf0 = 7 * (c >> 1) + 3 * (c & 1);
  q.voffH = lane * 16;
#define FFN_H(n, m, h) pk_bf16(relu(w.ag[n][m][2 * (h)]) * w.au[n][m][2 * (h)], relu(w.ag[n][m][2 * (h) + 1]) * w.au[n][m][2 * (h) + 1])
#define P2_ADDR() do { q.voffC = (lane >> 3) * 2048 + (((lane & 7) ^ (((wr & 1) * 4 + (lane >> 4)) & 7)) * 16); PIN(q.voffC); \
    q.aH = mk_l(49152 + GRP * 49152 + wm * 4096 + lane * 16); \
    _Pragma("unroll") for (int n = 0; n < 4; ++n) q.aW[n] = mk_l((4 * g + qq) * 256 + (((wn * 4 + n) ^ (4 * (g & 1) + qq)) << 5) + pp * 8); } while (0)
#define PACK_S() do { _Pragma("unroll") for (int m = 0; m < 4; ++m) { hq[m] = (u32x4){FFN_H(0, m, 0), FFN_H(0, m, 1), FFN_H(1, m, 0), FFN_H(1, m, 1)}; PIN(hq[m]); } \
    _Pragma("unroll") for (int m = 0; m < 4; m += 2) {     \
      const auto s0 = __builtin_amdgcn_permlane32_swap(FFN_H(2, m, 0), FFN_H(2, m + 1, 0), false, false); \
      const auto s1 = __builtin_amdgcn_permlane32_swap(FFN_H(2, m, 1), FFN_H(2, m + 1, 1), false, false); \
      hq[4 + m / 2] = (u32x4){s0[0], s1[0], s0[1], s1[1]}; PIN(hq[4 + m / 2]); } \
    ho[0] = lane * 16 + hb + (kf0 + ((c & 1) ? 3 : 0)) * 8192u; \
    ho[1] = (lane & 31) * 16 + (lane >> 5) * 1024 + (c & 1) * 512 + hb + (kf0 + ((c & 1) ? 0 : 3)) * 8192u; \
    ho[2] = (lane ^ ((lane >> 5) << 2)) * 16 + hb + (112 + c) * 8192u; } while (0)
  if constexpr (GRP == 0) {
    MFMA_DRAIN();
    STAMP();
#pragma unroll
    for (int f = 0; f < 2; ++f)
#pragma unroll
      for (int m = 0; m < 4; ++m) {
        __builtin_amdgcn_raw_buffer_store_b128((u32x4){FFN_H(2 * f, m, 0), FFN_H(2 * f, m, 1), FFN_H(2 * f + 1, m, 0), FFN_H(2 * f + 1, m, 1)}, rws,
                                               lane * 16 + m * 1024 + hb + (kf0 + 1 + f) * 8192u, 0, 16);
        SCHED();
      }
    STAMP();
    WAIT_V(0); arrive(A.ws, lane, flb + (c >> 3) * 128 + c * 4, 0, epoch);
    __builtin_amdgcn_s_setprio(0);
    STAMP();
    P2_ADDR();
    if (wr == 0) { wait_flags(A.ws, flc + kq * 160 + (lane & 7) * 4, epoch); lds_post(3); }
    lds_wait(3, 5u);
    STAMP();
    q.template stageA<0, 0>(); q.template stageA<0, 1>(); q.template stageA<1, 0>();
    STAMP();
  } else {
    bf16x8 xb[4], bs1[4], wg1[3], wu1[3];
#pragma unroll
    for (int m = 0; m < 4; ++m) { w.xa[m] = lds_b128(w.aX[0] + 32768 + m * 2048); xb[m] = lds_b128(w.aX[1] + 32768 + m * 2048); }
#pragma unroll
    for (int i = 0; i < 4; ++i) { w.bsf[i] = lds_b128(w.aB[0] + 8192 + i * 2048); bs1[i] = lds_b128(w.aB[1] + 8192 + i * 2048); }
    w.wgf[0] = lds_tr2(w.aW[0] + 32768, w.aW[0] + 32768 + 1024); w.wuf[0] = lds_tr2(w.aW[0] + 49152, w.aW[0] + 49152 + 1024);
    q.template stageB<1>(); WAIT_L0(); BAR();
    __builtin_amdgcn_s_setprio(1);
#pragma unroll
    for (int i = 0; i < 4; ++i)
#pragma unroll
      for (int m = 0; m < 4; ++m) MFMA(w.xa[m], w.bsf[i], w.as[i][m]);
#pragma unroll
    for (int i = 0; i < 4; ++i)
#pragma unroll
      for (int m = 0; m < 4; ++m) MFMA(xb[m], bs1[i], w.as[i][m]);
#pragma unroll
    for (int m = 0; m < 4; ++m) MFMA(w.wgf[0], w.xa[m], w.ag[0][m]);
#pragma unroll
    for (int m = 0; m < 4; ++m) MFMA(w.wuf[0], w.xa[m], w.au[0][m]);
    __builtin_amdgcn_s_setprio(0);
    BAR();
    STAMP();
    f32x4 pl[2], pm[2];
    f32x2 l8[2], cin[2], ea[2], agg[2];
#pragma unroll
    for (int pf = 0; pf < 2; ++pf) {
      const lptr pb = mk_l(L_PRM + (pf * 16 + r) * 64);
      pl[pf] = *(const LAS f32x4*)pb; pm[pf] = *(const LAS f32x4*)(pb + 16); l8[pf] = *(const LAS f32x2*)(pb + 32);
    }
#pragma unroll
    for (int n = 1; n < 3; ++n) { w.wgf[n] = lds_tr2(w.aW[n] + 32768, w.aW[n] + 32768 + 1024); w.wuf[n] = lds_tr2(w.aW[n] + 49152, w.aW[n] + 49152 + 1024); }
    SCHED();
#define PAIR(v, k) ((k) & 1 ? (f32x2){(v)[(k) >> 1][2], (v)[(k) >> 1][3]} : (f32x2){(v)[(k) >> 1][0], (v)[(k) >> 1][1]})
#define CSTEP(P, Q, pf, k) do { const f32x2 lx_ = {pl[pf][0], pl[pf][0]}, ly_ = {pl[pf][1], pl[pf][1]}; \
      const f32x2 a_ = __builtin_elementwise_fma(-ly_, Q, PAIR(w.as[2 * (pf)], k)), b_ = __builtin_elementwise_fma(ly_, P, PAIR(w.as[2 * (pf) + 1], k)); \
      P = __builtin_elementwise_fma(lx_, P, a_); Q = __builtin_elementwise_fma(lx_, Q, b_); } while (0)
    {
#pragma unroll
      for (int pf = 0; pf < 2; ++pf)
#pragma unroll
        for (int m = 0; m < 4; ++m) {
          const f32x4 re = w.as[2 * pf][m], im = w.as[2 * pf + 1][m];
          w.as[2 * pf][m] = re * pl[pf][2] - im * pl[pf][3]; w.as[2 * pf + 1][m] = -(re * pl[pf][3]) - im * pl[pf][2];
          PIN(w.as[2 * pf][m]); PIN(w.as[2 * pf + 1][m]);
        }
      f32x2 P[2], Q[2];
#pragma unroll
      for (int k = 0; k < 8; ++k)
#pragma unroll
        for (int pf = 0; pf < 2; ++pf) {
          if (k == 0) { P[pf] = PAIR(w.as[2 * pf], 0); Q[pf] = PAIR(w.as[2 * pf + 1], 0); } else CSTEP(P[pf], Q[pf], pf, k);
        }
#pragma unroll
      for (int pf = 0; pf < 2; ++pf) {
        ea[pf] = (f32x2){P[pf].x, Q[pf].x};
        const f32x2 e = cfma(l8[pf], ea[pf], (f32x2){P[pf].y, Q[pf].y});
        const f32x2 l16 = {pm[pf][0], pm[pf][1]}, l32 = {pm[pf][2], pm[pf][3]};
        float a0, a1, b0, b1; sw16(e.x, a0, a1); sw16(e.y, b0, b1);
        const f32x2 r0 = {a0, b0};
        const f32x2 xx = cfma(l16, r0, (f32x2){a1, b1});
        float c0, c1, d0, d1; sw32(xx.x, c0, c1); sw32(xx.y, d0, d1);
        const f32x2 s0 = {c0, d0};
        agg[pf] = cfma(l32, s0, (f32x2){c1, d1});
        const f32x2 c3 = cfma(l16, s0, r0);
        cin[pf] = g == 0 ? (f32x2){0.f, 0.f} : g == 1 ? r0 : g == 2 ? s0 : c3;
        PIN(ea[pf]);
      }
    }
    f32x2 aggq = (g & 1) ? agg[1] : agg[0]; PIN(aggq); PIN(cin[0]); PIN(cin[1]);
    if (wr < 3) {
      if (g < 2) *(LAS f32x2*)mk_l(L_WA + ((g * 16 + r) * 4 + wr) * 8) = aggq;
      WAIT_L0();
      *(volatile LAS unsigned*)mk_l(L_FLG + wr * 4) = 1u;
    }
    SCHED();
#pragma unroll
    for (int n = 0; n < 3; ++n) { wg1[n] = lds_tr2(w.aW[n] + 40960, w.aW[n] + 40960 + 1024); wu1[n] = lds_tr2(w.aW[n] + 57344, w.aW[n] + 57344 + 1024); }
    if (wr == 3) {
      const lptr fp = mk_l(L_FLG);
_Pragma("nounroll")
      for (unsigned spins = 0; spins < SPIN_CAP; ++spins) {
        const u32x4 f = *(volatile LAS u32x4*)fp;
        if (__builtin_amdgcn_readfirstlane(f[0] & f[1] & f[2])) break;
      }
      asm volatile("" ::: "memory");
      const lptr ap = mk_l(L_WA + ((g & 1) * 16 + r) * 32);
      const f32x4 a01 = *(const LAS f32x4*)ap; const f32x2 a2 = *(const LAS f32x2*)(ap + 16);
      const f32x2 lw = *(const LAS f32x2*)mk_l(L_PRM + ((g & 1) * 16 + r) * 64 + 40);
      const f32x2 t = cfma(lw, cfma(lw, cfma(lw, (f32x2){a01[0], a01[1]}, (f32x2){a01[2], a01[3]}), a2), aggq);
      if (rch < 7) __hip_atomic_store((GAS u64*)(A.ws + WS_AG) + (c * 8 + 7 - rch) * 64 + lane, ((u64)epoch << 32) | __float_as_uint((g & 2) ? t.y : t.x), RLX_AGENT);
    }
    WAIT_L0(); lds_post(3); SCHED();
    STAMP();
    __builtin_amdgcn_s_setprio(1);
#pragma unroll
    for (int n = 1; n < 3; ++n) {
#pragma unroll
      for (int m = 0; m < 4; ++m) MFMA(w.wgf[n], w.xa[m], w.ag[n][m]);
#pragma unroll
      for (int m = 0; m < 4; ++m) MFMA(w.wuf[n], w.xa[m], w.au[n][m]);
    }
#pragma unroll
    for (int n = 0; n < 3; ++n) {
#pragma unroll
      for (int m = 0; m < 4; ++m) MFMA(wg1[n], xb[m], w.ag[n][m]);
#pragma unroll
      for (int m = 0; m < 4; ++m) MFMA(wu1[n], xb[m], w.au[n][m]);
    }
    __builtin_amdgcn_s_setprio(2);
    SCHED();
    lane = fresh_lane(); r = lane & 15; g = lane >> 4; qq = (lane & 15) >> 2; pp = lane & 3;
    q.voffH = lane * 16;
    const rsrc_t rag = mk_rsrc(A.ws + WS_AG + c * 4096, 4096u);
    const unsigned ago = lane * 8 + (8 - rch) * 512;
    u32x2 gv_[7];
#pragma unroll
    for (int cc = 0; cc < 7; ++cc) gv_[cc] = __builtin_amdgcn_raw_buffer_load_b64(rag, ago + cc * 512, 0, 16);
    {
      const lptr fp = mk_l(L_FLG);
_Pragma("nounroll")
      for (unsigned spins = 0; spins < SPIN_CAP; ++spins) {
        const u32x4 f = *(volatile LAS u32x4*)fp;
        if (__builtin_amdgcn_readfirstlane(f[0] & f[1] & f[2])) break;
      }
      asm volatile("" ::: "memory");
    }
    STAMP();
    f32x4 p2[2], tb4[2], a01[2]; f32x2 a2[2];
    const f32x2 l256q = *(const LAS f32x2*)mk_l(L_PRM + ((g & 1) * 16 + r) * 64 + 48);
    {
      const lptr pb = mk_l(L_PRM + r * 64), ap = mk_l(L_WA + r * 32), tp = mk_l(L_TAB + wr * 2048 + g * 512 + r * 16);
#pragma unroll
      for (int pf = 0; pf < 2; ++pf) {
        pl[pf] = *(const LAS f32x4*)(pb + pf * 1024); p2[pf] = *(const LAS f32x4*)(pb + pf * 1024 + 32); tb4[pf] = *(const LAS f32x4*)(tp + pf * 256);
        a01[pf] = *(const LAS f32x4*)(ap + pf * 512); a2[pf] = *(const LAS f32x2*)(ap + pf * 512 + 16);
      }
    }
    SCHED();
    PACK_S();
    P2_ADDR();
    PIN(ho[0]); PIN(ho[1]); PIN(ho[2]);
    SCHED();
    f32x2 car[2], cai[2], cmr[2], cmi[2];
#pragma unroll
    for (int pf = 0; pf < 2; ++pf) {
      const f32x2 l8_ = {p2[pf][0], p2[pf][1]}, l64 = {p2[pf][2], p2[pf][3]};
      f32x2 sw = wr > 0 ? (f32x2){a01[pf][0], a01[pf][1]} : (f32x2){0.f, 0.f};
      if (wr > 1) sw = cfma(l64, sw, (f32x2){a01[pf][2], a01[pf][3]});
      if (wr > 2) sw = cfma(l64, sw, a2[pf]);
      const f32x2 ca = cfma((f32x2){tb4[pf][0], tb4[pf][1]}, sw, cin[pf]), cb_ = cfma(l8_, ca, ea[pf]), cm = {tb4[pf][2], tb4[pf][3]}, cn = cmul(l8_, cm);
      car[pf] = (f32x2){ca.x, cb_.x}; cai[pf] = (f32x2){ca.y, cb_.y}; cmr[pf] = (f32x2){cm.x, cn.x}; cmi[pf] = (f32x2){cm.y, cn.y};
      PIN(car[pf]); PIN(cai[pf]); PIN(cmr[pf]); PIN(cmi[pf]);
    }
    STAMP();
_Pragma("nounroll")
    for (unsigned spins = 0; spins < SPIN_CAP; ++spins) {
      __builtin_amdgcn_s_waitcnt(0x0F70);
      unsigned bad = 0;
#pragma unroll
      for (int cc = 0; cc < 7; ++cc) bad |= gv_[cc][1] ^ (cc < rch ? epoch : 0u);
      if (__all(bad == 0u)) break;
      __builtin_amdgcn_s_sleep(1);
      asm volatile("" ::: "memory");
#pragma unroll
      for (int cc = 0; cc < 7; ++cc) gv_[cc] = __builtin_amdgcn_raw_buffer_load_b64(rag, ago + cc * 512, 0, 16);
    }
    __builtin_amdgcn_s_waitcnt(0x0F70);
    STAMP();
    f32x2 cb;
    {
      float are[7], aim[7];
#pragma unroll
      for (int cc = 0; cc < 7; ++cc) { const unsigned v = gv_[cc][0]; sw32(__uint_as_float(v), are[cc], aim[cc]); }
      cb = (f32x2){are[6], aim[6]};
#pragma unroll
      for (int cc = 5; cc >= 0; --cc) cb = cfma(l256q, cb, (f32x2){are[cc], aim[cc]});
    }
    unsigned hr[2][8], hi[2][8];
    {
      float cx[2], cy[2]; sw16(cb.x, cx[0], cx[1]); sw16(cb.y, cy[0], cy[1]);
      f32x2 P[2], Q[2];
#pragma unroll
      for (int pf = 0; pf < 2; ++pf) { P[pf] = car[pf] + cmr[pf] * cx[pf] - cmi[pf] * cy[pf]; Q[pf] = cai[pf] + cmr[pf] * cy[pf] + cmi[pf] * cx[pf]; }
#pragma unroll
      for (int k = 0; k < 8; ++k)
#pragma unroll
        for (int pf = 0; pf < 2; ++pf) { CSTEP(P[pf], Q[pf], pf, k); hr[pf][k] = pk_bf16(P[pf].x, P[pf].y); hi[pf][k] = pk_bf16(Q[pf].x, Q[pf].y); }
    }
#pragma unroll
    for (int i = 0; i < 8; ++i) {
      const unsigned* h = (i >> 2) ? hi[(i >> 1) & 1] : hr[(i >> 1) & 1];
      hq[6 + i] = (u32x4){h[(i & 1) * 4], h[(i & 1) * 4 + 1], h[(i & 1) * 4 + 2], h[(i & 1) * 4 + 3]}; PIN(hq[6 + i]);
    }
    STAMP(); TS2();
    WAIT_V(0); lds_wait(1);
    STAMP();
    q.template stageA<1, 1>(); q.template stageA<2, 0>();
  }
  }
  if (GRP == 0) { STAMP(); TS2(); }
  constexpr int G = GRP;
  int r, g;
  unsigned fl = 0u;
#define FLAG_LOAD(off) asm volatile("global_load_dword %0, %1, %2 sc1" : "+v"(fl) : "v"(off), "s"(A.ws) : "memory")
#define FLAG_CHECK(off) do { PIN(fl); if (!__all(fl == epoch)) wait_flags(A.ws, off, epoch); } while (0)
#define HQ_ST(i) __builtin_amdgcn_raw_buffer_store_b128(hq[i], rws, (i) < 4 ? ho[0] + (i) * 1024 : (i) < 6 ? ho[1] + ((i) - 4) * 2048 : ho[2] + (((i) - 6) >> 2) * 262144 + (((i) - 6) & 3) * 1024, 0, 16)
#define XTRA(st) ((st) == 13 ? 4 : 0)
  float gv[4];
  u32x2 xv[4], dv; float gr[4];
  const int t0 = tt * 128 + wm * 64 + kq * 2, dc0 = dt * 128 + wn * 64;
  auto side = [&](auto ST_) {
    constexpr int ST = decltype(ST_)::value;
    if constexpr (ST == 13) {
      const int l = fresh_lane(), g_ = l >> 4, q_ = (l & 15) >> 2, p_ = l & 3;
#pragma unroll
      for (int e = 0; e < 2; ++e) {
        const unsigned b = 49152 + G * 49152 + wm * 4096 + (g_ >> 1) * 2048 + p_ * 256 + 8 * (g_ & 1) * 16 + q_ * 16 + (e ^ (kq & 1)) * 8;
        q.aS[e] = mk_l(b + ((p_ >> 1) << 6)); q.aS2[e] = mk_l(b + (((p_ >> 1) ^ 1) << 6));
        q.aC[e] = mk_l((wn * 64 + (l & 15)) * 128 + (((g_ + 4 * e) ^ (((l & 15) >> 1) & 7)) << 4));
      }
    }
    if constexpr (ST == 13) {
      const int l = fresh_lane();
      const unsigned go = (unsigned)WS_G + (tt * 128 + wm * 64 + ((l & 15) >> 2) * 16 + tj(l)) * 4;
#pragma unroll
      for (int j = 0; j < 4; ++j) asm volatile("global_load_dword %0, %1, %2 sc1" : "=v"(gv[j]) : "v"(go + (q.pj[j] >> 7)), "s"(A.ws) : "memory");
    }
    if constexpr (ST >= 16 && ST < 20) {
      constexpr int m = ST - 16;
      PIN(gv[m]);
#pragma unroll
      for (int n = 0; n < 4; ++n) { q.af[m][n] *= 1.f - gv[m]; PIN(q.af[m][n]); }
    }
    if constexpr (ST == NSTEP - 1) {
      const int l = fresh_lane();
      dv = *(const u32x2*)(A.dsk + dc0 + (l & 15) * 4);
#pragma unroll
      for (int i = 0; i < 4; ++i) {
        xv[i] = *(const u32x2*)(A.x + (t0 + i * 16 + tj(l >> 4)) * D_ + dc0 + (l & 15) * 4);
        gr[i] = __int_as_float(__builtin_amdgcn_ds_bpermute((4 * i + (l >> 4)) * 4, __float_as_int(gv[0])));
      }
#pragma unroll
      for (int j = 0; j < 4; ++j) { gv[j] *= 2.f; PIN(gv[j]); }
    }
  };
  __builtin_amdgcn_s_setprio(0);
  if constexpr (G == 0) {
    const unsigned f2 = flc + 512 + (8 * kq + (lane & 7)) * 4, f3 = flc + 640 + (lane & 31) * 4;
    WAIT_V(8); lds_post(1); lds_wait(1); STAMP();
    q.template load<0, 0>(); WAIT_V(10); WAIT_L0(); lds_post(2); SCHED();
    static_for<0, NSTEP - 1>([&](auto ST_) {
      constexpr int ST = decltype(ST_)::value;
      constexpr int prev = (ST + 2 < NSTEP ? 6 : 0) + (ST == 4 || ST == 10 ? 1 : 0) + XTRA(ST);
      q.template compute<ST, prev>();
      q.template load<ST + 1, 0>();
      if constexpr (ST + 1 == 4) FLAG_LOAD(f2);
      if constexpr (ST + 1 == 5) { WAIT_V(6); FLAG_CHECK(f2); }
      if constexpr (ST + 1 == 10) FLAG_LOAD(f3);
      if constexpr (ST + 1 == 11) { WAIT_V(6); FLAG_CHECK(f3); }
      if constexpr (ST + 1 == 20) { if (blockIdx.x == 255 && wr == 0 && lane == 0) __hip_atomic_store((GAS unsigned*)(A.ws + WS_EP), epoch, RLX_AGENT); }
      side(std::integral_constant<int, ST + 1>{});
      WAIT_L0(); BAR(); TS2();
      if constexpr (ST == 1 || ST == 13) STAMP();
    });
  } else {
    HQ_ST(0); HQ_ST(1); HQ_ST(2);
    lds_wait(2);
    static_for<0, NSTEP>([&](auto ST_) {
      constexpr int ST = decltype(ST_)::value;
      constexpr int nst = ST == 0 ? 3 : ST <= 4 ? 2 : 0;
      if constexpr (ST == 2 || ST == 6) {
        if (wr == 0 && lane == 0) __hip_atomic_store((GAS unsigned*)(A.ws + flb + (ST == 2 ? 512 : 640) + c * 4), epoch, RLX_AGENT);
      }
      q.template load<ST, 1>();
      if constexpr (ST == 0) { HQ_ST(3); HQ_ST(4); HQ_ST(5); }
      if constexpr (ST >= 1 && ST <= 4) { HQ_ST(4 + 2 * ST); HQ_ST(5 + 2 * ST); }
      side(ST_);
      WAIT_L0(); BAR();
      if constexpr (ST + 1 < NSTEP) {
        q.template compute<ST, (ST + 2 < NSTEP ? 2 : 0) + (ST + 3 < NSTEP ? 4 : 0) + nst + XTRA(ST)>(); TS2();
        if constexpr (ST == 1 || ST == 13) STAMP();
      }
    });
  }
  lane = fresh_lane(); r = lane & 15; g = lane >> 4;
  f32x4 o[4];
  {
    unsigned xo[3], pk[4];
#pragma unroll
    for (int j = 1; j < 4; ++j) xo[j - 1] = (unsigned)WS_XB + ((tile * 4 + (kq ^ j)) * 3 + (j - 1)) * 16384u + wid * 2048 + lane * 16;
    __builtin_amdgcn_s_setprio(1);
#pragma unroll
    for (int k = 0; k < 19; ++k) {
      if (k < 16) { const int m = ((k >> 2) + 1) & 3, n = k & 3; MFMA(q.bb[0][n], q.ha[0][m], q.as[m][n]); MFMA(q.bb[1][n], q.ha[1][m], q.as[m][n]); }
      if (k == 3 || k >= 16) asm volatile("s_nop 3");
      if (G == 0 && k == 14) RBAR();
      SCHED();
      if (k >= 3) {
        const int m = (((k - 3) >> 2) + 1) & 3, n = (k - 3) & 3;
        f32x4 v;
#pragma unroll
        for (int j = 0; j < 4; ++j) v[j] = __builtin_fmaf(q.as[m][n][j], gv[m], q.af[m][n][j]);
        if (m == 0) o[n] = v;
        else {
          pk[(n & 1) * 2] = pk_bf16(v[0], v[1]); pk[(n & 1) * 2 + 1] = pk_bf16(v[2], v[3]);
          if (n & 1) __builtin_amdgcn_raw_buffer_store_b128((u32x4){pk[0], pk[1], pk[2], pk[3]}, rws, xo[m - 1] + (n >> 1) * 1024, 0, 16);
        }
      }
      SCHED();
    }
    __builtin_amdgcn_s_setprio(0);
  }
  STAMP();
  WAIT_V(0);
  if (lane >= 1 && lane < 4) __hip_atomic_store((GAS unsigned*)(A.ws + WS_XF + ((tile * 4 + (kq ^ lane)) * 8 + wid) * XS) + lane, epoch, RLX_AGENT);
  STAMP();
  f32x4 sk[4];
  {
    const f32x4 df = {bf2f(dv[0] & 0xffffu), bfhi(dv[0]), bf2f(dv[1] & 0xffffu), bfhi(dv[1])};
#pragma unroll
    for (int i = 0; i < 4; ++i) { sk[i] = (f32x4){bf2f(xv[i][0] & 0xffffu), bfhi(xv[i][0]), bf2f(xv[i][1] & 0xffffu), bfhi(xv[i][1])} * df * gr[i]; PIN(sk[i]); }
  }
  const lptr tw = mk_l(49152 + G * 49152 + wr * 4096 + r * 256), tr_ = mk_l(49152 + G * 49152 + wr * 4096 + g * 256);
  wait_flags(A.ws, (unsigned)WS_XF + ((tile * 4 + kq) * 8 + wid) * XS + (1 + (lane & 3) % 3) * 4, epoch);
  STAMP();
  {
    u32x4 pv[6];
    const unsigned xi = (unsigned)WS_XB + (tile * 4 + kq) * 3 * 16384u + wid * 2048 + lane * 16;
#pragma unroll
    for (int i = 0; i < 6; ++i) pv[i] = __builtin_amdgcn_raw_buffer_load_b128(rws, xi + (i >> 1) * 16384 + (i & 1) * 1024, 0, 16);
#pragma unroll
    for (int i = 0; i < 6; ++i)
#pragma unroll
      for (int h = 0; h < 2; ++h) {
        const unsigned a0 = pv[i][2 * h], a1 = pv[i][2 * h + 1];
        o[(i & 1) * 2 + h] += (f32x4){bf2f(a0 & 0xffffu), bfhi(a0), bf2f(a1 & 0xffffu), bfhi(a1)};
      }
  }
#pragma unroll
  for (int n = 0; n < 4; ++n) *(LAS f32x4*)(tw + (((n * 4 + g) ^ (r & 7)) << 4)) = o[n];
  WAIT_L0();
#pragma unroll
  for (int i = 0; i < 4; ++i) {
    const f32x4 v = *(const LAS f32x4*)(tr_ + i * 1024 + ((r ^ ((4 * i + g) & 7)) << 4));
    __builtin_amdgcn_raw_buffer_store_b128(__builtin_bit_cast(u32x4, v + sk[i]), mk_rsrc(A.out, L_ * D_ * 4), ((t0 + i * 16 + tj(g)) * D_ + dc0 + r * 4) * 4, 0, 17);
  }
  STAMP();
  ST_DUMP(A.ws, wid);
  if (GRP == 0) asm volatile(".Lgm_end0:"); else asm volatile(".Lgm_end1:");
  __builtin_amdgcn_endpgm();
  __builtin_unreachable();
}

__global__ __launch_bounds__(512, 2) void fused_kernel(Args A) {
  const u64 t00 = 0;
  const int wid = __builtin_amdgcn_readfirstlane(threadIdx.x >> 6);
  {
    const Pre pre = mk_pre(threadIdx.x & 63, wid, blockIdx.x);
    const int G = wid >> 2;
    const rsrc_t rx = mk_rsrc(A.x, L_ * D_ * 2);
#pragma unroll
    for (int s = 0; s < 4; ++s) dma16(pre.ldsX + s * 2048, pre.voffX, rx, pre.sX + s * 4096);
    dma16(pre.ldsW + G * 1024, pre.voffW, mk_rsrc(A.wg, D_ * F_ * 2), pre.sW + G * 28672u);
    dma16(pre.ldsW + 16384 + G * 1024, pre.voffW, mk_rsrc(A.wu, D_ * F_ * 2), pre.sW + G * 28672u);
    dma16(pre.ldsB + G * 4096, pre.voffB, mk_rsrc((wid & 2) ? A.bim : A.bre, P_ * D_ * 2), pre.sB + G * 32768);
  }
  const char* code; unsigned code_len;
  asm volatile("s_getpc_b64 %0\n.Lgm_pc:\n\ts_mov_b32 %1, .Lgm_end0-.Lgm_pc\n\ts_max_u32 %1, %1, .Lgm_end1-.Lgm_pc" : "=s"(code), "=s"(code_len) :: "scc");
  if (wid < 4) body<0>(A, wid, code, code_len, t00); else body<1>(A, wid, code, code_len, t00);
}

extern "C" void kernel_launch(void* const* d_in, const int* in_sizes, int n_in, void* d_out, int out_size, void* d_ws, size_t ws_size,
                              hipStream_t stream) {
  Args a; a.x = (const u16*)d_in[0]; a.lre = (const u16*)d_in[1]; a.lim = (const u16*)d_in[2]; a.bre = (const u16*)d_in[3];
  a.bim = (const u16*)d_in[4]; a.cre = (const u16*)d_in[5]; a.cim = (const u16*)d_in[6]; a.ldt = (const u16*)d_in[7]; a.dsk = (const u16*)d_in[8];
  a.wg = (const u16*)d_in[10]; a.wu = (const u16*)d_in[11]; a.wd = (const u16*)d_in[12]; a.gw = (const u16*)d_in[13];
  a.ws = (char*)d_ws; a.out = (float*)d_out;
  (void)hipFuncSetAttribute((const void*)fused_kernel, hipFuncAttributeMaxDynamicSharedMemorySize, LDS_BYTES);
  fused_kernel<<<256, 512, LDS_BYTES, stream>>>(a);
}
```

```cpp
#define XS 16
#include <hip/hip_runtime.h>
#include <hip/hip_bf16.h>
#include <type_traits>
#define MAP2 1
#define ORD2 1
#define MAP1 1
#define SQ 1
#define SO 1
#define PFD 4

typedef unsigned short u16;
typedef short bf16x8 __attribute__((ext_vector_type(8)));
typedef short s16x4 __attribute__((ext_vector_type(4)));
typedef float f32x4 __attribute__((ext_vector_type(4)));
typedef float f32x2 __attribute__((ext_vector_type(2)));
typedef unsigned u32x4 __attribute__((ext_vector_type(4)));
typedef unsigned u32x2 __attribute__((ext_vector_type(2)));
typedef unsigned long long u64;
#define LAS __attribute__((address_space(3)))
#define GAS __attribute__((address_space(1)))
#define RLX_AGENT __ATOMIC_RELAXED, __HIP_MEMORY_SCOPE_AGENT
typedef __amdgpu_buffer_rsrc_t rsrc_t;

constexpr int L_ = 2048, D_ = 1024, P_ = 1024, F_ = 3584;
constexpr int NKF = 176;
constexpr size_t WS_H = 0;
constexpr size_t H_BYTES = (size_t)16 * NKF * 8 * 1024;
constexpr size_t WS_XB = WS_H + H_BYTES;
constexpr size_t XB_BYTES = (size_t)64 * 4 * 3 * 16384;
constexpr size_t WS_G = WS_XB + XB_BYTES;
constexpr size_t WS_AG = WS_G + 8192;
constexpr size_t WS_EP = WS_AG + (size_t)32 * 8 * 64 * 8;
constexpr size_t WS_FL = WS_EP + 512;
constexpr size_t WS_XF = WS_FL + 8192;
constexpr size_t WS_END = WS_XF + 256 * 8 * XS;
constexpr unsigned SPIN_CAP = 1u << 20;

__device__ __forceinline__ float bf2f(unsigned b) { return __uint_as_float(b << 16); }
__device__ __forceinline__ float bfhi(unsigned b) { return __uint_as_float(b & 0xffff0000u); }
__device__ __forceinline__ unsigned pk_bf16(float lo, float hi) {
  typedef __bf16 bf2_t __attribute__((ext_vector_type(2)));
  return __builtin_bit_cast(unsigned, __builtin_convertvector((f32x2){lo, hi}, bf2_t)); }
__device__ __forceinline__ float relu(float v) { const int b = __float_as_int(v); return __int_as_float(b > 0 ? b : 0); }
template <int CTRL, int BANKS> __device__ __forceinline__ float dpp(float old, float src) {
  return __int_as_float(__builtin_amdgcn_update_dpp(__float_as_int(old), __float_as_int(src), CTRL, 0xf, BANKS, false)); }
__device__ __forceinline__ rsrc_t mk_rsrc(const void* p, unsigned bytes) {
  return __builtin_amdgcn_make_buffer_rsrc((void*)p, (short)0, (int)bytes, 0x00020000); }
__device__ __forceinline__ void dma16(unsigned ldsaddr, unsigned voff, rsrc_t srd, unsigned soff) {
  asm volatile("s_mov_b32 m0, %0\n\ts_nop 0\n\tbuffer_load_dwordx4 %1, %2, %3 offen lds"
               :: "s"(ldsaddr), "v"(voff), "s"(srd), "s"(soff) : "memory"); }
__device__ __forceinline__ void dma16_dev(unsigned ldsaddr, unsigned voff, rsrc_t srd, unsigned soff) {
  asm volatile("s_mov_b32 m0, %0\n\ts_nop 0\n\tbuffer_load_dwordx4 %1, %2, %3 offen sc1 lds"
               :: "s"(ldsaddr), "v"(voff), "s"(srd), "s"(soff) : "memory"); }
#define WAIT_V(n) asm volatile("s_waitcnt vmcnt(" #n ")" ::: "memory")
#define WAIT_VN(n) asm volatile("s_waitcnt vmcnt(%0)" :: "i"(n) : "memory")
#define WAIT_L0() do { __builtin_amdgcn_s_waitcnt(0xC07F); asm volatile("" ::: "memory"); } while (0)
#define SCHED() __builtin_amdgcn_sched_barrier(0)
#define RBAR() do { __builtin_amdgcn_s_barrier(); asm volatile("" ::: "memory"); } while (0)
#define BAR() do { SCHED(); RBAR(); SCHED(); } while (0)
__device__ __forceinline__ int fresh_lane() { int l; asm volatile("v_mbcnt_lo_u32_b32 %0, -1, 0\n\tv_mbcnt_hi_u32_b32 %0, -1, %0" : "=v"(l)); return l; }
typedef LAS char* lptr;
__device__ __forceinline__ lptr mk_l(unsigned a) { lptr p = (lptr)(size_t)a; asm volatile("" : "+v"(p)); return p; }
__device__ __forceinline__ bf16x8 lds_b128(lptr a) { return *(const LAS bf16x8*)a; }
__device__ __forceinline__ bf16x8 lds_tr2(lptr a0, lptr a1) {
  s16x4 lo = __builtin_amdgcn_ds_read_tr16_b64_v4i16((LAS s16x4*)a0);
  s16x4 hi = __builtin_amdgcn_ds_read_tr16_b64_v4i16((LAS s16x4*)a1);
  return __builtin_shufflevector(lo, hi, 0, 1, 2, 3, 4, 5, 6, 7); }
#define MFMA(a, b, c) asm volatile("v_mfma_f32_16x16x32_bf16 %0, %1, %2, %0" : "+v"(c) : "v"(a), "v"(b))
#define MFMA0(a, b, c) asm volatile("v_mfma_f32_16x16x32_bf16 %0, %1, %2, 0" : "=&v"(c) : "v"(a), "v"(b))
#define PIN(x) asm volatile("" : "+v"(x))
#define MFMA_DRAIN() do { asm volatile("s_nop 7\n\ts_nop 7" ::: "memory"); SCHED(); } while (0)

#define ST_DECL
#define STAMP()
#define TS2()
#define ST_DUMP(ws, wid)

struct Args {
  const u16 *x, *lre, *lim, *bre, *bim, *cre, *cim, *ldt, *dsk, *wg, *wu, *wd, *gw;
  char* ws; float* out;
};

constexpr unsigned L_X = 0, L_W = 65536, L_BS = 131072, L_PRM = 151552  ,
                   L_WA = 153600  , L_FLG = 154624  , L_SINK = 154880, L_TAB = 155136  , LDS_BYTES = 163840;

__device__ __forceinline__ int tj(int j) { return (j & 1) * 8 + ((j >> 1) & 1); }
__device__ __forceinline__ f32x2 cmul(f32x2 a, f32x2 b) { return (f32x2){a.x * b.x - a.y * b.y, a.x * b.y + a.y * b.x}; }
__device__ __forceinline__ f32x2 cfma(f32x2 a, f32x2 b, f32x2 c) {
  return (f32x2){__builtin_fmaf(a.x, b.x, __builtin_fmaf(-a.y, b.y, c.x)), __builtin_fmaf(a.x, b.y, __builtin_fmaf(a.y, b.x, c.y))}; }
__device__ __forceinline__ void sw16(float v, float& r0, float& r1) { auto t = __builtin_amdgcn_permlane16_swap(__float_as_uint(v), __float_as_uint(v), false, false); r0 = __uint_as_float(t[0]); r1 = __uint_as_float(t[1]); }
__device__ __forceinline__ void sw32(float v, float& r0, float& r1) { auto t = __builtin_amdgcn_permlane32_swap(__float_as_uint(v), __float_as_uint(v), false, false); r0 = __uint_as_float(t[0]); r1 = __uint_as_float(t[1]); }

__device__ __forceinline__ void touch(const char* p) {
  asm volatile("s_mov_b32 m0, %0\n\ts_nop 0\n\tglobal_load_lds_dword %1, off" :: "s"(L_SINK), "v"(p) : "memory"); }
template <int GRP>
struct P1 {
  static constexpr int NW = GRP == 0 ? 4 : 3, NS = GRP == 0 ? 0 : 4;
  f32x4 ag[NW][4], au[NW][4], as[4][4];
  bf16x8 xa[4], wgf[NW], wuf[NW], bsf[4];
  lptr aX[2], aB[2], aW[NW];
  unsigned voffX, voffW, voffB;
  unsigned sX, sW, sB, ldsX, ldsW, ldsB;
  rsrc_t rx, rwg, rwu, rb;
  const char* pfp; unsigned pfs; int pfo, pfm;
  __device__ __forceinline__ void pull(int T) { const int i = T + pfo; touch(pfp + pfs * (unsigned)(i < 0 ? 0 : i > pfm ? pfm : i)); }

  template <int BUF, int KS, class M> __device__ __forceinline__ void load(M&& mem) {
    mem(0);
#pragma unroll
    for (int m = 0; m < 4; ++m) xa[m] = lds_b128(aX[KS] + BUF * 32768 + m * 2048);
    mem(1);
#pragma unroll
    for (int i = 0; i < NS; ++i) bsf[i] = lds_b128(aB[KS] + BUF * 8192 + i * 2048);
    if (NS) mem(2);
#pragma unroll
    for (int n = 0; n < NW; ++n) {
      wgf[n] = lds_tr2(aW[n] + BUF * 32768 + KS * 8192, aW[n] + BUF * 32768 + KS * 8192 + 1024);
      wuf[n] = lds_tr2(aW[n] + BUF * 32768 + 16384 + KS * 8192, aW[n] + BUF * 32768 + 16384 + KS * 8192 + 1024);
      mem(n + (NS ? 3 : 2));
    }
  }
  template <int BUF, int KS> __device__ __forceinline__ void load() { load<BUF, KS>([](int) {}); }
  template <int BUF> __device__ __forceinline__ void dmaX1(int T, int s) { dma16(ldsX + BUF * 32768 + s * 2048, voffX, rx, sX + T * 128 + s * 4096); }
  template <int BUF, int HH> __device__ __forceinline__ void dmaW1(int T, int i) {
    dma16(ldsW + BUF * 32768 + (i & 1) * 16384 + HH * 8192 + (i >> 1) * 1024, voffW, (i & 1) ? rwu : rwg, sW + (unsigned)(T * 64 + HH * 32 + (i >> 1) * 4) * 7168u);
  }
  template <int BUF> __device__ __forceinline__ void dmaB1(int T, int pf) { dma16(ldsB + BUF * 8192 + pf * 4096, voffB, rb, sB + pf * 32768 + T * 128); }
  template <int NV, bool FIRST = false> __device__ __forceinline__ void compute() {
    constexpr int NM = 4 * NS + 8 * NW;
    __builtin_amdgcn_s_setprio(1);
#pragma unroll
    for (int k = 0; k < NM; ++k) {
      const int f = k >> 2, m = (f & 1) ? 3 - (k & 3) : (k & 3);
      const int n = f < NS ? 0 : (f - NS) >> 1;
      const bf16x8& a = f < NS ? xa[m] : ((f - NS) & 1) ? wuf[n] : wgf[n];
      const bf16x8& b = f < NS ? bsf[f & 3] : xa[m];
      f32x4& c = f < NS ? as[f & 3][m] : ((f - NS) & 1) ? au[n][m] : ag[n][m];
      if (FIRST) MFMA0(a, b, c); else MFMA(a, b, c);
    }
    if (NV != -2) { if (NV >= 0) WAIT_VN(NV < 0 ? 0 : NV); RBAR(); }
    __builtin_amdgcn_s_setprio(0);
    SCHED();
  }
  template <int BUF> __device__ __forceinline__ void dmaX(int T) {
#pragma unroll
    for (int s = 0; s < 4; ++s) dma16(ldsX + BUF * 32768 + s * 2048, voffX, rx, sX + T * 128 + s * 4096);
  }
  template <int BUF, int HH> __device__ __forceinline__ void dmaW(int T) {
#pragma unroll
    for (int s = 0; s < 2; ++s) {
      const unsigned so = sW + (unsigned)(T * 64 + HH * 32 + s * 4) * 7168u;
      dma16(ldsW + BUF * 32768 + HH * 8192 + s * 1024, voffW, rwg, so);
      dma16(ldsW + BUF * 32768 + 16384 + HH * 8192 + s * 1024, voffW, rwu, so);
    }
  }
  template <int BUF> __device__ __forceinline__ void dmaB(int T) {
#pragma unroll
    for (int pf = 0; pf < 2; ++pf) dma16(ldsB + BUF * 8192 + pf * 4096, voffB, rb, sB + pf * 32768 + T * 128);
  }
};

template <int BUF> __device__ __forceinline__ void p1_tile(P1<0>& w, int T) {
  w.template compute<7>();
  w.template load<BUF, 1>([&](int i) { if (i < 4) w.template dmaW1<BUF ^ 1, 1>(T + 1, i); else if (i == 4) w.pull(T); }); WAIT_L0(); BAR();
  w.template compute<5>();
  w.template load<BUF ^ 1, 0>([&](int i) { if (i < 4) w.template dmaX1<BUF>(T + 2, i); else w.template dmaB1<BUF>(T + 2, i - 4); }); WAIT_L0(); BAR();
}
template <int BUF> __device__ __forceinline__ void p1_tile(P1<1>& w, int T) {
  w.template load<BUF, 0>([&](int i) { if (i >= SO && i < SO + 4) w.template dmaX1<BUF ^ 1>(T + 1, i - SO); }); WAIT_L0(); BAR();
  w.template compute<-1>();
  w.template load<BUF, 1>([&](int i) { if (i >= SO && i < SO + 4) w.template dmaW1<BUF, 0>(T + 2, i - SO); }); WAIT_V(4); WAIT_L0(); BAR();
  w.template compute<-1>();
}

constexpr int NSTEP = 22;
struct P2 {
  f32x4 af[4][4], as[4][4];
  bf16x8 ha[2][4], bb[2][4];
  lptr aH, aS[2], aS2[2], aW[4], aC[2];
  unsigned voffH, voffW, voffC;
  unsigned sH, sHs, sW, sC, ldsA, ldsB, kk, pj[4], ps;
  rsrc_t rh, rw, rc, rc2_;
  template <int ST> __device__ __forceinline__ unsigned kf() const {
    constexpr int e = 2 * (ST - 8), k0 = ST < 8 ? 7 * (ST >> 1) + 3 * (ST & 1) + 1 : 7 * (e / 3) + 3 * (e % 3), k1 = ST < 8 ? k0 + 1 : 7 * ((e + 1) / 3) + 3 * ((e + 1) % 3);
    return k0 + kk * (k1 - k0);
  }
  template <int ST, int GA> __device__ __forceinline__ void dmaA(int j) {
    const unsigned so = ST < 14 ? sH + kf<ST>() * 8192u : sHs + ((ST - 14) & 3) * 16384u + ((ST - 14) >> 2) * 262144u;
    dma16_dev(ldsA + GA * 49152 + (ST % 3) * 16384 + j * 1024, voffH, rh, so + GA * (NKF * 8192u) + (ST < 14 ? pj[j] : (unsigned)(j << 10) ^ ps));
  }
  template <int ST> __device__ __forceinline__ void dmaB(int s) {
    const unsigned l = ldsB + (ST % 3) * 16384 + s * 2048;
    if (ST >= 14) dma16(l, voffC, ST < 18 ? rc : rc2_, sC + ((ST - 14) & 3) * 128u + s * 32768u);
    else dma16(l, voffW, rw, sW + kf<ST>() * 65536u + s * 16384u);
  }
  template <int ST, int GA> __device__ __forceinline__ void stageA() {
#pragma unroll
    for (int j = 0; j < 4; ++j) dmaA<ST, GA>(j);
  }
  template <int ST> __device__ __forceinline__ void stageB() {
#pragma unroll
    for (int s = 0; s < 4; ++s) dmaB<ST>(s);
  }
  template <int ST, int G> __device__ __forceinline__ void load() {
    constexpr int SL = (ST % 3) * 16384;
#pragma unroll
    for (int k2 = 0; k2 < 2; ++k2) {
#pragma unroll
      for (int n = 0; n < 4; ++n) {
        const int i = k2 * 4 + n;
        if constexpr (G == 0) { if constexpr (ST + 2 < NSTEP) { if (i < 4) dmaB<ST + 2>(i); else if (i < 6) dmaA<ST + 2, 1>(i - 4); } }
        else { if constexpr (ST + 2 < NSTEP) { if (i < 2) dmaA<ST + 2, 1>(i + 2); } if constexpr (ST + 3 < NSTEP) { if (i >= 2 && i < 6) dmaA<ST + 3, 0>(i - 2); } }
        if (ST >= 14) bb[k2][n] = lds_b128(aC[k2] + SL + n * 2048);
        else bb[k2][n] = lds_tr2(aW[n] + SL + k2 * 8192, aW[n] + SL + k2 * 8192 + 4096);
        if (ST >= 14) ha[k2][n] = lds_tr2(aS[n & 1] + SL + k2 * 8192 + (n >> 1) * 1024, aS2[n & 1] + SL + k2 * 8192 + (n >> 1) * 1024);
        else ha[k2][n] = lds_b128(aH + SL + k2 * 8192 + n * 1024);
      }
    }
  }
  template <int ST, int NV> __device__ __forceinline__ void compute() {
    __builtin_amdgcn_s_setprio(1);
#pragma unroll
    for (int k = 0; k < 32; ++k) {
      const int k2 = k >> 4, m = (k >> 2) & 3, n = (m & 1) ? 3 - (k & 3) : (k & 3);
      f32x4& c = ST >= 14 ? as[m][n] : af[m][n];
      if ((ST == 0 || ST == 14) && k2 == 0) MFMA0(bb[k2][n], ha[k2][m], c); else MFMA(bb[k2][n], ha[k2][m], c);
    }
    WAIT_VN(NV); RBAR();
    __builtin_amdgcn_s_setprio(0);
    SCHED();
  }
};
template <int I, int N, class F> __device__ __forceinline__ void static_for(F&& f) {
  if constexpr (I < N) { f(std::integral_constant<int, I>{}); static_for<I + 1, N>(f); }
}

__device__ __forceinline__ void arrive(char* ws, int lane, unsigned flag_off, int ctr, unsigned epoch) {
  if (lane == 0) {
    const unsigned old = __hip_atomic_fetch_add((LAS unsigned*)mk_l(L_FLG + 16 + ctr * 4), 1u, __ATOMIC_RELAXED, __HIP_MEMORY_SCOPE_WORKGROUP);
    if (old == 3u) __hip_atomic_store((GAS unsigned*)(ws + flag_off), epoch, RLX_AGENT);
  }
}
__device__ __forceinline__ void lds_post(int ctr) {
  if (fresh_lane() == 0) __hip_atomic_fetch_add((LAS unsigned*)mk_l(L_FLG + 16 + ctr * 4), 1u, __ATOMIC_RELAXED, __HIP_MEMORY_SCOPE_WORKGROUP);
}
__device__ __forceinline__ void lds_wait(int ctr, unsigned n = 4u) {
  const lptr p = mk_l(L_FLG + 16 + ctr * 4);
_Pragma("nounroll")
  for (unsigned spins = 0; spins < (SPIN_CAP << 6); ++spins) {
    if (__builtin_amdgcn_readfirstlane(*(volatile LAS unsigned*)p) >= n) break;
    __builtin_amdgcn_s_sleep(1);
  }
  asm volatile("" ::: "memory");
}
__device__ __forceinline__ void wait_flags(const char* ws, unsigned off, unsigned epoch) {
_Pragma("nounroll")
  for (unsigned spins = 0; spins < SPIN_CAP; ++spins) {
    if (__all(__hip_atomic_load((const GAS unsigned*)(ws + off), RLX_AGENT) == epoch)) break;
    __builtin_amdgcn_s_sleep(1);
  }
  asm volatile("" ::: "memory");
}

struct Pre { unsigned voffX, voffW, voffB, sX, ldsX, sW, ldsW, sB, ldsB; };
__device__ __forceinline__ Pre mk_pre(int lane, int wid, int bid) {
  Pre pre;
  const int wr = wid & 3, G = wid >> 2, idx = bid >> 3, c = ((bid & 7) >> 1) * 8 + (idx & 7), rch = (bid & 1) * 4 + (idx >> 3);
  const unsigned sw8 = ((lane & 7) ^ (((wr & 1) * 4 + (lane >> 4)) & 7)) * 16;
  pre.voffX = ((lane >> 5) * 16 + tj(lane >> 3)) * 2048 + sw8;
  pre.voffB = (lane >> 3) * 2048 + sw8;
  pre.voffW = (lane >> 4) * 7168 + ((((lane & 15) >> 1) ^ ((lane >> 4) | ((wr & 1) << 2))) * 32) + (lane & 1) * 16;
  pre.sX = (unsigned)(rch * 256 + (G * 2 + (wr >> 1)) * 64 + (wr & 1) * 32) * 2048u;
  pre.ldsX = L_X + (G * 16 + (wr >> 1) * 8 + (wr & 1)) * 1024;
  const int ii = (wr & 1) * 2 + (wr >> 1) * 4;
  pre.sW = (unsigned)(ii * 4) * 7168u + (unsigned)((224 * c) & ~63);
  pre.ldsW = L_W + ii * 1024;
  pre.sB = (unsigned)(c * 32 + (wr & 1) * 8) * 2048u;
  pre.ldsB = L_BS + wr * 1024;
  return pre;
}
template <int GRP>
__device__ __forceinline__ void body(const Args& A, int wid, const char* code, const unsigned code_len, const u64 t00) {
  asm volatile("" : "+s"(wid));
  ST_DECL STAMP();
  const int xcd = blockIdx.x & 7, idx = blockIdx.x >> 3, c = (xcd >> 1) * 8 + (idx & 7), rch = (xcd & 1) * 4 + (idx >> 3);
  const int xrow = (xcd & 1) * 1024 + (xcd >> 1) * 256 + idx * 8;
  const int kq = xcd >> 1, rc2 = (xcd & 1) * 4 + (idx >> 3), dt = idx & 7;
  const int tt = 2 * rc2 + GRP, tile = rc2 * 8 + dt;
  const unsigned flb = (unsigned)WS_FL + rch * 1024, flc = (unsigned)WS_FL + rc2 * 1024;
  const int wr = wid & 3, wm = wr >> 1, wn = wr & 1;
  int lane = fresh_lane();
  unsigned epoch;
  P2 q;
  u32x4 hq[14]; unsigned ho[3];
  const rsrc_t rws = mk_rsrc(A.ws, (unsigned)WS_END);
  {
  P1<GRP> w;
  constexpr int NW = P1<GRP>::NW, NS = P1<GRP>::NS;
  int r = lane & 15, g = lane >> 4, qq = (lane & 15) >> 2, pp = lane & 3;
  const Pre pre = mk_pre(lane, wid, blockIdx.x);
  w.rx = mk_rsrc(A.x, L_ * D_ * 2); w.rwg = mk_rsrc(A.wg, D_ * F_ * 2); w.rwu = mk_rsrc(A.wu, D_ * F_ * 2);
  w.rb = mk_rsrc((wr >> 1) ? A.bim : A.bre, P_ * D_ * 2);
  w.voffX = pre.voffX; w.voffB = pre.voffB; w.voffW = pre.voffW;
  w.sX = pre.sX; w.ldsX = pre.ldsX; w.sW = pre.sW; w.ldsW = pre.ldsW; w.sB = pre.sB; w.ldsB = pre.ldsB;
  const char* wbase = (const char*)((wr & 1) ? A.wu : A.wg) + (idx * 2 + (wr >> 1)) * 7168 + xcd * 896;
  const char* rbase = wr < 2 ? (const char*)A.x + (xrow + wr * 4) * 2048 : (const char*)(wr == 2 ? A.bre : A.bim) + (xcd * 128 + idx * 4) * 2048;
  if (GRP == 1) {
    w.template dmaW<0, 1>(0); w.template dmaX<1>(1); w.template dmaW<1, 0>(1);
    const unsigned co = ((idx * 4 + wr) * 4 + (lane & 3)) * 128u;
    touch(lane < 16 ? wbase + (2 + (lane >> 3)) * 458752u + ((lane & 7) < 7 ? (lane & 7) : 0) * 128 : lane < 24 ? rbase + ((lane - 16) >> 1) * 2048 + (2 + (lane & 1)) * 128
                    : code + (co < code_len ? co : 0u));
  }
  {
#pragma unroll
    for (int ks = 0; ks < 2; ++ks) {
      const unsigned ch = ((g + 4 * ks) ^ ((r >> 1) & 7)) << 4;
      w.aX[ks] = mk_l(L_X + (wr * 64 + r) * 128 + ch);
      if (NS) w.aB[ks] = mk_l(L_BS + r * 128 + ch);
    }
    const unsigned swz = qq | ((g & 1) << 2);
#pragma unroll
    for (int n = 0; n < NW; ++n) {
      unsigned col;
      if (GRP == 0) col = (((2 + n) ^ swz) << 5) + pp * 8;
      else if (n < 2) col = (((((c & 1) ? 6 : 0) + n) ^ swz) << 5) + pp * 8;
      else col = (((((c & 1) ? 0 : 6) + (pp >> 1)) ^ swz) << 5) + (c & 1) * 16 + (pp & 1) * 8;
      w.aW[n] = mk_l(L_W + (8 * g + qq) * 256 + col);
    }
  }
  if constexpr (GRP == 0) {
    w.template dmaX<1>(1); w.template dmaB<1>(1);
    {
      const unsigned j = idx * 4 + wr, co = (j * 4 + (lane & 3)) * 128u;
      const int l = lane < 32 ? lane : 31;
      if (l < 7) { w.pfp = wbase + l * 128; w.pfs = 458752u; w.pfo = PFD; w.pfm = 15; }
      else if (l < 15) { w.pfp = rbase + ((l - 7) >> 1) * 2048 + ((l - 7) & 1) * 128; w.pfs = 256u; w.pfo = 2; w.pfm = 7; }
      else if (l < 23) { w.pfp = (const char*)A.wd + (kq * 896 + j) * 2048u + (xcd & 1) * 1024 + (l - 15) * 128; w.pfs = 262144u; w.pfo = -7; w.pfm = 6; }
      else if (l < 27) { w.pfp = (const char*)((j & 1) ? A.cim : A.cre) + ((xcd & 1) * 512 + (j >> 1)) * 2048u + kq * 512 + (l - 23) * 128; w.pfs = 131072u; w.pfo = 0; w.pfm = 7; }
      else { w.pfp = l < 31 ? code + (co < code_len ? co : 0u) : (const char*)((idx & 4) ? A.dsk : A.gw) + ((idx & 3) * 4 + wr) * 128; w.pfs = 0u; w.pfo = 0; w.pfm = 0; }
    }
    WAIT_V(6); BAR();
    w.template load<0, 0>();
    WAIT_L0(); BAR();
    STAMP(); TS2();
    w.template compute<-1, true>();
    w.template load<0, 1>([&](int i) { if (i < 4) w.template dmaW1<1, 1>(1, i); else if (i == 4) w.pull(0); }); WAIT_L0(); BAR();
    w.template compute<5>();
    w.template load<1, 0>([&](int i) { if (i < 4) w.template dmaX1<0>(2, i); else w.template dmaB1<0>(2, i - 4); }); WAIT_L0(); BAR(); TS2();
    p1_tile<1>(w, 1);
  } else {
    WAIT_V(20);
    const lptr rp = mk_l(L_TAB + wr * 2048 + (lane & 31) * 2);
    const float lr = bf2f(*(const LAS u16*)rp), li = bf2f(*(const LAS u16*)(rp + 64)), ld = bf2f(*(const LAS u16*)(rp + 128));
    epoch = __builtin_amdgcn_readfirstlane(*(const LAS unsigned*)mk_l(L_TAB + wr * 2048 + 192)) + 1u;
    const float dt_ = bf2f(pk_bf16(__expf(ld), 0.f) & 0xffffu);
    const float e = __expf(dt_ * lr), rev = __builtin_amdgcn_fractf(dt_ * li * 0.15915494309189535f);
    f32x2 lam = {e * __builtin_amdgcn_cosf(rev), -e * __builtin_amdgcn_sinf(rev)};
    PIN(lam);
    WAIT_V(13); BAR();
    {
      const float nr = lam.x - 1.f, rden = 1.f / (lr * lr + li * li);
      f32x2 l8 = cmul(lam, lam); l8 = cmul(l8, l8); l8 = cmul(l8, l8);
      const f32x2 l16 = cmul(l8, l8), l32 = cmul(l16, l16), l64 = cmul(l32, l32), l128 = cmul(l64, l64);
      f32x2 pw = (wr & 1) ? l64 : (f32x2){1.f, 0.f}; if (wr & 2) pw = cmul(pw, l128);
      const f32x2 q0 = (lane & 32) ? l32 : (f32x2){1.f, 0.f}, q1 = cmul(q0, l16), c0 = cmul(q0, pw), c1 = cmul(q1, pw);
      const lptr tp = mk_l(L_TAB + wr * 2048 + (lane >> 5) * 1024 + (lane & 31) * 16);
      *(LAS f32x4*)tp = (f32x4){q0.x, q0.y, c0.x, c0.y}; *(LAS f32x4*)(tp + 512) = (f32x4){q1.x, q1.y, c1.x, c1.y};
      const lptr pb = mk_l(L_PRM + (lane & 31) * 64);
      if (wr == 0) *(LAS f32x4*)pb = (f32x4){lam.x, lam.y, (nr * lr - lam.y * li) * rden, (-lam.y * lr - nr * li) * rden};
      else if (wr == 1) { *(LAS f32x4*)(pb + 16) = (f32x4){l16.x, l16.y, l32.x, l32.y}; *(LAS f32x4*)(pb + 32) = (f32x4){l8.x, l8.y, l64.x, l64.y}; *(LAS f32x2*)(pb + 48) = cmul(l128, l128); }
      else if (wr == 3) *(LAS unsigned*)mk_l(L_FLG + (lane & 7) * 4) = (lane & 7) == 3 ? epoch : 0u;
    }
    BAR();
    STAMP(); TS2();
    w.template load<0, 0>(); WAIT_V(9); WAIT_L0(); BAR();
    w.template compute<-1, true>();
    w.template load<0, 1>([&](int i) { if (i >= SO && i < SO + 4) w.template dmaW1<0, 0>(2, i - SO); }); WAIT_V(4);
    if (wr == 2) {
      __hip_atomic_store((GAS u64*)(A.ws + WS_AG) + (c * 8 + 7 - rch) * 64 + lane, (u64)(epoch - 1u) << 32, RLX_AGENT);
      if (lane < 6) __hip_atomic_store((GAS unsigned*)(A.ws + flb + lane * 128 + c * 4), epoch - 1u, RLX_AGENT);
      else if (lane >= 32 && (lane & 3)) __hip_atomic_store((GAS unsigned*)(A.ws + WS_XF + ((tile * 4 + (kq ^ (lane & 3))) * 8 + ((lane - 32) >> 2)) * XS) + (lane & 3), epoch - 1u, RLX_AGENT);
    }
    WAIT_L0(); BAR();
    w.template compute<-1>(); TS2();
    p1_tile<1>(w, 1);
  }
  for (int T = 2; T < 14; T += 2) {
    TS2(); p1_tile<0>(w, T); TS2();
    p1_tile<1>(w, T + 1);
  }
  if (GRP == 0) epoch = __builtin_amdgcn_readfirstlane(*(const LAS unsigned*)mk_l(L_FLG + 12));
  STAMP();
  lane = fresh_lane();
  q.rh = mk_rsrc(A.ws + WS_H, (unsigned)H_BYTES); q.rw = mk_rsrc(A.wd, F_ * D_ * 2); q.rc = mk_rsrc((kq >> 1) ? A.cim : A.cre, D_ * P_ * 2);
  q.voffW = (lane >> 4) * 2048 + ((((lane & 15) >> 1) ^ (((wr & 1) << 2) | (lane >> 4))) * 32) + (lane & 1) * 16;
  {
    const int i0 = (wr & 1) + (wr >> 1) * 8;
    q.kk = wr >> 1;
    q.sH = (unsigned)(2 * rc2 * NKF + 28 * kq) * 8192u + (wr & 1) * 4096u;
    q.sHs = (unsigned)(2 * rc2 * NKF + 112 + 16 * kq + (wr >> 1)) * 8192u + (wr & 1) * 4096u;
    q.sW = (unsigned)(28 * kq * 32 + (wr & 1) * 4) * 2048u + dt * 256u;
    q.sC = (unsigned)(dt * 128 + i0 * 8) * 2048u + (kq & 1) * 1024u;
#pragma unroll
    for (int j = 0; j < 4; ++j) q.pj[j] = (unsigned)(j ^ kq) << 10;
    q.rc = mk_rsrc(A.cre, D_ * P_ * 2); q.rc2_ = mk_rsrc(A.cim, D_ * P_ * 2);
    q.sHs = (unsigned)(2 * rc2 * NKF + 112 + 8 * kq + (wr >> 1)) * 8192u + (wr & 1) * 4096u;
    q.sC = (unsigned)(dt * 128 + i0 * 8) * 2048u + kq * 512u;
    q.ps = (unsigned)(kq >> 1) << 10;
    q.ldsA = 49152 + wr * 4096; q.ldsB = i0 * 1024;
  }
  u32x4 gx[4], gg[4];
  const int tg = rch * 256 + c * 8 + wr * 2 + (lane >> 5);
  if constexpr (GRP == 0) {
    w.template compute<7>();
    w.template load<0, 1>(); w.template dmaW<1, 1>(15);
#pragma unroll
    for (int i = 0; i < 4; ++i) gx[i] = *(const u32x4*)(A.x + tg * D_ + i * 256 + (lane & 31) * 8);
    WAIT_L0(); BAR();
    w.template compute<8>();
    w.template load<1, 0>(); q.template stageB<0>();
#pragma unroll
    for (int i = 0; i < 4; ++i) gg[i] = *(const u32x4*)(A.gw + i * 256 + (lane & 31) * 8);
    WAIT_L0(); BAR();
    w.template compute<12>();
    w.template load<1, 1>();
    {
      typedef __bf16 bf2_t __attribute__((ext_vector_type(2)));
      float s0 = 0.f, s1 = 0.f;
#pragma unroll
      for (int i = 0; i < 4; ++i)
#pragma unroll
        for (int j = 0; j < 4; j += 2) {
          const unsigned a0 = gx[i][j], b0 = gg[i][j], a1 = gx[i][j + 1], b1 = gg[i][j + 1];
          s0 = __builtin_amdgcn_fdot2_f32_bf16(__builtin_bit_cast(bf2_t, a0), __builtin_bit_cast(bf2_t, b0), s0, false);
          s1 = __builtin_amdgcn_fdot2_f32_bf16(__builtin_bit_cast(bf2_t, a1), __builtin_bit_cast(bf2_t, b1), s1, false);
        }
      float s = s0 + s1;
      s += dpp<0x128, 0xf>(0.f, s); s += dpp<0x124, 0xf>(0.f, s); s += dpp<0x122, 0xf>(0.f, s); s += dpp<0x121, 0xf>(0.f, s);
      float t0, t1; sw16(s, t0, t1); s = t0 + t1;
      if ((lane & 31) == 0) __hip_atomic_store((GAS unsigned*)(A.ws + WS_G) + tg, __float_as_uint(1.f / (1.f + __expf(-s))), RLX_AGENT);
    }
    WAIT_L0(); BAR();
    w.template compute<-2>();
    __builtin_amdgcn_s_setprio(3);
  } else {
    w.template load<0, 0>(); w.template dmaX<1>(15); WAIT_L0(); BAR();
    w.template compute<-1>();
    w.template load<0, 1>(); WAIT_V(0); WAIT_L0(); BAR();
    w.template compute<-1>();
  }
  lane = fresh_lane(); r = lane & 15; g = lane >> 4; qq = (lane & 15) >> 2; pp = lane & 3;
  const unsigned hb = (unsigned)WS_H + (unsigned)(2 * rch + (wr >> 1)) * NKF * 8192u + (wr & 1) * 4096u;
  const int kf0 = 7 * (c >> 1) + 3 * (c & 1);
  q.voffH = lane * 16;
#define FFN_H(n, m, h) pk_bf16(relu(w.ag[n][m][2 * (h)]) * w.au[n][m][2 * (h)], relu(w.ag[n][m][2 * (h) + 1]) * w.au[n][m][2 * (h) + 1])
#define P2_ADDR() do { q.voffC = (lane >> 3) * 2048 + (((lane & 7) ^ (((wr & 1) * 4 + (lane >> 4)) & 7)) * 16); PIN(q.voffC); \
    q.aH = mk_l(49152 + GRP * 49152 + wm * 4096 + lane * 16); \
    _Pragma("unroll") for (int n = 0; n < 4; ++n) q.aW[n] = mk_l((4 * g + qq) * 256 + (((wn * 4 + n) ^ (4 * (g & 1) + qq)) << 5) + pp * 8); } while (0)
#define PACK_S() do { _Pragma("unroll") for (int m = 0; m < 4; ++m) { hq[m] = (u32x4){FFN_H(0, m, 0), FFN_H(0, m, 1), FFN_H(1, m, 0), FFN_H(1, m, 1)}; PIN(hq[m]); } \
    _Pragma("unroll") for (int m = 0; m < 4; m += 2) {     \
      const auto s0 = __builtin_amdgcn_permlane32_swap(FFN_H(2, m, 0), FFN_H(2, m + 1, 0), false, false); \
      const auto s1 = __builtin_amdgcn_permlane32_swap(FFN_H(2, m, 1), FFN_H(2, m + 1, 1), false, false); \
      hq[4 + m / 2] = (u32x4){s0[0], s1[0], s0[1], s1[1]}; PIN(hq[4 + m / 2]); } \
    ho[0] = lane * 16 + hb + (kf0 + ((c & 1) ? 3 : 0)) * 8192u; \
    ho[1] = (lane & 31) * 16 + (lane >> 5) * 1024 + (c & 1) * 512 + hb + (kf0 + ((c & 1) ? 0 : 3)) * 8192u; \
    ho[2] = (lane ^ ((lane >> 5) << 2)) * 16 + hb + (112 + c) * 8192u; } while (0)
  if constexpr (GRP == 0) {
    MFMA_DRAIN();
    STAMP();
#pragma unroll
    for (int f = 0; f < 2; ++f)
#pragma unroll
      for (int m = 0; m < 4; ++m) {
        __builtin_amdgcn_raw_buffer_store_b128((u32x4){FFN_H(2 * f, m, 0), FFN_H(2 * f, m, 1), FFN_H(2 * f + 1, m, 0), FFN_H(2 * f + 1, m, 1)}, rws,
                                               lane * 16 + m * 1024 + hb + (kf0 + 1 + f) * 8192u, 0, 16);
        SCHED();
      }
    STAMP();
    WAIT_V(0); arrive(A.ws, lane, flb + (c >> 3) * 128 + c * 4, 0, epoch);
    __builtin_amdgcn_s_setprio(0);
    STAMP();
    P2_ADDR();
    if (wr == 0) { wait_flags(A.ws, flc + kq * 160 + (lane & 7) * 4, epoch); lds_post(3); }
    lds_wait(3, 5u);
    STAMP();
    q.template stageA<0, 0>(); q.template stageA<0, 1>(); q.template stageA<1, 0>();
    STAMP();
  } else {
    bf16x8 xb[4], bs1[4], wg1[3], wu1[3];
#pragma unroll
    for (int m = 0; m < 4; ++m) { w.xa[m] = lds_b128(w.aX[0] + 32768 + m * 2048); xb[m] = lds_b128(w.aX[1] + 32768 + m * 2048); }
#pragma unroll
    for (int i = 0; i < 4; ++i) { w.bsf[i] = lds_b128(w.aB[0] + 8192 + i * 2048); bs1[i] = lds_b128(w.aB[1] + 8192 + i * 2048); }
    w.wgf[0] = lds_tr2(w.aW[0] + 32768, w.aW[0] + 32768 + 1024); w.wuf[0] = lds_tr2(w.aW[0] + 49152, w.aW[0] + 49152 + 1024);
    q.template stageB<1>(); WAIT_L0(); BAR();
    __builtin_amdgcn_s_setprio(1);
#pragma unroll
    for (int i = 0; i < 4; ++i)
#pragma unroll
      for (int m = 0; m < 4; ++m) MFMA(w.xa[m], w.bsf[i], w.as[i][m]);
#pragma unroll
    for (int i = 0; i < 4; ++i)
#pragma unroll
      for (int m = 0; m < 4; ++m) MFMA(xb[m], bs1[i], w.as[i][m]);
#pragma unroll
    for (int m = 0; m < 4; ++m) MFMA(w.wgf[0], w.xa[m], w.ag[0][m]);
#pragma unroll
    for (int m = 0; m < 4; ++m) MFMA(w.wuf[0], w.xa[m], w.au[0][m]);
    __builtin_amdgcn_s_setprio(0);
    BAR();
    STAMP();
    f32x4 pl[2], pm[2];
    f32x2 l8[2], cin[2], ea[2], agg[2];
#pragma unroll
    for (int pf = 0; pf < 2; ++pf) {
      const lptr pb = mk_l(L_PRM + (pf * 16 + r) * 64);
      pl[pf] = *(const LAS f32x4*)pb; pm[pf] = *(const LAS f32x4*)(pb + 16); l8[pf] = *(const LAS f32x2*)(pb + 32);
    }
#pragma unroll
    for (int n = 1; n < 3; ++n) { w.wgf[n] = lds_tr2(w.aW[n] + 32768, w.aW[n] + 32768 + 1024); w.wuf[n] = lds_tr2(w.aW[n] + 49152, w.aW[n] + 49152 + 1024); }
    SCHED();
#define PAIR(v, k) ((k) & 1 ? (f32x2){(v)[(k) >> 1][2], (v)[(k) >> 1][3]} : (f32x2){(v)[(k) >> 1][0], (v)[(k) >> 1][1]})
#define CSTEP(P, Q, pf, k) do { const f32x2 lx_ = {pl[pf][0], pl[pf][0]}, ly_ = {pl[pf][1], pl[pf][1]}; \
      const f32x2 a_ = __builtin_elementwise_fma(-ly_, Q, PAIR(w.as[2 * (pf)], k)), b_ = __builtin_elementwise_fma(ly_, P, PAIR(w.as[2 * (pf) + 1], k)); \
      P = __builtin_elementwise_fma(lx_, P, a_); Q = __builtin_elementwise_fma(lx_, Q, b_); } while (0)
    {
#pragma unroll
      for (int pf = 0; pf < 2; ++pf)
#pragma unroll
        for (int m = 0; m < 4; ++m) {
          const f32x4 re = w.as[2 * pf][m], im = w.as[2 * pf + 1][m];
          w.as[2 * pf][m] = re * pl[pf][2] - im * pl[pf][3]; w.as[2 * pf + 1][m] = -(re * pl[pf][3]) - im * pl[pf][2];
          PIN(w.as[2 * pf][m]); PIN(w.as[2 * pf + 1][m]);
        }
      f32x2 P[2], Q[2];
#pragma unroll
      for (int k = 0; k < 8; ++k)
#pragma unroll
        for (int pf = 0; pf < 2; ++pf) {
          if (k == 0) { P[pf] = PAIR(w.as[2 * pf], 0); Q[pf] = PAIR(w.as[2 * pf + 1], 0); } else CSTEP(P[pf], Q[pf], pf, k);
        }
#pragma unroll
      for (int pf = 0; pf < 2; ++pf) {
        ea[pf] = (f32x2){P[pf].x, Q[pf].x};
        const f32x2 e = cfma(l8[pf], ea[pf], (f32x2){P[pf].y, Q[pf].y});
        const f32x2 l16 = {pm[pf][0], pm[pf][1]}, l32 = {pm[pf][2], pm[pf][3]};
        float a0, a1, b0, b1; sw16(e.x, a0, a1); sw16(e.y, b0, b1);
        const f32x2 r0 = {a0, b0};
        const f32x2 xx = cfma(l16, r0, (f32x2){a1, b1});
        float c0, c1, d0, d1; sw32(xx.x, c0, c1); sw32(xx.y, d0, d1);
        const f32x2 s0 = {c0, d0};
        agg[pf] = cfma(l32, s0, (f32x2){c1, d1});
        const f32x2 c3 = cfma(l16, s0, r0);
        cin[pf] = g == 0 ? (f32x2){0.f, 0.f} : g == 1 ? r0 : g == 2 ? s0 : c3;
        PIN(ea[pf]);
      }
    }
    f32x2 aggq = (g & 1) ? agg[1] : agg[0]; PIN(aggq); PIN(cin[0]); PIN(cin[1]);
    if (wr < 3) {
      if (g < 2) *(LAS f32x2*)mk_l(L_WA + ((g * 16 + r) * 4 + wr) * 8) = aggq;
      WAIT_L0();
      *(volatile LAS unsigned*)mk_l(L_FLG + wr * 4) = 1u;
    }
    SCHED();
#pragma unroll
    for (int n = 0; n < 3; ++n) { wg1[n] = lds_tr2(w.aW[n] + 40960, w.aW[n] + 40960 + 1024); wu1[n] = lds_tr2(w.aW[n] + 57344, w.aW[n] + 57344 + 1024); }
    if (wr == 3) {
      const lptr fp = mk_l(L_FLG);
_Pragma("nounroll")
      for (unsigned spins = 0; spins < SPIN_CAP; ++spins) {
        const u32x4 f = *(volatile LAS u32x4*)fp;
        if (__builtin_amdgcn_readfirstlane(f[0] & f[1] & f[2])) break;
      }
      asm volatile("" ::: "memory");
      const lptr ap = mk_l(L_WA + ((g & 1) * 16 + r) * 32);
      const f32x4 a01 = *(const LAS f32x4*)ap; const f32x2 a2 = *(const LAS f32x2*)(ap + 16);
      const f32x2 lw = *(const LAS f32x2*)mk_l(L_PRM + ((g & 1) * 16 + r) * 64 + 40);
      const f32x2 t = cfma(lw, cfma(lw, cfma(lw, (f32x2){a01[0], a01[1]}, (f32x2){a01[2], a01[3]}), a2), aggq);
      if (rch < 7) __hip_atomic_store((GAS u64*)(A.ws + WS_AG) + (c * 8 + 7 - rch) * 64 + lane, ((u64)epoch << 32) | __float_as_uint((g & 2) ? t.y : t.x), RLX_AGENT);
    }
    WAIT_L0(); lds_post(3); SCHED();
    STAMP();
    __builtin_amdgcn_s_setprio(1);
#pragma unroll
    for (int n = 1; n < 3; ++n) {
#pragma unroll
      for (int m = 0; m < 4; ++m) MFMA(w.wgf[n], w.xa[m], w.ag[n][m]);
#pragma unroll
      for (int m = 0; m < 4; ++m) MFMA(w.wuf[n], w.xa[m], w.au[n][m]);
    }
#pragma unroll
    for (int n = 0; n < 3; ++n) {
#pragma unroll
      for (int m = 0; m < 4; ++m) MFMA(wg1[n], xb[m], w.ag[n][m]);
#pragma unroll
      for (int m = 0; m < 4; ++m) MFMA(wu1[n], xb[m], w.au[n][m]);
    }
    __builtin_amdgcn_s_setprio(2);
    SCHED();
    lane = fresh_lane(); r = lane & 15; g = lane >> 4; qq = (lane & 15) >> 2; pp = lane & 3;
    q.voffH = lane * 16;
    const rsrc_t rag = mk_rsrc(A.ws + WS_AG + c * 4096, 4096u);
    const unsigned ago = lane * 8 + (8 - rch) * 512;
    u32x2 gv_[7];
#pragma unroll
    for (int cc = 0; cc < 7; ++cc) gv_[cc] = __builtin_amdgcn_raw_buffer_load_b64(rag, ago + cc * 512, 0, 16);
    {
      const lptr fp = mk_l(L_FLG);
_Pragma("nounroll")
      for (unsigned spins = 0; spins < SPIN_CAP; ++spins) {
        const u32x4 f = *(volatile LAS u32x4*)fp;
        if (__builtin_amdgcn_readfirstlane(f[0] & f[1] & f[2])) break;
      }
      asm volatile("" ::: "memory");
    }
    STAMP();
    f32x4 p2[2], tb4[2], a01[2]; f32x2 a2[2];
    const f32x2 l256q = *(const LAS f32x2*)mk_l(L_PRM + ((g & 1) * 16 + r) * 64 + 48);
    {
      const lptr pb = mk_l(L_PRM + r * 64), ap = mk_l(L_WA + r * 32), tp = mk_l(L_TAB + wr * 2048 + g * 512 + r * 16);
#pragma unroll
      for (int pf = 0; pf < 2; ++pf) {
        pl[pf] = *(const LAS f32x4*)(pb + pf * 1024); p2[pf] = *(const LAS f32x4*)(pb + pf * 1024 + 32); tb4[pf] = *(const LAS f32x4*)(tp + pf * 256);
        a01[pf] = *(const LAS f32x4*)(ap + pf * 512); a2[pf] = *(const LAS f32x2*)(ap + pf * 512 + 16);
      }
    }
    SCHED();
    PACK_S();
    P2_ADDR();
    PIN(ho[0]); PIN(ho[1]); PIN(ho[2]);
    SCHED();
    f32x2 car[2], cai[2], cmr[2], cmi[2];
#pragma unroll
    for (int pf = 0; pf < 2; ++pf) {
      const f32x2 l8_ = {p2[pf][0], p2[pf][1]}, l64 = {p2[pf][2], p2[pf][3]};
      f32x2 sw = wr > 0 ? (f32x2){a01[pf][0], a01[pf][1]} : (f32x2){0.f, 0.f};
      if (wr > 1) sw = cfma(l64, sw, (f32x2){a01[pf][2], a01[pf][3]});
      if (wr > 2) sw = cfma(l64, sw, a2[pf]);
      const f32x2 ca = cfma((f32x2){tb4[pf][0], tb4[pf][1]}, sw, cin[pf]), cb_ = cfma(l8_, ca, ea[pf]), cm = {tb4[pf][2], tb4[pf][3]}, cn = cmul(l8_, cm);
      car[pf] = (f32x2){ca.x, cb_.x}; cai[pf] = (f32x2){ca.y, cb_.y}; cmr[pf] = (f32x2){cm.x, cn.x}; cmi[pf] = (f32x2){cm.y, cn.y};
      PIN(car[pf]); PIN(cai[pf]); PIN(cmr[pf]); PIN(cmi[pf]);
    }
    STAMP();
_Pragma("nounroll")
    for (unsigned spins = 0; spins < SPIN_CAP; ++spins) {
      __builtin_amdgcn_s_waitcnt(0x0F70);
      unsigned bad = 0;
#pragma unroll
      for (int cc = 0; cc < 7; ++cc) bad |= gv_[cc][1] ^ (cc < rch ? epoch : 0u);
      if (__all(bad == 0u)) break;
      __builtin_amdgcn_s_sleep(1);
      asm volatile("" ::: "memory");
#pragma unroll
      for (int cc = 0; cc < 7; ++cc) gv_[cc] = __builtin_amdgcn_raw_buffer_load_b64(rag, ago + cc * 512, 0, 16);
    }
    __builtin_amdgcn_s_waitcnt(0x0F70);
    STAMP();
    f32x2 cb;
    {
      float are[7], aim[7];
#pragma unroll
      for (int cc = 0; cc < 7; ++cc) { const unsigned v = gv_[cc][0]; sw32(__uint_as_float(v), are[cc], aim[cc]); }
      cb = (f32x2){are[6], aim[6]};
#pragma unroll
      for (int cc = 5; cc >= 0; --cc) cb = cfma(l256q, cb, (f32x2){are[cc], aim[cc]});
    }
    unsigned hr[2][8], hi[2][8];
    {
      float cx[2], cy[2]; sw16(cb.x, cx[0], cx[1]); sw16(cb.y, cy[0], cy[1]);
      f32x2 P[2], Q[2];
#pragma unroll
      for (int pf = 0; pf < 2; ++pf) { P[pf] = car[pf] + cmr[pf] * cx[pf] - cmi[pf] * cy[pf]; Q[pf] = cai[pf] + cmr[pf] * cy[pf] + cmi[pf] * cx[pf]; }
#pragma unroll
      for (int k = 0; k < 8; ++k)
#pragma unroll
        for (int pf = 0; pf < 2; ++pf) { CSTEP(P[pf], Q[pf], pf, k); hr[pf][k] = pk_bf16(P[pf].x, P[pf].y); hi[pf][k] = pk_bf16(Q[pf].x, Q[pf].y); }
    }
#pragma unroll
    for (int i = 0; i < 8; ++i) {
      const unsigned* h = (i >> 2) ? hi[(i >> 1) & 1] : hr[(i >> 1) & 1];
      hq[6 + i] = (u32x4){h[(i & 1) * 4], h[(i & 1) * 4 + 1], h[(i & 1) * 4 + 2], h[(i & 1) * 4 + 3]}; PIN(hq[6 + i]);
    }
    STAMP(); TS2();
    WAIT_V(0); lds_wait(1);
    STAMP();
    q.template stageA<1, 1>(); q.template stageA<2, 0>();
  }
  }
  if (GRP == 0) { STAMP(); TS2(); }
  constexpr int G = GRP;
  int r, g;
  unsigned fl = 0u;
#define FLAG_LOAD(off) asm volatile("global_load_dword %0, %1, %2 sc1" : "+v"(fl) : "v"(off), "s"(A.ws) : "memory")
#define FLAG_CHECK(off) do { PIN(fl); if (!__all(fl == epoch)) wait_flags(A.ws, off, epoch); } while (0)
#define HQ_ST(i) __builtin_amdgcn_raw_buffer_store_b128(hq[i], rws, (i) < 4 ? ho[0] + (i) * 1024 : (i) < 6 ? ho[1] + ((i) - 4) * 2048 : ho[2] + (((i) - 6) >> 2) * 262144 + (((i) - 6) & 3) * 1024, 0, 16)
#define XTRA(st) ((st) == 13 ? 4 : 0)
  float gv[4];
  u32x2 xv[4], dv; float gr[4];
  const int t0 = tt * 128 + wm * 64 + kq * 2, dc0 = dt * 128 + wn * 64;
  auto side = [&](auto ST_) {
    constexpr int ST = decltype(ST_)::value;
    if constexpr (ST == 13) {
      const int l = fresh_lane(), g_ = l >> 4, q_ = (l & 15) >> 2, p_ = l & 3;
#pragma unroll
      for (int e = 0; e < 2; ++e) {
        const unsigned b = 49152 + G * 49152 + wm * 4096 + (g_ >> 1) * 2048 + p_ * 256 + 8 * (g_ & 1) * 16 + q_ * 16 + (e ^ (kq & 1)) * 8;
        q.aS[e] = mk_l(b + ((p_ >> 1) << 6)); q.aS2[e] = mk_l(b + (((p_ >> 1) ^ 1) << 6));
        q.aC[e] = mk_l((wn * 64 + (l & 15)) * 128 + (((g_ + 4 * e) ^ (((l & 15) >> 1) & 7)) << 4));
      }
    }
    if constexpr (ST == 13) {
      const int l = fresh_lane();
      const unsigned go = (unsigned)WS_G + (tt * 128 + wm * 64 + ((l & 15) >> 2) * 16 + tj(l)) * 4;
#pragma unroll
      for (int j = 0; j < 4; ++j) asm volatile("global_load_dword %0, %1, %2 sc1" : "=v"(gv[j]) : "v"(go + (q.pj[j] >> 7)), "s"(A.ws) : "memory");
    }
    if constexpr (ST >= 16 && ST < 20) {
      constexpr int m = ST - 16;
      PIN(gv[m]);
#pragma unroll
      for (int n = 0; n < 4; ++n) { q.af[m][n] *= 1.f - gv[m]; PIN(q.af[m][n]); }
    }
    if constexpr (ST == NSTEP - 1) {
      const int l = fresh_lane();
      dv = *(const u32x2*)(A.dsk + dc0 + (l & 15) * 4);
#pragma unroll
      for (int i = 0; i < 4; ++i) {
        xv[i] = *(const u32x2*)(A.x + (t0 + i * 16 + tj(l >> 4)) * D_ + dc0 + (l & 15) * 4);
        gr[i] = __int_as_float(__builtin_amdgcn_ds_bpermute((4 * i + (l >> 4)) * 4, __float_as_int(gv[0])));
      }
#pragma unroll
      for (int j = 0; j < 4; ++j) { gv[j] *= 2.f; PIN(gv[j]); }
    }
  };
  __builtin_amdgcn_s_setprio(0);
  if constexpr (G == 0) {
    const unsigned f2 = flc + 512 + (8 * kq + (lane & 7)) * 4, f3 = flc + 640 + (lane & 31) * 4;
    WAIT_V(8); lds_post(1); lds_wait(1); STAMP();
    q.template load<0, 0>(); WAIT_V(10); WAIT_L0(); lds_post(2); SCHED();
    static_for<0, NSTEP - 1>([&](auto ST_) {
      constexpr int ST = decltype(ST_)::value;
      constexpr int prev = (ST + 2 < NSTEP ? 6 : 0) + (ST == 4 || ST == 10 ? 1 : 0) + XTRA(ST);
      q.template compute<ST, prev>();
      q.template load<ST + 1, 0>();
      if constexpr (ST + 1 == 4) FLAG_LOAD(f2);
      if constexpr (ST + 1 == 5) { WAIT_V(6); FLAG_CHECK(f2); }
      if constexpr (ST + 1 == 10) FLAG_LOAD(f3);
      if constexpr (ST + 1 == 11) { WAIT_V(6); FLAG_CHECK(f3); }
      if constexpr (ST + 1 == 20) { if (blockIdx.x == 255 && wr == 0 && lane == 0) __hip_atomic_store((GAS unsigned*)(A.ws + WS_EP), epoch, RLX_AGENT); }
      side(std::integral_constant<int, ST + 1>{});
      WAIT_L0(); BAR(); TS2();
      if constexpr (ST == 1 || ST == 13) STAMP();
    });
  } else {
    HQ_ST(0); HQ_ST(1); HQ_ST(2);
    lds_wait(2);
    static_for<0, NSTEP>([&](auto ST_) {
      constexpr int ST = decltype(ST_)::value;
      constexpr int nst = ST == 0 ? 3 : ST <= 4 ? 2 : 0;
      if constexpr (ST == 2 || ST == 6) {
        if (wr == 0 && lane == 0) __hip_atomic_store((GAS unsigned*)(A.ws + flb + (ST == 2 ? 512 : 640) + c * 4), epoch, RLX_AGENT);
      }
      q.template load<ST, 1>();
      if constexpr (ST == 0) { HQ_ST(3); HQ_ST(4); HQ_ST(5); }
      if constexpr (ST >= 1 && ST <= 4) { HQ_ST(4 + 2 * ST); HQ_ST(5 + 2 * ST); }
      side(ST_);
      WAIT_L0(); BAR();
      if constexpr (ST + 1 < NSTEP) {
        q.template compute<ST, (ST + 2 < NSTEP ? 2 : 0) + (ST + 3 < NSTEP ? 4 : 0) + nst + XTRA(ST)>(); TS2();
        if constexpr (ST == 1 || ST == 13) STAMP();
      }
    });
  }
  lane = fresh_lane(); r = lane & 15; g = lane >> 4;
  f32x4 o[4];
  {
    unsigned xo[3], pk[4];
#pragma unroll
    for (int j = 1; j < 4; ++j) xo[j - 1] = (unsigned)WS_XB + ((tile * 4 + (kq ^ j)) * 3 + (j - 1)) * 16384u + wid * 2048 + lane * 16;
    __builtin_amdgcn_s_setprio(1);
#pragma unroll
    for (int k = 0; k < 19; ++k) {
      if (k < 16) { const int m = ((k >> 2) + 1) & 3, n = k & 3; MFMA(q.bb[0][n], q.ha[0][m], q.as[m][n]); MFMA(q.bb[1][n], q.ha[1][m], q.as[m][n]); }
      if (k == 3 || k >= 16) asm volatile("s_nop 3");
      if (G == 0 && k == 14) RBAR();
      SCHED();
      if (k >= 3) {
        const int m = (((k - 3) >> 2) + 1) & 3, n = (k - 3) & 3;
        f32x4 v;
#pragma unroll
        for (int j = 0; j < 4; ++j) v[j] = __builtin_fmaf(q.as[m][n][j], gv[m], q.af[m][n][j]);
        if (m == 0) o[n] = v;
        else {
          pk[(n & 1) * 2] = pk_bf16(v[0], v[1]); pk[(n & 1) * 2 + 1] = pk_bf16(v[2], v[3]);
          if (n & 1) __builtin_amdgcn_raw_buffer_store_b128((u32x4){pk[0], pk[1], pk[2], pk[3]}, rws, xo[m - 1] + (n >> 1) * 1024, 0, 16);
        }
      }
      SCHED();
    }
    __builtin_amdgcn_s_setprio(0);
  }
  STAMP();
  WAIT_V(0);
  if (lane >= 1 && lane < 4) __hip_atomic_store((GAS unsigned*)(A.ws + WS_XF + ((tile * 4 + (kq ^ lane)) * 8 + wid) * XS) + lane, epoch, RLX_AGENT);
  STAMP();
  f32x4 sk[4];
  {
    const f32x4 df = {bf2f(dv[0] & 0xffffu), bfhi(dv[0]), bf2f(dv[1] & 0xffffu), bfhi(dv[1])};
#pragma unroll
    for (int i = 0; i < 4; ++i) { sk[i] = (f32x4){bf2f(xv[i][0] & 0xffffu), bfhi(xv[i][0]), bf2f(xv[i][1] & 0xffffu), bfhi(xv[i][1])} * df * gr[i]; PIN(sk[i]); }
  }
  const lptr tw = mk_l(49152 + G * 49152 + wr * 4096 + r * 256), tr_ = mk_l(49152 + G * 49152 + wr * 4096 + g * 256);
  wait_flags(A.ws, (unsigned)WS_XF + ((tile * 4 + kq) * 8 + wid) * XS + (1 + (lane & 3) % 3) * 4, epoch);
  STAMP();
  {
    u32x4 pv[6];
    const unsigned xi = (unsigned)WS_XB + (tile * 4 + kq) * 3 * 16384u + wid * 2048 + lane * 16;
#pragma unroll
    for (int i = 0; i < 6; ++i) pv[i] = __builtin_amdgcn_raw_buffer_load_b128(rws, xi + (i >> 1) * 16384 + (i & 1) * 1024, 0, 16);
#pragma unroll
    for (int i = 0; i < 6; ++i)
#pragma unroll
      for (int h = 0; h < 2; ++h) {
        const unsigned a0 = pv[i][2 * h], a1 = pv[i][2 * h + 1];
        o[(i & 1) * 2 + h] += (f32x4){bf2f(a0 & 0xffffu), bfhi(a0), bf2f(a1 & 0xffffu), bfhi(a1)};
      }
  }
#pragma unroll
  for (int n = 0; n < 4; ++n) *(LAS f32x4*)(tw + (((n * 4 + g) ^ (r & 7)) << 4)) = o[n];
  WAIT_L0();
#pragma unroll
  for (int i = 0; i < 4; ++i) {
    const f32x4 v = *(const LAS f32x4*)(tr_ + i * 1024 + ((r ^ ((4 * i + g) & 7)) << 4));
    __builtin_amdgcn_raw_buffer_store_b128(__builtin_bit_cast(u32x4, v + sk[i]), mk_rsrc(A.out, L_ * D_ * 4), ((t0 + i * 16 + tj(g)) * D_ + dc0 + r * 4) * 4, 0, 17);
  }
  STAMP();
  ST_DUMP(A.ws, wid);
  if (GRP == 0) asm volatile(".Lgm_end0:"); else asm volatile(".Lgm_end1:");
  __builtin_amdgcn_endpgm();
  __builtin_unreachable();
}

__global__ __launch_bounds__(512, 2) void fused_kernel(Args A) {
  const u64 t00 = 0;
  const int wid = __builtin_amdgcn_readfirstlane(threadIdx.x >> 6);
  {
    const Pre pre = mk_pre(threadIdx.x & 63, wid, blockIdx.x);
    const int G = wid >> 2;
    if (G) {
      const int l = threadIdx.x & 63;
      const char* p = (l < 16 ? (const char*)A.lre : l < 32 ? (const char*)A.lim : (const char*)A.ldt) + (pre.sB >> 16) * 64 + (l & 15) * 4;
      if (l >= 48) p = A.ws + WS_EP;
      asm volatile("s_mov_b32 m0, %0\n\ts_nop 0\n\tglobal_load_lds_dword %1, off sc1" :: "s"(L_TAB + (wid & 3) * 2048), "v"(p) : "memory");
    }
    const rsrc_t rx = mk_rsrc(A.x, L_ * D_ * 2);
#pragma unroll
    for (int s = 0; s < 4; ++s) dma16(pre.ldsX + s * 2048, pre.voffX, rx, pre.sX + s * 4096);
    dma16(pre.ldsW + G * 1024, pre.voffW, mk_rsrc(A.wg, D_ * F_ * 2), pre.sW + G * 28672u);
    dma16(pre.ldsW + 16384 + G * 1024, pre.voffW, mk_rsrc(A.wu, D_ * F_ * 2), pre.sW + G * 28672u);
    dma16(pre.ldsB + G * 4096, pre.voffB, mk_rsrc((wid & 2) ? A.bim : A.bre, P_ * D_ * 2), pre.sB + G * 32768);
  }
  const char* code; unsigned code_len;
  asm volatile("s_getpc_b64 %0\n.Lgm_pc:\n\ts_mov_b32 %1, .Lgm_end0-.Lgm_pc\n\ts_max_u32 %1, %1, .Lgm_end1-.Lgm_pc" : "=s"(code), "=s"(code_len) :: "scc");
  if (wid < 4) body<0>(A, wid, code, code_len, t00); else body<1>(A, wid, code, code_len, t00);
}

extern "C" void kernel_launch(void* const* d_in, const int* in_sizes, int n_in, void* d_out, int out_size, void* d_ws, size_t ws_size,
                              hipStream_t stream) {
  Args a; a.x = (const u16*)d_in[0]; a.lre = (const u16*)d_in[1]; a.lim = (const u16*)d_in[2]; a.bre = (const u16*)d_in[3];
  a.bim = (const u16*)d_in[4]; a.cre = (const u16*)d_in[5]; a.cim = (const u16*)d_in[6]; a.ldt = (const u16*)d_in[7]; a.dsk = (const u16*)d_in[8];
  a.wg = (const u16*)d_in[10]; a.wu = (const u16*)d_in[11]; a.wd = (const u16*)d_in[12]; a.gw = (const u16*)d_in[13];
  a.ws = (char*)d_ws; a.out = (float*)d_out;
  (void)hipFuncSetAttribute((const void*)fused_kernel, hipFuncAttributeMaxDynamicSharedMemorySize, LDS_BYTES);
  fused_kernel<<<256, 512, LDS_BYTES, stream>>>(a);
}
```

```cpp
#define XS 16
#include <hip/hip_runtime.h>
#include <hip/hip_bf16.h>
#include <type_traits>
#define MAP2 1
#define ORD2 1
#define MAP1 1
#define SQ 1
#define SO 1
#define PFD 4

typedef unsigned short u16;
typedef short bf16x8 __attribute__((ext_vector_type(8)));
typedef short s16x4 __attribute__((ext_vector_type(4)));
typedef float f32x4 __attribute__((ext_vector_type(4)));
typedef float f32x2 __attribute__((ext_vector_type(2)));
typedef unsigned u32x4 __attribute__((ext_vector_type(4)));
typedef unsigned u32x2 __attribute__((ext_vector_type(2)));
typedef unsigned long long u64;
#define LAS __attribute__((address_space(3)))
#define GAS __attribute__((address_space(1)))
#define RLX_AGENT __ATOMIC_RELAXED, __HIP_MEMORY_SCOPE_AGENT
typedef __amdgpu_buffer_rsrc_t rsrc_t;

constexpr int L_ = 2048, D_ = 1024, P_ = 1024, F_ = 3584;
constexpr int NKF = 176;
constexpr size_t WS_H = 0;
constexpr size_t H_BYTES = (size_t)16 * NKF * 8 * 1024;
constexpr size_t WS_XB = WS_H + H_BYTES;
constexpr size_t XB_BYTES = (size_t)64 * 4 * 3 * 16384;
constexpr size_t WS_G = WS_XB + XB_BYTES;
constexpr size_t WS_AG = WS_G + 8192;
constexpr size_t WS_EP = WS_AG + (size_t)32 * 8 * 64 * 8;
constexpr size_t WS_FL = WS_EP + 512;
constexpr size_t WS_XF = WS_FL + 8192;
constexpr size_t WS_END = WS_XF + 256 * 8 * XS;
constexpr unsigned SPIN_CAP = 1u << 20;

__device__ __forceinline__ float bf2f(unsigned b) { return __uint_as_float(b << 16); }
__device__ __forceinline__ float bfhi(unsigned b) { return __uint_as_float(b & 0xffff0000u); }
__device__ __forceinline__ unsigned pk_bf16(float lo, float hi) {
  typedef __bf16 bf2_t __attribute__((ext_vector_type(2)));
  return __builtin_bit_cast(unsigned, __builtin_convertvector((f32x2){lo, hi}, bf2_t)); }
__device__ __forceinline__ float relu(float v) { const int b = __float_as_int(v); return __int_as_float(b > 0 ? b : 0); }
template <int CTRL, int BANKS> __device__ __forceinline__ float dpp(float old, float src) {
  return __int_as_float(__builtin_amdgcn_update_dpp(__float_as_int(old), __float_as_int(src), CTRL, 0xf, BANKS, false)); }
__device__ __forceinline__ rsrc_t mk_rsrc(const void* p, unsigned bytes) {
  return __builtin_amdgcn_make_buffer_rsrc((void*)p, (short)0, (int)bytes, 0x00020000); }
__device__ __forceinline__ void dma16(unsigned ldsaddr, unsigned voff, rsrc_t srd, unsigned soff) {
  asm volatile("s_mov_b32 m0, %0\n\ts_nop 0\n\tbuffer_load_dwordx4 %1, %2, %3 offen lds"
               :: "s"(ldsaddr), "v"(voff), "s"(srd), "s"(soff) : "memory"); }
__device__ __forceinline__ void dma16_dev(unsigned ldsaddr, unsigned voff, rsrc_t srd, unsigned soff) {
  asm volatile("s_mov_b32 m0, %0\n\ts_nop 0\n\tbuffer_load_dwordx4 %1, %2, %3 offen sc1 lds"
               :: "s"(ldsaddr), "v"(voff), "s"(srd), "s"(soff) : "memory"); }
#define WAIT_V(n) asm volatile("s_waitcnt vmcnt(" #n ")" ::: "memory")
#define WAIT_VN(n) asm volatile("s_waitcnt vmcnt(%0)" :: "i"(n) : "memory")
#define WAIT_L0() do { __builtin_amdgcn_s_waitcnt(0xC07F); asm volatile("" ::: "memory"); } while (0)
#define SCHED() __builtin_amdgcn_sched_barrier(0)
#define RBAR() do { __builtin_amdgcn_s_barrier(); asm volatile("" ::: "memory"); } while (0)
#define BAR() do { SCHED(); RBAR(); SCHED(); } while (0)
__device__ __forceinline__ int fresh_lane() { int l; asm volatile("v_mbcnt_lo_u32_b32 %0, -1, 0\n\tv_mbcnt_hi_u32_b32 %0, -1, %0" : "=v"(l)); return l; }
typedef LAS char* lptr;
__device__ __forceinline__ lptr mk_l(unsigned a) { lptr p = (lptr)(size_t)a; asm volatile("" : "+v"(p)); return p; }
__device__ __forceinline__ bf16x8 lds_b128(lptr a) { return *(const LAS bf16x8*)a; }
__device__ __forceinline__ bf16x8 lds_tr2(lptr a0, lptr a1) {
  s16x4 lo = __builtin_amdgcn_ds_read_tr16_b64_v4i16((LAS s16x4*)a0);
  s16x4 hi = __builtin_amdgcn_ds_read_tr16_b64_v4i16((LAS s16x4*)a1);
  return __builtin_shufflevector(lo, hi, 0, 1, 2, 3, 4, 5, 6, 7); }
#define MFMA(a, b, c) asm volatile("v_mfma_f32_16x16x32_bf16 %0, %1, %2, %0" : "+v"(c) : "v"(a), "v"(b))
#define MFMA0(a, b, c) asm volatile("v_mfma_f32_16x16x32_bf16 %0, %1, %2, 0" : "=&v"(c) : "v"(a), "v"(b))
#define PIN(x) asm volatile("" : "+v"(x))
#define MFMA_DRAIN() do { asm volatile("s_nop 7\n\ts_nop 7" ::: "memory"); SCHED(); } while (0)

#define ST_DECL
#define STAMP()
#define TS2()
#define ST_DUMP(ws, wid)

struct Args {
  const u16 *x, *lre, *lim, *bre, *bim, *cre, *cim, *ldt, *dsk, *wg, *wu, *wd, *gw;
  char* ws; float* out;
};

constexpr unsigned L_X = 0, L_W = 65536, L_BS = 131072, L_PRM = 151552  ,
                   L_WA = 153600  , L_FLG = 154624  , L_SINK = 154880, L_TAB = 155136  , LDS_BYTES = 163840;

__device__ __forceinline__ int tj(int j) { return (j & 1) * 8 + ((j >> 1) & 1); }
__device__ __forceinline__ f32x2 cmul(f32x2 a, f32x2 b) { return (f32x2){a.x * b.x - a.y * b.y, a.x * b.y + a.y * b.x}; }
__device__ __forceinline__ f32x2 cfma(f32x2 a, f32x2 b, f32x2 c) {
  return (f32x2){__builtin_fmaf(a.x, b.x, __builtin_fmaf(-a.y, b.y, c.x)), __builtin_fmaf(a.x, b.y, __builtin_fmaf(a.y, b.x, c.y))}; }
__device__ __forceinline__ void sw16(float v, float& r0, float& r1) { auto t = __builtin_amdgcn_permlane16_swap(__float_as_uint(v), __float_as_uint(v), false, false); r0 = __uint_as_float(t[0]); r1 = __uint_as_float(t[1]); }
__device__ __forceinline__ void sw32(float v, float& r0, float& r1) { auto t = __builtin_amdgcn_permlane32_swap(__float_as_uint(v), __float_as_uint(v), false, false); r0 = __uint_as_float(t[0]); r1 = __uint_as_float(t[1]); }

__device__ __forceinline__ void touch(const char* p) {
  asm volatile("s_mov_b32 m0, %0\n\ts_nop 0\n\tglobal_load_lds_dword %1, off" :: "s"(L_SINK), "v"(p) : "memory"); }
template <int GRP>
struct P1 {
  static constexpr int NW = GRP == 0 ? 4 : 3, NS = GRP == 0 ? 0 : 4;
  f32x4 ag[NW][4], au[NW][4], as[4][4];
  bf16x8 xa[4], wgf[NW], wuf[NW], bsf[4];
  lptr aX[2], aB[2], aW[NW];
  unsigned voffX, voffW, voffB;
  unsigned sX, sW, sB, ldsX, ldsW, ldsB;
  rsrc_t rx, rwg, rwu, rb;
  const char* pfp; unsigned pfs; int pfo, pfm;
  __device__ __forceinline__ void pull(int T) { const int i = T + pfo; touch(pfp + pfs * (unsigned)(i < 0 ? 0 : i > pfm ? pfm : i)); }

  template <int BUF, int KS, class M> __device__ __forceinline__ void load(M&& mem) {
    mem(0);
#pragma unroll
    for (int m = 0; m < 4; ++m) xa[m] = lds_b128(aX[KS] + BUF * 32768 + m * 2048);
    mem(1);
#pragma unroll
    for (int i = 0; i < NS; ++i) bsf[i] = lds_b128(aB[KS] + BUF * 8192 + i * 2048);
    if (NS) mem(2);
#pragma unroll
    for (int n = 0; n < NW; ++n) {
      wgf[n] = lds_tr2(aW[n] + BUF * 32768 + KS * 8192, aW[n] + BUF * 32768 + KS * 8192 + 1024);
      wuf[n] = lds_tr2(aW[n] + BUF * 32768 + 16384 + KS * 8192, aW[n] + BUF * 32768 + 16384 + KS * 8192 + 1024);
      mem(n + (NS ? 3 : 2));
    }
  }
  template <int BUF, int KS> __device__ __forceinline__ void load() { load<BUF, KS>([](int) {}); }
  template <int BUF> __device__ __forceinline__ void dmaX1(int T, int s) { dma16(ldsX + BUF * 32768 + s * 2048, voffX, rx, sX + T * 128 + s * 4096); }
  template <int BUF, int HH> __device__ __forceinline__ void dmaW1(int T, int i) {
    dma16(ldsW + BUF * 32768 + (i & 1) * 16384 + HH * 8192 + (i >> 1) * 1024, voffW, (i & 1) ? rwu : rwg, sW + (unsigned)(T * 64 + HH * 32 + (i >> 1) * 4) * 7168u);
  }
  template <int BUF> __device__ __forceinline__ void dmaB1(int T, int pf) { dma16(ldsB + BUF * 8192 + pf * 4096, voffB, rb, sB + pf * 32768 + T * 128); }
  template <int NV, bool FIRST = false> __device__ __forceinline__ void compute() {
    constexpr int NM = 4 * NS + 8 * NW;
    __builtin_amdgcn_s_setprio(1);
#pragma unroll
    for (int k = 0; k < NM; ++k) {
      const int f = k >> 2, m = (f & 1) ? 3 - (k & 3) : (k & 3);
      const int n = f < NS ? 0 : (f - NS) >> 1;
      const bf16x8& a = f < NS ? xa[m] : ((f - NS) & 1) ? wuf[n] : wgf[n];
      const bf16x8& b = f < NS ? bsf[f & 3] : xa[m];
      f32x4& c = f < NS ? as[f & 3][m] : ((f - NS) & 1) ? au[n][m] : ag[n][m];
      if (FIRST) MFMA0(a, b, c); else MFMA(a, b, c);
    }
    if (NV != -2) { if (NV >= 0) WAIT_VN(NV < 0 ? 0 : NV); RBAR(); }
    __builtin_amdgcn_s_setprio(0);
    SCHED();
  }
  template <int BUF> __device__ __forceinline__ void dmaX(int T) {
#pragma unroll
    for (int s = 0; s < 4; ++s) dma16(ldsX + BUF * 32768 + s * 2048, voffX, rx, sX + T * 128 + s * 4096);
  }
  template <int BUF, int HH> __device__ __forceinline__ void dmaW(int T) {
#pragma unroll
    for (int s = 0; s < 2; ++s) {
      const unsigned so = sW + (unsigned)(T * 64 + HH * 32 + s * 4) * 7168u;
      dma16(ldsW + BUF * 32768 + HH * 8192 + s * 1024, voffW, rwg, so);
      dma16(ldsW + BUF * 32768 + 16384 + HH * 8192 + s * 1024, voffW, rwu, so);
    }
  }
  template <int BUF> __device__ __forceinline__ void dmaB(int T) {
#pragma unroll
    for (int pf = 0; pf < 2; ++pf) dma16(ldsB + BUF * 8192 + pf * 4096, voffB, rb, sB + pf * 32768 + T * 128);
  }
};

template <int BUF> __device__ __forceinline__ void p1_tile(P1<0>& w, int T) {
  w.template compute<7>();
  w.template load<BUF, 1>([&](int i) { if (i < 4) w.template dmaW1<BUF ^ 1, 1>(T + 1, i); else if (i == 4) w.pull(T); }); WAIT_L0(); BAR();
  w.template compute<5>();
  w.template load<BUF ^ 1, 0>([&](int i) { if (i < 4) w.template dmaX1<BUF>(T + 2, i); else w.template dmaB1<BUF>(T + 2, i - 4); }); WAIT_L0(); BAR();
}
template <int BUF> __device__ __forceinline__ void p1_tile(P1<1>& w, int T) {
  w.template load<BUF, 0>([&](int i) { if (i >= SO && i < SO + 4) w.template dmaX1<BUF ^ 1>(T + 1, i - SO); }); WAIT_L0(); BAR();
  w.template compute<-1>();
  w.template load<BUF, 1>([&](int i) { if (i >= SO && i < SO + 4) w.template dmaW1<BUF, 0>(T + 2, i - SO); }); WAIT_V(4); WAIT_L0(); BAR();
  w.template compute<-1>();
}

constexpr int NSTEP = 22;
struct P2 {
  f32x4 af[4][4], as[4][4];
  bf16x8 ha[2][4], bb[2][4];
  lptr aH, aS[2], aS2[2], aW[4], aC[2];
  unsigned voffH, voffW, voffC;
  unsigned sH, sHs, sW, sC, ldsA, ldsB, kk, pj[4], ps, rot;
  rsrc_t rh, rw, rc, rc2_;
  template <int ST> __device__ __forceinline__ unsigned kf() const {
    constexpr int e = 2 * (ST - 8), k0 = ST < 8 ? 7 * (ST >> 1) + 3 * (ST & 1) + 1 : 7 * (e / 3) + 3 * (e % 3), k1 = ST < 8 ? k0 + 1 : 7 * ((e + 1) / 3) + 3 * ((e + 1) % 3);
    if (ST < 8) return ((7u * ((ST + rot) & 7u)) >> 1) + 1u + kk;
    return k0 + kk * (k1 - k0);
  }
  template <int ST, int GA> __device__ __forceinline__ void dmaA(int j) {
    const unsigned so = ST < 14 ? sH + kf<ST>() * 8192u : sHs + ((ST - 14) & 3) * 16384u + ((ST - 14) >> 2) * 262144u;
    dma16_dev(ldsA + GA * 49152 + (ST % 3) * 16384 + j * 1024, voffH, rh, so + GA * (NKF * 8192u) + (ST < 14 ? pj[j] : (unsigned)(j << 10) ^ ps));
  }
  template <int ST> __device__ __forceinline__ void dmaB(int s) {
    const unsigned l = ldsB + (ST % 3) * 16384 + s * 2048;
    if (ST >= 14) dma16(l, voffC, ST < 18 ? rc : rc2_, sC + ((ST - 14) & 3) * 128u + s * 32768u);
    else dma16(l, voffW, rw, sW + kf<ST>() * 65536u + s * 16384u);
  }
  template <int ST, int GA> __device__ __forceinline__ void stageA() {
#pragma unroll
    for (int j = 0; j < 4; ++j) dmaA<ST, GA>(j);
  }
  template <int ST> __device__ __forceinline__ void stageB() {
#pragma unroll
    for (int s = 0; s < 4; ++s) dmaB<ST>(s);
  }
  template <int ST, int G> __device__ __forceinline__ void load() {
    constexpr int SL = (ST % 3) * 16384;
#pragma unroll
    for (int k2 = 0; k2 < 2; ++k2) {
#pragma unroll
      for (int n = 0; n < 4; ++n) {
        const int i = k2 * 4 + n;
        if constexpr (G == 0) { if constexpr (ST + 2 < NSTEP) { if (i < 4) dmaB<ST + 2>(i); else if (i < 6) dmaA<ST + 2, 1>(i - 4); } }
        else { if constexpr (ST + 2 < NSTEP) { if (i < 2) dmaA<ST + 2, 1>(i + 2); } if constexpr (ST + 3 < NSTEP) { if (i >= 2 && i < 6) dmaA<ST + 3, 0>(i - 2); } }
        if (ST >= 14) bb[k2][n] = lds_b128(aC[k2] + SL + n * 2048);
        else bb[k2][n] = lds_tr2(aW[n] + SL + k2 * 8192, aW[n] + SL + k2 * 8192 + 4096);
        if (ST >= 14) ha[k2][n] = lds_tr2(aS[n & 1] + SL + k2 * 8192 + (n >> 1) * 1024, aS2[n & 1] + SL + k2 * 8192 + (n >> 1) * 1024);
        else ha[k2][n] = lds_b128(aH + SL + k2 * 8192 + n * 1024);
      }
    }
  }
  template <int ST, int NV> __device__ __forceinline__ void compute() {
    __builtin_amdgcn_s_setprio(1);
#pragma unroll
    for (int k = 0; k < 32; ++k) {
      const int k2 = k >> 4, m = (k >> 2) & 3, n = (m & 1) ? 3 - (k & 3) : (k & 3);
      f32x4& c = ST >= 14 ? as[m][n] : af[m][n];
      if ((ST == 0 || ST == 14) && k2 == 0) MFMA0(bb[k2][n], ha[k2][m], c); else MFMA(bb[k2][n], ha[k2][m], c);
    }
    WAIT_VN(NV); RBAR();
    __builtin_amdgcn_s_setprio(0);
    SCHED();
  }
};
template <int I, int N, class F> __device__ __forceinline__ void static_for(F&& f) {
  if constexpr (I < N) { f(std::integral_constant<int, I>{}); static_for<I + 1, N>(f); }
}

__device__ __forceinline__ void arrive(char* ws, int lane, unsigned flag_off, int ctr, unsigned epoch) {
  if (lane == 0) {
    const unsigned old = __hip_atomic_fetch_add((LAS unsigned*)mk_l(L_FLG + 16 + ctr * 4), 1u, __ATOMIC_RELAXED, __HIP_MEMORY_SCOPE_WORKGROUP);
    if (old == 3u) __hip_atomic_store((GAS unsigned*)(ws + flag_off), epoch, RLX_AGENT);
  }
}
__device__ __forceinline__ void lds_post(int ctr) {
  if (fresh_lane() == 0) __hip_atomic_fetch_add((LAS unsigned*)mk_l(L_FLG + 16 + ctr * 4), 1u, __ATOMIC_RELAXED, __HIP_MEMORY_SCOPE_WORKGROUP);
}
__device__ __forceinline__ void lds_wait(int ctr, unsigned n = 4u) {
  const lptr p = mk_l(L_FLG + 16 + ctr * 4);
_Pragma("nounroll")
  for (unsigned spins = 0; spins < (SPIN_CAP << 6); ++spins) {
    if (__builtin_amdgcn_readfirstlane(*(volatile LAS unsigned*)p) >= n) break;
    __builtin_amdgcn_s_sleep(1);
  }
  asm volatile("" ::: "memory");
}
__device__ __forceinline__ void wait_flags(const char* ws, unsigned off, unsigned epoch) {
_Pragma("nounroll")
  for (unsigned spins = 0; spins < SPIN_CAP; ++spins) {
    if (__all(__hip_atomic_load((const GAS unsigned*)(ws + off), RLX_AGENT) == epoch)) break;
    __builtin_amdgcn_s_sleep(1);
  }
  asm volatile("" ::: "memory");
}

struct Pre { unsigned voffX, voffW, voffB, sX, ldsX, sW, ldsW, sB, ldsB; };
__device__ __forceinline__ Pre mk_pre(int lane, int wid, int bid) {
  Pre pre;
  const int wr = wid & 3, G = wid >> 2, idx = bid >> 3, c = ((bid & 7) >> 1) * 8 + (idx & 7), rch = (bid & 1) * 4 + (idx >> 3);
  const unsigned sw8 = ((lane & 7) ^ (((wr & 1) * 4 + (lane >> 4)) & 7)) * 16;
  pre.voffX = ((lane >> 5) * 16 + tj(lane >> 3)) * 2048 + sw8;
  pre.voffB = (lane >> 3) * 2048 + sw8;
  pre.voffW = (lane >> 4) * 7168 + ((((lane & 15) >> 1) ^ ((lane >> 4) | ((wr & 1) << 2))) * 32) + (lane & 1) * 16;
  pre.sX = (unsigned)(rch * 256 + (G * 2 + (wr >> 1)) * 64 + (wr & 1) * 32) * 2048u;
  pre.ldsX = L_X + (G * 16 + (wr >> 1) * 8 + (wr & 1)) * 1024;
  const int ii = (wr & 1) * 2 + (wr >> 1) * 4;
  pre.sW = (unsigned)(ii * 4) * 7168u + (unsigned)((224 * c) & ~63);
  pre.ldsW = L_W + ii * 1024;
  pre.sB = (unsigned)(c * 32 + (wr & 1) * 8) * 2048u;
  pre.ldsB = L_BS + wr * 1024;
  return pre;
}
template <int GRP>
__device__ __forceinline__ void body(const Args& A, int wid, const char* code, const unsigned code_len, const u64 t00) {
  asm volatile("" : "+s"(wid));
  ST_DECL STAMP();
  const int xcd = blockIdx.x & 7, idx = blockIdx.x >> 3, c = (xcd >> 1) * 8 + (idx & 7), rch = (xcd & 1) * 4 + (idx >> 3);
  const int xrow = (xcd & 1) * 1024 + (xcd >> 1) * 256 + idx * 8;
  const int kq = xcd >> 1, rc2 = (xcd & 1) * 4 + (idx >> 3), dt = idx & 7;
  const int tt = 2 * rc2 + GRP, tile = rc2 * 8 + dt;
  const unsigned flb = (unsigned)WS_FL + rch * 1024, flc = (unsigned)WS_FL + rc2 * 1024;
  const int wr = wid & 3, wm = wr >> 1, wn = wr & 1;
  int lane = fresh_lane();
  unsigned epoch;
  P2 q;
  u32x4 hq[14]; unsigned ho[3];
  const rsrc_t rws = mk_rsrc(A.ws, (unsigned)WS_END);
  {
  P1<GRP> w;
  constexpr int NW = P1<GRP>::NW, NS = P1<GRP>::NS;
  int r = lane & 15, g = lane >> 4, qq = (lane & 15) >> 2, pp = lane & 3;
  const Pre pre = mk_pre(lane, wid, blockIdx.x);
  w.rx = mk_rsrc(A.x, L_ * D_ * 2); w.rwg = mk_rsrc(A.wg, D_ * F_ * 2); w.rwu = mk_rsrc(A.wu, D_ * F_ * 2);
  w.rb = mk_rsrc((wr >> 1) ? A.bim : A.bre, P_ * D_ * 2);
  w.voffX = pre.voffX; w.voffB = pre.voffB; w.voffW = pre.voffW;
  w.sX = pre.sX; w.ldsX = pre.ldsX; w.sW = pre.sW; w.ldsW = pre.ldsW; w.sB = pre.sB; w.ldsB = pre.ldsB;
  const char* wbase = (const char*)((wr & 1) ? A.wu : A.wg) + (idx * 2 + (wr >> 1)) * 7168 + xcd * 896;
  const char* rbase = wr < 2 ? (const char*)A.x + (xrow + wr * 4) * 2048 : (const char*)(wr == 2 ? A.bre : A.bim) + (xcd * 128 + idx * 4) * 2048;
  if (GRP == 1) {
    w.template dmaW<0, 1>(0); w.template dmaW<1, 0>(1);
    const unsigned co = ((idx * 4 + wr) * 4 + (lane & 3)) * 128u;
    touch(lane < 16 ? wbase + (2 + (lane >> 3)) * 458752u + ((lane & 7) < 7 ? (lane & 7) : 0) * 128 : lane < 24 ? rbase + ((lane - 16) >> 1) * 2048 + (2 + (lane & 1)) * 128
                    : code + (co < code_len ? co : 0u));
  }
  {
#pragma unroll
    for (int ks = 0; ks < 2; ++ks) {
      const unsigned ch = ((g + 4 * ks) ^ ((r >> 1) & 7)) << 4;
      w.aX[ks] = mk_l(L_X + (wr * 64 + r) * 128 + ch);
      if (NS) w.aB[ks] = mk_l(L_BS + r * 128 + ch);
    }
    const unsigned swz = qq | ((g & 1) << 2);
#pragma unroll
    for (int n = 0; n < NW; ++n) {
      unsigned col;
      if (GRP == 0) col = (((2 + n) ^ swz) << 5) + pp * 8;
      else if (n < 2) col = (((((c & 1) ? 6 : 0) + n) ^ swz) << 5) + pp * 8;
      else col = (((((c & 1) ? 0 : 6) + (pp >> 1)) ^ swz) << 5) + (c & 1) * 16 + (pp & 1) * 8;
      w.aW[n] = mk_l(L_W + (8 * g + qq) * 256 + col);
    }
  }
  if constexpr (GRP == 0) {
    {
      const unsigned j = idx * 4 + wr, co = (j * 4 + (lane & 3)) * 128u;
      const int l = lane < 32 ? lane : 31;
      if (l < 7) { w.pfp = wbase + l * 128; w.pfs = 458752u; w.pfo = PFD; w.pfm = 15; }
      else if (l < 15) { w.pfp = rbase + ((l - 7) >> 1) * 2048 + ((l - 7) & 1) * 128; w.pfs = 256u; w.pfo = 2; w.pfm = 7; }
      else if (l < 23) { w.pfp = (const char*)A.wd + (kq * 896 + j) * 2048u + (xcd & 1) * 1024 + (l - 15) * 128; w.pfs = 262144u; w.pfo = -7; w.pfm = 6; }
      else if (l < 27) { w.pfp = (const char*)((j & 1) ? A.cim : A.cre) + ((xcd & 1) * 512 + (j >> 1)) * 2048u + kq * 512 + (l - 23) * 128; w.pfs = 131072u; w.pfo = 0; w.pfm = 7; }
      else { w.pfp = l < 31 ? code + (co < code_len ? co : 0u) : (const char*)((idx & 4) ? A.dsk : A.gw) + ((idx & 3) * 4 + wr) * 128; w.pfs = 0u; w.pfo = 0; w.pfm = 0; }
    }
    WAIT_V(0); BAR();
    w.template load<0, 0>([&](int i) { if (i < 4) w.template dmaX1<1>(1, i); else w.template dmaB1<1>(1, i - 4); });
    WAIT_L0(); BAR();
    STAMP(); TS2();
    w.template compute<-1, true>();
    w.template load<0, 1>([&](int i) { if (i < 4) w.template dmaW1<1, 1>(1, i); else if (i == 4) w.pull(0); }); WAIT_L0(); BAR();
    w.template compute<5>();
    w.template load<1, 0>([&](int i) { if (i < 4) w.template dmaX1<0>(2, i); else w.template dmaB1<0>(2, i - 4); }); WAIT_L0(); BAR(); TS2();
    p1_tile<1>(w, 1);
  } else {
    WAIT_V(16);
    const lptr rp = mk_l(L_TAB + wr * 2048 + (lane & 31) * 2);
    const float lr = bf2f(*(const LAS u16*)rp), li = bf2f(*(const LAS u16*)(rp + 64)), ld = bf2f(*(const LAS u16*)(rp + 128));
    epoch = __builtin_amdgcn_readfirstlane(*(const LAS unsigned*)mk_l(L_TAB + wr * 2048 + 192)) + 1u;
    const float dt_ = bf2f(pk_bf16(__expf(ld), 0.f) & 0xffffu);
    const float e = __expf(dt_ * lr), rev = __builtin_amdgcn_fractf(dt_ * li * 0.15915494309189535f);
    f32x2 lam = {e * __builtin_amdgcn_cosf(rev), -e * __builtin_amdgcn_sinf(rev)};
    PIN(lam);
    WAIT_V(9); BAR();
    {
      const float nr = lam.x - 1.f, rden = 1.f / (lr * lr + li * li);
      f32x2 l8 = cmul(lam, lam); l8 = cmul(l8, l8); l8 = cmul(l8, l8);
      const f32x2 l16 = cmul(l8, l8), l32 = cmul(l16, l16), l64 = cmul(l32, l32), l128 = cmul(l64, l64);
      f32x2 pw = (wr & 1) ? l64 : (f32x2){1.f, 0.f}; if (wr & 2) pw = cmul(pw, l128);
      const f32x2 q0 = (lane & 32) ? l32 : (f32x2){1.f, 0.f}, q1 = cmul(q0, l16), c0 = cmul(q0, pw), c1 = cmul(q1, pw);
      const lptr tp = mk_l(L_TAB + wr * 2048 + (lane >> 5) * 1024 + (lane & 31) * 16);
      *(LAS f32x4*)tp = (f32x4){q0.x, q0.y, c0.x, c0.y}; *(LAS f32x4*)(tp + 512) = (f32x4){q1.x, q1.y, c1.x, c1.y};
      const lptr pb = mk_l(L_PRM + (lane & 31) * 64);
      if (wr == 0) *(LAS f32x4*)pb = (f32x4){lam.x, lam.y, (nr * lr - lam.y * li) * rden, (-lam.y * lr - nr * li) * rden};
      else if (wr == 1) { *(LAS f32x4*)(pb + 16) = (f32x4){l16.x, l16.y, l32.x, l32.y}; *(LAS f32x4*)(pb + 32) = (f32x4){l8.x, l8.y, l64.x, l64.y}; *(LAS f32x2*)(pb + 48) = cmul(l128, l128); }
      else if (wr == 3) *(LAS unsigned*)mk_l(L_FLG + (lane & 7) * 4) = (lane & 7) == 3 ? epoch : 0u;
    }
    BAR();
    STAMP(); TS2();
    w.template load<0, 0>([&](int i) { if (i >= SO && i < SO + 4) w.template dmaX1<1>(1, i - SO); }); WAIT_V(9); WAIT_L0(); BAR();
    w.template compute<-1, true>();
    w.template load<0, 1>([&](int i) { if (i >= SO && i < SO + 4) w.template dmaW1<0, 0>(2, i - SO); }); WAIT_V(4);
    if (wr == 2) {
      __hip_atomic_store((GAS u64*)(A.ws + WS_AG) + (c * 8 + 7 - rch) * 64 + lane, (u64)(epoch - 1u) << 32, RLX_AGENT);
      if (lane < 6) __hip_atomic_store((GAS unsigned*)(A.ws + flb + lane * 128 + c * 4), epoch - 1u, RLX_AGENT);
      else if (lane >= 32 && (lane & 3)) __hip_atomic_store((GAS unsigned*)(A.ws + WS_XF + ((tile * 4 + (kq ^ (lane & 3))) * 8 + ((lane - 32) >> 2)) * XS) + (lane & 3), epoch - 1u, RLX_AGENT);
    }
    WAIT_L0(); BAR();
    w.template compute<-1>(); TS2();
    p1_tile<1>(w, 1);
  }
  for (int T = 2; T < 14; T += 2) {
    TS2(); p1_tile<0>(w, T); TS2();
    p1_tile<1>(w, T + 1);
  }
  if (GRP == 0) epoch = __builtin_amdgcn_readfirstlane(*(const LAS unsigned*)mk_l(L_FLG + 12));
  STAMP();
  lane = fresh_lane();
  q.rh = mk_rsrc(A.ws + WS_H, (unsigned)H_BYTES); q.rw = mk_rsrc(A.wd, F_ * D_ * 2); q.rc = mk_rsrc((kq >> 1) ? A.cim : A.cre, D_ * P_ * 2);
  q.voffW = (lane >> 4) * 2048 + ((((lane & 15) >> 1) ^ (((wr & 1) << 2) | (lane >> 4))) * 32) + (lane & 1) * 16;
  {
    const int i0 = (wr & 1) + (wr >> 1) * 8;
    q.kk = wr >> 1; q.rot = dt + 1;
    q.sH = (unsigned)(2 * rc2 * NKF + 28 * kq) * 8192u + (wr & 1) * 4096u;
    q.sHs = (unsigned)(2 * rc2 * NKF + 112 + 16 * kq + (wr >> 1)) * 8192u + (wr & 1) * 4096u;
    q.sW = (unsigned)(28 * kq * 32 + (wr & 1) * 4) * 2048u + dt * 256u;
    q.sC = (unsigned)(dt * 128 + i0 * 8) * 2048u + (kq & 1) * 1024u;
#pragma unroll
    for (int j = 0; j < 4; ++j) q.pj[j] = (unsigned)(j ^ kq) << 10;
    q.rc = mk_rsrc(A.cre, D_ * P_ * 2); q.rc2_ = mk_rsrc(A.cim, D_ * P_ * 2);
    q.sHs = (unsigned)(2 * rc2 * NKF + 112 + 8 * kq + (wr >> 1)) * 8192u + (wr & 1) * 4096u;
    q.sC = (unsigned)(dt * 128 + i0 * 8) * 2048u + kq * 512u;
    q.ps = (unsigned)(kq >> 1) << 10;
    q.ldsA = 49152 + wr * 4096; q.ldsB = i0 * 1024;
  }
  u32x4 gx[4], gg[4];
  const int tg = rch * 256 + c * 8 + wr * 2 + (lane >> 5);
  if constexpr (GRP == 0) {
    w.template compute<7>();
    w.template load<0, 1>(); w.template dmaW<1, 1>(15);
#pragma unroll
    for (int i = 0; i < 4; ++i) gx[i] = *(const u32x4*)(A.x + tg * D_ + i * 256 + (lane & 31) * 8);
    WAIT_L0(); BAR();
    w.template compute<8>();
    w.template load<1, 0>(); q.template stageB<0>();
#pragma unroll
    for (int i = 0; i < 4; ++i) gg[i] = *(const u32x4*)(A.gw + i * 256 + (lane & 31) * 8);
    WAIT_L0(); BAR();
    w.template compute<12>();
    w.template load<1, 1>();
    {
      typedef __bf16 bf2_t __attribute__((ext_vector_type(2)));
      float s0 = 0.f, s1 = 0.f;
#pragma unroll
      for (int i = 0; i < 4; ++i)
#pragma unroll
        for (int j = 0; j < 4; j += 2) {
          const unsigned a0 = gx[i][j], b0 = gg[i][j], a1 = gx[i][j + 1], b1 = gg[i][j + 1];
          s0 = __builtin_amdgcn_fdot2_f32_bf16(__builtin_bit_cast(bf2_t, a0), __builtin_bit_cast(bf2_t, b0), s0, false);
          s1 = __builtin_amdgcn_fdot2_f32_bf16(__builtin_bit_cast(bf2_t, a1), __builtin_bit_cast(bf2_t, b1), s1, false);
        }
      float s = s0 + s1;
      s += dpp<0x128, 0xf>(0.f, s); s += dpp<0x124, 0xf>(0.f, s); s += dpp<0x122, 0xf>(0.f, s); s += dpp<0x121, 0xf>(0.f, s);
      float t0, t1; sw16(s, t0, t1); s = t0 + t1;
      if ((lane & 31) == 0) __hip_atomic_store((GAS unsigned*)(A.ws + WS_G) + tg, __float_as_uint(1.f / (1.f + __expf(-s))), RLX_AGENT);
    }
    WAIT_L0(); BAR();
    w.template compute<-2>();
    __builtin_amdgcn_s_setprio(3);
  } else {
    w.template load<0, 0>(); w.template dmaX<1>(15); WAIT_L0(); BAR();
    w.template compute<-1>();
    w.template load<0, 1>(); WAIT_V(0); WAIT_L0(); BAR();
    w.template compute<-1>();
  }
  lane = fresh_lane(); r = lane & 15; g = lane >> 4; qq = (lane & 15) >> 2; pp = lane & 3;
  const unsigned hb = (unsigned)WS_H + (unsigned)(2 * rch + (wr >> 1)) * NKF * 8192u + (wr & 1) * 4096u;
  const int kf0 = 7 * (c >> 1) + 3 * (c & 1);
  q.voffH = lane * 16;
#define FFN_H(n, m, h) pk_bf16(relu(w.ag[n][m][2 * (h)]) * w.au[n][m][2 * (h)], relu(w.ag[n][m][2 * (h) + 1]) * w.au[n][m][2 * (h) + 1])
#define P2_ADDR() do { q.voffC = (lane >> 3) * 2048 + (((lane & 7) ^ (((wr & 1) * 4 + (lane >> 4)) & 7)) * 16); PIN(q.voffC); \
    q.aH = mk_l(49152 + GRP * 49152 + wm * 4096 + lane * 16); \
    _Pragma("unroll") for (int n = 0; n < 4; ++n) q.aW[n] = mk_l((4 * g + qq) * 256 + (((wn * 4 + n) ^ (4 * (g & 1) + qq)) << 5) + pp * 8); } while (0)
#define PACK_S() do { _Pragma("unroll") for (int m = 0; m < 4; ++m) { hq[m] = (u32x4){FFN_H(0, m, 0), FFN_H(0, m, 1), FFN_H(1, m, 0), FFN_H(1, m, 1)}; PIN(hq[m]); } \
    _Pragma("unroll") for (int m = 0; m < 4; m += 2) {     \
      const auto s0 = __builtin_amdgcn_permlane32_swap(FFN_H(2, m, 0), FFN_H(2, m + 1, 0), false, false); \
      const auto s1 = __builtin_amdgcn_permlane32_swap(FFN_H(2, m, 1), FFN_H(2, m + 1, 1), false, false); \
      hq[4 + m / 2] = (u32x4){s0[0], s1[0], s0[1], s1[1]}; PIN(hq[4 + m / 2]); } \
    ho[0] = lane * 16 + hb + (kf0 + ((c & 1) ? 3 : 0)) * 8192u; \
    ho[1] = (lane & 31) * 16 + (lane >> 5) * 1024 + (c & 1) * 512 + hb + (kf0 + ((c & 1) ? 0 : 3)) * 8192u; \
    ho[2] = (lane ^ ((lane >> 5) << 2)) * 16 + hb + (112 + c) * 8192u; } while (0)
  if constexpr (GRP == 0) {
    MFMA_DRAIN();
    STAMP();
#pragma unroll
    for (int f = 0; f < 2; ++f)
#pragma unroll
      for (int m = 0; m < 4; ++m) {
        __builtin_amdgcn_raw_buffer_store_b128((u32x4){FFN_H(2 * f, m, 0), FFN_H(2 * f, m, 1), FFN_H(2 * f + 1, m, 0), FFN_H(2 * f + 1, m, 1)}, rws,
                                               lane * 16 + m * 1024 + hb + (kf0 + 1 + f) * 8192u, 0, 16);
        SCHED();
      }
    STAMP();
    WAIT_V(0); arrive(A.ws, lane, flb + (c >> 3) * 128 + c * 4, 0, epoch);
    __builtin_amdgcn_s_setprio(0);
    STAMP();
    P2_ADDR();
    if (wr == 0) { wait_flags(A.ws, flc + kq * 160 + ((dt + 1 + (lane & 3)) & 7) * 4, epoch); lds_post(3); }
    lds_wait(3, 5u);
    STAMP();
    q.template stageA<0, 0>(); q.template stageA<0, 1>(); q.template stageA<1, 0>();
    STAMP();
  } else {
    bf16x8 xb[4], bs1[4], wg1[3], wu1[3];
#pragma unroll
    for (int m = 0; m < 4; ++m) { w.xa[m] = lds_b128(w.aX[0] + 32768 + m * 2048); xb[m] = lds_b128(w.aX[1] + 32768 + m * 2048); }
#pragma unroll
    for (int i = 0; i < 4; ++i) { w.bsf[i] = lds_b128(w.aB[0] + 8192 + i * 2048); bs1[i] = lds_b128(w.aB[1] + 8192 + i * 2048); }
    w.wgf[0] = lds_tr2(w.aW[0] + 32768, w.aW[0] + 32768 + 1024); w.wuf[0] = lds_tr2(w.aW[0] + 49152, w.aW[0] + 49152 + 1024);
    q.template stageB<1>(); WAIT_L0(); BAR();
    __builtin_amdgcn_s_setprio(1);
#pragma unroll
    for (int i = 0; i < 4; ++i)
#pragma unroll
      for (int m = 0; m < 4; ++m) MFMA(w.xa[m], w.bsf[i], w.as[i][m]);
#pragma unroll
    for (int i = 0; i < 4; ++i)
#pragma unroll
      for (int m = 0; m < 4; ++m) MFMA(xb[m], bs1[i], w.as[i][m]);
#pragma unroll
    for (int m = 0; m < 4; ++m) MFMA(w.wgf[0], w.xa[m], w.ag[0][m]);
#pragma unroll
    for (int m = 0; m < 4; ++m) MFMA(w.wuf[0], w.xa[m], w.au[0][m]);
    __builtin_amdgcn_s_setprio(0);
    BAR();
    STAMP();
    f32x4 pl[2], pm[2];
    f32x2 l8[2], cin[2], ea[2], agg[2];
#pragma unroll
    for (int pf = 0; pf < 2; ++pf) {
      const lptr pb = mk_l(L_PRM + (pf * 16 + r) * 64);
      pl[pf] = *(const LAS f32x4*)pb; pm[pf] = *(const LAS f32x4*)(pb + 16); l8[pf] = *(const LAS f32x2*)(pb + 32);
    }
#pragma unroll
    for (int n = 1; n < 3; ++n) { w.wgf[n] = lds_tr2(w.aW[n] + 32768, w.aW[n] + 32768 + 1024); w.wuf[n] = lds_tr2(w.aW[n] + 49152, w.aW[n] + 49152 + 1024); }
    SCHED();
#define PAIR(v, k) ((k) & 1 ? (f32x2){(v)[(k) >> 1][2], (v)[(k) >> 1][3]} : (f32x2){(v)[(k) >> 1][0], (v)[(k) >> 1][1]})
#define CSTEP(P, Q, pf, k) do { const f32x2 lx_ = {pl[pf][0], pl[pf][0]}, ly_ = {pl[pf][1], pl[pf][1]}; \
      const f32x2 a_ = __builtin_elementwise_fma(-ly_, Q, PAIR(w.as[2 * (pf)], k)), b_ = __builtin_elementwise_fma(ly_, P, PAIR(w.as[2 * (pf) + 1], k)); \
      P = __builtin_elementwise_fma(lx_, P, a_); Q = __builtin_elementwise_fma(lx_, Q, b_); } while (0)
    {
#pragma unroll
      for (int pf = 0; pf < 2; ++pf)
#pragma unroll
        for (int m = 0; m < 4; ++m) {
          const f32x4 re = w.as[2 * pf][m], im = w.as[2 * pf + 1][m];
          w.as[2 * pf][m] = re * pl[pf][2] - im * pl[pf][3]; w.as[2 * pf + 1][m] = -(re * pl[pf][3]) - im * pl[pf][2];
          PIN(w.as[2 * pf][m]); PIN(w.as[2 * pf + 1][m]);
        }
      f32x2 P[2], Q[2];
#pragma unroll
      for (int k = 0; k < 8; ++k)
#pragma unroll
        for (int pf = 0; pf < 2; ++pf) {
          if (k == 0) { P[pf] = PAIR(w.as[2 * pf], 0); Q[pf] = PAIR(w.as[2 * pf + 1], 0); } else CSTEP(P[pf], Q[pf], pf, k);
        }
#pragma unroll
      for (int pf = 0; pf < 2; ++pf) {
        ea[pf] = (f32x2){P[pf].x, Q[pf].x};
        const f32x2 e = cfma(l8[pf], ea[pf], (f32x2){P[pf].y, Q[pf].y});
        const f32x2 l16 = {pm[pf][0], pm[pf][1]}, l32 = {pm[pf][2], pm[pf][3]};
        float a0, a1, b0, b1; sw16(e.x, a0, a1); sw16(e.y, b0, b1);
        const f32x2 r0 = {a0, b0};
        const f32x2 xx = cfma(l16, r0, (f32x2){a1, b1});
        float c0, c1, d0, d1; sw32(xx.x, c0, c1); sw32(xx.y, d0, d1);
        const f32x2 s0 = {c0, d0};
        agg[pf] = cfma(l32, s0, (f32x2){c1, d1});
        const f32x2 c3 = cfma(l16, s0, r0);
        cin[pf] = g == 0 ? (f32x2){0.f, 0.f} : g == 1 ? r0 : g == 2 ? s0 : c3;
        PIN(ea[pf]);
      }
    }
    f32x2 aggq = (g & 1) ? agg[1] : agg[0]; PIN(aggq); PIN(cin[0]); PIN(cin[1]);
    if (wr < 3) {
      if (g < 2) *(LAS f32x2*)mk_l(L_WA + ((g * 16 + r) * 4 + wr) * 8) = aggq;
      WAIT_L0();
      *(volatile LAS unsigned*)mk_l(L_FLG + wr * 4) = 1u;
    }
    SCHED();
#pragma unroll
    for (int n = 0; n < 3; ++n) { wg1[n] = lds_tr2(w.aW[n] + 40960, w.aW[n] + 40960 + 1024); wu1[n] = lds_tr2(w.aW[n] + 57344, w.aW[n] + 57344 + 1024); }
    if (wr == 3) {
      const lptr fp = mk_l(L_FLG);
_Pragma("nounroll")
      for (unsigned spins = 0; spins < SPIN_CAP; ++spins) {
        const u32x4 f = *(volatile LAS u32x4*)fp;
        if (__builtin_amdgcn_readfirstlane(f[0] & f[1] & f[2])) break;
      }
      asm volatile("" ::: "memory");
      const lptr ap = mk_l(L_WA + ((g & 1) * 16 + r) * 32);
      const f32x4 a01 = *(const LAS f32x4*)ap; const f32x2 a2 = *(const LAS f32x2*)(ap + 16);
      const f32x2 lw = *(const LAS f32x2*)mk_l(L_PRM + ((g & 1) * 16 + r) * 64 + 40);
      const f32x2 t = cfma(lw, cfma(lw, cfma(lw, (f32x2){a01[0], a01[1]}, (f32x2){a01[2], a01[3]}), a2), aggq);
      if (rch < 7) __hip_atomic_store((GAS u64*)(A.ws + WS_AG) + (c * 8 + 7 - rch) * 64 + lane, ((u64)epoch << 32) | __float_as_uint((g & 2) ? t.y : t.x), RLX_AGENT);
    }
    WAIT_L0(); lds_post(3); SCHED();
    STAMP();
    __builtin_amdgcn_s_setprio(1);
#pragma unroll
    for (int n = 1; n < 3; ++n) {
#pragma unroll
      for (int m = 0; m < 4; ++m) MFMA(w.wgf[n], w.xa[m], w.ag[n][m]);
#pragma unroll
      for (int m = 0; m < 4; ++m) MFMA(w.wuf[n], w.xa[m], w.au[n][m]);
    }
#pragma unroll
    for (int n = 0; n < 3; ++n) {
#pragma unroll
      for (int m = 0; m < 4; ++m) MFMA(wg1[n], xb[m], w.ag[n][m]);
#pragma unroll
      for (int m = 0; m < 4; ++m) MFMA(wu1[n], xb[m], w.au[n][m]);
    }
    __builtin_amdgcn_s_setprio(2);
    SCHED();
    lane = fresh_lane(); r = lane & 15; g = lane >> 4; qq = (lane & 15) >> 2; pp = lane & 3;
    q.voffH = lane * 16;
    const rsrc_t rag = mk_rsrc(A.ws + WS_AG + c * 4096, 4096u);
    const unsigned ago = lane * 8 + (8 - rch) * 512;
    u32x2 gv_[7];
#pragma unroll
    for (int cc = 0; cc < 7; ++cc) gv_[cc] = __builtin_amdgcn_raw_buffer_load_b64(rag, ago + cc * 512, 0, 16);
    {
      const lptr fp = mk_l(L_FLG);
_Pragma("nounroll")
      for (unsigned spins = 0; spins < SPIN_CAP; ++spins) {
        const u32x4 f = *(volatile LAS u32x4*)fp;
        if (__builtin_amdgcn_readfirstlane(f[0] & f[1] & f[2])) break;
      }
      asm volatile("" ::: "memory");
    }
    STAMP();
    f32x4 p2[2], tb4[2], a01[2]; f32x2 a2[2];
    const f32x2 l256q = *(const LAS f32x2*)mk_l(L_PRM + ((g & 1) * 16 + r) * 64 + 48);
    {
      const lptr pb = mk_l(L_PRM + r * 64), ap = mk_l(L_WA + r * 32), tp = mk_l(L_TAB + wr * 2048 + g * 512 + r * 16);
#pragma unroll
      for (int pf = 0; pf < 2; ++pf) {
        pl[pf] = *(const LAS f32x4*)(pb + pf * 1024); p2[pf] = *(const LAS f32x4*)(pb + pf * 1024 + 32); tb4[pf] = *(const LAS f32x4*)(tp + pf * 256);
        a01[pf] = *(const LAS f32x4*)(ap + pf * 512); a2[pf] = *(const LAS f32x2*)(ap + pf * 512 + 16);
      }
    }
    SCHED();
    PACK_S();
    P2_ADDR();
    PIN(ho[0]); PIN(ho[1]); PIN(ho[2]);
    SCHED();
    f32x2 car[2], cai[2], cmr[2], cmi[2];
#pragma unroll
    for (int pf = 0; pf < 2; ++pf) {
      const f32x2 l8_ = {p2[pf][0], p2[pf][1]}, l64 = {p2[pf][2], p2[pf][3]};
      f32x2 sw = wr > 0 ? (f32x2){a01[pf][0], a01[pf][1]} : (f32x2){0.f, 0.f};
      if (wr > 1) sw = cfma(l64, sw, (f32x2){a01[pf][2], a01[pf][3]});
      if (wr > 2) sw = cfma(l64, sw, a2[pf]);
      const f32x2 ca = cfma((f32x2){tb4[pf][0], tb4[pf][1]}, sw, cin[pf]), cb_ = cfma(l8_, ca, ea[pf]), cm = {tb4[pf][2], tb4[pf][3]}, cn = cmul(l8_, cm);
      car[pf] = (f32x2){ca.x, cb_.x}; cai[pf] = (f32x2){ca.y, cb_.y}; cmr[pf] = (f32x2){cm.x, cn.x}; cmi[pf] = (f32x2){cm.y, cn.y};
      PIN(car[pf]); PIN(cai[pf]); PIN(cmr[pf]); PIN(cmi[pf]);
    }
    STAMP();
_Pragma("nounroll")
    for (unsigned spins = 0; spins < SPIN_CAP; ++spins) {
      __builtin_amdgcn_s_waitcnt(0x0F70);
      unsigned bad = 0;
#pragma unroll
      for (int cc = 0; cc < 7; ++cc) bad |= gv_[cc][1] ^ (cc < rch ? epoch : 0u);
      if (__all(bad == 0u)) break;
      __builtin_amdgcn_s_sleep(1);
      asm volatile("" ::: "memory");
#pragma unroll
      for (int cc = 0; cc < 7; ++cc) gv_[cc] = __builtin_amdgcn_raw_buffer_load_b64(rag, ago + cc * 512, 0, 16);
    }
    __builtin_amdgcn_s_waitcnt(0x0F70);
    STAMP();
    f32x2 cb;
    {
      float are[7], aim[7];
#pragma unroll
      for (int cc = 0; cc < 7; ++cc) { const unsigned v = gv_[cc][0]; sw32(__uint_as_float(v), are[cc], aim[cc]); }
      cb = (f32x2){are[6], aim[6]};
#pragma unroll
      for (int cc = 5; cc >= 0; --cc) cb = cfma(l256q, cb, (f32x2){are[cc], aim[cc]});
    }
    unsigned hr[2][8], hi[2][8];
    {
      float cx[2], cy[2]; sw16(cb.x, cx[0], cx[1]); sw16(cb.y, cy[0], cy[1]);
      f32x2 P[2], Q[2];
#pragma unroll
      for (int pf = 0; pf < 2; ++pf) { P[pf] = car[pf] + cmr[pf] * cx[pf] - cmi[pf] * cy[pf]; Q[pf] = cai[pf] + cmr[pf] * cy[pf] + cmi[pf] * cx[pf]; }
#pragma unroll
      for (int k = 0; k < 8; ++k)
#pragma unroll
        for (int pf = 0; pf < 2; ++pf) { CSTEP(P[pf], Q[pf], pf, k); hr[pf][k] = pk_bf16(P[pf].x, P[pf].y); hi[pf][k] = pk_bf16(Q[pf].x, Q[pf].y); }
    }
#pragma unroll
    for (int i = 0; i < 8; ++i) {
      const unsigned* h = (i >> 2) ? hi[(i >> 1) & 1] : hr[(i >> 1) & 1];
      hq[6 + i] = (u32x4){h[(i & 1) * 4], h[(i & 1) * 4 + 1], h[(i & 1) * 4 + 2], h[(i & 1) * 4 + 3]}; PIN(hq[6 + i]);
    }
    STAMP(); TS2();
    WAIT_V(0); lds_wait(1);
    STAMP();
    q.template stageA<1, 1>(); q.template stageA<2, 0>();
  }
  }
  if (GRP == 0) { STAMP(); TS2(); }
  constexpr int G = GRP;
  int r, g;
  unsigned fl = 0u;
#define FLAG_LOAD(off) asm volatile("global_load_dword %0, %1, %2 sc1" : "+v"(fl) : "v"(off), "s"(A.ws) : "memory")
#define FLAG_CHECK(off) do { PIN(fl); if (!__all(fl == epoch)) wait_flags(A.ws, off, epoch); } while (0)
#define HQ_ST(i) __builtin_amdgcn_raw_buffer_store_b128(hq[i], rws, (i) < 4 ? ho[0] + (i) * 1024 : (i) < 6 ? ho[1] + ((i) - 4) * 2048 : ho[2] + (((i) - 6) >> 2) * 262144 + (((i) - 6) & 3) * 1024, 0, 16)
#define XTRA(st) ((st) == 13 ? 4 : 0)
  float gv[4];
  u32x2 xv[4], dv; float gr[4];
  const int t0 = tt * 128 + wm * 64 + kq * 2, dc0 = dt * 128 + wn * 64;
  auto side = [&](auto ST_) {
    constexpr int ST = decltype(ST_)::value;
    if constexpr (ST == 13) {
      const int l = fresh_lane(), g_ = l >> 4, q_ = (l & 15) >> 2, p_ = l & 3;
#pragma unroll
      for (int e = 0; e < 2; ++e) {
        const unsigned b = 49152 + G * 49152 + wm * 4096 + (g_ >> 1) * 2048 + p_ * 256 + 8 * (g_ & 1) * 16 + q_ * 16 + (e ^ (kq & 1)) * 8;
        q.aS[e] = mk_l(b + ((p_ >> 1) << 6)); q.aS2[e] = mk_l(b + (((p_ >> 1) ^ 1) << 6));
        q.aC[e] = mk_l((wn * 64 + (l & 15)) * 128 + (((g_ + 4 * e) ^ (((l & 15) >> 1) & 7)) << 4));
      }
    }
    if constexpr (ST == 13) {
      const int l = fresh_lane();
      const unsigned go = (unsigned)WS_G + (tt * 128 + wm * 64 + ((l & 15) >> 2) * 16 + tj(l)) * 4;
#pragma unroll
      for (int j = 0; j < 4; ++j) asm volatile("global_load_dword %0, %1, %2 sc1" : "=v"(gv[j]) : "v"(go + (q.pj[j] >> 7)), "s"(A.ws) : "memory");
    }
    if constexpr (ST >= 16 && ST < 20) {
      constexpr int m = ST - 16;
      PIN(gv[m]);
#pragma unroll
      for (int n = 0; n < 4; ++n) { q.af[m][n] *= 1.f - gv[m]; PIN(q.af[m][n]); }
    }
    if constexpr (ST == NSTEP - 1) {
      const int l = fresh_lane();
      dv = *(const u32x2*)(A.dsk + dc0 + (l & 15) * 4);
#pragma unroll
      for (int i = 0; i < 4; ++i) {
        xv[i] = *(const u32x2*)(A.x + (t0 + i * 16 + tj(l >> 4)) * D_ + dc0 + (l & 15) * 4);
        gr[i] = __int_as_float(__builtin_amdgcn_ds_bpermute((4 * i + (l >> 4)) * 4, __float_as_int(gv[0])));
      }
#pragma unroll
      for (int j = 0; j < 4; ++j) { gv[j] *= 2.f; PIN(gv[j]); }
    }
  };
  __builtin_amdgcn_s_setprio(0);
  if constexpr (G == 0) {
    const unsigned f2 = flc + 512 + (8 * kq + (lane & 7)) * 4, f3 = flc + 640 + (lane & 31) * 4;
    WAIT_V(8); lds_post(1); lds_wait(1); STAMP();
    const unsigned f1 = flc + kq * 160 + ((dt + 1 + lane) & 7) * 4;
    q.template load<0, 0>(); WAIT_V(10); FLAG_LOAD(f1); WAIT_L0(); lds_post(2); SCHED();
    static_for<0, NSTEP - 1>([&](auto ST_) {
      constexpr int ST = decltype(ST_)::value;
      constexpr int prev = (ST + 2 < NSTEP ? 6 : 0) + (ST <= 4 || ST == 10 ? 1 : 0) + XTRA(ST);
      q.template compute<ST, prev>();
      q.template load<ST + 1, 0>();
      if constexpr (ST + 1 <= 4) {
        WAIT_V(6); PIN(fl);
        if (!((__ballot(fl == epoch) >> (ST + 4)) & 1ull)) wait_flags(A.ws, flc + kq * 160 + ((dt + 5 + ST) & 7) * 4, epoch);
        if constexpr (ST + 1 < 4) FLAG_LOAD(f1);
      }
      if constexpr (ST + 1 == 4) FLAG_LOAD(f2);
      if constexpr (ST + 1 == 5) { WAIT_V(6); FLAG_CHECK(f2); }
      if constexpr (ST + 1 == 10) FLAG_LOAD(f3);
      if constexpr (ST + 1 == 11) { WAIT_V(6); FLAG_CHECK(f3); }
      if constexpr (ST + 1 == 20) { if (blockIdx.x == 255 && wr == 0 && lane == 0) __hip_atomic_store((GAS unsigned*)(A.ws + WS_EP), epoch, RLX_AGENT); }
      side(std::integral_constant<int, ST + 1>{});
      WAIT_L0(); BAR(); TS2();
      if constexpr (ST == 1 || ST == 13) STAMP();
    });
  } else {
    HQ_ST(0); HQ_ST(1); HQ_ST(2);
    lds_wait(2);
    static_for<0, NSTEP>([&](auto ST_) {
      constexpr int ST = decltype(ST_)::value;
      constexpr int nst = ST == 0 ? 3 : ST <= 4 ? 2 : 0;
      if constexpr (ST == 2 || ST == 6) {
        if (wr == 0 && lane == 0) __hip_atomic_store((GAS unsigned*)(A.ws + flb + (ST == 2 ? 512 : 640) + c * 4), epoch, RLX_AGENT);
      }
      q.template load<ST, 1>();
      if constexpr (ST == 0) { HQ_ST(3); HQ_ST(4); HQ_ST(5); }
      if constexpr (ST >= 1 && ST <= 4) { HQ_ST(4 + 2 * ST); HQ_ST(5 + 2 * ST); }
      side(ST_);
      WAIT_L0(); BAR();
      if constexpr (ST + 1 < NSTEP) {
        q.template compute<ST, (ST + 2 < NSTEP ? 2 : 0) + (ST + 3 < NSTEP ? 4 : 0) + nst + XTRA(ST)>(); TS2();
        if constexpr (ST == 1 || ST == 13) STAMP();
      }
    });
  }
  lane = fresh_lane(); r = lane & 15; g = lane >> 4;
  f32x4 o[4];
  {
    unsigned xo[3], pk[4];
#pragma unroll
    for (int j = 1; j < 4; ++j) xo[j - 1] = (unsigned)WS_XB + ((tile * 4 + (kq ^ j)) * 3 + (j - 1)) * 16384u + wid * 2048 + lane * 16;
    __builtin_amdgcn_s_setprio(1);
#pragma unroll
    for (int k = 0; k < 19; ++k) {
      if (k < 16) { const int m = ((k >> 2) + 1) & 3, n = k & 3; MFMA(q.bb[0][n], q.ha[0][m], q.as[m][n]); MFMA(q.bb[1][n], q.ha[1][m], q.as[m][n]); }
      if (k == 3 || k >= 16) asm volatile("s_nop 3");
      if (G == 0 && k == 14) RBAR();
      SCHED();
      if (k >= 3) {
        const int m = (((k - 3) >> 2) + 1) & 3, n = (k - 3) & 3;
        f32x4 v;
#pragma unroll
        for (int j = 0; j < 4; ++j) v[j] = __builtin_fmaf(q.as[m][n][j], gv[m], q.af[m][n][j]);
        if (m == 0) o[n] = v;
        else {
          pk[(n & 1) * 2] = pk_bf16(v[0], v[1]); pk[(n & 1) * 2 + 1] = pk_bf16(v[2], v[3]);
          if (n & 1) __builtin_amdgcn_raw_buffer_store_b128((u32x4){pk[0], pk[1], pk[2], pk[3]}, rws, xo[m - 1] + (n >> 1) * 1024, 0, 16);
        }
      }
      SCHED();
    }
    __builtin_amdgcn_s_setprio(0);
  }
  STAMP();
  WAIT_V(0);
  if (lane >= 1 && lane < 4) __hip_atomic_store((GAS unsigned*)(A.ws + WS_XF + ((tile * 4 + (kq ^ lane)) * 8 + wid) * XS) + lane, epoch, RLX_AGENT);
  STAMP();
  f32x4 sk[4];
  {
    const f32x4 df = {bf2f(dv[0] & 0xffffu), bfhi(dv[0]), bf2f(dv[1] & 0xffffu), bfhi(dv[1])};
#pragma unroll
    for (int i = 0; i < 4; ++i) { sk[i] = (f32x4){bf2f(xv[i][0] & 0xffffu), bfhi(xv[i][0]), bf2f(xv[i][1] & 0xffffu), bfhi(xv[i][1])} * df * gr[i]; PIN(sk[i]); }
  }
  const lptr tw = mk_l(49152 + G * 49152 + wr * 4096 + r * 256), tr_ = mk_l(49152 + G * 49152 + wr * 4096 + g * 256);
  wait_flags(A.ws, (unsigned)WS_XF + ((tile * 4 + kq) * 8 + wid) * XS + (1 + (lane & 3) % 3) * 4, epoch);
  STAMP();
  {
    u32x4 pv[6];
    const unsigned xi = (unsigned)WS_XB + (tile * 4 + kq) * 3 * 16384u + wid * 2048 + lane * 16;
#pragma unroll
    for (int i = 0; i < 6; ++i) pv[i] = __builtin_amdgcn_raw_buffer_load_b128(rws, xi + (i >> 1) * 16384 + (i & 1) * 1024, 0, 16);
#pragma unroll
    for (int i = 0; i < 6; ++i)
#pragma unroll
      for (int h = 0; h < 2; ++h) {
        const unsigned a0 = pv[i][2 * h], a1 = pv[i][2 * h + 1];
        o[(i & 1) * 2 + h] += (f32x4){bf2f(a0 & 0xffffu), bfhi(a0), bf2f(a1 & 0xffffu), bfhi(a1)};
      }
  }
#pragma unroll
  for (int n = 0; n < 4; ++n) *(LAS f32x4*)(tw + (((n * 4 + g) ^ (r & 7)) << 4)) = o[n];
  WAIT_L0();
#pragma unroll
  for (int i = 0; i < 4; ++i) {
    const f32x4 v = *(const LAS f32x4*)(tr_ + i * 1024 + ((r ^ ((4 * i + g) & 7)) << 4));
    __builtin_amdgcn_raw_buffer_store_b128(__builtin_bit_cast(u32x4, v + sk[i]), mk_rsrc(A.out, L_ * D_ * 4), ((t0 + i * 16 + tj(g)) * D_ + dc0 + r * 4) * 4, 0, 17);
  }
  STAMP();
  ST_DUMP(A.ws, wid);
  if (GRP == 0) asm volatile(".Lgm_end0:"); else asm volatile(".Lgm_end1:");
  __builtin_amdgcn_endpgm();
  __builtin_unreachable();
}

__global__ __launch_bounds__(512, 2) void fused_kernel(Args A) {
  const u64 t00 = 0;
  const int wid = __builtin_amdgcn_readfirstlane(threadIdx.x >> 6);
  {
    const Pre pre = mk_pre(threadIdx.x & 63, wid, blockIdx.x);
    const int G = wid >> 2;
    if (G) {
      const int l = threadIdx.x & 63;
      const char* p = (l < 16 ? (const char*)A.lre : l < 32 ? (const char*)A.lim : (const char*)A.ldt) + (pre.sB >> 16) * 64 + (l & 15) * 4;
      if (l >= 48) p = A.ws + WS_EP;
      asm volatile("s_mov_b32 m0, %0\n\ts_nop 0\n\tglobal_load_lds_dword %1, off sc1" :: "s"(L_TAB + (wid & 3) * 2048), "v"(p) : "memory");
    }
    const rsrc_t rx = mk_rsrc(A.x, L_ * D_ * 2);
#pragma unroll
    for (int s = 0; s < 4; ++s) dma16(pre.ldsX + s * 2048, pre.voffX, rx, pre.sX + s * 4096);
    dma16(pre.ldsW + G * 1024, pre.voffW, mk_rsrc(A.wg, D_ * F_ * 2), pre.sW + G * 28672u);
    dma16(pre.ldsW + 16384 + G * 1024, pre.voffW, mk_rsrc(A.wu, D_ * F_ * 2), pre.sW + G * 28672u);
    dma16(pre.ldsB + G * 4096, pre.voffB, mk_rsrc((wid & 2) ? A.bim : A.bre, P_ * D_ * 2), pre.sB + G * 32768);
  }
  const char* code; unsigned code_len;
  asm volatile("s_getpc_b64 %0\n.Lgm_pc:\n\ts_mov_b32 %1, .Lgm_end0-.Lgm_pc\n\ts_max_u32 %1, %1, .Lgm_end1-.Lgm_pc" : "=s"(code), "=s"(code_len) :: "scc");
  if (wid < 4) body<0>(A, wid, code, code_len, t00); else body<1>(A, wid, code, code_len, t00);
}

extern "C" void kernel_launch(void* const* d_in, const int* in_sizes, int n_in, void* d_out, int out_size, void* d_ws, size_t ws_size,
                              hipStream_t stream) {
  Args a; a.x = (const u16*)d_in[0]; a.lre = (const u16*)d_in[1]; a.lim = (const u16*)d_in[2]; a.bre = (const u16*)d_in[3];
  a.bim = (const u16*)d_in[4]; a.cre = (const u16*)d_in[5]; a.cim = (const u16*)d_in[6]; a.ldt = (const u16*)d_in[7]; a.dsk = (const u16*)d_in[8];
  a.wg = (const u16*)d_in[10]; a.wu = (const u16*)d_in[11]; a.wd = (const u16*)d_in[12]; a.gw = (const u16*)d_in[13];
  a.ws = (char*)d_ws; a.out = (float*)d_out;
  (void)hipFuncSetAttribute((const void*)fused_kernel, hipFuncAttributeMaxDynamicSharedMemorySize, LDS_BYTES);
  fused_kernel<<<256, 512, LDS_BYTES, stream>>>(a);
}
```

```cpp
#define XS 16
#include <hip/hip_runtime.h>
#include <hip/hip_bf16.h>
#include <type_traits>
#define MAP2 1
#define ORD2 1
#define MAP1 1
#define SQ 1
#define SO 1
#define PFD 4

typedef unsigned short u16;
typedef short bf16x8 __attribute__((ext_vector_type(8)));
typedef short s16x4 __attribute__((ext_vector_type(4)));
typedef float f32x4 __attribute__((ext_vector_type(4)));
typedef float f32x2 __attribute__((ext_vector_type(2)));
typedef unsigned u32x4 __attribute__((ext_vector_type(4)));
typedef unsigned u32x2 __attribute__((ext_vector_type(2)));
typedef unsigned long long u64;
#define LAS __attribute__((address_space(3)))
#define GAS __attribute__((address_space(1)))
#define RLX_AGENT __ATOMIC_RELAXED, __HIP_MEMORY_SCOPE_AGENT
typedef __amdgpu_buffer_rsrc_t rsrc_t;

constexpr int L_ = 2048, D_ = 1024, P_ = 1024, F_ = 3584;
constexpr int NKF = 176;
constexpr size_t WS_H = 0;
constexpr size_t H_BYTES = (size_t)16 * NKF * 8 * 1024;
constexpr size_t WS_XB = WS_H + H_BYTES;
constexpr size_t XB_BYTES = (size_t)64 * 4 * 3 * 16384;
constexpr size_t WS_G = WS_XB + XB_BYTES;
constexpr size_t WS_AG = WS_G + 8192;
constexpr size_t WS_EP = WS_AG + (size_t)32 * 8 * 64 * 8;
constexpr size_t WS_FL = WS_EP + 512;
constexpr size_t WS_XF = WS_FL + 8192;
constexpr size_t WS_END = WS_XF + 256 * 8 * XS;
constexpr unsigned SPIN_CAP = 1u << 20;

__device__ __forceinline__ float bf2f(unsigned b) { return __uint_as_float(b << 16); }
__device__ __forceinline__ float bfhi(unsigned b) { return __uint_as_float(b & 0xffff0000u); }
__device__ __forceinline__ unsigned pk_bf16(float lo, float hi) {
  typedef __bf16 bf2_t __attribute__((ext_vector_type(2)));
  return __builtin_bit_cast(unsigned, __builtin_convertvector((f32x2){lo, hi}, bf2_t)); }
__device__ __forceinline__ float relu(float v) { const int b = __float_as_int(v); return __int_as_float(b > 0 ? b : 0); }
template <int CTRL, int BANKS> __device__ __forceinline__ float dpp(float old, float src) {
  return __int_as_float(__builtin_amdgcn_update_dpp(__float_as_int(old), __float_as_int(src), CTRL, 0xf, BANKS, false)); }
__device__ __forceinline__ rsrc_t mk_rsrc(const void* p, unsigned bytes) {
  return __builtin_amdgcn_make_buffer_rsrc((void*)p, (short)0, (int)bytes, 0x00020000); }
__device__ __forceinline__ void dma16(unsigned ldsaddr, unsigned voff, rsrc_t srd, unsigned soff) {
  asm volatile("s_mov_b32 m0, %0\n\ts_nop 0\n\tbuffer_load_dwordx4 %1, %2, %3 offen lds"
               :: "s"(ldsaddr), "v"(voff), "s"(srd), "s"(soff) : "memory"); }
__device__ __forceinline__ void dma16_dev(unsigned ldsaddr, unsigned voff, rsrc_t srd, unsigned soff) {
  asm volatile("s_mov_b32 m0, %0\n\ts_nop 0\n\tbuffer_load_dwordx4 %1, %2, %3 offen sc1 lds"
               :: "s"(ldsaddr), "v"(voff), "s"(srd), "s"(soff) : "memory"); }
#define WAIT_V(n) asm volatile("s_waitcnt vmcnt(" #n ")" ::: "memory")
#define WAIT_VN(n) asm volatile("s_waitcnt vmcnt(%0)" :: "i"(n) : "memory")
#define WAIT_L0() do { __builtin_amdgcn_s_waitcnt(0xC07F); asm volatile("" ::: "memory"); } while (0)
#define SCHED() __builtin_amdgcn_sched_barrier(0)
#define RBAR() do { __builtin_amdgcn_s_barrier(); asm volatile("" ::: "memory"); } while (0)
#define BAR() do { SCHED(); RBAR(); SCHED(); } while (0)
__device__ __forceinline__ int fresh_lane() { int l; asm volatile("v_mbcnt_lo_u32_b32 %0, -1, 0\n\tv_mbcnt_hi_u32_b32 %0, -1, %0" : "=v"(l)); return l; }
typedef LAS char* lptr;
__device__ __forceinline__ lptr mk_l(unsigned a) { lptr p = (lptr)(size_t)a; asm volatile("" : "+v"(p)); return p; }
__device__ __forceinline__ bf16x8 lds_b128(lptr a) { return *(const LAS bf16x8*)a; }
__device__ __forceinline__ bf16x8 lds_tr2(lptr a0, lptr a1) {
  s16x4 lo = __builtin_amdgcn_ds_read_tr16_b64_v4i16((LAS s16x4*)a0);
  s16x4 hi = __builtin_amdgcn_ds_read_tr16_b64_v4i16((LAS s16x4*)a1);
  return __builtin_shufflevector(lo, hi, 0, 1, 2, 3, 4, 5, 6, 7); }
#define MFMA(a, b, c) asm volatile("v_mfma_f32_16x16x32_bf16 %0, %1, %2, %0" : "+v"(c) : "v"(a), "v"(b))
#define MFMA0(a, b, c) asm volatile("v_mfma_f32_16x16x32_bf16 %0, %1, %2, 0" : "=&v"(c) : "v"(a), "v"(b))
#define PIN(x) asm volatile("" : "+v"(x))
#define MFMA_DRAIN() do { asm volatile("s_nop 7\n\ts_nop 7" ::: "memory"); SCHED(); } while (0)

#define ST_DECL
#define STAMP()
#define TS2()
#define ST_DUMP(ws, wid)

struct Args {
  const u16 *x, *lre, *lim, *bre, *bim, *cre, *cim, *ldt, *dsk, *wg, *wu, *wd, *gw;
  char* ws; float* out;
};

constexpr unsigned L_X = 0, L_W = 65536, L_BS = 131072, L_PRM = 151552  ,
                   L_WA = 153600  , L_FLG = 154624  , L_SINK = 154880, L_TAB = 155136  , LDS_BYTES = 163840;

__device__ __forceinline__ int tj(int j) { return (j & 1) * 8 + ((j >> 1) & 1); }
__device__ __forceinline__ f32x2 cmul(f32x2 a, f32x2 b) { return (f32x2){a.x * b.x - a.y * b.y, a.x * b.y + a.y * b.x}; }
__device__ __forceinline__ f32x2 cfma(f32x2 a, f32x2 b, f32x2 c) {
  return (f32x2){__builtin_fmaf(a.x, b.x, __builtin_fmaf(-a.y, b.y, c.x)), __builtin_fmaf(a.x, b.y, __builtin_fmaf(a.y, b.x, c.y))}; }
__device__ __forceinline__ void sw16(float v, float& r0, float& r1) { auto t = __builtin_amdgcn_permlane16_swap(__float_as_uint(v), __float_as_uint(v), false, false); r0 = __uint_as_float(t[0]); r1 = __uint_as_float(t[1]); }
__device__ __forceinline__ void sw32(float v, float& r0, float& r1) { auto t = __builtin_amdgcn_permlane32_swap(__float_as_uint(v), __float_as_uint(v), false, false); r0 = __uint_as_float(t[0]); r1 = __uint_as_float(t[1]); }

__device__ __forceinline__ void touch(const char* p) {
  asm volatile("s_mov_b32 m0, %0\n\ts_nop 0\n\tglobal_load_lds_dword %1, off" :: "s"(L_SINK), "v"(p) : "memory"); }
template <int GRP>
struct P1 {
  static constexpr int NW = GRP == 0 ? 4 : 3, NS = GRP == 0 ? 0 : 4;
  f32x4 ag[NW][4], au[NW][4], as[4][4];
  bf16x8 xa[4], wgf[NW], wuf[NW], bsf[4];
  lptr aX[2], aB[2], aW[NW];
  unsigned voffX, voffW, voffB;
  unsigned sX, sW, sB, ldsX, ldsW, ldsB;
  rsrc_t rx, rwg, rwu, rb;
  const char* pfp; unsigned pfs; int pfo, pfm;
  __device__ __forceinline__ void pull(int T) { const int i = T + pfo; touch(pfp + pfs * (unsigned)(i < 0 ? 0 : i > pfm ? pfm : i)); }

  template <int BUF, int KS, class M> __device__ __forceinline__ void load(M&& mem) {
    mem(0);
#pragma unroll
    for (int m = 0; m < 4; ++m) xa[m] = lds_b128(aX[KS] + BUF * 32768 + m * 2048);
    mem(1);
#pragma unroll
    for (int i = 0; i < NS; ++i) bsf[i] = lds_b128(aB[KS] + BUF * 8192 + i * 2048);
    if (NS) mem(2);
#pragma unroll
    for (int n = 0; n < NW; ++n) {
      wgf[n] = lds_tr2(aW[n] + BUF * 32768 + KS * 8192, aW[n] + BUF * 32768 + KS * 8192 + 1024);
      wuf[n] = lds_tr2(aW[n] + BUF * 32768 + 16384 + KS * 8192, aW[n] + BUF * 32768 + 16384 + KS * 8192 + 1024);
      mem(n + (NS ? 3 : 2));
    }
  }
  template <int BUF, int KS> __device__ __forceinline__ void load() { load<BUF, KS>([](int) {}); }
  template <int BUF> __device__ __forceinline__ void dmaX1(int T, int s) { dma16(ldsX + BUF * 32768 + s * 2048, voffX, rx, sX + T * 128 + s * 4096); }
  template <int BUF, int HH> __device__ __forceinline__ void dmaW1(int T, int i) {
    dma16(ldsW + BUF * 32768 + (i & 1) * 16384 + HH * 8192 + (i >> 1) * 1024, voffW, (i & 1) ? rwu : rwg, sW + (unsigned)(T * 64 + HH * 32 + (i >> 1) * 4) * 7168u);
  }
  template <int BUF> __device__ __forceinline__ void dmaB1(int T, int pf) { dma16(ldsB + BUF * 8192 + pf * 4096, voffB, rb, sB + pf * 32768 + T * 128); }
  template <int NV, bool FIRST = false> __device__ __forceinline__ void compute() {
    constexpr int NM = 4 * NS + 8 * NW;
    __builtin_amdgcn_s_setprio(1);
#pragma unroll
    for (int k = 0; k < NM; ++k) {
      const int f = k >> 2, m = (f & 1) ? 3 - (k & 3) : (k & 3);
      const int n = f < NS ? 0 : (f - NS) >> 1;
      const bf16x8& a = f < NS ? xa[m] : ((f - NS) & 1) ? wuf[n] : wgf[n];
      const bf16x8& b = f < NS ? bsf[f & 3] : xa[m];
      f32x4& c = f < NS ? as[f & 3][m] : ((f - NS) & 1) ? au[n][m] : ag[n][m];
      if (FIRST) MFMA0(a, b, c); else MFMA(a, b, c);
    }
    if (NV != -2) { if (NV >= 0) WAIT_VN(NV < 0 ? 0 : NV); RBAR(); }
    __builtin_amdgcn_s_setprio(0);
    SCHED();
  }
  template <int BUF> __device__ __forceinline__ void dmaX(int T) {
#pragma unroll
    for (int s = 0; s < 4; ++s) dma16(ldsX + BUF * 32768 + s * 2048, voffX, rx, sX + T * 128 + s * 4096);
  }
  template <int BUF, int HH> __device__ __forceinline__ void dmaW(int T) {
#pragma unroll
    for (int s = 0; s < 2; ++s) {
      const unsigned so = sW + (unsigned)(T * 64 + HH * 32 + s * 4) * 7168u;
      dma16(ldsW + BUF * 32768 + HH * 8192 + s * 1024, voffW, rwg, so);
      dma16(ldsW + BUF * 32768 + 16384 + HH * 8192 + s * 1024, voffW, rwu, so);
    }
  }
  template <int BUF> __device__ __forceinline__ void dmaB(int T) {
#pragma unroll
    for (int pf = 0; pf < 2; ++pf) dma16(ldsB + BUF * 8192 + pf * 4096, voffB, rb, sB + pf * 32768 + T * 128);
  }
};

template <int BUF> __device__ __forceinline__ void p1_tile(P1<0>& w, int T) {
  w.template compute<7>();
  w.template load<BUF, 1>([&](int i) { if (i < 4) w.template dmaW1<BUF ^ 1, 1>(T + 1, i); else if (i == 4) w.pull(T); }); WAIT_L0(); BAR();
  w.template compute<5>();
  w.template load<BUF ^ 1, 0>([&](int i) { if (i < 4) w.template dmaX1<BUF>(T + 2, i); else w.template dmaB1<BUF>(T + 2, i - 4); }); WAIT_L0(); BAR();
}
template <int BUF> __device__ __forceinline__ void p1_tile(P1<1>& w, int T) {
  w.template load<BUF, 0>([&](int i) { if (i >= SO && i < SO + 4) w.template dmaX1<BUF ^ 1>(T + 1, i - SO); }); WAIT_L0(); BAR();
  w.template compute<-1>();
  w.template load<BUF, 1>([&](int i) { if (i >= SO && i < SO + 4) w.template dmaW1<BUF, 0>(T + 2, i - SO); }); WAIT_V(4); WAIT_L0(); BAR();
  w.template compute<-1>();
}

constexpr int NSTEP = 22;
struct P2 {
  f32x4 af[4][4], as[4][4];
  bf16x8 ha[2][4], bb[2][4];
  lptr aH, aS[2], aS2[2], aW[4], aC[2];
  unsigned voffH, voffW, voffC;
  unsigned sH, sHs, sW, sC, ldsA, ldsB, kk, pj[4], ps;
  rsrc_t rh, rw, rc, rc2_;
  template <int ST> __device__ __forceinline__ unsigned kf() const {
    constexpr int e = 2 * (ST - 8), k0 = ST < 8 ? 7 * (ST >> 1) + 3 * (ST & 1) + 1 : 7 * (e / 3) + 3 * (e % 3), k1 = ST < 8 ? k0 + 1 : 7 * ((e + 1) / 3) + 3 * ((e + 1) % 3);
    return k0 + kk * (k1 - k0);
  }
  template <int ST, int GA> __device__ __forceinline__ void dmaA(int j) {
    const unsigned so = ST < 14 ? sH + kf<ST>() * 8192u : sHs + ((ST - 14) & 3) * 16384u + ((ST - 14) >> 2) * 262144u;
    dma16_dev(ldsA + GA * 49152 + (ST % 3) * 16384 + j * 1024, voffH, rh, so + GA * (NKF * 8192u) + (ST < 14 ? pj[j] : (unsigned)(j << 10) ^ ps));
  }
  template <int ST> __device__ __forceinline__ void dmaB(int s) {
    const unsigned l = ldsB + (ST % 3) * 16384 + s * 2048;
    if (ST >= 14) dma16(l, voffC, ST < 18 ? rc : rc2_, sC + ((ST - 14) & 3) * 128u + s * 32768u);
    else dma16(l, voffW, rw, sW + kf<ST>() * 65536u + s * 16384u);
  }
  template <int ST, int GA> __device__ __forceinline__ void stageA() {
#pragma unroll
    for (int j = 0; j < 4; ++j) dmaA<ST, GA>(j);
  }
  template <int ST> __device__ __forceinline__ void stageB() {
#pragma unroll
    for (int s = 0; s < 4; ++s) dmaB<ST>(s);
  }
  template <int ST, int G> __device__ __forceinline__ void load() {
    constexpr int SL = (ST % 3) * 16384;
#pragma unroll
    for (int k2 = 0; k2 < 2; ++k2) {
#pragma unroll
      for (int n = 0; n < 4; ++n) {
        const int i = k2 * 4 + n;
        if constexpr (G == 0) { if constexpr (ST + 2 < NSTEP) { if (i < 4) dmaB<ST + 2>(i); else if (i < 6) dmaA<ST + 2, 1>(i - 4); } }
        else { if constexpr (ST + 2 < NSTEP) { if (i < 2) dmaA<ST + 2, 1>(i + 2); } if constexpr (ST + 3 < NSTEP) { if (i >= 2 && i < 6) dmaA<ST + 3, 0>(i - 2); } }
        if (ST >= 14) bb[k2][n] = lds_b128(aC[k2] + SL + n * 2048);
        else bb[k2][n] = lds_tr2(aW[n] + SL + k2 * 8192, aW[n] + SL + k2 * 8192 + 4096);
        if (ST >= 14) ha[k2][n] = lds_tr2(aS[n & 1] + SL + k2 * 8192 + (n >> 1) * 1024, aS2[n & 1] + SL + k2 * 8192 + (n >> 1) * 1024);
        else ha[k2][n] = lds_b128(aH + SL + k2 * 8192 + n * 1024);
      }
    }
  }
  template <int ST, int NV> __device__ __forceinline__ void compute() {
    __builtin_amdgcn_s_setprio(1);
#pragma unroll
    for (int k = 0; k < 32; ++k) {
      const int k2 = k >> 4, m = (k >> 2) & 3, n = (m & 1) ? 3 - (k & 3) : (k & 3);
      f32x4& c = ST >= 14 ? as[m][n] : af[m][n];
      if ((ST == 0 || ST == 14) && k2 == 0) MFMA0(bb[k2][n], ha[k2][m], c); else MFMA(bb[k2][n], ha[k2][m], c);
    }
    WAIT_VN(NV); RBAR();
    __builtin_amdgcn_s_setprio(0);
    SCHED();
  }
};
template <int I, int N, class F> __device__ __forceinline__ void static_for(F&& f) {
  if constexpr (I < N) { f(std::integral_constant<int, I>{}); static_for<I + 1, N>(f); }
}

__device__ __forceinline__ void arrive(char* ws, int lane, unsigned flag_off, int ctr, unsigned epoch) {
  if (lane == 0) {
    const unsigned old = __hip_atomic_fetch_add((LAS unsigned*)mk_l(L_FLG + 16 + ctr * 4), 1u, __ATOMIC_RELAXED, __HIP_MEMORY_SCOPE_WORKGROUP);
    if (old == 3u) __hip_atomic_store((GAS unsigned*)(ws + flag_off), epoch, RLX_AGENT);
  }
}
__device__ __forceinline__ void lds_post(int ctr) {
  if (fresh_lane() == 0) __hip_atomic_fetch_add((LAS unsigned*)mk_l(L_FLG + 16 + ctr * 4), 1u, __ATOMIC_RELAXED, __HIP_MEMORY_SCOPE_WORKGROUP);
}
__device__ __forceinline__ void lds_wait(int ctr, unsigned n = 4u) {
  const lptr p = mk_l(L_FLG + 16 + ctr * 4);
_Pragma("nounroll")
  for (unsigned spins = 0; spins < (SPIN_CAP << 6); ++spins) {
    if (__builtin_amdgcn_readfirstlane(*(volatile LAS unsigned*)p) >= n) break;
    __builtin_amdgcn_s_sleep(1);
  }
  asm volatile("" ::: "memory");
}
__device__ __forceinline__ void wait_flags(const char* ws, unsigned off, unsigned epoch) {
_Pragma("nounroll")
  for (unsigned spins = 0; spins < SPIN_CAP; ++spins) {
    if (__all(__hip_atomic_load((const GAS unsigned*)(ws + off), RLX_AGENT) == epoch)) break;
    __builtin_amdgcn_s_sleep(1);
  }
  asm volatile("" ::: "memory");
}

struct Pre { unsigned voffX, voffW, voffB, sX, ldsX, sW, ldsW, sB, ldsB; };
__device__ __forceinline__ Pre mk_pre(int lane, int wid, int bid) {
  Pre pre;
  const int wr = wid & 3, G = wid >> 2, idx = bid >> 3, c = ((bid & 7) >> 1) * 8 + (idx & 7), rch = (bid & 1) * 4 + (idx >> 3);
  const unsigned sw8 = ((lane & 7) ^ (((wr & 1) * 4 + (lane >> 4)) & 7)) * 16;
  pre.voffX = ((lane >> 5) * 16 + tj(lane >> 3)) * 2048 + sw8;
  pre.voffB = (lane >> 3) * 2048 + sw8;
  pre.voffW = (lane >> 4) * 7168 + ((((lane & 15) >> 1) ^ ((lane >> 4) | ((wr & 1) << 2))) * 32) + (lane & 1) * 16;
  pre.sX = (unsigned)(rch * 256 + (G * 2 + (wr >> 1)) * 64 + (wr & 1) * 32) * 2048u;
  pre.ldsX = L_X + (G * 16 + (wr >> 1) * 8 + (wr & 1)) * 1024;
  const int ii = (wr & 1) * 2 + (wr >> 1) * 4;
  pre.sW = (unsigned)(ii * 4) * 7168u + (unsigned)((224 * c) & ~63);
  pre.ldsW = L_W + ii * 1024;
  pre.sB = (unsigned)(c * 32 + (wr & 1) * 8) * 2048u;
  pre.ldsB = L_BS + wr * 1024;
  return pre;
}
template <int GRP>
__device__ __forceinline__ void body(const Args& A, int wid, const char* code, const unsigned code_len, const u64 t00) {
  asm volatile("" : "+s"(wid));
  ST_DECL STAMP();
  const int xcd = blockIdx.x & 7, idx = blockIdx.x >> 3, c = (xcd >> 1) * 8 + (idx & 7), rch = (xcd & 1) * 4 + (idx >> 3);
  const int xrow = (xcd & 1) * 1024 + (xcd >> 1) * 256 + idx * 8;
  const int kq = xcd >> 1, rc2 = (xcd & 1) * 4 + (idx >> 3), dt = idx & 7;
  const int tt = 2 * rc2 + GRP, tile = rc2 * 8 + dt;
  const unsigned flb = (unsigned)WS_FL + rch * 1024, flc = (unsigned)WS_FL + rc2 * 1024;
  const int wr = wid & 3, wm = wr >> 1, wn = wr & 1;
  int lane = fresh_lane();
  unsigned epoch;
  P2 q;
  u32x4 hq[14]; unsigned ho[3];
  const rsrc_t rws = mk_rsrc(A.ws, (unsigned)WS_END);
  {
  P1<GRP> w;
  constexpr int NW = P1<GRP>::NW, NS = P1<GRP>::NS;
  int r = lane & 15, g = lane >> 4, qq = (lane & 15) >> 2, pp = lane & 3;
  const Pre pre = mk_pre(lane, wid, blockIdx.x);
  w.rx = mk_rsrc(A.x, L_ * D_ * 2); w.rwg = mk_rsrc(A.wg, D_ * F_ * 2); w.rwu = mk_rsrc(A.wu, D_ * F_ * 2);
  w.rb = mk_rsrc((wr >> 1) ? A.bim : A.bre, P_ * D_ * 2);
  w.voffX = pre.voffX; w.voffB = pre.voffB; w.voffW = pre.voffW;
  w.sX = pre.sX; w.ldsX = pre.ldsX; w.sW = pre.sW; w.ldsW = pre.ldsW; w.sB = pre.sB; w.ldsB = pre.ldsB;
  const char* wbase = (const char*)((wr & 1) ? A.wu : A.wg) + (idx * 2 + (wr >> 1)) * 7168 + xcd * 896;
  const char* rbase = wr < 2 ? (const char*)A.x + (xrow + wr * 4) * 2048 : (const char*)(wr == 2 ? A.bre : A.bim) + (xcd * 128 + idx * 4) * 2048;
  if (GRP == 1) {
    w.template dmaW<0, 1>(0); w.template dmaW<1, 0>(1);
    const unsigned co = ((idx * 4 + wr) * 4 + (lane & 3)) * 128u;
    touch(lane < 16 ? wbase + (2 + (lane >> 3)) * 458752u + ((lane & 7) < 7 ? (lane & 7) : 0) * 128 : lane < 24 ? rbase + ((lane - 16) >> 1) * 2048 + (2 + (lane & 1)) * 128
                    : code + (co < code_len ? co : 0u));
  }
  {
#pragma unroll
    for (int ks = 0; ks < 2; ++ks) {
      const unsigned ch = ((g + 4 * ks) ^ ((r >> 1) & 7)) << 4;
      w.aX[ks] = mk_l(L_X + (wr * 64 + r) * 128 + ch);
      if (NS) w.aB[ks] = mk_l(L_BS + r * 128 + ch);
    }
    const unsigned swz = qq | ((g & 1) << 2);
#pragma unroll
    for (int n = 0; n < NW; ++n) {
      unsigned col;
      if (GRP == 0) col = (((2 + n) ^ swz) << 5) + pp * 8;
      else if (n < 2) col = (((((c & 1) ? 6 : 0) + n) ^ swz) << 5) + pp * 8;
      else col = (((((c & 1) ? 0 : 6) + (pp >> 1)) ^ swz) << 5) + (c & 1) * 16 + (pp & 1) * 8;
      w.aW[n] = mk_l(L_W + (8 * g + qq) * 256 + col);
    }
  }
  if constexpr (GRP == 0) {
    {
      const unsigned j = idx * 4 + wr, co = (j * 4 + (lane & 3)) * 128u;
      const int l = lane < 32 ? lane : 31;
      if (l < 7) { w.pfp = wbase + l * 128; w.pfs = 458752u; w.pfo = PFD; w.pfm = 15; }
      else if (l < 15) { w.pfp = rbase + ((l - 7) >> 1) * 2048 + ((l - 7) & 1) * 128; w.pfs = 256u; w.pfo = 2; w.pfm = 7; }
      else if (l < 23) { w.pfp = (const char*)A.wd + (kq * 896 + j) * 2048u + (xcd & 1) * 1024 + (l - 15) * 128; w.pfs = 262144u; w.pfo = -7; w.pfm = 6; }
      else if (l < 27) { w.pfp = (const char*)((j & 1) ? A.cim : A.cre) + ((xcd & 1) * 512 + (j >> 1)) * 2048u + kq * 512 + (l - 23) * 128; w.pfs = 131072u; w.pfo = 0; w.pfm = 7; }
      else { w.pfp = l < 31 ? code + (co < code_len ? co : 0u) : (const char*)((idx & 4) ? A.dsk : A.gw) + ((idx & 3) * 4 + wr) * 128; w.pfs = 0u; w.pfo = 0; w.pfm = 0; }
    }
    WAIT_V(0); BAR();
    w.template load<0, 0>([&](int i) { if (i < 4) w.template dmaX1<1>(1, i); else w.template dmaB1<1>(1, i - 4); });
    WAIT_L0(); BAR();
    STAMP(); TS2();
    w.template compute<-1, true>();
    w.template load<0, 1>([&](int i) { if (i < 4) w.template dmaW1<1, 1>(1, i); else if (i == 4) w.pull(0); }); WAIT_L0(); BAR();
    w.template compute<5>();
    w.template load<1, 0>([&](int i) { if (i < 4) w.template dmaX1<0>(2, i); else w.template dmaB1<0>(2, i - 4); }); WAIT_L0(); BAR(); TS2();
    p1_tile<1>(w, 1);
  } else {
    WAIT_V(16);
    const lptr rp = mk_l(L_TAB + wr * 2048 + (lane & 31) * 2);
    const float lr = bf2f(*(const LAS u16*)rp), li = bf2f(*(const LAS u16*)(rp + 64)), ld = bf2f(*(const LAS u16*)(rp + 128));
    epoch = __builtin_amdgcn_readfirstlane(*(const LAS unsigned*)mk_l(L_TAB + wr * 2048 + 192)) + 1u;
    const float dt_ = bf2f(pk_bf16(__expf(ld), 0.f) & 0xffffu);
    const float e = __expf(dt_ * lr), rev = __builtin_amdgcn_fractf(dt_ * li * 0.15915494309189535f);
    f32x2 lam = {e * __builtin_amdgcn_cosf(rev), -e * __builtin_amdgcn_sinf(rev)};
    PIN(lam);
    WAIT_V(9); BAR();
    {
      const float nr = lam.x - 1.f, rden = 1.f / (lr * lr + li * li);
      f32x2 l8 = cmul(lam, lam); l8 = cmul(l8, l8); l8 = cmul(l8, l8);
      const f32x2 l16 = cmul(l8, l8), l32 = cmul(l16, l16), l64 = cmul(l32, l32), l128 = cmul(l64, l64);
      f32x2 pw = (wr & 1) ? l64 : (f32x2){1.f, 0.f}; if (wr & 2) pw = cmul(pw, l128);
      const f32x2 q0 = (lane & 32) ? l32 : (f32x2){1.f, 0.f}, q1 = cmul(q0, l16), c0 = cmul(q0, pw), c1 = cmul(q1, pw);
      const lptr tp = mk_l(L_TAB + wr * 2048 + (lane >> 5) * 1024 + (lane & 31) * 16);
      *(LAS f32x4*)tp = (f32x4){q0.x, q0.y, c0.x, c0.y}; *(LAS f32x4*)(tp + 512) = (f32x4){q1.x, q1.y, c1.x, c1.y};
      const lptr pb = mk_l(L_PRM + (lane & 31) * 64);
      if (wr == 0) *(LAS f32x4*)pb = (f32x4){lam.x, lam.y, (nr * lr - lam.y * li) * rden, (-lam.y * lr - nr * li) * rden};
      else if (wr == 1) { *(LAS f32x4*)(pb + 16) = (f32x4){l16.x, l16.y, l32.x, l32.y}; *(LAS f32x4*)(pb + 32) = (f32x4){l8.x, l8.y, l64.x, l64.y}; *(LAS f32x2*)(pb + 48) = cmul(l128, l128); }
      else if (wr == 3) *(LAS unsigned*)mk_l(L_FLG + (lane & 7) * 4) = (lane & 7) == 3 ? epoch : 0u;
    }
    BAR();
    STAMP(); TS2();
    w.template load<0, 0>([&](int i) { if (i >= SO && i < SO + 4) w.template dmaX1<1>(1, i - SO); }); WAIT_V(9); WAIT_L0(); BAR();
    w.template compute<-1, true>();
    w.template load<0, 1>([&](int i) { if (i >= SO && i < SO + 4) w.template dmaW1<0, 0>(2, i - SO); }); WAIT_V(4);
    if (wr == 2) {
      __hip_atomic_store((GAS u64*)(A.ws + WS_AG) + (c * 8 + 7 - rch) * 64 + lane, (u64)(epoch - 1u) << 32, RLX_AGENT);
      if (lane < 6) __hip_atomic_store((GAS unsigned*)(A.ws + flb + lane * 128 + c * 4), epoch - 1u, RLX_AGENT);
      else if (lane >= 32 && (lane & 3)) __hip_atomic_store((GAS unsigned*)(A.ws + WS_XF + ((tile * 4 + (kq ^ (lane & 3))) * 8 + ((lane - 32) >> 2)) * XS) + (lane & 3), epoch - 1u, RLX_AGENT);
    }
    WAIT_L0(); BAR();
    w.template compute<-1>(); TS2();
    p1_tile<1>(w, 1);
  }
  for (int T = 2; T < 14; T += 2) {
    TS2(); p1_tile<0>(w, T); TS2();
    p1_tile<1>(w, T + 1);
  }
  if (GRP == 0) epoch = __builtin_amdgcn_readfirstlane(*(const LAS unsigned*)mk_l(L_FLG + 12));
  STAMP();
  lane = fresh_lane();
  q.rh = mk_rsrc(A.ws + WS_H, (unsigned)H_BYTES); q.rw = mk_rsrc(A.wd, F_ * D_ * 2); q.rc = mk_rsrc((kq >> 1) ? A.cim : A.cre, D_ * P_ * 2);
  q.voffW = (lane >> 4) * 2048 + ((((lane & 15) >> 1) ^ (((wr & 1) << 2) | (lane >> 4))) * 32) + (lane & 1) * 16;
  {
    const int i0 = (wr & 1) + (wr >> 1) * 8;
    q.kk = wr >> 1;
    q.sH = (unsigned)(2 * rc2 * NKF + 28 * kq) * 8192u + (wr & 1) * 4096u;
    q.sHs = (unsigned)(2 * rc2 * NKF + 112 + 16 * kq + (wr >> 1)) * 8192u + (wr & 1) * 4096u;
    q.sW = (unsigned)(28 * kq * 32 + (wr & 1) * 4) * 2048u + dt * 256u;
    q.sC = (unsigned)(dt * 128 + i0 * 8) * 2048u + (kq & 1) * 1024u;
#pragma unroll
    for (int j = 0; j < 4; ++j) q.pj[j] = (unsigned)(j ^ kq) << 10;
    q.rc = mk_rsrc(A.cre, D_ * P_ * 2); q.rc2_ = mk_rsrc(A.cim, D_ * P_ * 2);
    q.sHs = (unsigned)(2 * rc2 * NKF + 112 + 8 * kq + (wr >> 1)) * 8192u + (wr & 1) * 4096u;
    q.sC = (unsigned)(dt * 128 + i0 * 8) * 2048u + kq * 512u;
    q.ps = (unsigned)(kq >> 1) << 10;
    q.ldsA = 49152 + wr * 4096; q.ldsB = i0 * 1024;
  }
  u32x4 gx[4], gg[4];
  const int tg = rch * 256 + c * 8 + wr * 2 + (lane >> 5);
  if constexpr (GRP == 0) {
    w.template compute<7>();
    w.template load<0, 1>(); w.template dmaW<1, 1>(15);
#pragma unroll
    for (int i = 0; i < 4; ++i) gx[i] = *(const u32x4*)(A.x + tg * D_ + i * 256 + (lane & 31) * 8);
    WAIT_L0(); BAR();
    w.template compute<8>();
    w.template load<1, 0>(); q.template stageB<0>();
#pragma unroll
    for (int i = 0; i < 4; ++i) gg[i] = *(const u32x4*)(A.gw + i * 256 + (lane & 31) * 8);
    WAIT_L0(); BAR();
    w.template compute<12>();
    w.template load<1, 1>();
    {
      typedef __bf16 bf2_t __attribute__((ext_vector_type(2)));
      float s0 = 0.f, s1 = 0.f;
#pragma unroll
      for (int i = 0; i < 4; ++i)
#pragma unroll
        for (int j = 0; j < 4; j += 2) {
          const unsigned a0 = gx[i][j], b0 = gg[i][j], a1 = gx[i][j + 1], b1 = gg[i][j + 1];
          s0 = __builtin_amdgcn_fdot2_f32_bf16(__builtin_bit_cast(bf2_t, a0), __builtin_bit_cast(bf2_t, b0), s0, false);
          s1 = __builtin_amdgcn_fdot2_f32_bf16(__builtin_bit_cast(bf2_t, a1), __builtin_bit_cast(bf2_t, b1), s1, false);
        }
      float s = s0 + s1;
      s += dpp<0x128, 0xf>(0.f, s); s += dpp<0x124, 0xf>(0.f, s); s += dpp<0x122, 0xf>(0.f, s); s += dpp<0x121, 0xf>(0.f, s);
      float t0, t1; sw16(s, t0, t1); s = t0 + t1;
      if ((lane & 31) == 0) __hip_atomic_store((GAS unsigned*)(A.ws + WS_G) + tg, __float_as_uint(1.f / (1.f + __expf(-s))), RLX_AGENT);
    }
    WAIT_L0(); BAR();
    w.template compute<-2>();
    __builtin_amdgcn_s_setprio(3);
  } else {
    w.template load<0, 0>(); w.template dmaX<1>(15); WAIT_L0(); BAR();
    w.template compute<-1>();
    w.template load<0, 1>(); WAIT_V(0); WAIT_L0(); BAR();
    w.template compute<-1>();
  }
  lane = fresh_lane(); r = lane & 15; g = lane >> 4; qq = (lane & 15) >> 2; pp = lane & 3;
  const unsigned hb = (unsigned)WS_H + (unsigned)(2 * rch + (wr >> 1)) * NKF * 8192u + (wr & 1) * 4096u;
  const int kf0 = 7 * (c >> 1) + 3 * (c & 1);
  q.voffH = lane * 16;
#define FFN_H(n, m, h) pk_bf16(relu(w.ag[n][m][2 * (h)]) * w.au[n][m][2 * (h)], relu(w.ag[n][m][2 * (h) + 1]) * w.au[n][m][2 * (h) + 1])
#define P2_ADDR() do { q.voffC = (lane >> 3) * 2048 + (((lane & 7) ^ (((wr & 1) * 4 + (lane >> 4)) & 7)) * 16); PIN(q.voffC); \
    q.aH = mk_l(49152 + GRP * 49152 + wm * 4096 + lane * 16); \
    _Pragma("unroll") for (int n = 0; n < 4; ++n) q.aW[n] = mk_l((4 * g + qq) * 256 + (((wn * 4 + n) ^ (4 * (g & 1) + qq)) << 5) + pp * 8); } while (0)
#define PACK_S() do { _Pragma("unroll") for (int m = 0; m < 4; ++m) { hq[m] = (u32x4){FFN_H(0, m, 0), FFN_H(0, m, 1), FFN_H(1, m, 0), FFN_H(1, m, 1)}; PIN(hq[m]); } \
    _Pragma("unroll") for (int m = 0; m < 4; m += 2) {     \
      const auto s0 = __builtin_amdgcn_permlane32_swap(FFN_H(2, m, 0), FFN_H(2, m + 1, 0), false, false); \
      const auto s1 = __builtin_amdgcn_permlane32_swap(FFN_H(2, m, 1), FFN_H(2, m + 1, 1), false, false); \
      hq[4 + m / 2] = (u32x4){s0[0], s1[0], s0[1], s1[1]}; PIN(hq[4 + m / 2]); } \
    ho[0] = lane * 16 + hb + (kf0 + ((c & 1) ? 3 : 0)) * 8192u; \
    ho[1] = (lane & 31) * 16 + (lane >> 5) * 1024 + (c & 1) * 512 + hb + (kf0 + ((c & 1) ? 0 : 3)) * 8192u; \
    ho[2] = (lane ^ ((lane >> 5) << 2)) * 16 + hb + (112 + c) * 8192u; } while (0)
  if constexpr (GRP == 0) {
    MFMA_DRAIN();
    STAMP();
#pragma unroll
    for (int f = 0; f < 2; ++f)
#pragma unroll
      for (int m = 0; m < 4; ++m) {
        __builtin_amdgcn_raw_buffer_store_b128((u32x4){FFN_H(2 * f, m, 0), FFN_H(2 * f, m, 1), FFN_H(2 * f + 1, m, 0), FFN_H(2 * f + 1, m, 1)}, rws,
                                               lane * 16 + m * 1024 + hb + (kf0 + 1 + f) * 8192u, 0, 16);
        SCHED();
      }
    STAMP();
    WAIT_V(0); arrive(A.ws, lane, flb + (c >> 3) * 128 + c * 4, 0, epoch);
    __builtin_amdgcn_s_setprio(0);
    STAMP();
    P2_ADDR();
    if (wr == 0) { wait_flags(A.ws, flc + kq * 160 + (lane & 7) * 4, epoch); lds_post(3); }
    lds_wait(3, 5u);
    STAMP();
    q.template stageA<0, 0>(); q.template stageA<0, 1>(); q.template stageA<1, 0>();
    STAMP();
  } else {
    bf16x8 xb[4], bs1[4], wg1[3], wu1[3];
#pragma unroll
    for (int m = 0; m < 4; ++m) { w.xa[m] = lds_b128(w.aX[0] + 32768 + m * 2048); xb[m] = lds_b128(w.aX[1] + 32768 + m * 2048); }
#pragma unroll
    for (int i = 0; i < 4; ++i) { w.bsf[i] = lds_b128(w.aB[0] + 8192 + i * 2048); bs1[i] = lds_b128(w.aB[1] + 8192 + i * 2048); }
    w.wgf[0] = lds_tr2(w.aW[0] + 32768, w.aW[0] + 32768 + 1024); w.wuf[0] = lds_tr2(w.aW[0] + 49152, w.aW[0] + 49152 + 1024);
    q.template stageB<1>(); WAIT_L0(); BAR();
    __builtin_amdgcn_s_setprio(1);
#pragma unroll
    for (int i = 0; i < 4; ++i)
#pragma unroll
      for (int m = 0; m < 4; ++m) MFMA(w.xa[m], w.bsf[i], w.as[i][m]);
#pragma unroll
    for (int i = 0; i < 4; ++i)
#pragma unroll
      for (int m = 0; m < 4; ++m) MFMA(xb[m], bs1[i], w.as[i][m]);
#pragma unroll
    for (int m = 0; m < 4; ++m) MFMA(w.wgf[0], w.xa[m], w.ag[0][m]);
#pragma unroll
    for (int m = 0; m < 4; ++m) MFMA(w.wuf[0], w.xa[m], w.au[0][m]);
    __builtin_amdgcn_s_setprio(0);
    BAR();
    STAMP();
    f32x4 pl[2], pm[2];
    f32x2 l8[2], cin[2], ea[2], agg[2];
#pragma unroll
    for (int pf = 0; pf < 2; ++pf) {
      const lptr pb = mk_l(L_PRM + (pf * 16 + r) * 64);
      pl[pf] = *(const LAS f32x4*)pb; pm[pf] = *(const LAS f32x4*)(pb + 16); l8[pf] = *(const LAS f32x2*)(pb + 32);
    }
#pragma unroll
    for (int n = 1; n < 3; ++n) { w.wgf[n] = lds_tr2(w.aW[n] + 32768, w.aW[n] + 32768 + 1024); w.wuf[n] = lds_tr2(w.aW[n] + 49152, w.aW[n] + 49152 + 1024); }
    SCHED();
#define PAIR(v, k) ((k) & 1 ? (f32x2){(v)[(k) >> 1][2], (v)[(k) >> 1][3]} : (f32x2){(v)[(k) >> 1][0], (v)[(k) >> 1][1]})
#define CSTEP(P, Q, pf, k) do { const f32x2 lx_ = {pl[pf][0], pl[pf][0]}, ly_ = {pl[pf][1], pl[pf][1]}; \
      const f32x2 a_ = __builtin_elementwise_fma(-ly_, Q, PAIR(w.as[2 * (pf)], k)), b_ = __builtin_elementwise_fma(ly_, P, PAIR(w.as[2 * (pf) + 1], k)); \
      P = __builtin_elementwise_fma(lx_, P, a_); Q = __builtin_elementwise_fma(lx_, Q, b_); } while (0)
    {
#pragma unroll
      for (int pf = 0; pf < 2; ++pf)
#pragma unroll
        for (int m = 0; m < 4; ++m) {
          const f32x4 re = w.as[2 * pf][m], im = w.as[2 * pf + 1][m];
          w.as[2 * pf][m] = re * pl[pf][2] - im * pl[pf][3]; w.as[2 * pf + 1][m] = -(re * pl[pf][3]) - im * pl[pf][2];
          PIN(w.as[2 * pf][m]); PIN(w.as[2 * pf + 1][m]);
        }
      f32x2 P[2], Q[2];
#pragma unroll
      for (int k = 0; k < 8; ++k)
#pragma unroll
        for (int pf = 0; pf < 2; ++pf) {
          if (k == 0) { P[pf] = PAIR(w.as[2 * pf], 0); Q[pf] = PAIR(w.as[2 * pf + 1], 0); } else CSTEP(P[pf], Q[pf], pf, k);
        }
#pragma unroll
      for (int pf = 0; pf < 2; ++pf) {
        ea[pf] = (f32x2){P[pf].x, Q[pf].x};
        const f32x2 e = cfma(l8[pf], ea[pf], (f32x2){P[pf].y, Q[pf].y});
        const f32x2 l16 = {pm[pf][0], pm[pf][1]}, l32 = {pm[pf][2], pm[pf][3]};
        float a0, a1, b0, b1; sw16(e.x, a0, a1); sw16(e.y, b0, b1);
        const f32x2 r0 = {a0, b0};
        const f32x2 xx = cfma(l16, r0, (f32x2){a1, b1});
        float c0, c1, d0, d1; sw32(xx.x, c0, c1); sw32(xx.y, d0, d1);
        const f32x2 s0 = {c0, d0};
        agg[pf] = cfma(l32, s0, (f32x2){c1, d1});
        const f32x2 c3 = cfma(l16, s0, r0);
        cin[pf] = g == 0 ? (f32x2){0.f, 0.f} : g == 1 ? r0 : g == 2 ? s0 : c3;
        PIN(ea[pf]);
      }
    }
    f32x2 aggq = (g & 1) ? agg[1] : agg[0]; PIN(aggq); PIN(cin[0]); PIN(cin[1]);
    if (wr < 3) {
      if (g < 2) *(LAS f32x2*)mk_l(L_WA + ((g * 16 + r) * 4 + wr) * 8) = aggq;
      WAIT_L0();
      *(volatile LAS unsigned*)mk_l(L_FLG + wr * 4) = 1u;
    }
    SCHED();
#pragma unroll
    for (int n = 0; n < 3; ++n) { wg1[n] = lds_tr2(w.aW[n] + 40960, w.aW[n] + 40960 + 1024); wu1[n] = lds_tr2(w.aW[n] + 57344, w.aW[n] + 57344 + 1024); }
    if (wr == 3) {
      const lptr fp = mk_l(L_FLG);
_Pragma("nounroll")
      for (unsigned spins = 0; spins < SPIN_CAP; ++spins) {
        const u32x4 f = *(volatile LAS u32x4*)fp;
        if (__builtin_amdgcn_readfirstlane(f[0] & f[1] & f[2])) break;
      }
      asm volatile("" ::: "memory");
      const lptr ap = mk_l(L_WA + ((g & 1) * 16 + r) * 32);
      const f32x4 a01 = *(const LAS f32x4*)ap; const f32x2 a2 = *(const LAS f32x2*)(ap + 16);
      const f32x2 lw = *(const LAS f32x2*)mk_l(L_PRM + ((g & 1) * 16 + r) * 64 + 40);
      const f32x2 t = cfma(lw, cfma(lw, cfma(lw, (f32x2){a01[0], a01[1]}, (f32x2){a01[2], a01[3]}), a2), aggq);
      if (rch < 7) __hip_atomic_store((GAS u64*)(A.ws + WS_AG) + (c * 8 + 7 - rch) * 64 + lane, ((u64)epoch << 32) | __float_as_uint((g & 2) ? t.y : t.x), RLX_AGENT);
    }
    WAIT_L0(); lds_post(3); SCHED();
    STAMP();
    __builtin_amdgcn_s_setprio(1);
#pragma unroll
    for (int n = 1; n < 3; ++n) {
#pragma unroll
      for (int m = 0; m < 4; ++m) MFMA(w.wgf[n], w.xa[m], w.ag[n][m]);
#pragma unroll
      for (int m = 0; m < 4; ++m) MFMA(w.wuf[n], w.xa[m], w.au[n][m]);
    }
#pragma unroll
    for (int n = 0; n < 3; ++n) {
#pragma unroll
      for (int m = 0; m < 4; ++m) MFMA(wg1[n], xb[m], w.ag[n][m]);
#pragma unroll
      for (int m = 0; m < 4; ++m) MFMA(wu1[n], xb[m], w.au[n][m]);
    }
    __builtin_amdgcn_s_setprio(2);
    SCHED();
    lane = fresh_lane(); r = lane & 15; g = lane >> 4; qq = (lane & 15) >> 2; pp = lane & 3;
    q.voffH = lane * 16;
    const rsrc_t rag = mk_rsrc(A.ws + WS_AG + c * 4096, 4096u);
    const unsigned ago = lane * 8 + (8 - rch) * 512;
    u32x2 gv_[7];
#pragma unroll
    for (int cc = 0; cc < 7; ++cc) gv_[cc] = __builtin_amdgcn_raw_buffer_load_b64(rag, ago + cc * 512, 0, 16);
    {
      const lptr fp = mk_l(L_FLG);
_Pragma("nounroll")
      for (unsigned spins = 0; spins < SPIN_CAP; ++spins) {
        const u32x4 f = *(volatile LAS u32x4*)fp;
        if (__builtin_amdgcn_readfirstlane(f[0] & f[1] & f[2])) break;
      }
      asm volatile("" ::: "memory");
    }
    STAMP();
    f32x4 p2[2], tb4[2], a01[2]; f32x2 a2[2];
    const f32x2 l256q = *(const LAS f32x2*)mk_l(L_PRM + ((g & 1) * 16 + r) * 64 + 48);
    {
      const lptr pb = mk_l(L_PRM + r * 64), ap = mk_l(L_WA + r * 32), tp = mk_l(L_TAB + wr * 2048 + g * 512 + r * 16);
#pragma unroll
      for (int pf = 0; pf < 2; ++pf) {
        pl[pf] = *(const LAS f32x4*)(pb + pf * 1024); p2[pf] = *(const LAS f32x4*)(pb + pf * 1024 + 32); tb4[pf] = *(const LAS f32x4*)(tp + pf * 256);
        a01[pf] = *(const LAS f32x4*)(ap + pf * 512); a2[pf] = *(const LAS f32x2*)(ap + pf * 512 + 16);
      }
    }
    SCHED();
    PACK_S();
    P2_ADDR();
    PIN(ho[0]); PIN(ho[1]); PIN(ho[2]);
    SCHED();
    f32x2 car[2], cai[2], cmr[2], cmi[2];
#pragma unroll
    for (int pf = 0; pf < 2; ++pf) {
      const f32x2 l8_ = {p2[pf][0], p2[pf][1]}, l64 = {p2[pf][2], p2[pf][3]};
      f32x2 sw = wr > 0 ? (f32x2){a01[pf][0], a01[pf][1]} : (f32x2){0.f, 0.f};
      if (wr > 1) sw = cfma(l64, sw, (f32x2){a01[pf][2], a01[pf][3]});
      if (wr > 2) sw = cfma(l64, sw, a2[pf]);
      const f32x2 ca = cfma((f32x2){tb4[pf][0], tb4[pf][1]}, sw, cin[pf]), cb_ = cfma(l8_, ca, ea[pf]), cm = {tb4[pf][2], tb4[pf][3]}, cn = cmul(l8_, cm);
      car[pf] = (f32x2){ca.x, cb_.x}; cai[pf] = (f32x2){ca.y, cb_.y}; cmr[pf] = (f32x2){cm.x, cn.x}; cmi[pf] = (f32x2){cm.y, cn.y};
      PIN(car[pf]); PIN(cai[pf]); PIN(cmr[pf]); PIN(cmi[pf]);
    }
    STAMP();
_Pragma("nounroll")
    for (unsigned spins = 0; spins < SPIN_CAP; ++spins) {
      __builtin_amdgcn_s_waitcnt(0x0F70);
      unsigned bad = 0;
#pragma unroll
      for (int cc = 0; cc < 7; ++cc) bad |= gv_[cc][1] ^ (cc < rch ? epoch : 0u);
      if (__all(bad == 0u)) break;
      __builtin_amdgcn_s_sleep(1);
      asm volatile("" ::: "memory");
#pragma unroll
      for (int cc = 0; cc < 7; ++cc) gv_[cc] = __builtin_amdgcn_raw_buffer_load_b64(rag, ago + cc * 512, 0, 16);
    }
    __builtin_amdgcn_s_waitcnt(0x0F70);
    STAMP();
    f32x2 cb;
    {
      float are[7], aim[7];
#pragma unroll
      for (int cc = 0; cc < 7; ++cc) { const unsigned v = gv_[cc][0]; sw32(__uint_as_float(v), are[cc], aim[cc]); }
      cb = (f32x2){are[6], aim[6]};
#pragma unroll
      for (int cc = 5; cc >= 0; --cc) cb = cfma(l256q, cb, (f32x2){are[cc], aim[cc]});
    }
    unsigned hr[2][8], hi[2][8];
    {
      float cx[2], cy[2]; sw16(cb.x, cx[0], cx[1]); sw16(cb.y, cy[0], cy[1]);
      f32x2 P[2], Q[2];
#pragma unroll
      for (int pf = 0; pf < 2; ++pf) { P[pf] = car[pf] + cmr[pf] * cx[pf] - cmi[pf] * cy[pf]; Q[pf] = cai[pf] + cmr[pf] * cy[pf] + cmi[pf] * cx[pf]; }
#pragma unroll
      for (int k = 0; k < 8; ++k)
#pragma unroll
        for (int pf = 0; pf < 2; ++pf) { CSTEP(P[pf], Q[pf], pf, k); hr[pf][k] = pk_bf16(P[pf].x, P[pf].y); hi[pf][k] = pk_bf16(Q[pf].x, Q[pf].y); }
    }
#pragma unroll
    for (int i = 0; i < 8; ++i) {
      const unsigned* h = (i >> 2) ? hi[(i >> 1) & 1] : hr[(i >> 1) & 1];
      hq[6 + i] = (u32x4){h[(i & 1) * 4], h[(i & 1) * 4 + 1], h[(i & 1) * 4 + 2], h[(i & 1) * 4 + 3]}; PIN(hq[6 + i]);
    }
    STAMP(); TS2();
    WAIT_V(0); lds_wait(1);
    STAMP();
    q.template stageA<1, 1>(); q.template stageA<2, 0>();
  }
  }
  if (GRP == 0) { STAMP(); TS2(); }
  constexpr int G = GRP;
  int r, g;
  unsigned fl = 0u;
#define FLAG_LOAD(off) asm volatile("global_load_dword %0, %1, %2 sc1" : "+v"(fl) : "v"(off), "s"(A.ws) : "memory")
#define FLAG_CHECK(off) do { PIN(fl); if (!__all(fl == epoch)) wait_flags(A.ws, off, epoch); } while (0)
#define HQ_ST(i) __builtin_amdgcn_raw_buffer_store_b128(hq[i], rws, (i) < 4 ? ho[0] + (i) * 1024 : (i) < 6 ? ho[1] + ((i) - 4) * 2048 : ho[2] + (((i) - 6) >> 2) * 262144 + (((i) - 6) & 3) * 1024, 0, 16)
#define XTRA(st) ((st) == 13 ? 4 : 0)
  float gv[4];
  u32x2 xv[4], dv; float gr[4];
  const int t0 = tt * 128 + wm * 64 + kq * 2, dc0 = dt * 128 + wn * 64;
  auto side = [&](auto ST_) {
    constexpr int ST = decltype(ST_)::value;
    if constexpr (ST == 13) {
      const int l = fresh_lane(), g_ = l >> 4, q_ = (l & 15) >> 2, p_ = l & 3;
#pragma unroll
      for (int e = 0; e < 2; ++e) {
        const unsigned b = 49152 + G * 49152 + wm * 4096 + (g_ >> 1) * 2048 + p_ * 256 + 8 * (g_ & 1) * 16 + q_ * 16 + (e ^ (kq & 1)) * 8;
        q.aS[e] = mk_l(b + ((p_ >> 1) << 6)); q.aS2[e] = mk_l(b + (((p_ >> 1) ^ 1) << 6));
        q.aC[e] = mk_l((wn * 64 + (l & 15)) * 128 + (((g_ + 4 * e) ^ (((l & 15) >> 1) & 7)) << 4));
      }
    }
    if constexpr (ST == 13) {
      const int l = fresh_lane();
      const unsigned go = (unsigned)WS_G + (tt * 128 + wm * 64 + ((l & 15) >> 2) * 16 + tj(l)) * 4;
#pragma unroll
      for (int j = 0; j < 4; ++j) asm volatile("global_load_dword %0, %1, %2 sc1" : "=v"(gv[j]) : "v"(go + (q.pj[j] >> 7)), "s"(A.ws) : "memory");
    }
    if constexpr (ST >= 16 && ST < 20) {
      constexpr int m = ST - 16;
      PIN(gv[m]);
#pragma unroll
      for (int n = 0; n < 4; ++n) { q.af[m][n] *= 1.f - gv[m]; PIN(q.af[m][n]); }
    }
    if constexpr (ST == NSTEP - 1) {
      const int l = fresh_lane();
      dv = *(const u32x2*)(A.dsk + dc0 + (l & 15) * 4);
#pragma unroll
      for (int i = 0; i < 4; ++i) {
        xv[i] = *(const u32x2*)(A.x + (t0 + i * 16 + tj(l >> 4)) * D_ + dc0 + (l & 15) * 4);
        gr[i] = __int_as_float(__builtin_amdgcn_ds_bpermute((4 * i + (l >> 4)) * 4, __float_as_int(gv[0])));
      }
#pragma unroll
      for (int j = 0; j < 4; ++j) { gv[j] *= 2.f; PIN(gv[j]); }
    }
  };
  __builtin_amdgcn_s_setprio(0);
  if constexpr (G == 0) {
    const unsigned f2 = flc + 512 + (8 * kq + (lane & 7)) * 4, f3 = flc + 640 + (lane & 31) * 4;
    WAIT_V(8); lds_post(1); lds_wait(1); STAMP();
    q.template load<0, 0>(); WAIT_V(10); WAIT_L0(); lds_post(2); SCHED();
    static_for<0, NSTEP - 1>([&](auto ST_) {
      constexpr int ST = decltype(ST_)::value;
      constexpr int prev = (ST + 2 < NSTEP ? 6 : 0) + (ST == 4 || ST == 10 ? 1 : 0) + XTRA(ST);
      q.template compute<ST, prev>();
      q.template load<ST + 1, 0>();
      if constexpr (ST + 1 == 4) FLAG_LOAD(f2);
      if constexpr (ST + 1 == 5) { WAIT_V(6); FLAG_CHECK(f2); }
      if constexpr (ST + 1 == 10) FLAG_LOAD(f3);
      if constexpr (ST + 1 == 11) { WAIT_V(6); FLAG_CHECK(f3); }
      if constexpr (ST + 1 == 20) { if (blockIdx.x == 255 && wr == 0 && lane == 0) __hip_atomic_store((GAS unsigned*)(A.ws + WS_EP), epoch, RLX_AGENT); }
      side(std::integral_constant<int, ST + 1>{});
      WAIT_L0(); BAR(); TS2();
      if constexpr (ST == 1 || ST == 13) STAMP();
    });
  } else {
    HQ_ST(0); HQ_ST(1); HQ_ST(2);
    lds_wait(2);
    static_for<0, NSTEP>([&](auto ST_) {
      constexpr int ST = decltype(ST_)::value;
      constexpr int nst = ST == 0 ? 3 : ST <= 4 ? 2 : 0;
      if constexpr (ST == 2 || ST == 6) {
        if (wr == 0 && lane == 0) __hip_atomic_store((GAS unsigned*)(A.ws + flb + (ST == 2 ? 512 : 640) + c * 4), epoch, RLX_AGENT);
      }
      q.template load<ST, 1>();
      if constexpr (ST == 0) { HQ_ST(3); HQ_ST(4); HQ_ST(5); }
      if constexpr (ST >= 1 && ST <= 4) { HQ_ST(4 + 2 * ST); HQ_ST(5 + 2 * ST); }
      side(ST_);
      WAIT_L0(); BAR();
      if constexpr (ST + 1 < NSTEP) {
        q.template compute<ST, (ST + 2 < NSTEP ? 2 : 0) + (ST + 3 < NSTEP ? 4 : 0) + nst + XTRA(ST)>(); TS2();
        if constexpr (ST == 1 || ST == 13) STAMP();
      }
    });
  }
  lane = fresh_lane(); r = lane & 15; g = lane >> 4;
  f32x4 o[4];
  {
    unsigned xo[3], pk[4];
#pragma unroll
    for (int j = 1; j < 4; ++j) xo[j - 1] = (unsigned)WS_XB + ((tile * 4 + (kq ^ j)) * 3 + (j - 1)) * 16384u + wid * 2048 + lane * 16;
    __builtin_amdgcn_s_setprio(1);
#pragma unroll
    for (int k = 0; k < 19; ++k) {
      if (k < 16) { const int m = ((k >> 2) + 1) & 3, n = k & 3; MFMA(q.bb[0][n], q.ha[0][m], q.as[m][n]); MFMA(q.bb[1][n], q.ha[1][m], q.as[m][n]); }
      if (k == 3 || k >= 16) asm volatile("s_nop 3");
      if (G == 0 && k == 14) RBAR();
      SCHED();
      if (k >= 3) {
        const int m = (((k - 3) >> 2) + 1) & 3, n = (k - 3) & 3;
        f32x4 v;
#pragma unroll
        for (int j = 0; j < 4; ++j) v[j] = __builtin_fmaf(q.as[m][n][j], gv[m], q.af[m][n][j]);
        if (m == 0) o[n] = v;
        else {
          pk[(n & 1) * 2] = pk_bf16(v[0], v[1]); pk[(n & 1) * 2 + 1] = pk_bf16(v[2], v[3]);
          if (n & 1) __builtin_amdgcn_raw_buffer_store_b128((u32x4){pk[0], pk[1], pk[2], pk[3]}, rws, xo[m - 1] + (n >> 1) * 1024, 0, 16);
        }
      }
      SCHED();
    }
    __builtin_amdgcn_s_setprio(0);
  }
  STAMP();
  WAIT_V(0);
  if (lane >= 1 && lane < 4) __hip_atomic_store((GAS unsigned*)(A.ws + WS_XF + ((tile * 4 + (kq ^ lane)) * 8 + wid) * XS) + lane, epoch, RLX_AGENT);
  STAMP();
  f32x4 sk[4];
  {
    const f32x4 df = {bf2f(dv[0] & 0xffffu), bfhi(dv[0]), bf2f(dv[1] & 0xffffu), bfhi(dv[1])};
#pragma unroll
    for (int i = 0; i < 4; ++i) { sk[i] = (f32x4){bf2f(xv[i][0] & 0xffffu), bfhi(xv[i][0]), bf2f(xv[i][1] & 0xffffu), bfhi(xv[i][1])} * df * gr[i]; PIN(sk[i]); }
  }
  const lptr tw = mk_l(49152 + G * 49152 + wr * 4096 + r * 256), tr_ = mk_l(49152 + G * 49152 + wr * 4096 + g * 256);
  wait_flags(A.ws, (unsigned)WS_XF + ((tile * 4 + kq) * 8 + wid) * XS + (1 + (lane & 3) % 3) * 4, epoch);
  STAMP();
  {
    u32x4 pv[6];
    const unsigned xi = (unsigned)WS_XB + (tile * 4 + kq) * 3 * 16384u + wid * 2048 + lane * 16;
#pragma unroll
    for (int i = 0; i < 6; ++i) pv[i] = __builtin_amdgcn_raw_buffer_load_b128(rws, xi + (i >> 1) * 16384 + (i & 1) * 1024, 0, 16);
#pragma unroll
    for (int i = 0; i < 6; ++i)
#pragma unroll
      for (int h = 0; h < 2; ++h) {
        const unsigned a0 = pv[i][2 * h], a1 = pv[i][2 * h + 1];
        o[(i & 1) * 2 + h] += (f32x4){bf2f(a0 & 0xffffu), bfhi(a0), bf2f(a1 & 0xffffu), bfhi(a1)};
      }
  }
#pragma unroll
  for (int n = 0; n < 4; ++n) *(LAS f32x4*)(tw + (((n * 4 + g) ^ (r & 7)) << 4)) = o[n];
  WAIT_L0();
#pragma unroll
  for (int i = 0; i < 4; ++i) {
    const f32x4 v = *(const LAS f32x4*)(tr_ + i * 1024 + ((r ^ ((4 * i + g) & 7)) << 4));
    __builtin_amdgcn_raw_buffer_store_b128(__builtin_bit_cast(u32x4, v + sk[i]), mk_rsrc(A.out, L_ * D_ * 4), ((t0 + i * 16 + tj(g)) * D_ + dc0 + r * 4) * 4, 0, 17);
  }
  STAMP();
  ST_DUMP(A.ws, wid);
  if (GRP == 0) asm volatile(".Lgm_end0:"); else asm volatile(".Lgm_end1:");
  __builtin_amdgcn_endpgm();
  __builtin_unreachable();
}

__global__ __launch_bounds__(512, 2) void fused_kernel(Args A) {
  const u64 t00 = 0;
  const int wid = __builtin_amdgcn_readfirstlane(threadIdx.x >> 6);
  {
    const Pre pre = mk_pre(threadIdx.x & 63, wid, blockIdx.x);
    const int G = wid >> 2;
    if (G) {
      const int l = threadIdx.x & 63;
      const char* p = (l < 16 ? (const char*)A.lre : l < 32 ? (const char*)A.lim : (const char*)A.ldt) + (pre.sB >> 16) * 64 + (l & 15) * 4;
      if (l >= 48) p = A.ws + WS_EP;
      asm volatile("s_mov_b32 m0, %0\n\ts_nop 0\n\tglobal_load_lds_dword %1, off sc1" :: "s"(L_TAB + (wid & 3) * 2048), "v"(p) : "memory");
    }
    const rsrc_t rx = mk_rsrc(A.x, L_ * D_ * 2);
#pragma unroll
    for (int s = 0; s < 4; ++s) dma16(pre.ldsX + s * 2048, pre.voffX, rx, pre.sX + s * 4096);
    dma16(pre.ldsW + G * 1024, pre.voffW, mk_rsrc(A.wg, D_ * F_ * 2), pre.sW + G * 28672u);
    dma16(pre.ldsW + 16384 + G * 1024, pre.voffW, mk_rsrc(A.wu, D_ * F_ * 2), pre.sW + G * 28672u);
    dma16(pre.ldsB + G * 4096, pre.voffB, mk_rsrc((wid & 2) ? A.bim : A.bre, P_ * D_ * 2), pre.sB + G * 32768);
  }
  const char* code; unsigned code_len;
  asm volatile("s_getpc_b64 %0\n.Lgm_pc:\n\ts_mov_b32 %1, .Lgm_end0-.Lgm_pc\n\ts_max_u32 %1, %1, .Lgm_end1-.Lgm_pc" : "=s"(code), "=s"(code_len) :: "scc");
  if (wid < 4) body<0>(A, wid, code, code_len, t00); else body<1>(A, wid, code, code_len, t00);
}

extern "C" void kernel_launch(void* const* d_in, const int* in_sizes, int n_in, void* d_out, int out_size, void* d_ws, size_t ws_size,
                              hipStream_t stream) {
  Args a; a.x = (const u16*)d_in[0]; a.lre = (const u16*)d_in[1]; a.lim = (const u16*)d_in[2]; a.bre = (const u16*)d_in[3];
  a.bim = (const u16*)d_in[4]; a.cre = (const u16*)d_in[5]; a.cim = (const u16*)d_in[6]; a.ldt = (const u16*)d_in[7]; a.dsk = (const u16*)d_in[8];
  a.wg = (const u16*)d_in[10]; a.wu = (const u16*)d_in[11]; a.wd = (const u16*)d_in[12]; a.gw = (const u16*)d_in[13];
  a.ws = (char*)d_ws; a.out = (float*)d_out;
  (void)hipFuncSetAttribute((const void*)fused_kernel, hipFuncAttributeMaxDynamicSharedMemorySize, LDS_BYTES);
  fused_kernel<<<256, 512, LDS_BYTES, stream>>>(a);
}
```

```cpp
#include <hip/hip_runtime.h>
#include <hip/hip_bf16.h>
#include <type_traits>

typedef unsigned short u16;
typedef short bf16x8 __attribute__((ext_vector_type(8)));
typedef short s16x4 __attribute__((ext_vector_type(4)));
typedef float f32x4 __attribute__((ext_vector_type(4)));
typedef float f32x2 __attribute__((ext_vector_type(2)));
typedef unsigned u32x4 __attribute__((ext_vector_type(4)));
typedef unsigned u32x2 __attribute__((ext_vector_type(2)));
typedef unsigned long long u64;
#define LAS __attribute__((address_space(3)))
#define GAS __attribute__((address_space(1)))
#define RLX_AGENT __ATOMIC_RELAXED, __HIP_MEMORY_SCOPE_AGENT
typedef __amdgpu_buffer_rsrc_t rsrc_t;

constexpr int XS = 16;
constexpr int SO = 1;
constexpr int PFD = 4;
constexpr int L_ = 2048, D_ = 1024, P_ = 1024, F_ = 3584;
constexpr int NKF = 176;
constexpr size_t WS_H = 0;
constexpr size_t H_BYTES = (size_t)16 * NKF * 8 * 1024;
constexpr size_t WS_XB = WS_H + H_BYTES;
constexpr size_t XB_BYTES = (size_t)64 * 4 * 3 * 16384;
constexpr size_t WS_G = WS_XB + XB_BYTES;
constexpr size_t WS_AG = WS_G + 8192;
constexpr size_t WS_EP = WS_AG + (size_t)32 * 8 * 64 * 8;
constexpr size_t WS_FL = WS_EP + 512;
constexpr size_t WS_XF = WS_FL + 8192;
constexpr size_t WS_END = WS_XF + 256 * 8 * XS;
constexpr unsigned SPIN_CAP = 1u << 20;

__device__ __forceinline__ float bf2f(unsigned b) { return __uint_as_float(b << 16); }
__device__ __forceinline__ float bfhi(unsigned b) { return __uint_as_float(b & 0xffff0000u); }
__device__ __forceinline__ unsigned pk_bf16(float lo, float hi) {
  typedef __bf16 bf2_t __attribute__((ext_vector_type(2)));
  return __builtin_bit_cast(unsigned, __builtin_convertvector((f32x2){lo, hi}, bf2_t)); }
__device__ __forceinline__ float relu(float v) { const int b = __float_as_int(v); return __int_as_float(b > 0 ? b : 0); }
template <int CTRL, int BANKS> __device__ __forceinline__ float dpp(float old, float src) {
  return __int_as_float(__builtin_amdgcn_update_dpp(__float_as_int(old), __float_as_int(src), CTRL, 0xf, BANKS, false)); }
__device__ __forceinline__ rsrc_t mk_rsrc(const void* p, unsigned bytes) {
  return __builtin_amdgcn_make_buffer_rsrc((void*)p, (short)0, (int)bytes, 0x00020000); }
__device__ __forceinline__ void dma16(unsigned ldsaddr, unsigned voff, rsrc_t srd, unsigned soff) {
  asm volatile("s_mov_b32 m0, %0\n\ts_nop 0\n\tbuffer_load_dwordx4 %1, %2, %3 offen lds"
               :: "s"(ldsaddr), "v"(voff), "s"(srd), "s"(soff) : "memory"); }
__device__ __forceinline__ void dma16_dev(unsigned ldsaddr, unsigned voff, rsrc_t srd, unsigned soff) {
  asm volatile("s_mov_b32 m0, %0\n\ts_nop 0\n\tbuffer_load_dwordx4 %1, %2, %3 offen sc1 lds"
               :: "s"(ldsaddr), "v"(voff), "s"(srd), "s"(soff) : "memory"); }
#define WAIT_V(n) asm volatile("s_waitcnt vmcnt(" #n ")" ::: "memory")
#define WAIT_VN(n) asm volatile("s_waitcnt vmcnt(%0)" :: "i"(n) : "memory")
#define WAIT_L0() do { __builtin_amdgcn_s_waitcnt(0xC07F); asm volatile("" ::: "memory"); } while (0)
#define SCHED() __builtin_amdgcn_sched_barrier(0)
#define RBAR() do { __builtin_amdgcn_s_barrier(); asm volatile("" ::: "memory"); } while (0)
#define BAR() do { SCHED(); RBAR(); SCHED(); } while (0)
__device__ __forceinline__ int fresh_lane() { int l; asm volatile("v_mbcnt_lo_u32_b32 %0, -1, 0\n\tv_mbcnt_hi_u32_b32 %0, -1, %0" : "=v"(l)); return l; }
typedef LAS char* lptr;
__device__ __forceinline__ lptr mk_l(unsigned a) { lptr p = (lptr)(size_t)a; asm volatile("" : "+v"(p)); return p; }
__device__ __forceinline__ bf16x8 lds_b128(lptr a) { return *(const LAS bf16x8*)a; }
__device__ __forceinline__ bf16x8 lds_tr2(lptr a0, lptr a1) {
  s16x4 lo = __builtin_amdgcn_ds_read_tr16_b64_v4i16((LAS s16x4*)a0);
  s16x4 hi = __builtin_amdgcn_ds_read_tr16_b64_v4i16((LAS s16x4*)a1);
  return __builtin_shufflevector(lo, hi, 0, 1, 2, 3, 4, 5, 6, 7); }
#define MFMA(a, b, c) asm volatile("v_mfma_f32_16x16x32_bf16 %0, %1, %2, %0" : "+v"(c) : "v"(a), "v"(b))
#define MFMA0(a, b, c) asm volatile("v_mfma_f32_16x16x32_bf16 %0, %1, %2, 0" : "=&v"(c) : "v"(a), "v"(b))
#define PIN(x) asm volatile("" : "+v"(x))
#define MFMA_DRAIN() do { asm volatile("s_nop 7\n\ts_nop 7" ::: "memory"); SCHED(); } while (0)

struct Args {
  const u16 *x, *lre, *lim, *bre, *bim, *cre, *cim, *ldt, *dsk, *wg, *wu, *wd, *gw;
  char* ws; float* out;
};

constexpr unsigned L_X = 0, L_W = 65536, L_BS = 131072, L_PRM = 151552  ,
                   L_WA = 153600  , L_FLG = 154624  , L_SINK = 154880, L_TAB = 155136  , LDS_BYTES = 163840;

__device__ __forceinline__ int tj(int j) { return (j & 1) * 8 + ((j >> 1) & 1); }
__device__ __forceinline__ f32x2 cmul(f32x2 a, f32x2 b) { return (f32x2){a.x * b.x - a.y * b.y, a.x * b.y + a.y * b.x}; }
__device__ __forceinline__ f32x2 cfma(f32x2 a, f32x2 b, f32x2 c) {
  return (f32x2){__builtin_fmaf(a.x, b.x, __builtin_fmaf(-a.y, b.y, c.x)), __builtin_fmaf(a.x, b.y, __builtin_fmaf(a.y, b.x, c.y))}; }
__device__ __forceinline__ void sw16(float v, float& r0, float& r1) { auto t = __builtin_amdgcn_permlane16_swap(__float_as_uint(v), __float_as_uint(v), false, false); r0 = __uint_as_float(t[0]); r1 = __uint_as_float(t[1]); }
__device__ __forceinline__ void sw32(float v, float& r0, float& r1) { auto t = __builtin_amdgcn_permlane32_swap(__float_as_uint(v), __float_as_uint(v), false, false); r0 = __uint_as_float(t[0]); r1 = __uint_as_float(t[1]); }

__device__ __forceinline__ void touch(const char* p) {
  asm volatile("s_mov_b32 m0, %0\n\ts_nop 0\n\tglobal_load_lds_dword %1, off" :: "s"(L_SINK), "v"(p) : "memory"); }
template <int GRP>
struct P1 {
  static constexpr int NW = GRP == 0 ? 4 : 3, NS = GRP == 0 ? 0 : 4;
  f32x4 ag[NW][4], au[NW][4], as[4][4];
  bf16x8 xa[4], wgf[NW], wuf[NW], bsf[4];
  lptr aX[2], aB[2], aW[NW];
  unsigned voffX, voffW, voffB;
  unsigned sX, sW, sB, ldsX, ldsW, ldsB;
  rsrc_t rx, rwg, rwu, rb;
  const char* pfp; unsigned pfs; int pfo, pfm;
  __device__ __forceinline__ void pull(int T) { const int i = T + pfo; touch(pfp + pfs * (unsigned)(i < 0 ? 0 : i > pfm ? pfm : i)); }

  template <int BUF, int KS, class M> __device__ __forceinline__ void load(M&& mem) {
    mem(0);
#pragma unroll
    for (int m = 0; m < 4; ++m) xa[m] = lds_b128(aX[KS] + BUF * 32768 + m * 2048);
    mem(1);
#pragma unroll
    for (int i = 0; i < NS; ++i) bsf[i] = lds_b128(aB[KS] + BUF * 8192 + i * 2048);
    if (NS) mem(2);
#pragma unroll
    for (int n = 0; n < NW; ++n) {
      wgf[n] = lds_tr2(aW[n] + BUF * 32768 + KS * 8192, aW[n] + BUF * 32768 + KS * 8192 + 1024);
      wuf[n] = lds_tr2(aW[n] + BUF * 32768 + 16384 + KS * 8192, aW[n] + BUF * 32768 + 16384 + KS * 8192 + 1024);
      mem(n + (NS ? 3 : 2));
    }
  }
  template <int BUF, int KS> __device__ __forceinline__ void load() { load<BUF, KS>([](int) {}); }
  template <int BUF> __device__ __forceinline__ void dmaX1(int T, int s) { dma16(ldsX + BUF * 32768 + s * 2048, voffX, rx, sX + T * 128 + s * 4096); }
  template <int BUF, int HH> __device__ __forceinline__ void dmaW1(int T, int i) {
    dma16(ldsW + BUF * 32768 + (i & 1) * 16384 + HH * 8192 + (i >> 1) * 1024, voffW, (i & 1) ? rwu : rwg, sW + (unsigned)(T * 64 + HH * 32 + (i >> 1) * 4) * 7168u);
  }
  template <int BUF> __device__ __forceinline__ void dmaB1(int T, int pf) { dma16(ldsB + BUF * 8192 + pf * 4096, voffB, rb, sB + pf * 32768 + T * 128); }
  template <int NV, bool FIRST = false> __device__ __forceinline__ void compute() {
    constexpr int NM = 4 * NS + 8 * NW;
    __builtin_amdgcn_s_setprio(1);
#pragma unroll
    for (int k = 0; k < NM; ++k) {
      const int f = k >> 2, m = (f & 1) ? 3 - (k & 3) : (k & 3);
      const int n = f < NS ? 0 : (f - NS) >> 1;
      const bf16x8& a = f < NS ? xa[m] : ((f - NS) & 1) ? wuf[n] : wgf[n];
      const bf16x8& b = f < NS ? bsf[f & 3] : xa[m];
      f32x4& c = f < NS ? as[f & 3][m] : ((f - NS) & 1) ? au[n][m] : ag[n][m];
      if (FIRST) MFMA0(a, b, c); else MFMA(a, b, c);
    }
    if (NV != -2) { if (NV >= 0) WAIT_VN(NV < 0 ? 0 : NV); RBAR(); }
    __builtin_amdgcn_s_setprio(0);
    SCHED();
  }
  template <int BUF> __device__ __forceinline__ void dmaX(int T) {
#pragma unroll
    for (int s = 0; s < 4; ++s) dma16(ldsX + BUF * 32768 + s * 2048, voffX, rx, sX + T * 128 + s * 4096);
  }
  template <int BUF, int HH> __device__ __forceinline__ void dmaW(int T) {
#pragma unroll
    for (int s = 0; s < 2; ++s) {
      const unsigned so = sW + (unsigned)(T * 64 + HH * 32 + s * 4) * 7168u;
      dma16(ldsW + BUF * 32768 + HH * 8192 + s * 1024, voffW, rwg, so);
      dma16(ldsW + BUF * 32768 + 16384 + HH * 8192 + s * 1024, voffW, rwu, so);
    }
  }
  template <int BUF> __device__ __forceinline__ void dmaB(int T) {
#pragma unroll
    for (int pf = 0; pf < 2; ++pf) dma16(ldsB + BUF * 8192 + pf * 4096, voffB, rb, sB + pf * 32768 + T * 128);
  }
};

template <int BUF> __device__ __forceinline__ void p1_tile(P1<0>& w, int T) {
  w.template compute<7>();
  w.template load<BUF, 1>([&](int i) { if (i < 4) w.template dmaW1<BUF ^ 1, 1>(T + 1, i); else if (i == 4) w.pull(T); }); WAIT_L0(); BAR();
  w.template compute<5>();
  w.template load<BUF ^ 1, 0>([&](int i) { if (i < 4) w.template dmaX1<BUF>(T + 2, i); else w.template dmaB1<BUF>(T + 2, i - 4); }); WAIT_L0(); BAR();
}
template <int BUF> __device__ __forceinline__ void p1_tile(P1<1>& w, int T) {
  w.template load<BUF, 0>([&](int i) { if (i >= SO && i < SO + 4) w.template dmaX1<BUF ^ 1>(T + 1, i - SO); }); WAIT_L0(); BAR();
  w.template compute<-1>();
  w.template load<BUF, 1>([&](int i) { if (i >= SO && i < SO + 4) w.template dmaW1<BUF, 0>(T + 2, i - SO); }); WAIT_V(4); WAIT_L0(); BAR();
  w.template compute<-1>();
}

constexpr int NSTEP = 22;
struct P2 {
  f32x4 af[4][4], as[4][4];
  bf16x8 ha[2][4], bb[2][4];
  lptr aH, aS[2], aS2[2], aW[4], aC[2];
  unsigned voffH, voffW, voffC;
  unsigned sH, sHs, sW, sC, ldsA, ldsB, kk, pj[4], ps;
  rsrc_t rh, rw, rcr, rci;
  template <int ST> __device__ __forceinline__ unsigned kf() const {
    constexpr int e = 2 * (ST - 8), k0 = ST < 8 ? 7 * (ST >> 1) + 3 * (ST & 1) + 1 : 7 * (e / 3) + 3 * (e % 3), k1 = ST < 8 ? k0 + 1 : 7 * ((e + 1) / 3) + 3 * ((e + 1) % 3);
    return k0 + kk * (k1 - k0);
  }
  template <int ST, int GA> __device__ __forceinline__ void dmaA(int j) {
    const unsigned so = ST < 14 ? sH + kf<ST>() * 8192u : sHs + ((ST - 14) & 3) * 16384u + ((ST - 14) >> 2) * 262144u;
    dma16_dev(ldsA + GA * 49152 + (ST % 3) * 16384 + j * 1024, voffH, rh, so + GA * (NKF * 8192u) + (ST < 14 ? pj[j] : (unsigned)(j << 10) ^ ps));
  }
  template <int ST> __device__ __forceinline__ void dmaB(int s) {
    const unsigned l = ldsB + (ST % 3) * 16384 + s * 2048;
    if (ST >= 14) dma16(l, voffC, ST < 18 ? rcr : rci, sC + ((ST - 14) & 3) * 128u + s * 32768u);
    else dma16(l, voffW, rw, sW + kf<ST>() * 65536u + s * 16384u);
  }
  template <int ST, int GA> __device__ __forceinline__ void stageA() {
#pragma unroll
    for (int j = 0; j < 4; ++j) dmaA<ST, GA>(j);
  }
  template <int ST> __device__ __forceinline__ void stageB() {
#pragma unroll
    for (int s = 0; s < 4; ++s) dmaB<ST>(s);
  }
  template <int ST, int G> __device__ __forceinline__ void load() {
    constexpr int SL = (ST % 3) * 16384;
#pragma unroll
    for (int k2 = 0; k2 < 2; ++k2) {
#pragma unroll
      for (int n = 0; n < 4; ++n) {
        const int i = k2 * 4 + n;
        if constexpr (G == 0) { if constexpr (ST + 2 < NSTEP) { if (i < 4) dmaB<ST + 2>(i); else if (i < 6) dmaA<ST + 2, 1>(i - 4); } }
        else { if constexpr (ST + 2 < NSTEP) { if (i < 2) dmaA<ST + 2, 1>(i + 2); } if constexpr (ST + 3 < NSTEP) { if (i >= 2 && i < 6) dmaA<ST + 3, 0>(i - 2); } }
        if (ST >= 14) bb[k2][n] = lds_b128(aC[k2] + SL + n * 2048);
        else bb[k2][n] = lds_tr2(aW[n] + SL + k2 * 8192, aW[n] + SL + k2 * 8192 + 4096);
        if (ST >= 14) ha[k2][n] = lds_tr2(aS[n & 1] + SL + k2 * 8192 + (n >> 1) * 1024, aS2[n & 1] + SL + k2 * 8192 + (n >> 1) * 1024);
        else ha[k2][n] = lds_b128(aH + SL + k2 * 8192 + n * 1024);
      }
    }
  }
  template <int ST, int NV> __device__ __forceinline__ void compute() {
    __builtin_amdgcn_s_setprio(1);
#pragma unroll
    for (int k = 0; k < 32; ++k) {
      const int k2 = k >> 4, m = (k >> 2) & 3, n = (m & 1) ? 3 - (k & 3) : (k & 3);
      f32x4& c = ST >= 14 ? as[m][n] : af[m][n];
      if ((ST == 0 || ST == 14) && k2 == 0) MFMA0(bb[k2][n], ha[k2][m], c); else MFMA(bb[k2][n], ha[k2][m], c);
    }
    WAIT_VN(NV); RBAR();
    __builtin_amdgcn_s_setprio(0);
    SCHED();
  }
};
template <int I, int N, class F> __device__ __forceinline__ void static_for(F&& f) {
  if constexpr (I < N) { f(std::integral_constant<int, I>{}); static_for<I + 1, N>(f); }
}

__device__ __forceinline__ void arrive(char* ws, int lane, unsigned flag_off, int ctr, unsigned epoch) {
  if (lane == 0) {
    const unsigned old = __hip_atomic_fetch_add((LAS unsigned*)mk_l(L_FLG + 16 + ctr * 4), 1u, __ATOMIC_RELAXED, __HIP_MEMORY_SCOPE_WORKGROUP);
    if (old == 3u) __hip_atomic_store((GAS unsigned*)(ws + flag_off), epoch, RLX_AGENT);
  }
}
__device__ __forceinline__ void lds_post(int ctr) {
  if (fresh_lane() == 0) __hip_atomic_fetch_add((LAS unsigned*)mk_l(L_FLG + 16 + ctr * 4), 1u, __ATOMIC_RELAXED, __HIP_MEMORY_SCOPE_WORKGROUP);
}
__device__ __forceinline__ void lds_wait(int ctr, unsigned n = 4u) {
  const lptr p = mk_l(L_FLG + 16 + ctr * 4);
_Pragma("nounroll")
  for (unsigned spins = 0; spins < (SPIN_CAP << 6); ++spins) {
    if (__builtin_amdgcn_readfirstlane(*(volatile LAS unsigned*)p) >= n) break;
    __builtin_amdgcn_s_sleep(1);
  }
  asm volatile("" ::: "memory");
}
__device__ __forceinline__ void wait_flags(const char* ws, unsigned off, unsigned epoch) {
_Pragma("nounroll")
  for (unsigned spins = 0; spins < SPIN_CAP; ++spins) {
    if (__all(__hip_atomic_load((const GAS unsigned*)(ws + off), RLX_AGENT) == epoch)) break;
    __builtin_amdgcn_s_sleep(1);
  }
  asm volatile("" ::: "memory");
}

struct Pre { unsigned voffX, voffW, voffB, sX, ldsX, sW, ldsW, sB, ldsB; };
__device__ __forceinline__ Pre mk_pre(int lane, int wid, int bid) {
  Pre pre;
  const int wr = wid & 3, G = wid >> 2, idx = bid >> 3, c = ((bid & 7) >> 1) * 8 + (idx & 7), rch = (bid & 1) * 4 + (idx >> 3);
  const unsigned sw8 = ((lane & 7) ^ (((wr & 1) * 4 + (lane >> 4)) & 7)) * 16;
  pre.voffX = ((lane >> 5) * 16 + tj(lane >> 3)) * 2048 + sw8;
  pre.voffB = (lane >> 3) * 2048 + sw8;
  pre.voffW = (lane >> 4) * 7168 + ((((lane & 15) >> 1) ^ ((lane >> 4) | ((wr & 1) << 2))) * 32) + (lane & 1) * 16;
  pre.sX = (unsigned)(rch * 256 + (G * 2 + (wr >> 1)) * 64 + (wr & 1) * 32) * 2048u;
  pre.ldsX = L_X + (G * 16 + (wr >> 1) * 8 + (wr & 1)) * 1024;
  const int ii = (wr & 1) * 2 + (wr >> 1) * 4;
  pre.sW = (unsigned)(ii * 4) * 7168u + (unsigned)((224 * c) & ~63);
  pre.ldsW = L_W + ii * 1024;
  pre.sB = (unsigned)(c * 32 + (wr & 1) * 8) * 2048u;
  pre.ldsB = L_BS + wr * 1024;
  return pre;
}
template <int GRP>
__device__ __forceinline__ void body(const Args& A, int wid, const char* code, const unsigned code_len) {
  asm volatile("" : "+s"(wid));
  const int xcd = blockIdx.x & 7, idx = blockIdx.x >> 3, c = (xcd >> 1) * 8 + (idx & 7), rch = (xcd & 1) * 4 + (idx >> 3);
  const int xrow = (xcd & 1) * 1024 + (xcd >> 1) * 256 + idx * 8;
  const int kq = xcd >> 1, dt = idx & 7;
  const int tt = 2 * rch + GRP, tile = rch * 8 + dt;
  const unsigned flb = (unsigned)WS_FL + rch * 1024;
  const int wr = wid & 3, wm = wr >> 1, wn = wr & 1;
  int lane = fresh_lane();
  unsigned epoch;
  P2 q;
  u32x4 hq[14]; unsigned ho[3];
  const rsrc_t rws = mk_rsrc(A.ws, (unsigned)WS_END);
  {
  P1<GRP> w;
  constexpr int NW = P1<GRP>::NW, NS = P1<GRP>::NS;
  int r = lane & 15, g = lane >> 4, qq = (lane & 15) >> 2, pp = lane & 3;
  const Pre pre = mk_pre(lane, wid, blockIdx.x);
  w.rx = mk_rsrc(A.x, L_ * D_ * 2); w.rwg = mk_rsrc(A.wg, D_ * F_ * 2); w.rwu = mk_rsrc(A.wu, D_ * F_ * 2);
  w.rb = mk_rsrc((wr >> 1) ? A.bim : A.bre, P_ * D_ * 2);
  w.voffX = pre.voffX; w.voffB = pre.voffB; w.voffW = pre.voffW;
  w.sX = pre.sX; w.ldsX = pre.ldsX; w.sW = pre.sW; w.ldsW = pre.ldsW; w.sB = pre.sB; w.ldsB = pre.ldsB;
  const char* wbase = (const char*)((wr & 1) ? A.wu : A.wg) + (idx * 2 + (wr >> 1)) * 7168 + xcd * 896;
  const char* rbase = wr < 2 ? (const char*)A.x + (xrow + wr * 4) * 2048 : (const char*)(wr == 2 ? A.bre : A.bim) + (xcd * 128 + idx * 4) * 2048;
  if (GRP == 1) {
    w.template dmaW<0, 1>(0); w.template dmaW<1, 0>(1);
    const unsigned co = ((idx * 4 + wr) * 4 + (lane & 3)) * 128u;
    touch(lane < 16 ? wbase + (2 + (lane >> 3)) * 458752u + ((lane & 7) < 7 ? (lane & 7) : 0) * 128 : lane < 24 ? rbase + ((lane - 16) >> 1) * 2048 + (2 + (lane & 1)) * 128
                    : code + (co < code_len ? co : 0u));
  }
  {
#pragma unroll
    for (int ks = 0; ks < 2; ++ks) {
      const unsigned ch = ((g + 4 * ks) ^ ((r >> 1) & 7)) << 4;
      w.aX[ks] = mk_l(L_X + (wr * 64 + r) * 128 + ch);
      if (NS) w.aB[ks] = mk_l(L_BS + r * 128 + ch);
    }
    const unsigned swz = qq | ((g & 1) << 2);
#pragma unroll
    for (int n = 0; n < NW; ++n) {
      unsigned col;
      if (GRP == 0) col = (((2 + n) ^ swz) << 5) + pp * 8;
      else if (n < 2) col = (((((c & 1) ? 6 : 0) + n) ^ swz) << 5) + pp * 8;
      else col = (((((c & 1) ? 0 : 6) + (pp >> 1)) ^ swz) << 5) + (c & 1) * 16 + (pp & 1) * 8;
      w.aW[n] = mk_l(L_W + (8 * g + qq) * 256 + col);
    }
  }
  if constexpr (GRP == 0) {
    {
      const unsigned j = idx * 4 + wr, co = (j * 4 + (lane & 3)) * 128u;
      const int l = lane < 32 ? lane : 31;
      if (l < 7) { w.pfp = wbase + l * 128; w.pfs = 458752u; w.pfo = PFD; w.pfm = 15; }
      else if (l < 15) { w.pfp = rbase + ((l - 7) >> 1) * 2048 + ((l - 7) & 1) * 128; w.pfs = 256u; w.pfo = 2; w.pfm = 7; }
      else if (l < 23) { w.pfp = (const char*)A.wd + (kq * 896 + j) * 2048u + (xcd & 1) * 1024 + (l - 15) * 128; w.pfs = 262144u; w.pfo = -7; w.pfm = 6; }
      else if (l < 27) { w.pfp = (const char*)((j & 1) ? A.cim : A.cre) + ((xcd & 1) * 512 + (j >> 1)) * 2048u + kq * 512 + (l - 23) * 128; w.pfs = 131072u; w.pfo = 0; w.pfm = 7; }
      else { w.pfp = l < 31 ? code + (co < code_len ? co : 0u) : (const char*)((idx & 4) ? A.dsk : A.gw) + ((idx & 3) * 4 + wr) * 128; w.pfs = 0u; w.pfo = 0; w.pfm = 0; }
    }
    WAIT_V(0); BAR();
    w.template load<0, 0>([&](int i) { if (i < 4) w.template dmaX1<1>(1, i); else w.template dmaB1<1>(1, i - 4); });
    WAIT_L0(); BAR();
    w.template compute<-1, true>();
    w.template load<0, 1>([&](int i) { if (i < 4) w.template dmaW1<1, 1>(1, i); else if (i == 4) w.pull(0); }); WAIT_L0(); BAR();
    w.template compute<5>();
    w.template load<1, 0>([&](int i) { if (i < 4) w.template dmaX1<0>(2, i); else w.template dmaB1<0>(2, i - 4); }); WAIT_L0(); BAR();
    p1_tile<1>(w, 1);
  } else {
    WAIT_V(16);
    const lptr rp = mk_l(L_TAB + wr * 2048 + (lane & 31) * 2);
    const float lr = bf2f(*(const LAS u16*)rp), li = bf2f(*(const LAS u16*)(rp + 64)), ld = bf2f(*(const LAS u16*)(rp + 128));
    epoch = __builtin_amdgcn_readfirstlane(*(const LAS unsigned*)mk_l(L_TAB + wr * 2048 + 192)) + 1u;
    const float dt_ = bf2f(pk_bf16(__expf(ld), 0.f) & 0xffffu);
    const float e = __expf(dt_ * lr), rev = __builtin_amdgcn_fractf(dt_ * li * 0.15915494309189535f);
    f32x2 lam = {e * __builtin_amdgcn_cosf(rev), -e * __builtin_amdgcn_sinf(rev)};
    PIN(lam);
    WAIT_V(9); BAR();
    {
      const float nr = lam.x - 1.f, rden = 1.f / (lr * lr + li * li);
      f32x2 l8 = cmul(lam, lam); l8 = cmul(l8, l8); l8 = cmul(l8, l8);
      const f32x2 l16 = cmul(l8, l8), l32 = cmul(l16, l16), l64 = cmul(l32, l32), l128 = cmul(l64, l64);
      f32x2 pw = (wr & 1) ? l64 : (f32x2){1.f, 0.f}; if (wr & 2) pw = cmul(pw, l128);
      const f32x2 q0 = (lane & 32) ? l32 : (f32x2){1.f, 0.f}, q1 = cmul(q0, l16), c0 = cmul(q0, pw), c1 = cmul(q1, pw);
      const lptr tp = mk_l(L_TAB + wr * 2048 + (lane >> 5) * 1024 + (lane & 31) * 16);
      *(LAS f32x4*)tp = (f32x4){q0.x, q0.y, c0.x, c0.y}; *(LAS f32x4*)(tp + 512) = (f32x4){q1.x, q1.y, c1.x, c1.y};
      const lptr pb = mk_l(L_PRM + (lane & 31) * 64);
      if (wr == 0) *(LAS f32x4*)pb = (f32x4){lam.x, lam.y, (nr * lr - lam.y * li) * rden, (-lam.y * lr - nr * li) * rden};
      else if (wr == 1) { *(LAS f32x4*)(pb + 16) = (f32x4){l16.x, l16.y, l32.x, l32.y}; *(LAS f32x4*)(pb + 32) = (f32x4){l8.x, l8.y, l64.x, l64.y}; *(LAS f32x2*)(pb + 48) = cmul(l128, l128); }
      else if (wr == 3) *(LAS unsigned*)mk_l(L_FLG + (lane & 7) * 4) = (lane & 7) == 3 ? epoch : 0u;
    }
    BAR();
    w.template load<0, 0>([&](int i) { if (i >= SO && i < SO + 4) w.template dmaX1<1>(1, i - SO); }); WAIT_V(9); WAIT_L0(); BAR();
    w.template compute<-1, true>();
    w.template load<0, 1>([&](int i) { if (i >= SO && i < SO + 4) w.template dmaW1<0, 0>(2, i - SO); }); WAIT_V(4);
    if (wr == 2) {
      __hip_atomic_store((GAS u64*)(A.ws + WS_AG) + (c * 8 + 7 - rch) * 64 + lane, (u64)(epoch - 1u) << 32, RLX_AGENT);
      if (lane < 6) __hip_atomic_store((GAS unsigned*)(A.ws + flb + lane * 128 + c * 4), epoch - 1u, RLX_AGENT);
      else if (lane >= 32 && (lane & 3)) __hip_atomic_store((GAS unsigned*)(A.ws + WS_XF + ((tile * 4 + (kq ^ (lane & 3))) * 8 + ((lane - 32) >> 2)) * XS) + (lane & 3), epoch - 1u, RLX_AGENT);
    }
    WAIT_L0(); BAR();
    w.template compute<-1>();
    p1_tile<1>(w, 1);
  }
  for (int T = 2; T < 14; T += 2) {
    p1_tile<0>(w, T);
    p1_tile<1>(w, T + 1);
  }
  if (GRP == 0) epoch = __builtin_amdgcn_readfirstlane(*(const LAS unsigned*)mk_l(L_FLG + 12));
  lane = fresh_lane();
  q.rh = mk_rsrc(A.ws + WS_H, (unsigned)H_BYTES); q.rw = mk_rsrc(A.wd, F_ * D_ * 2);
  q.voffW = (lane >> 4) * 2048 + ((((lane & 15) >> 1) ^ (((wr & 1) << 2) | (lane >> 4))) * 32) + (lane & 1) * 16;
  {
    const int i0 = (wr & 1) + (wr >> 1) * 8;
    q.kk = wr >> 1;
    q.sH = (unsigned)(2 * rch * NKF + 28 * kq) * 8192u + (wr & 1) * 4096u;
    q.sW = (unsigned)(28 * kq * 32 + (wr & 1) * 4) * 2048u + dt * 256u;
#pragma unroll
    for (int j = 0; j < 4; ++j) q.pj[j] = (unsigned)(j ^ kq) << 10;
    q.rcr = mk_rsrc(A.cre, D_ * P_ * 2); q.rci = mk_rsrc(A.cim, D_ * P_ * 2);
    q.sHs = (unsigned)(2 * rch * NKF + 112 + 8 * kq + (wr >> 1)) * 8192u + (wr & 1) * 4096u;
    q.sC = (unsigned)(dt * 128 + i0 * 8) * 2048u + kq * 512u;
    q.ps = (unsigned)(kq >> 1) << 10;
    q.ldsA = 49152 + wr * 4096; q.ldsB = i0 * 1024;
  }
  u32x4 gx[4], gg[4];
  const int tg = rch * 256 + c * 8 + wr * 2 + (lane >> 5);
  if constexpr (GRP == 0) {
    w.template compute<7>();
    w.template load<0, 1>(); w.template dmaW<1, 1>(15);
#pragma unroll
    for (int i = 0; i < 4; ++i) gx[i] = *(const u32x4*)(A.x + tg * D_ + i * 256 + (lane & 31) * 8);
    WAIT_L0(); BAR();
    w.template compute<8>();
    w.template load<1, 0>(); q.template stageB<0>();
#pragma unroll
    for (int i = 0; i < 4; ++i) gg[i] = *(const u32x4*)(A.gw + i * 256 + (lane & 31) * 8);
    WAIT_L0(); BAR();
    w.template compute<12>();
    w.template load<1, 1>();
    {
      typedef __bf16 bf2_t __attribute__((ext_vector_type(2)));
      float s0 = 0.f, s1 = 0.f;
#pragma unroll
      for (int i = 0; i < 4; ++i)
#pragma unroll
        for (int j = 0; j < 4; j += 2) {
          const unsigned a0 = gx[i][j], b0 = gg[i][j], a1 = gx[i][j + 1], b1 = gg[i][j + 1];
          s0 = __builtin_amdgcn_fdot2_f32_bf16(__builtin_bit_cast(bf2_t, a0), __builtin_bit_cast(bf2_t, b0), s0, false);
          s1 = __builtin_amdgcn_fdot2_f32_bf16(__builtin_bit_cast(bf2_t, a1), __builtin_bit_cast(bf2_t, b1), s1, false);
        }
      float s = s0 + s1;
      s += dpp<0x128, 0xf>(0.f, s); s += dpp<0x124, 0xf>(0.f, s); s += dpp<0x122, 0xf>(0.f, s); s += dpp<0x121, 0xf>(0.f, s);
      float t0, t1; sw16(s, t0, t1); s = t0 + t1;
      if ((lane & 31) == 0) __hip_atomic_store((GAS unsigned*)(A.ws + WS_G) + tg, __float_as_uint(1.f / (1.f + __expf(-s))), RLX_AGENT);
    }
    WAIT_L0(); BAR();
    w.template compute<-2>();
    __builtin_amdgcn_s_setprio(3);
  } else {
    w.template load<0, 0>(); w.template dmaX<1>(15); WAIT_L0(); BAR();
    w.template compute<-1>();
    w.template load<0, 1>(); WAIT_V(0); WAIT_L0(); BAR();
    w.template compute<-1>();
  }
  lane = fresh_lane(); r = lane & 15; g = lane >> 4; qq = (lane & 15) >> 2; pp = lane & 3;
  const unsigned hb = (unsigned)WS_H + (unsigned)(2 * rch + (wr >> 1)) * NKF * 8192u + (wr & 1) * 4096u;
  const int kf0 = 7 * (c >> 1) + 3 * (c & 1);
  q.voffH = lane * 16;
#define FFN_H(n, m, h) pk_bf16(relu(w.ag[n][m][2 * (h)]) * w.au[n][m][2 * (h)], relu(w.ag[n][m][2 * (h) + 1]) * w.au[n][m][2 * (h) + 1])
#define P2_ADDR() do { q.voffC = (lane >> 3) * 2048 + (((lane & 7) ^ (((wr & 1) * 4 + (lane >> 4)) & 7)) * 16); PIN(q.voffC); \
    q.aH = mk_l(49152 + GRP * 49152 + wm * 4096 + lane * 16); \
    _Pragma("unroll") for (int n = 0; n < 4; ++n) q.aW[n] = mk_l((4 * g + qq) * 256 + (((wn * 4 + n) ^ (4 * (g & 1) + qq)) << 5) + pp * 8); } while (0)
#define PACK_S() do { _Pragma("unroll") for (int m = 0; m < 4; ++m) { hq[m] = (u32x4){FFN_H(0, m, 0), FFN_H(0, m, 1), FFN_H(1, m, 0), FFN_H(1, m, 1)}; PIN(hq[m]); } \
    _Pragma("unroll") for (int m = 0; m < 4; m += 2) {     \
      const auto s0 = __builtin_amdgcn_permlane32_swap(FFN_H(2, m, 0), FFN_H(2, m + 1, 0), false, false); \
      const auto s1 = __builtin_amdgcn_permlane32_swap(FFN_H(2, m, 1), FFN_H(2, m + 1, 1), false, false); \
      hq[4 + m / 2] = (u32x4){s0[0], s1[0], s0[1], s1[1]}; PIN(hq[4 + m / 2]); } \
    ho[0] = lane * 16 + hb + (kf0 + ((c & 1) ? 3 : 0)) * 8192u; \
    ho[1] = (lane & 31) * 16 + (lane >> 5) * 1024 + (c & 1) * 512 + hb + (kf0 + ((c & 1) ? 0 : 3)) * 8192u; \
    ho[2] = (lane ^ ((lane >> 5) << 2)) * 16 + hb + (112 + c) * 8192u; } while (0)
  if constexpr (GRP == 0) {
    MFMA_DRAIN();
#pragma unroll
    for (int f = 0; f < 2; ++f)
#pragma unroll
      for (int m = 0; m < 4; ++m) {
        __builtin_amdgcn_raw_buffer_store_b128((u32x4){FFN_H(2 * f, m, 0), FFN_H(2 * f, m, 1), FFN_H(2 * f + 1, m, 0), FFN_H(2 * f + 1, m, 1)}, rws,
                                               lane * 16 + m * 1024 + hb + (kf0 + 1 + f) * 8192u, 0, 16);
        SCHED();
      }
    WAIT_V(0); arrive(A.ws, lane, flb + (c >> 3) * 128 + c * 4, 0, epoch);
    __builtin_amdgcn_s_setprio(0);
    P2_ADDR();
    if (wr == 0) { wait_flags(A.ws, flb + kq * 160 + (lane & 7) * 4, epoch); lds_post(3); }
    lds_wait(3, 5u);
    q.template stageA<0, 0>(); q.template stageA<0, 1>(); q.template stageA<1, 0>();
  } else {
    bf16x8 xb[4], bs1[4], wg1[3], wu1[3];
#pragma unroll
    for (int m = 0; m < 4; ++m) { w.xa[m] = lds_b128(w.aX[0] + 32768 + m * 2048); xb[m] = lds_b128(w.aX[1] + 32768 + m * 2048); }
#pragma unroll
    for (int i = 0; i < 4; ++i) { w.bsf[i] = lds_b128(w.aB[0] + 8192 + i * 2048); bs1[i] = lds_b128(w.aB[1] + 8192 + i * 2048); }
    w.wgf[0] = lds_tr2(w.aW[0] + 32768, w.aW[0] + 32768 + 1024); w.wuf[0] = lds_tr2(w.aW[0] + 49152, w.aW[0] + 49152 + 1024);
    q.template stageB<1>(); WAIT_L0(); BAR();
    __builtin_amdgcn_s_setprio(1);
#pragma unroll
    for (int i = 0; i < 4; ++i)
#pragma unroll
      for (int m = 0; m < 4; ++m) MFMA(w.xa[m], w.bsf[i], w.as[i][m]);
#pragma unroll
    for (int i = 0; i < 4; ++i)
#pragma unroll
      for (int m = 0; m < 4; ++m) MFMA(xb[m], bs1[i], w.as[i][m]);
#pragma unroll
    for (int m = 0; m < 4; ++m) MFMA(w.wgf[0], w.xa[m], w.ag[0][m]);
#pragma unroll
    for (int m = 0; m < 4; ++m) MFMA(w.wuf[0], w.xa[m], w.au[0][m]);
    __builtin_amdgcn_s_setprio(0);
    BAR();
    f32x4 pl[2], pm[2];
    f32x2 l8[2], cin[2], ea[2], agg[2];
#pragma unroll
    for (int pf = 0; pf < 2; ++pf) {
      const lptr pb = mk_l(L_PRM + (pf * 16 + r) * 64);
      pl[pf] = *(const LAS f32x4*)pb; pm[pf] = *(const LAS f32x4*)(pb + 16); l8[pf] = *(const LAS f32x2*)(pb + 32);
    }
#pragma unroll
    for (int n = 1; n < 3; ++n) { w.wgf[n] = lds_tr2(w.aW[n] + 32768, w.aW[n] + 32768 + 1024); w.wuf[n] = lds_tr2(w.aW[n] + 49152, w.aW[n] + 49152 + 1024); }
    SCHED();
#define PAIR(v, k) ((k) & 1 ? (f32x2){(v)[(k) >> 1][2], (v)[(k) >> 1][3]} : (f32x2){(v)[(k) >> 1][0], (v)[(k) >> 1][1]})
#define CSTEP(P, Q, pf, k) do { const f32x2 lx_ = {pl[pf][0], pl[pf][0]}, ly_ = {pl[pf][1], pl[pf][1]}; \
      const f32x2 a_ = __builtin_elementwise_fma(-ly_, Q, PAIR(w.as[2 * (pf)], k)), b_ = __builtin_elementwise_fma(ly_, P, PAIR(w.as[2 * (pf) + 1], k)); \
      P = __builtin_elementwise_fma(lx_, P, a_); Q = __builtin_elementwise_fma(lx_, Q, b_); } while (0)
    {
#pragma unroll
      for (int pf = 0; pf < 2; ++pf)
#pragma unroll
        for (int m = 0; m < 4; ++m) {
          const f32x4 re = w.as[2 * pf][m], im = w.as[2 * pf + 1][m];
          w.as[2 * pf][m] = re * pl[pf][2] - im * pl[pf][3]; w.as[2 * pf + 1][m] = -(re * pl[pf][3]) - im * pl[pf][2];
          PIN(w.as[2 * pf][m]); PIN(w.as[2 * pf + 1][m]);
        }
      f32x2 P[2], Q[2];
#pragma unroll
      for (int k = 0; k < 8; ++k)
#pragma unroll
        for (int pf = 0; pf < 2; ++pf) {
          if (k == 0) { P[pf] = PAIR(w.as[2 * pf], 0); Q[pf] = PAIR(w.as[2 * pf + 1], 0); } else CSTEP(P[pf], Q[pf], pf, k);
        }
#pragma unroll
      for (int pf = 0; pf < 2; ++pf) {
        ea[pf] = (f32x2){P[pf].x, Q[pf].x};
        const f32x2 e = cfma(l8[pf], ea[pf], (f32x2){P[pf].y, Q[pf].y});
        const f32x2 l16 = {pm[pf][0], pm[pf][1]}, l32 = {pm[pf][2], pm[pf][3]};
        float a0, a1, b0, b1; sw16(e.x, a0, a1); sw16(e.y, b0, b1);
        const f32x2 r0 = {a0, b0};
        const f32x2 xx = cfma(l16, r0, (f32x2){a1, b1});
        float c0, c1, d0, d1; sw32(xx.x, c0, c1); sw32(xx.y, d0, d1);
        const f32x2 s0 = {c0, d0};
        agg[pf] = cfma(l32, s0, (f32x2){c1, d1});
        const f32x2 c3 = cfma(l16, s0, r0);
        cin[pf] = g == 0 ? (f32x2){0.f, 0.f} : g == 1 ? r0 : g == 2 ? s0 : c3;
        PIN(ea[pf]);
      }
    }
    f32x2 aggq = (g & 1) ? agg[1] : agg[0]; PIN(aggq); PIN(cin[0]); PIN(cin[1]);
    if (wr < 3) {
      if (g < 2) *(LAS f32x2*)mk_l(L_WA + ((g * 16 + r) * 4 + wr) * 8) = aggq;
      WAIT_L0();
      *(volatile LAS unsigned*)mk_l(L_FLG + wr * 4) = 1u;
    }
    SCHED();
#pragma unroll
    for (int n = 0; n < 3; ++n) { wg1[n] = lds_tr2(w.aW[n] + 40960, w.aW[n] + 40960 + 1024); wu1[n] = lds_tr2(w.aW[n] + 57344, w.aW[n] + 57344 + 1024); }
    if (wr == 3) {
      const lptr fp = mk_l(L_FLG);
_Pragma("nounroll")
      for (unsigned spins = 0; spins < SPIN_CAP; ++spins) {
        const u32x4 f = *(volatile LAS u32x4*)fp;
        if (__builtin_amdgcn_readfirstlane(f[0] & f[1] & f[2])) break;
      }
      asm volatile("" ::: "memory");
      const lptr ap = mk_l(L_WA + ((g & 1) * 16 + r) * 32);
      const f32x4 a01 = *(const LAS f32x4*)ap; const f32x2 a2 = *(const LAS f32x2*)(ap + 16);
      const f32x2 lw = *(const LAS f32x2*)mk_l(L_PRM + ((g & 1) * 16 + r) * 64 + 40);
      const f32x2 t = cfma(lw, cfma(lw, cfma(lw, (f32x2){a01[0], a01[1]}, (f32x2){a01[2], a01[3]}), a2), aggq);
      if (rch < 7) __hip_atomic_store((GAS u64*)(A.ws + WS_AG) + (c * 8 + 7 - rch) * 64 + lane, ((u64)epoch << 32) | __float_as_uint((g & 2) ? t.y : t.x), RLX_AGENT);
    }
    WAIT_L0(); lds_post(3); SCHED();
    __builtin_amdgcn_s_setprio(1);
#pragma unroll
    for (int n = 1; n < 3; ++n) {
#pragma unroll
      for (int m = 0; m < 4; ++m) MFMA(w.wgf[n], w.xa[m], w.ag[n][m]);
#pragma unroll
      for (int m = 0; m < 4; ++m) MFMA(w.wuf[n], w.xa[m], w.au[n][m]);
    }
#pragma unroll
    for (int n = 0; n < 3; ++n) {
#pragma unroll
      for (int m = 0; m < 4; ++m) MFMA(wg1[n], xb[m], w.ag[n][m]);
#pragma unroll
      for (int m = 0; m < 4; ++m) MFMA(wu1[n], xb[m], w.au[n][m]);
    }
    __builtin_amdgcn_s_setprio(2);
    SCHED();
    lane = fresh_lane(); r = lane & 15; g = lane >> 4; qq = (lane & 15) >> 2; pp = lane & 3;
    q.voffH = lane * 16;
    const rsrc_t rag = mk_rsrc(A.ws + WS_AG + c * 4096, 4096u);
    const unsigned ago = lane * 8 + (8 - rch) * 512;
    u32x2 gv_[7];
#pragma unroll
    for (int cc = 0; cc < 7; ++cc) gv_[cc] = __builtin_amdgcn_raw_buffer_load_b64(rag, ago + cc * 512, 0, 16);
    {
      const lptr fp = mk_l(L_FLG);
_Pragma("nounroll")
      for (unsigned spins = 0; spins < SPIN_CAP; ++spins) {
        const u32x4 f = *(volatile LAS u32x4*)fp;
        if (__builtin_amdgcn_readfirstlane(f[0] & f[1] & f[2])) break;
      }
      asm volatile("" ::: "memory");
    }
    f32x4 p2[2], tb4[2], a01[2]; f32x2 a2[2];
    const f32x2 l256q = *(const LAS f32x2*)mk_l(L_PRM + ((g & 1) * 16 + r) * 64 + 48);
    {
      const lptr pb = mk_l(L_PRM + r * 64), ap = mk_l(L_WA + r * 32), tp = mk_l(L_TAB + wr * 2048 + g * 512 + r * 16);
#pragma unroll
      for (int pf = 0; pf < 2; ++pf) {
        pl[pf] = *(const LAS f32x4*)(pb + pf * 1024); p2[pf] = *(const LAS f32x4*)(pb + pf * 1024 + 32); tb4[pf] = *(const LAS f32x4*)(tp + pf * 256);
        a01[pf] = *(const LAS f32x4*)(ap + pf * 512); a2[pf] = *(const LAS f32x2*)(ap + pf * 512 + 16);
      }
    }
    SCHED();
    PACK_S();
    P2_ADDR();
    PIN(ho[0]); PIN(ho[1]); PIN(ho[2]);
    SCHED();
    f32x2 car[2], cai[2], cmr[2], cmi[2];
#pragma unroll
    for (int pf = 0; pf < 2; ++pf) {
      const f32x2 l8_ = {p2[pf][0], p2[pf][1]}, l64 = {p2[pf][2], p2[pf][3]};
      f32x2 sw = wr > 0 ? (f32x2){a01[pf][0], a01[pf][1]} : (f32x2){0.f, 0.f};
      if (wr > 1) sw = cfma(l64, sw, (f32x2){a01[pf][2], a01[pf][3]});
      if (wr > 2) sw = cfma(l64, sw, a2[pf]);
      const f32x2 ca = cfma((f32x2){tb4[pf][0], tb4[pf][1]}, sw, cin[pf]), cb_ = cfma(l8_, ca, ea[pf]), cm = {tb4[pf][2], tb4[pf][3]}, cn = cmul(l8_, cm);
      car[pf] = (f32x2){ca.x, cb_.x}; cai[pf] = (f32x2){ca.y, cb_.y}; cmr[pf] = (f32x2){cm.x, cn.x}; cmi[pf] = (f32x2){cm.y, cn.y};
      PIN(car[pf]); PIN(cai[pf]); PIN(cmr[pf]); PIN(cmi[pf]);
    }
_Pragma("nounroll")
    for (unsigned spins = 0; spins < SPIN_CAP; ++spins) {
      __builtin_amdgcn_s_waitcnt(0x0F70);
      unsigned bad = 0;
#pragma unroll
      for (int cc = 0; cc < 7; ++cc) bad |= gv_[cc][1] ^ (cc < rch ? epoch : 0u);
      if (__all(bad == 0u)) break;
      __builtin_amdgcn_s_sleep(1);
      asm volatile("" ::: "memory");
#pragma unroll
      for (int cc = 0; cc < 7; ++cc) gv_[cc] = __builtin_amdgcn_raw_buffer_load_b64(rag, ago + cc * 512, 0, 16);
    }
    __builtin_amdgcn_s_waitcnt(0x0F70);
    f32x2 cb;
    {
      float are[7], aim[7];
#pragma unroll
      for (int cc = 0; cc < 7; ++cc) { const unsigned v = gv_[cc][0]; sw32(__uint_as_float(v), are[cc], aim[cc]); }
      cb = (f32x2){are[6], aim[6]};
#pragma unroll
      for (int cc = 5; cc >= 0; --cc) cb = cfma(l256q, cb, (f32x2){are[cc], aim[cc]});
    }
    unsigned hr[2][8], hi[2][8];
    {
      float cx[2], cy[2]; sw16(cb.x, cx[0], cx[1]); sw16(cb.y, cy[0], cy[1]);
      f32x2 P[2], Q[2];
#pragma unroll
      for (int pf = 0; pf < 2; ++pf) { P[pf] = car[pf] + cmr[pf] * cx[pf] - cmi[pf] * cy[pf]; Q[pf] = cai[pf] + cmr[pf] * cy[pf] + cmi[pf] * cx[pf]; }
#pragma unroll
      for (int k = 0; k < 8; ++k)
#pragma unroll
        for (int pf = 0; pf < 2; ++pf) { CSTEP(P[pf], Q[pf], pf, k); hr[pf][k] = pk_bf16(P[pf].x, P[pf].y); hi[pf][k] = pk_bf16(Q[pf].x, Q[pf].y); }
    }
#pragma unroll
    for (int i = 0; i < 8; ++i) {
      const unsigned* h = (i >> 2) ? hi[(i >> 1) & 1] : hr[(i >> 1) & 1];
      hq[6 + i] = (u32x4){h[(i & 1) * 4], h[(i & 1) * 4 + 1], h[(i & 1) * 4 + 2], h[(i & 1) * 4 + 3]}; PIN(hq[6 + i]);
    }
    WAIT_V(0); lds_wait(1);
    q.template stageA<1, 1>(); q.template stageA<2, 0>();
  }
  }
  constexpr int G = GRP;
  int r, g;
  unsigned fl = 0u;
#define FLAG_LOAD(off) asm volatile("global_load_dword %0, %1, %2 sc1" : "+v"(fl) : "v"(off), "s"(A.ws) : "memory")
#define FLAG_CHECK(off) do { PIN(fl); if (!__all(fl == epoch)) wait_flags(A.ws, off, epoch); } while (0)
#define HQ_ST(i) __builtin_amdgcn_raw_buffer_store_b128(hq[i], rws, (i) < 4 ? ho[0] + (i) * 1024 : (i) < 6 ? ho[1] + ((i) - 4) * 2048 : ho[2] + (((i) - 6) >> 2) * 262144 + (((i) - 6) & 3) * 1024, 0, 16)
#define XTRA(st) ((st) == 13 ? 4 : 0)
  float gv[4];
  u32x2 xv[4], dv; float gr[4];
  const int t0 = tt * 128 + wm * 64 + kq * 2, dc0 = dt * 128 + wn * 64;
  auto side = [&](auto ST_) {
    constexpr int ST = decltype(ST_)::value;
    if constexpr (ST == 13) {
      const int l = fresh_lane(), g_ = l >> 4, q_ = (l & 15) >> 2, p_ = l & 3;
#pragma unroll
      for (int e = 0; e < 2; ++e) {
        const unsigned b = 49152 + G * 49152 + wm * 4096 + (g_ >> 1) * 2048 + p_ * 256 + 8 * (g_ & 1) * 16 + q_ * 16 + (e ^ (kq & 1)) * 8;
        q.aS[e] = mk_l(b + ((p_ >> 1) << 6)); q.aS2[e] = mk_l(b + (((p_ >> 1) ^ 1) << 6));
        q.aC[e] = mk_l((wn * 64 + (l & 15)) * 128 + (((g_ + 4 * e) ^ (((l & 15) >> 1) & 7)) << 4));
      }
    }
    if constexpr (ST == 13) {
      const int l = fresh_lane();
      const unsigned go = (unsigned)WS_G + (tt * 128 + wm * 64 + ((l & 15) >> 2) * 16 + tj(l)) * 4;
#pragma unroll
      for (int j = 0; j < 4; ++j) asm volatile("global_load_dword %0, %1, %2 sc1" : "=v"(gv[j]) : "v"(go + (q.pj[j] >> 7)), "s"(A.ws) : "memory");
    }
    if constexpr (ST >= 16 && ST < 20) {
      constexpr int m = ST - 16;
      PIN(gv[m]);
#pragma unroll
      for (int n = 0; n < 4; ++n) { q.af[m][n] *= 1.f - gv[m]; PIN(q.af[m][n]); }
    }
    if constexpr (ST == NSTEP - 1) {
      const int l = fresh_lane();
      dv = *(const u32x2*)(A.dsk + dc0 + (l & 15) * 4);
#pragma unroll
      for (int i = 0; i < 4; ++i) {
        xv[i] = *(const u32x2*)(A.x + (t0 + i * 16 + tj(l >> 4)) * D_ + dc0 + (l & 15) * 4);
        gr[i] = __int_as_float(__builtin_amdgcn_ds_bpermute((4 * i + (l >> 4)) * 4, __float_as_int(gv[0])));
      }
#pragma unroll
      for (int j = 0; j < 4; ++j) { gv[j] *= 2.f; PIN(gv[j]); }
    }
  };
  __builtin_amdgcn_s_setprio(0);
  if constexpr (G == 0) {
    const unsigned f2 = flb + 512 + (8 * kq + (lane & 7)) * 4, f3 = flb + 640 + (lane & 31) * 4;
    WAIT_V(8); lds_post(1); lds_wait(1);
    q.template load<0, 0>(); WAIT_V(10); WAIT_L0(); lds_post(2); SCHED();
    static_for<0, NSTEP - 1>([&](auto ST_) {
      constexpr int ST = decltype(ST_)::value;
      constexpr int prev = (ST + 2 < NSTEP ? 6 : 0) + (ST == 4 || ST == 10 ? 1 : 0) + XTRA(ST);
      q.template compute<ST, prev>();
      q.template load<ST + 1, 0>();
      if constexpr (ST + 1 == 4) FLAG_LOAD(f2);
      if constexpr (ST + 1 == 5) { WAIT_V(6); FLAG_CHECK(f2); }
      if constexpr (ST + 1 == 10) FLAG_LOAD(f3);
      if constexpr (ST + 1 == 11) { WAIT_V(6); FLAG_CHECK(f3); }
      if constexpr (ST + 1 == 20) { if (blockIdx.x == 255 && wr == 0 && lane == 0) __hip_atomic_store((GAS unsigned*)(A.ws + WS_EP), epoch, RLX_AGENT); }
      side(std::integral_constant<int, ST + 1>{});
      WAIT_L0(); BAR();
    });
  } else {
    HQ_ST(0); HQ_ST(1); HQ_ST(2);
    lds_wait(2);
    static_for<0, NSTEP>([&](auto ST_) {
      constexpr int ST = decltype(ST_)::value;
      constexpr int nst = ST == 0 ? 3 : ST <= 4 ? 2 : 0;
      if constexpr (ST == 2 || ST == 6) {
        if (wr == 0 && lane == 0) __hip_atomic_store((GAS unsigned*)(A.ws + flb + (ST == 2 ? 512 : 640) + c * 4), epoch, RLX_AGENT);
      }
      q.template load<ST, 1>();
      if constexpr (ST == 0) { HQ_ST(3); HQ_ST(4); HQ_ST(5); }
      if constexpr (ST >= 1 && ST <= 4) { HQ_ST(4 + 2 * ST); HQ_ST(5 + 2 * ST); }
      side(ST_);
      WAIT_L0(); BAR();
      if constexpr (ST + 1 < NSTEP) {
        q.template compute<ST, (ST + 2 < NSTEP ? 2 : 0) + (ST + 3 < NSTEP ? 4 : 0) + nst + XTRA(ST)>();
      }
    });
  }
  lane = fresh_lane(); r = lane & 15; g = lane >> 4;
  f32x4 o[4];
  {
    unsigned xo[3], pk[4];
#pragma unroll
    for (int j = 1; j < 4; ++j) xo[j - 1] = (unsigned)WS_XB + ((tile * 4 + (kq ^ j)) * 3 + (j - 1)) * 16384u + wid * 2048 + lane * 16;
    __builtin_amdgcn_s_setprio(1);
#pragma unroll
    for (int k = 0; k < 19; ++k) {
      if (k < 16) { const int m = ((k >> 2) + 1) & 3, n = k & 3; MFMA(q.bb[0][n], q.ha[0][m], q.as[m][n]); MFMA(q.bb[1][n], q.ha[1][m], q.as[m][n]); }
      if (k == 3 || k >= 16) asm volatile("s_nop 3");
      if (G == 0 && k == 14) RBAR();
      SCHED();
      if (k >= 3) {
        const int m = (((k - 3) >> 2) + 1) & 3, n = (k - 3) & 3;
        f32x4 v;
#pragma unroll
        for (int j = 0; j < 4; ++j) v[j] = __builtin_fmaf(q.as[m][n][j], gv[m], q.af[m][n][j]);
        if (m == 0) o[n] = v;
        else {
          pk[(n & 1) * 2] = pk_bf16(v[0], v[1]); pk[(n & 1) * 2 + 1] = pk_bf16(v[2], v[3]);
          if (n & 1) __builtin_amdgcn_raw_buffer_store_b128((u32x4){pk[0], pk[1], pk[2], pk[3]}, rws, xo[m - 1] + (n >> 1) * 1024, 0, 16);
        }
      }
      SCHED();
    }
    __builtin_amdgcn_s_setprio(0);
  }
  WAIT_V(0);
  if (lane >= 1 && lane < 4) __hip_atomic_store((GAS unsigned*)(A.ws + WS_XF + ((tile * 4 + (kq ^ lane)) * 8 + wid) * XS) + lane, epoch, RLX_AGENT);
  f32x4 sk[4];
  {
    const f32x4 df = {bf2f(dv[0] & 0xffffu), bfhi(dv[0]), bf2f(dv[1] & 0xffffu), bfhi(dv[1])};
#pragma unroll
    for (int i = 0; i < 4; ++i) { sk[i] = (f32x4){bf2f(xv[i][0] & 0xffffu), bfhi(xv[i][0]), bf2f(xv[i][1] & 0xffffu), bfhi(xv[i][1])} * df * gr[i]; PIN(sk[i]); }
  }
  const lptr tw = mk_l(49152 + G * 49152 + wr * 4096 + r * 256), tr_ = mk_l(49152 + G * 49152 + wr * 4096 + g * 256);
  wait_flags(A.ws, (unsigned)WS_XF + ((tile * 4 + kq) * 8 + wid) * XS + (1 + (lane & 3) % 3) * 4, epoch);
  {
    u32x4 pv[6];
    const unsigned xi = (unsigned)WS_XB + (tile * 4 + kq) * 3 * 16384u + wid * 2048 + lane * 16;
#pragma unroll
    for (int i = 0; i < 6; ++i) pv[i] = __builtin_amdgcn_raw_buffer_load_b128(rws, xi + (i >> 1) * 16384 + (i & 1) * 1024, 0, 16);
#pragma unroll
    for (int i = 0; i < 6; ++i)
#pragma unroll
      for (int h = 0; h < 2; ++h) {
        const unsigned a0 = pv[i][2 * h], a1 = pv[i][2 * h + 1];
        o[(i & 1) * 2 + h] += (f32x4){bf2f(a0 & 0xffffu), bfhi(a0), bf2f(a1 & 0xffffu), bfhi(a1)};
      }
  }
#pragma unroll
  for (int n = 0; n < 4; ++n) *(LAS f32x4*)(tw + (((n * 4 + g) ^ (r & 7)) << 4)) = o[n];
  WAIT_L0();
#pragma unroll
  for (int i = 0; i < 4; ++i) {
    const f32x4 v = *(const LAS f32x4*)(tr_ + i * 1024 + ((r ^ ((4 * i + g) & 7)) << 4));
    __builtin_amdgcn_raw_buffer_store_b128(__builtin_bit_cast(u32x4, v + sk[i]), mk_rsrc(A.out, L_ * D_ * 4), ((t0 + i * 16 + tj(g)) * D_ + dc0 + r * 4) * 4, 0, 17);
  }
  if (GRP == 0) asm volatile(".Lgm_end0:"); else asm volatile(".Lgm_end1:");
  __builtin_amdgcn_endpgm();
  __builtin_unreachable();
}

__global__ __launch_bounds__(512, 2) void fused_kernel(Args A) {
  const int wid = __builtin_amdgcn_readfirstlane(threadIdx.x >> 6);
  {
    const Pre pre = mk_pre(threadIdx.x & 63, wid, blockIdx.x);
    const int G = wid >> 2;
    if (G) {
      const int l = threadIdx.x & 63;
      const char* p = (l < 16 ? (const char*)A.lre : l < 32 ? (const char*)A.lim : (const char*)A.ldt) + (pre.sB >> 16) * 64 + (l & 15) * 4;
      if (l >= 48) p = A.ws + WS_EP;
      asm volatile("s_mov_b32 m0, %0\n\ts_nop 0\n\tglobal_load_lds_dword %1, off sc1" :: "s"(L_TAB + (wid & 3) * 2048), "v"(p) : "memory");
    }
    const rsrc_t rx = mk_rsrc(A.x, L_ * D_ * 2);
#pragma unroll
    for (int s = 0; s < 4; ++s) dma16(pre.ldsX + s * 2048, pre.voffX, rx, pre.sX + s * 4096);
    dma16(pre.ldsW + G * 1024, pre.voffW, mk_rsrc(A.wg, D_ * F_ * 2), pre.sW + G * 28672u);
    dma16(pre.ldsW + 16384 + G * 1024, pre.voffW, mk_rsrc(A.wu, D_ * F_ * 2), pre.sW + G * 28672u);
    dma16(pre.ldsB + G * 4096, pre.voffB, mk_rsrc((wid & 2) ? A.bim : A.bre, P_ * D_ * 2), pre.sB + G * 32768);
  }
  const char* code; unsigned code_len;
  asm volatile("s_getpc_b64 %0\n.Lgm_pc:\n\ts_mov_b32 %1, .Lgm_end0-.Lgm_pc\n\ts_max_u32 %1, %1, .Lgm_end1-.Lgm_pc" : "=s"(code), "=s"(code_len) :: "scc");
  if (wid < 4) body<0>(A, wid, code, code_len); else body<1>(A, wid, code, code_len);
}

extern "C" void kernel_launch(void* const* d_in, const int* in_sizes, int n_in, void* d_out, int out_size, void* d_ws, size_t ws_size,
                              hipStream_t stream) {
  Args a; a.x = (const u16*)d_in[0]; a.lre = (const u16*)d_in[1]; a.lim = (const u16*)d_in[2]; a.bre = (const u16*)d_in[3];
  a.bim = (const u16*)d_in[4]; a.cre = (const u16*)d_in[5]; a.cim = (const u16*)d_in[6]; a.ldt = (const u16*)d_in[7]; a.dsk = (const u16*)d_in[8];
  a.wg = (const u16*)d_in[10]; a.wu = (const u16*)d_in[11]; a.wd = (const u16*)d_in[12]; a.gw = (const u16*)d_in[13];
  a.ws = (char*)d_ws; a.out = (float*)d_out;
  (void)hipFuncSetAttribute((const void*)fused_kernel, hipFuncAttributeMaxDynamicSharedMemorySize, LDS_BYTES);
  fused_kernel<<<256, 512, LDS_BYTES, stream>>>(a);
}
```
